```python
import torch

def _softmax(x, axis=-1):
    return torch.softmax(x, dim=axis)
def _gelu(x):
    return torch.nn.functional.gelu(x, approximate="tanh")
def _silu(x):
    return torch.nn.functional.silu(x)
def _sigmoid(x):
    return torch.sigmoid(x)
def _layernorm(x, axis=-1, eps=1e-5):
    m = x.mean(dim=axis, keepdim=True)
    v = x.var(dim=axis, keepdim=True, unbiased=False)
    return (x - m) / torch.sqrt(v + eps)
def _scatter_add(tgt, idx, src, axis=0):
    return tgt.clone().index_add_(axis, idx, src)
def _segment_sum(src, seg, n):
    out = torch.zeros((n,) + src.shape[1:], dtype=src.dtype)
    return out.index_add_(0, seg, src)
def _pad(x, pw):
    flat = tuple(v for pair in reversed(pw) for v in pair)
    return torch.nn.functional.pad(x, flat)

kernel_name = 'recurrent_scan__sliding_block_cached__dense_activation_relu__prompt'

def setup_inputs():

    _w = (lambda a: (lambda t: t.to(torch.bfloat16) if t.is_floating_point() else t)(torch.as_tensor(a)))
    def _A_setup():
        B, Sq, D = 1, 2048, 576
        Hq, Hkv, Sk, Dh = 32, 16, 2048, 128
        assert Hq % Hkv == 0, (Hq, Hkv)
        return {
            "x": _w((lambda *s: torch.randn(*s, dtype=torch.bfloat16) * 0.05)(B, Sq, D)),
            "k_cache": _w((lambda *s: torch.randn(*s, dtype=torch.bfloat16) * 0.8)(B, Hkv, Sk, Dh)),
            "v_cache": _w((lambda *s: torch.randn(*s, dtype=torch.bfloat16) * 0.2)(B, Hkv, Sk, Dh)),
            "wq": _w((lambda *s: torch.randn(*s, dtype=torch.bfloat16) * 0.8)(D, Hq * Dh) * (D ** -0.5)),
            "wo": _w((lambda *s: torch.randn(*s, dtype=torch.bfloat16) * 0.2)(Hq * Dh, D) * ((Hq * Dh) ** -0.5)),
        }
    _A_in = _A_setup()

    def _B_setup():
        B, S, D, Dff = 1, 2048, 576, 4608
        return {
            "x": _w((lambda *s: torch.randn(*s, dtype=torch.bfloat16) * 0.05)(B, S, D)),
            "w1": _w((lambda *s: torch.randn(*s, dtype=torch.bfloat16) * 0.1)(D, Dff)),
            "w2": _w((lambda *s: torch.randn(*s, dtype=torch.bfloat16) * 0.1)(Dff, D)),
        }
    _B_in = _B_setup()

    return {
        "A_x": _A_in["x"],
        "A_k_cache": _A_in["k_cache"],
        "A_v_cache": _A_in["v_cache"],
        "A_wq": _A_in["wq"],
        "A_wo": _A_in["wo"],
        "B_x": _B_in["x"],
        "B_w1": _B_in["w1"],
        "B_w2": _B_in["w2"],
    }

def reference(A_x, A_k_cache, A_v_cache, A_wq, A_wo, B_x, B_w1, B_w2):
    def _A_ref(x, k_cache, v_cache, wq, wo):
        B, Sq, D = x.shape
        Hq, Dh = 32, 128
        Hkv, Sk = k_cache.shape[1], k_cache.shape[2]
        W = min(4096, max(256, Sk // 4))
        rep = Hq // Hkv
        q = (lambda x, *d: x.permute(*d))((x @ wq).reshape(B, Sq, Hq, Dh), 0, 2, 1, 3)
        k = (lambda x, n, axis: torch.repeat_interleave(x, n, dim=axis))(k_cache[:, :, Sk - W:, :], rep, axis=1)
        v = (lambda x, n, axis: torch.repeat_interleave(x, n, dim=axis))(v_cache[:, :, Sk - W:, :], rep, axis=1)
        scores = torch.einsum("bhqd,bhkd->bhqk", q, k) * (Dh ** -0.5)
        p = _softmax(scores, axis=-1)
        o = torch.einsum("bhqk,bhkd->bhqd", p, v)
        return (lambda x, *d: x.permute(*d))(o, 0, 2, 1, 3).reshape(B, Sq, Hq * Dh) @ wo

    def _B_ref(x, w1, w2):
        h = torch.einsum("bsd,df->bsf", x, w1)
        zero = torch.zeros_like(h)
        h = torch.maximum(h, zero)
        return torch.einsum("bsf,fd->bsd", h, w2)

    x = A_x
    xn = _layernorm(x, axis=-1)
    s = _A_ref(xn, A_k_cache, A_v_cache, A_wq, A_wo)
    g = _silu(_B_ref(xn, B_w1, B_w2))
    return x + s * g
```

```cpp
#include <hip/hip_runtime.h>
#include <hip/hip_bf16.h>
typedef unsigned short u16;
typedef __attribute__((ext_vector_type(8))) __bf16 bf16x8;
typedef __attribute__((ext_vector_type(16))) float f32x16;
__device__ __forceinline__ float b2f(u16 v){ return __uint_as_float(((unsigned)v)<<16); }
__device__ __forceinline__ u16 f2b(float f){ __bf16 b=(__bf16)f; return __builtin_bit_cast(u16,b); }
__device__ __forceinline__ float rb(float f){ return b2f(f2b(f)); }

__global__ void ln_kernel(const u16* x, u16* xn){
  int row = blockIdx.x*4 + (threadIdx.x>>6), lane = threadIdx.x&63;
  float v[9]; float s=0;
  for(int i=0;i<9;i++){ v[i]=b2f(x[row*576+lane+64*i]); s+=v[i]; }
  for(int o=32;o;o>>=1) s+=__shfl_xor(s,o);
  float m=s/576.f; float q=0;
  for(int i=0;i<9;i++){ float d=v[i]-m; q+=d*d; }
  for(int o=32;o;o>>=1) q+=__shfl_xor(q,o);
  float mb=rb(m), vb=rb(q/576.f);
  float den=rb(sqrtf(rb(vb+1e-5f)));
  for(int i=0;i<9;i++) xn[row*576+lane+64*i]=f2b(rb(v[i]-mb)/den);
}
template<bool BT,int EPI>
__global__ __launch_bounds__(256) void gemm(const u16* A,long lda,long sA,const u16* B,long ldb,long sB,int bdiv,void* C,long ldc,long sC,int K,float scale){
  __shared__ u16 As[64][40]; __shared__ u16 Bs[64][40];
  int z=blockIdx.z; A+=z*sA; B+=(z/bdiv)*sB;
  int m0=blockIdx.y*64,n0=blockIdx.x*64,t=threadIdx.x,w=t>>6,l=t&63,wr=w>>1,wc=w&1;
  f32x16 acc={};
  for(int k0=0;k0<K;k0+=32){
    { int r=t>>2,c=(t&3)*8; *(uint4*)&As[r][c]=*(const uint4*)&A[(long)(m0+r)*lda+k0+c]; }
    if(BT){ int r=t>>2,c=(t&3)*8; *(uint4*)&Bs[r][c]=*(const uint4*)&B[(long)(n0+r)*ldb+k0+c]; }
    else { int k=t>>3,c=(t&7)*8; uint4 v=*(const uint4*)&B[(long)(k0+k)*ldb+n0+c]; u16* p=(u16*)&v; for(int i=0;i<8;i++) Bs[c+i][k]=p[i]; }
    __syncthreads();
    for(int kk=0;kk<2;kk++){
      bf16x8 a=*(bf16x8*)&As[wr*32+(l&31)][kk*16+(l>>5)*8];
      bf16x8 b=*(bf16x8*)&Bs[wc*32+(l&31)][kk*16+(l>>5)*8];
      acc=__builtin_amdgcn_mfma_f32_32x32x16_bf16(a,b,acc,0,0,0);
    }
    __syncthreads();
  }
  for(int r=0;r<16;r++){
    int row=m0+wr*32+(r&3)+8*(r>>2)+4*(l>>5), col=n0+wc*32+(l&31);
    long idx=z*sC+(long)row*ldc+col;
    if(EPI==0) ((u16*)C)[idx]=f2b(acc[r]);
    else if(EPI==1) ((u16*)C)[idx]=f2b(fmaxf(acc[r],0.f));
    else ((float*)C)[idx]=rb(rb(acc[r])*scale);
  }
}
__global__ void softmax_k(const float* S,u16* P){
  long row = blockIdx.x*4L + (threadIdx.x>>6); int lane=threadIdx.x&63;
  float v[8],m=-1e30f; for(int i=0;i<8;i++){ v[i]=S[row*512+lane+64*i]; m=fmaxf(m,v[i]); }
  for(int o=32;o;o>>=1) m=fmaxf(m,__shfl_xor(m,o));
  float s=0; for(int i=0;i<8;i++){ v[i]=__expf(v[i]-m); s+=v[i]; }
  for(int o=32;o;o>>=1) s+=__shfl_xor(s,o);
  for(int i=0;i<8;i++) P[row*512+lane+64*i]=f2b(v[i]/s);
}
__global__ void fin_k(const u16* x,const u16* s,const u16* g,u16* out,int n){
  int i=blockIdx.x*256+threadIdx.x; if(i>=n) return;
  float gv=b2f(g[i]); float sl=rb(gv/(1.f+__expf(-gv)));
  out[i]=f2b(b2f(x[i])+rb(b2f(s[i])*sl));
}
extern "C" void kernel_launch(void* const* d_in, const int* in_sizes, int n_in, void* d_out, int out_size, void* d_ws, size_t ws_size, hipStream_t stream) {
  const u16* x=(const u16*)d_in[0]; const u16* kc=(const u16*)d_in[1]; const u16* vc=(const u16*)d_in[2];
  const u16* wq=(const u16*)d_in[3]; const u16* wo=(const u16*)d_in[4]; const u16* w1=(const u16*)d_in[6]; const u16* w2=(const u16*)d_in[7];
  char* ws=(char*)d_ws;
  u16* xn=(u16*)ws; ws+=2048*576*2;
  u16* q=(u16*)ws; ws+=2048L*4096*2;
  u16* o=q==0?0:(u16*)ws; ws+=2048L*4096*2;
  u16* h=(u16*)ws; ws+=2048L*4608*2;
  u16* s=(u16*)ws; ws+=2048*576*2;
  u16* g=(u16*)ws; ws+=2048*576*2;
  u16* P=(u16*)ws; ws+=32L*2048*512*2;
  float* S=(float*)ws;
  ln_kernel<<<512,256,0,stream>>>(x,xn);
  gemm<false,0><<<dim3(64,32,1),256,0,stream>>>(xn,576,0,wq,4096,0,1,q,4096,0,576,1.f);
  gemm<true,2><<<dim3(8,32,32),256,0,stream>>>(q,4096,128,kc+1536*128,128,2048L*128,2,S,512,2048L*512,128,0.08838834764f);
  softmax_k<<<32*2048/4,256,0,stream>>>(S,P);
  gemm<false,0><<<dim3(2,32,32),256,0,stream>>>(P,512,2048L*512,vc+1536*128,128,2048L*128,2,o,4096,128,512,1.f);
  gemm<false,0><<<dim3(9,32,1),256,0,stream>>>(o,4096,0,wo,576,0,1,s,576,0,4096,1.f);
  gemm<false,1><<<dim3(72,32,1),256,0,stream>>>(xn,576,0,w1,4608,0,1,h,4608,0,576,1.f);
  gemm<false,0><<<dim3(9,32,1),256,0,stream>>>(h,4608,0,w2,576,0,1,g,576,0,4608,1.f);
  fin_k<<<(2048*576+255)/256,256,0,stream>>>(x,s,g,(u16*)d_out,2048*576);
}
```

```cpp
#include <hip/hip_runtime.h>
#include <hip/hip_bf16.h>
constexpr size_t WS_BASE = (size_t)0 << 20;
constexpr size_t WS_WP = 38010880, WS_FL = 73203712, WS_DBG = 73269248, FL_END = 49216;
#define NST1 0
#define STNAMES ""
#define LO(p) ((unsigned)(size_t)(p))
#define RFL(x) __builtin_amdgcn_readfirstlane(x)
#define HI(p) ((unsigned)((size_t)(p) >> 32))
__global__ __launch_bounds__(256, 1) void km_main(void* ws, const void* w1, const void* wq, const void* kc, const void* vc, const void* wo, const void* w2, const void* x, void* out) {
  const unsigned w = __builtin_amdgcn_readfirstlane(threadIdx.x >> 6);
  asm volatile(
  "km_code:\n"
  "s_getpc_b64 s[26:27]\n"
  "v_mbcnt_lo_u32_b32 v0, -1, 0\n"
  "v_mbcnt_hi_u32_b32 v0, -1, v0\n"
  "s_mov_b32 s56, %14\n"
  "s_and_b32 s57, %15, 0xffff\n"
  "s_mov_b32 s58, 2359296\n"
  "s_mov_b32 s59, 0x20000\n"
  "s_mov_b32 s82, %18\n"
  "s_mov_b32 s93, %19\n"
  "s_lshl_b32 s79, s93, 2\n"
  "s_add_u32 s79, s79, s82\n"
  "s_mul_i32 s79, s79, 2304\n"
  "v_lshrrev_b32 v140, 5, v0\n"
  "v_and_b32 v141, 31, v0\n"
  "v_mul_u32_u24 v142, 1152, v140\n"
  "v_lshl_add_u32 v142, v141, 3, v142\n"
  "buffer_load_dwordx2 v[100:101], v142, s[56:59], s79 offen offset:0\n"
  "buffer_load_dwordx2 v[102:103], v142, s[56:59], s79 offen offset:256\n"
  "buffer_load_dwordx2 v[104:105], v142, s[56:59], s79 offen offset:512\n"
  "buffer_load_dwordx2 v[106:107], v142, s[56:59], s79 offen offset:768\n"
  "v_cmp_gt_u32 s[28:29], 16, v141\n"
  "v_mov_b32 v143, 0x7ffffff0\n"
  "s_nop 0\n"
  "v_cndmask_b32 v143, v143, v142, s[28:29]\n"
  "buffer_load_dwordx2 v[108:109], v143, s[56:59], s79 offen offset:1024\n"
  "s_mov_b32 s36, %0\n"
  "s_and_b32 s37, %1, 0xffff\n"
  "s_mov_b32 s38, 74317824\n"
  "s_mov_b32 s39, 0x20000\n"
  "v_mov_b32 v144, 73252864\n"
  "buffer_load_dword v145, v144, s[36:39], 0 offen offset:0 sc1\n"
  "v_lshlrev_b32 v1, 4, v0\n"
  "s_mov_b32 s40, %2\n"
  "s_and_b32 s41, %3, 0xffff\n"
  "s_mov_b32 s42, 5308416\n"
  "s_mov_b32 s43, 0x20000\n"
  "s_mov_b32 s44, %4\n"
  "s_and_b32 s45, %5, 0xffff\n"
  "s_mov_b32 s46, 4718592\n"
  "s_mov_b32 s47, 0x20000\n"
  "s_mov_b32 s48, %6\n"
  "s_and_b32 s49, %7, 0xffff\n"
  "s_mov_b32 s50, 8388608\n"
  "s_mov_b32 s51, 0x20000\n"
  "s_mov_b32 s52, %8\n"
  "s_and_b32 s53, %9, 0xffff\n"
  "s_mov_b32 s54, 8388608\n"
  "s_mov_b32 s55, 0x20000\n"
  "s_lshr_b32 s79, s93, 3\n"
  "s_lshl_b32 s79, s79, 6\n"
  "v_add_u32 v149, s79, v0\n"
  "s_mov_b32 s84, s26\n"
  "s_and_b32 s85, s27, 0xffff\n"
  "s_mov_b32 s87, 0x20000\n"
  "s_cmp_eq_u32 s82, 3\n s_cselect_b32 s86, 69632, 0\n"
  "v_lshlrev_b32 v146, 7, v149\n"
  "buffer_load_dword v219, v146, s[84:87], 0 offen offset:0\n"
  "s_lshl_b32 s80, s82, 11\n"
  "s_add_u32 s80, s80, 8192\n"
  "s_add_u32 s26, s26, s80\n"
  "s_addc_u32 s27, s27, 0\n"
  "s_lshr_b32 s83, s93, 5\n"
  "s_and_b32 s81, s93, 7\n"
  "s_lshl_b32 s81, s81, 2\n"
  "s_bfe_u32 s80, s93, 131075\n"
  "s_add_u32 s81, s81, s80\n"
  "s_lshl_b32 s79, s83, 4\n"
  "s_lshl_b32 s80, s82, 2\n"
  "s_add_u32 s79, s79, s80\n"
  "s_mul_i32 s60, s79, 18432\n"
  "s_add_u32 s61, s60, 18432\n"
  "s_add_u32 s62, s60, 36864\n"
  "s_add_u32 s63, s60, 55296\n"
  "s_mul_i32 s64, s81, 288\n"
  "s_mul_i32 s80, s82, 294912\n"
  "s_add_u32 s64, s64, s80\n"
  "s_mul_i32 s67, s82, 9216\n"
  "v_add_u32 v80, 0, v0\n"
  "v_mul_u32_u24 v81, 3641, v80\n"
  "v_lshrrev_b32 v81, 16, v81\n"
  "v_mul_u32_u24 v81, 8928, v81\n"
  "v_add_u32 v81, 0, v81\n"
  "v_add_u32 v2, v81, v1\n"
  "v_add_u32 v80, 64, v0\n"
  "v_mul_u32_u24 v81, 3641, v80\n"
  "v_lshrrev_b32 v81, 16, v81\n"
  "v_mul_u32_u24 v81, 8928, v81\n"
  "v_add_u32 v81, 0, v81\n"
  "v_add_u32 v3, v81, v1\n"
  "v_add_u32 v80, 128, v0\n"
  "v_mul_u32_u24 v81, 3641, v80\n"
  "v_lshrrev_b32 v81, 16, v81\n"
  "v_mul_u32_u24 v81, 8928, v81\n"
  "v_add_u32 v81, 0, v81\n"
  "v_add_u32 v4, v81, v1\n"
  "v_add_u32 v80, 192, v0\n"
  "v_mul_u32_u24 v81, 3641, v80\n"
  "v_lshrrev_b32 v81, 16, v81\n"
  "v_mul_u32_u24 v81, 8928, v81\n"
  "v_add_u32 v81, 0, v81\n"
  "v_add_u32 v5, v81, v1\n"
  "v_add_u32 v80, 256, v0\n"
  "v_mul_u32_u24 v81, 3641, v80\n"
  "v_lshrrev_b32 v81, 16, v81\n"
  "v_mul_u32_u24 v81, 8928, v81\n"
  "v_add_u32 v81, 4096, v81\n"
  "v_add_u32 v6, v81, v1\n"
  "v_add_u32 v80, 320, v0\n"
  "v_mul_u32_u24 v81, 3641, v80\n"
  "v_lshrrev_b32 v81, 16, v81\n"
  "v_mul_u32_u24 v81, 8928, v81\n"
  "v_add_u32 v81, 4096, v81\n"
  "v_add_u32 v7, v81, v1\n"
  "v_add_u32 v80, 384, v0\n"
  "v_mul_u32_u24 v81, 3641, v80\n"
  "v_lshrrev_b32 v81, 16, v81\n"
  "v_mul_u32_u24 v81, 8928, v81\n"
  "v_add_u32 v81, 4096, v81\n"
  "v_add_u32 v8, v81, v1\n"
  "v_add_u32 v80, 448, v0\n"
  "v_mul_u32_u24 v81, 3641, v80\n"
  "v_lshrrev_b32 v81, 16, v81\n"
  "v_mul_u32_u24 v81, 8928, v81\n"
  "v_add_u32 v81, 4096, v81\n"
  "v_add_u32 v9, v81, v1\n"
  "v_add_u32 v80, 512, v0\n"
  "v_mul_u32_u24 v81, 3641, v80\n"
  "v_lshrrev_b32 v81, 16, v81\n"
  "v_mul_u32_u24 v81, 8928, v81\n"
  "v_add_u32 v81, 8192, v81\n"
  "v_add_u32 v10, v81, v1\n"
  "v_lshrrev_b32 v80, 3, v141\n"
  "v_lshlrev_b32 v152, 9, v80\n"
  "v_and_b32 v80, 3, v141\n"
  "v_lshl_add_u32 v152, v80, 7, v152\n"
  "v_bfe_u32 v80, v141, 2, 1\n"
  "v_lshl_add_u32 v152, v80, 3, v152\n"
  "v_lshl_add_u32 v152, v140, 4, v152\n"
  "s_lshl_b32 s80, s82, 5\n"
  "s_add_u32 s80, s80, 147456\n"
  "v_add_u32 v152, s80, v152\n"
  "s_lshl_b32 s80, s82, 10\n"
  "s_add_u32 s80, s80, 147456\n"
  "v_add_u32 v153, s80, v1\n"
  "v_lshrrev_b32 v80, 3, v0\n"
  "v_lshlrev_b32 v80, 8, v80\n"
  "v_and_b32 v81, 7, v0\n"
  "v_lshl_add_u32 v154, v81, 4, v80\n"
  "s_lshl_b32 s80, s82, 11\n"
  "v_add_u32 v154, s80, v154\n"
  "s_cmp_eq_u32 s82, 0\n s_cselect_b64 vcc, -1, 0\n"
  "v_mov_b32 v80, 0x7ffffff0\n"
  "v_cndmask_b32 v155, v80, v154, vcc\n"
  "v_min_u32 v30, 1, v0\n"
  "v_mul_u32_u24 v30, 0x7ffff0, v30\n"
  "v_lshlrev_b32 v30, 8, v30\n"
  "v_cndmask_b32 v156, v80, v30, vcc\n"
  "v_lshlrev_b32 v157, 6, v141\n"
  "s_waitcnt vmcnt(6)\n"
  "v_lshlrev_b32 v112, 16, v100\n"
  "v_and_b32 v113, 0xffff0000, v100\n"
  "v_lshlrev_b32 v114, 16, v101\n"
  "v_and_b32 v115, 0xffff0000, v101\n"
  "s_waitcnt vmcnt(5)\n"
  "v_lshlrev_b32 v116, 16, v102\n"
  "v_and_b32 v117, 0xffff0000, v102\n"
  "v_lshlrev_b32 v118, 16, v103\n"
  "v_and_b32 v119, 0xffff0000, v103\n"
  "s_waitcnt vmcnt(4)\n"
  "v_lshlrev_b32 v120, 16, v104\n"
  "v_and_b32 v121, 0xffff0000, v104\n"
  "v_lshlrev_b32 v122, 16, v105\n"
  "v_and_b32 v123, 0xffff0000, v105\n"
  "s_waitcnt vmcnt(3)\n"
  "v_lshlrev_b32 v124, 16, v106\n"
  "v_and_b32 v125, 0xffff0000, v106\n"
  "v_lshlrev_b32 v126, 16, v107\n"
  "v_and_b32 v127, 0xffff0000, v107\n"
  "s_waitcnt vmcnt(2)\n"
  "v_lshlrev_b32 v128, 16, v108\n"
  "v_and_b32 v129, 0xffff0000, v108\n"
  "v_lshlrev_b32 v130, 16, v109\n"
  "v_and_b32 v131, 0xffff0000, v109\n"
  "v_pk_add_f32 v[160:161], v[112:113], v[114:115]\n"
  "v_pk_add_f32 v[160:161], v[160:161], v[116:117]\n"
  "v_pk_add_f32 v[160:161], v[160:161], v[118:119]\n"
  "v_pk_add_f32 v[160:161], v[160:161], v[120:121]\n"
  "v_pk_add_f32 v[160:161], v[160:161], v[122:123]\n"
  "v_pk_add_f32 v[160:161], v[160:161], v[124:125]\n"
  "v_pk_add_f32 v[160:161], v[160:161], v[126:127]\n"
  "v_pk_add_f32 v[160:161], v[160:161], v[128:129]\n"
  "v_pk_add_f32 v[160:161], v[160:161], v[130:131]\n"
  "v_add_f32 v160, v160, v161\n"
  "s_nop 1\n"
  "v_add_f32_dpp v160, v160, v160 row_ror:8 row_mask:0xf bank_mask:0xf\n"
  "s_nop 1\n"
  "v_add_f32_dpp v160, v160, v160 row_ror:4 row_mask:0xf bank_mask:0xf\n"
  "s_nop 1\n"
  "v_add_f32_dpp v160, v160, v160 row_ror:2 row_mask:0xf bank_mask:0xf\n"
  "s_nop 1\n"
  "v_add_f32_dpp v160, v160, v160 row_ror:1 row_mask:0xf bank_mask:0xf\n"
  "v_mov_b32 v161, v160\n"
  "s_nop 1\n"
  "v_permlane16_swap_b32 v160, v161\n"
  "v_add_f32 v160, v160, v161\n"
  "v_mul_f32 v162, 0xbae38e39, v160\n"
  "v_mov_b32 v163, v162\n"
  "v_mov_b32 v164, 0\n"
  "v_mov_b32 v165, 0\n"
  "v_pk_add_f32 v[166:167], v[112:113], v[162:163]\n"
  "v_pk_fma_f32 v[164:165], v[166:167], v[166:167], v[164:165]\n"
  "v_pk_add_f32 v[166:167], v[114:115], v[162:163]\n"
  "v_pk_fma_f32 v[164:165], v[166:167], v[166:167], v[164:165]\n"
  "v_pk_add_f32 v[166:167], v[116:117], v[162:163]\n"
  "v_pk_fma_f32 v[164:165], v[166:167], v[166:167], v[164:165]\n"
  "v_pk_add_f32 v[166:167], v[118:119], v[162:163]\n"
  "v_pk_fma_f32 v[164:165], v[166:167], v[166:167], v[164:165]\n"
  "v_pk_add_f32 v[166:167], v[120:121], v[162:163]\n"
  "v_pk_fma_f32 v[164:165], v[166:167], v[166:167], v[164:165]\n"
  "v_pk_add_f32 v[166:167], v[122:123], v[162:163]\n"
  "v_pk_fma_f32 v[164:165], v[166:167], v[166:167], v[164:165]\n"
  "v_pk_add_f32 v[166:167], v[124:125], v[162:163]\n"
  "v_pk_fma_f32 v[164:165], v[166:167], v[166:167], v[164:165]\n"
  "v_pk_add_f32 v[166:167], v[126:127], v[162:163]\n"
  "v_pk_fma_f32 v[164:165], v[166:167], v[166:167], v[164:165]\n"
  "v_pk_add_f32 v[166:167], v[128:129], v[162:163]\n"
  "v_cndmask_b32 v166, 0, v166, s[28:29]\n"
  "v_cndmask_b32 v167, 0, v167, s[28:29]\n"
  "v_pk_fma_f32 v[164:165], v[166:167], v[166:167], v[164:165]\n"
  "v_pk_add_f32 v[166:167], v[130:131], v[162:163]\n"
  "v_cndmask_b32 v166, 0, v166, s[28:29]\n"
  "v_cndmask_b32 v167, 0, v167, s[28:29]\n"
  "v_pk_fma_f32 v[164:165], v[166:167], v[166:167], v[164:165]\n"
  "v_add_f32 v160, v164, v165\n"
  "s_nop 1\n"
  "v_add_f32_dpp v160, v160, v160 row_ror:8 row_mask:0xf bank_mask:0xf\n"
  "s_nop 1\n"
  "v_add_f32_dpp v160, v160, v160 row_ror:4 row_mask:0xf bank_mask:0xf\n"
  "s_nop 1\n"
  "v_add_f32_dpp v160, v160, v160 row_ror:2 row_mask:0xf bank_mask:0xf\n"
  "s_nop 1\n"
  "v_add_f32_dpp v160, v160, v160 row_ror:1 row_mask:0xf bank_mask:0xf\n"
  "v_mov_b32 v161, v160\n"
  "s_nop 1\n"
  "v_permlane16_swap_b32 v160, v161\n"
  "v_add_f32 v160, v160, v161\n"
  "v_mul_f32 v160, 0x3ae38e39, v160\n"
  "v_cvt_pk_bf16_f32 v160, v160, v160\n"
  "v_lshlrev_b32 v160, 16, v160\n"
  "v_add_f32 v160, 0x3727c5ac, v160\n"
  "v_cvt_pk_bf16_f32 v160, v160, v160\n"
  "v_lshlrev_b32 v160, 16, v160\n"
  "v_sqrt_f32 v160, v160\n"
  "s_nop 0\n"
  "v_cvt_pk_bf16_f32 v160, v160, v160\n"
  "v_lshlrev_b32 v160, 16, v160\n"
  "v_rcp_f32 v160, v160\n"
  "v_cvt_pk_bf16_f32 v162, v162, v162\n"
  "v_lshlrev_b32 v162, 16, v162\n"
  "v_mov_b32 v163, v162\n"
  "v_mov_b32 v161, v160\n"
  "v_pk_add_f32 v[112:113], v[112:113], v[162:163]\n"
  "v_cvt_pk_bf16_f32 v100, v112, v113\n"
  "v_lshlrev_b32 v112, 16, v100\n"
  "v_and_b32 v113, 0xffff0000, v100\n"
  "v_pk_mul_f32 v[112:113], v[112:113], v[160:161]\n"
  "v_cvt_pk_bf16_f32 v100, v112, v113\n"
  "v_pk_add_f32 v[114:115], v[114:115], v[162:163]\n"
  "v_cvt_pk_bf16_f32 v101, v114, v115\n"
  "v_lshlrev_b32 v114, 16, v101\n"
  "v_and_b32 v115, 0xffff0000, v101\n"
  "v_pk_mul_f32 v[114:115], v[114:115], v[160:161]\n"
  "v_cvt_pk_bf16_f32 v101, v114, v115\n"
  "v_pk_add_f32 v[116:117], v[116:117], v[162:163]\n"
  "v_cvt_pk_bf16_f32 v102, v116, v117\n"
  "v_lshlrev_b32 v116, 16, v102\n"
  "v_and_b32 v117, 0xffff0000, v102\n"
  "v_pk_mul_f32 v[116:117], v[116:117], v[160:161]\n"
  "v_cvt_pk_bf16_f32 v102, v116, v117\n"
  "v_pk_add_f32 v[118:119], v[118:119], v[162:163]\n"
  "v_cvt_pk_bf16_f32 v103, v118, v119\n"
  "v_lshlrev_b32 v118, 16, v103\n"
  "v_and_b32 v119, 0xffff0000, v103\n"
  "v_pk_mul_f32 v[118:119], v[118:119], v[160:161]\n"
  "v_cvt_pk_bf16_f32 v103, v118, v119\n"
  "v_pk_add_f32 v[120:121], v[120:121], v[162:163]\n"
  "v_cvt_pk_bf16_f32 v104, v120, v121\n"
  "v_lshlrev_b32 v120, 16, v104\n"
  "v_and_b32 v121, 0xffff0000, v104\n"
  "v_pk_mul_f32 v[120:121], v[120:121], v[160:161]\n"
  "v_cvt_pk_bf16_f32 v104, v120, v121\n"
  "v_pk_add_f32 v[122:123], v[122:123], v[162:163]\n"
  "v_cvt_pk_bf16_f32 v105, v122, v123\n"
  "v_lshlrev_b32 v122, 16, v105\n"
  "v_and_b32 v123, 0xffff0000, v105\n"
  "v_pk_mul_f32 v[122:123], v[122:123], v[160:161]\n"
  "v_cvt_pk_bf16_f32 v105, v122, v123\n"
  "v_pk_add_f32 v[124:125], v[124:125], v[162:163]\n"
  "v_cvt_pk_bf16_f32 v106, v124, v125\n"
  "v_lshlrev_b32 v124, 16, v106\n"
  "v_and_b32 v125, 0xffff0000, v106\n"
  "v_pk_mul_f32 v[124:125], v[124:125], v[160:161]\n"
  "v_cvt_pk_bf16_f32 v106, v124, v125\n"
  "v_pk_add_f32 v[126:127], v[126:127], v[162:163]\n"
  "v_cvt_pk_bf16_f32 v107, v126, v127\n"
  "v_lshlrev_b32 v126, 16, v107\n"
  "v_and_b32 v127, 0xffff0000, v107\n"
  "v_pk_mul_f32 v[126:127], v[126:127], v[160:161]\n"
  "v_cvt_pk_bf16_f32 v107, v126, v127\n"
  "v_pk_add_f32 v[128:129], v[128:129], v[162:163]\n"
  "v_cvt_pk_bf16_f32 v108, v128, v129\n"
  "v_lshlrev_b32 v128, 16, v108\n"
  "v_and_b32 v129, 0xffff0000, v108\n"
  "v_pk_mul_f32 v[128:129], v[128:129], v[160:161]\n"
  "v_cvt_pk_bf16_f32 v108, v128, v129\n"
  "v_pk_add_f32 v[130:131], v[130:131], v[162:163]\n"
  "v_cvt_pk_bf16_f32 v109, v130, v131\n"
  "v_lshlrev_b32 v130, 16, v109\n"
  "v_and_b32 v131, 0xffff0000, v109\n"
  "v_pk_mul_f32 v[130:131], v[130:131], v[160:161]\n"
  "v_cvt_pk_bf16_f32 v109, v130, v131\n"
  "ds_write_b64 v152, v[100:101] offset:0\n"
  "ds_write_b64 v152, v[102:103] offset:2048\n"
  "ds_write_b64 v152, v[104:105] offset:4096\n"
  "ds_write_b64 v152, v[106:107] offset:6144\n"
  "ds_write_b64 v152, v[108:109] offset:8192\n"
  "s_waitcnt vmcnt(1)\n"
  "v_readfirstlane_b32 s24, v145\n"
  "s_waitcnt lgkmcnt(0)\n"
  "s_barrier\n"
  "ds_read_b128 v[112:115], v153 offset:0\n"
  "ds_read_b128 v[116:119], v153 offset:4096\n"
  "ds_read_b128 v[120:123], v153 offset:8192\n"
  "s_add_u32 s24, s24, 1\n"
  "s_lshr_b32 s80, s93, 1\n"
  "s_mul_i32 s80, s80, 18432\n"
  "s_and_b32 s97, s93, 1\n"
  "s_lshl_b32 s97, s97, 7\n"
  "s_add_u32 s80, s80, s97\n"
  "s_add_u32 s97, s80, 8192\n"
  "s_add_u32 s34, s80, 16384\n"
  "s_waitcnt lgkmcnt(2)\n"
  "buffer_store_dwordx4 v[112:115], v154, s[36:39], s80 offen offset:0 sc1\n"
  "s_waitcnt lgkmcnt(1)\n"
  "buffer_store_dwordx4 v[116:119], v154, s[36:39], s97 offen offset:0 sc1\n"
  "s_waitcnt lgkmcnt(0)\n"
  "buffer_store_dwordx4 v[120:123], v155, s[36:39], s34 offen offset:0 sc1\n"
  "s_add_u32 m0, s67, 0\n"
  "s_add_u32 s66, s64, 0\n"
  "buffer_load_dwordx4 v2, s[40:43], s66 offen offset:0 lds\n"
  "buffer_load_dwordx4 v3, s[40:43], s66 offen offset:1024 lds\n"
  "buffer_load_dwordx4 v4, s[40:43], s66 offen offset:2048 lds\n"
  "buffer_load_dwordx4 v5, s[40:43], s66 offen offset:3072 lds\n"
  "s_add_u32 m0, s67, 4096\n"
  "s_nop 0\n"
  "buffer_load_dwordx4 v6, s[40:43], s66 offen offset:0 lds\n"
  "buffer_load_dwordx4 v7, s[40:43], s66 offen offset:1024 lds\n"
  "buffer_load_dwordx4 v8, s[40:43], s66 offen offset:2048 lds\n"
  "buffer_load_dwordx4 v9, s[40:43], s66 offen offset:3072 lds\n"
  "s_add_u32 m0, s67, 8192\n"
  "s_nop 0\n"
  "buffer_load_dwordx4 v10, s[40:43], s66 offen offset:0 lds\n"
  "v_mov_b32 v140, s24\n"
  "s_lshl_b32 s80, s93, 6\n"
  "s_add_u32 s80, s80, 73236480\n"
  "s_waitcnt vmcnt(9)\n"
  "s_barrier\n"
  "buffer_store_dword v140, v156, s[36:39], s80 offen offset:0 sc1\n"
  "s_lshl_b32 s65, s81, 8\n"
  "s_lshl_b32 s80, s82, 18\n"
  "s_add_u32 s65, s65, s80\n"
  "s_sub_u32 s65, s65, 4718592\n"
  "s_and_b32 s97, s81, 1\n"
  "s_lshr_b32 s80, s81, 1\n"
  "s_mul_i32 s80, s80, 9216\n"
  "s_mul_i32 s68, s79, 147456\n"
  "s_add_u32 s68, s68, s80\n"
  "s_add_u32 s68, s68, 2359296\n"
  "s_mul_i32 s80, s97, 5120\n"
  "s_add_u32 s68, s68, s80\n"
  "s_mul_i32 s72, s97, 5112\n"
  "s_sub_u32 s72, 4096, s72\n"
  "s_lshl_b32 s73, s79, 17\n"
  "s_lshl_b32 s80, s81, 12\n"
  "s_add_u32 s73, s73, s80\n"
  "s_add_u32 s73, s73, 21233664\n"
  "s_add_u32 s69, s68, 147456\n"
  "s_add_u32 s74, s73, 131072\n"
  "s_add_u32 s70, s68, 294912\n"
  "s_add_u32 s75, s73, 262144\n"
  "s_add_u32 s71, s68, 442368\n"
  "s_add_u32 s76, s73, 393216\n"
  "s_lshr_b32 s80, s81, 1\n"
  "s_lshl_b32 s77, s80, 19\n"
  "s_add_u32 s77, s77, 393216\n"
  "s_lshl_b32 s78, s82, 12\n"
  "s_add_u32 s77, s77, s78\n"
  "s_lshl_b32 s94, s83, 2\n"
  "s_add_u32 s94, s94, s82\n"
  "s_lshl_b32 s94, s94, 2\n"
  "s_lshr_b32 s80, s81, 3\n"
  "s_add_u32 s94, s94, s80\n"
  "s_lshl_b32 s94, s94, 6\n"
  "s_and_b32 s80, s81, 7\n"
  "s_lshl_b32 s80, s80, 2\n"
  "s_add_u32 s94, s94, s80\n"
  "s_add_u32 s94, s94, 73203712\n"
  "s_mul_i32 s88, s93, 9216\n"
  "s_mul_i32 s80, s82, 2506752\n"
  "s_add_u32 s88, s88, s80\n"
  "s_add_u32 s88, s88, 38010880\n"
  "s_sub_u32 s80, s93, 128\n"
  "s_cmp_lt_u32 s93, 128\n"
  "s_cselect_b32 s84, %10, %12\n"
  "s_cselect_b32 s85, %11, %13\n"
  "s_cselect_b32 s86, 0, 1\n"
  "s_cselect_b32 s87, s93, s80\n"
  "s_mul_i32 s86, s86, 589824\n"
  "s_add_u32 s86, s86, 4718592\n"
  "s_mul_i32 s87, s87, 36864\n"
  "s_add_u32 s87, s87, s67\n"
  "s_lshl_b32 s96, s82, 10\n"
  "s_lshl_b32 s80, s93, 2\n"
  "s_add_u32 s96, s96, s80\n"
  "s_add_u32 s96, s96, 73211904\n"
  "s_cmp_lt_u32 s93, 144\n s_cselect_b32 s25, 5308416, 0\n"
  "s_mov_b32 s22, %12\n"
  "s_mov_b32 s23, %13\n"
  "s_mov_b32 s89, %14\n"
  "s_mov_b32 s90, %15\n"
  "s_mov_b32 s91, %16\n"
  "s_mov_b32 s92, %17\n"
  "s_mov_b32 s34, 0\n"
  "v_lshrrev_b32 v80, 4, v0\n"
  "v_bfe_u32 v81, v0, 2, 2\n"
  "v_and_b32 v82, 3, v0\n"
  "v_and_b32 v83, 15, v0\n"
  "v_lshl_add_u32 v84, v80, 2, v81\n"
  "v_mul_u32_u24 v85, 288, v84\n"
  "v_lshl_add_u32 v20, v82, 3, v85\n"
  "s_lshl_b32 s80, s97, 8\n"
  "s_sub_u32 s80, 256, s80\n"
  "v_add_u32 v21, s80, v20\n"
  "s_lshl_b32 s80, s97, 5\n"
  "v_add_u32 v20, s80, v20\n"
  "v_lshlrev_b32 v85, 2, v80\n"
  "v_xor_b32 v85, v85, v83\n"
  "v_lshlrev_b32 v85, 4, v85\n"
  "v_lshl_add_u32 v11, v80, 13, v85\n"
  "v_add_u32 v12, 31744, v11\n"
  "v_add_u32 v13, 63488, v11\n"
  "v_add_u32 v14, 95232, v11\n"
  "v_add_u32 v15, 131072, v11\n"
  "v_add_u32 v16, 162816, v11\n"
  "v_add_u32 v17, 194560, v11\n"
  "v_add_u32 v18, 226304, v11\n"
  "v_mov_b32 v19, 0x7ffffff0\n"
  "v_lshlrev_b32 v86, 8, v84\n"
  "v_lshl_add_u32 v86, v82, 4, v86\n"
  "v_xor_b32 v85, 0, v81\n"
  "v_lshl_add_u32 v44, v85, 6, v86\n"
  "v_xor_b32 v85, 1, v81\n"
  "v_lshl_add_u32 v45, v85, 6, v86\n"
  "v_xor_b32 v85, 2, v81\n"
  "v_lshl_add_u32 v46, v85, 6, v86\n"
  "v_xor_b32 v85, 3, v81\n"
  "v_lshl_add_u32 v47, v85, 6, v86\n"
  "v_lshrrev_b32 v80, 4, v0\n"
  "v_bfe_u32 v81, v0, 2, 2\n"
  "v_and_b32 v82, 3, v0\n"
  "v_and_b32 v83, 15, v0\n"
  "v_lshl_add_u32 v84, v80, 2, v81\n"
  "v_add_u32 v85, 0, v80\n"
  "v_xor_b32 v85, v85, v83\n"
  "v_lshlrev_b32 v85, 4, v85\n"
  "v_lshl_add_u32 v22, v80, 8, v85\n"
  "v_add_u32 v85, 4, v80\n"
  "v_xor_b32 v85, v85, v83\n"
  "v_lshlrev_b32 v85, 4, v85\n"
  "v_lshl_add_u32 v23, v80, 8, v85\n"
  "v_add_u32 v85, 8, v80\n"
  "v_xor_b32 v85, v85, v83\n"
  "v_lshlrev_b32 v85, 4, v85\n"
  "v_lshl_add_u32 v24, v80, 8, v85\n"
  "v_add_u32 v85, 12, v80\n"
  "v_xor_b32 v85, v85, v83\n"
  "v_lshlrev_b32 v85, 4, v85\n"
  "v_lshl_add_u32 v25, v80, 8, v85\n"
  "v_add_u32 v85, 0, v80\n"
  "v_lshlrev_b32 v85, 1, v85\n"
  "v_xor_b32 v85, v85, v83\n"
  "v_lshlrev_b32 v85, 4, v85\n"
  "v_lshl_add_u32 v26, v80, 8, v85\n"
  "v_add_u32 v85, 4, v80\n"
  "v_lshlrev_b32 v85, 1, v85\n"
  "v_xor_b32 v85, v85, v83\n"
  "v_lshlrev_b32 v85, 4, v85\n"
  "v_lshl_add_u32 v27, v80, 8, v85\n"
  "v_add_u32 v85, 0, v80\n"
  "v_xor_b32 v85, v85, v83\n"
  "v_lshlrev_b32 v85, 4, v85\n"
  "v_lshl_add_u32 v32, v83, 8, v85\n"
  "v_add_u32 v85, 4, v80\n"
  "v_xor_b32 v85, v85, v83\n"
  "v_lshlrev_b32 v85, 4, v85\n"
  "v_lshl_add_u32 v33, v83, 8, v85\n"
  "v_add_u32 v85, 8, v80\n"
  "v_xor_b32 v85, v85, v83\n"
  "v_lshlrev_b32 v85, 4, v85\n"
  "v_lshl_add_u32 v34, v83, 8, v85\n"
  "v_add_u32 v85, 12, v80\n"
  "v_xor_b32 v85, v85, v83\n"
  "v_lshlrev_b32 v85, 4, v85\n"
  "v_lshl_add_u32 v35, v83, 8, v85\n"
  "v_add_u32 v32, 131072, v32\n"
  "v_add_u32 v33, 131072, v33\n"
  "v_add_u32 v34, 131072, v34\n"
  "v_add_u32 v35, 131072, v35\n"
  "v_mul_u32_u24 v85, 1152, v84\n"
  "v_lshl_add_u32 v85, v82, 3, v85\n"
  "s_mul_i32 s80, s82, 288\n"
  "v_add_u32 v28, s80, v85\n"
  "v_and_b32 v86, 7, v84\n"
  "v_lshlrev_b32 v84, 8, v84\n"
  "v_lshl_add_u32 v84, v82, 3, v84\n"
  "v_xor_b32 v85, 0, v86\n"
  "v_lshl_add_u32 v36, v85, 5, v84\n"
  "v_xor_b32 v85, 1, v86\n"
  "v_lshl_add_u32 v37, v85, 5, v84\n"
  "v_xor_b32 v85, 2, v86\n"
  "v_lshl_add_u32 v38, v85, 5, v84\n"
  "v_xor_b32 v85, 3, v86\n"
  "v_lshl_add_u32 v39, v85, 5, v84\n"
  "v_xor_b32 v85, 4, v86\n"
  "v_lshl_add_u32 v40, v85, 5, v84\n"
  "v_xor_b32 v85, 5, v86\n"
  "v_lshl_add_u32 v41, v85, 5, v84\n"
  "v_xor_b32 v85, 6, v86\n"
  "v_lshl_add_u32 v42, v85, 5, v84\n"
  "v_xor_b32 v85, 7, v86\n"
  "v_lshl_add_u32 v43, v85, 5, v84\n"
  "s_cmp_eq_u32 s82, 0\n s_cbranch_scc0 Lpe_%=\n"
  "s_lshr_b32 s80, s93, 5\n"
  "s_lshl_b32 s80, s80, 11\n"
  "s_add_u32 s80, s80, 73236480\n"
  "s_mov_b32 s97, 0x20000\n"
  "Lxp_%=:\n"
  "buffer_load_dword v140, v157, s[36:39], s80 offen sc1\n"
  "s_sub_u32 s97, s97, 1\n"
  "s_waitcnt vmcnt(0)\n"
  "s_cbranch_scc1 Lpe_%=\n"
  "v_cmp_ne_u32 vcc, s24, v140\n"
  "s_nop 1\n"
  "s_cbranch_vccnz Lxp_%=\n"
  "Lpe_%=:\n"
  "s_barrier\n"
  "buffer_load_dwordx4 a[0:3], v1, s[36:39], s60 offen offset:0 sc1\n"
  "buffer_load_dwordx4 a[4:7], v1, s[36:39], s61 offen offset:0 sc1\n"
  "buffer_load_dwordx4 a[8:11], v1, s[36:39], s62 offen offset:0 sc1\n"
  "buffer_load_dwordx4 a[12:15], v1, s[36:39], s63 offen offset:0 sc1\n"
  "buffer_load_dwordx4 a[16:19], v1, s[36:39], s60 offen offset:1024 sc1\n"
  "buffer_load_dwordx4 a[20:23], v1, s[36:39], s61 offen offset:1024 sc1\n"
  "buffer_load_dwordx4 a[24:27], v1, s[36:39], s62 offen offset:1024 sc1\n"
  "buffer_load_dwordx4 a[28:31], v1, s[36:39], s63 offen offset:1024 sc1\n"
  "s_waitcnt vmcnt(9)\n"
  "s_barrier\n"
  "ds_read_b64_tr_b16 v[48:49], v20 offset:0\n"
  "ds_read_b64_tr_b16 v[50:51], v20 offset:4608\n"
  "ds_read_b64_tr_b16 v[52:53], v20 offset:32\n"
  "ds_read_b64_tr_b16 v[54:55], v20 offset:4640\n"
  "ds_read_b64_tr_b16 v[56:57], v20 offset:64\n"
  "ds_read_b64_tr_b16 v[58:59], v20 offset:4672\n"
  "ds_read_b64_tr_b16 v[60:61], v20 offset:96\n"
  "ds_read_b64_tr_b16 v[62:63], v20 offset:4704\n"
  "ds_read_b64_tr_b16 v[64:65], v20 offset:128\n"
  "ds_read_b64_tr_b16 v[66:67], v20 offset:4736\n"
  "s_waitcnt vmcnt(7) lgkmcnt(8)\n"
  "v_mfma_f32_16x16x32_bf16 v[80:83], v[48:51], a[0:3], 0\n"
  "s_waitcnt vmcnt(6)\n"
  "buffer_load_dwordx4 a[32:35], v1, s[36:39], s60 offen offset:2048 sc1\n"
  "v_mfma_f32_16x16x32_bf16 v[84:87], v[48:51], a[4:7], 0\n"
  "s_waitcnt vmcnt(6)\n"
  "v_mfma_f32_16x16x32_bf16 v[88:91], v[48:51], a[8:11], 0\n"
  "s_waitcnt vmcnt(5)\n"
  "ds_read_b64_tr_b16 v[68:69], v20 offset:160\n"
  "v_mfma_f32_16x16x32_bf16 v[92:95], v[48:51], a[12:15], 0\n"
  "s_waitcnt lgkmcnt(3)\n"
  "ds_read_b64_tr_b16 v[70:71], v20 offset:4768\n"
  "v_mfma_f32_16x16x32_bf16 v[96:99], v[52:55], a[0:3], 0\n"
  "v_mfma_f32_16x16x32_bf16 v[100:103], v[52:55], a[4:7], 0\n"
  "buffer_load_dwordx4 a[36:39], v1, s[36:39], s61 offen offset:2048 sc1\n"
  "v_mfma_f32_16x16x32_bf16 v[104:107], v[52:55], a[8:11], 0\n"
  "ds_read_b64_tr_b16 v[72:73], v20 offset:192\n"
  "v_mfma_f32_16x16x32_bf16 v[108:111], v[52:55], a[12:15], 0\n"
  "ds_read_b64_tr_b16 v[74:75], v20 offset:4800\n"
  "v_mfma_f32_16x16x32_bf16 v[112:115], v[56:59], a[0:3], 0\n"
  "v_mfma_f32_16x16x32_bf16 v[116:119], v[56:59], a[4:7], 0\n"
  "v_mfma_f32_16x16x32_bf16 v[120:123], v[56:59], a[8:11], 0\n"
  "ds_read_b64_tr_b16 v[76:77], v20 offset:224\n"
  "v_mfma_f32_16x16x32_bf16 v[124:127], v[56:59], a[12:15], 0\n"
  "ds_read_b64_tr_b16 v[78:79], v20 offset:4832\n"
  "v_mfma_f32_16x16x32_bf16 v[128:131], v[60:63], a[0:3], 0\n"
  "buffer_load_dwordx4 a[40:43], v1, s[36:39], s62 offen offset:2048 sc1\n"
  "v_mfma_f32_16x16x32_bf16 v[132:135], v[60:63], a[4:7], 0\n"
  "v_mfma_f32_16x16x32_bf16 v[136:139], v[60:63], a[8:11], 0\n"
  "ds_read_b64_tr_b16 v[48:49], v21 offset:0\n"
  "v_mfma_f32_16x16x32_bf16 v[140:143], v[60:63], a[12:15], 0\n"
  "s_waitcnt lgkmcnt(3)\n"
  "ds_read_b64_tr_b16 v[50:51], v21 offset:4608\n"
  "v_mfma_f32_16x16x32_bf16 v[144:147], v[64:67], a[0:3], 0\n"
  "buffer_load_dwordx4 a[44:47], v1, s[36:39], s63 offen offset:2048 sc1\n"
  "v_mfma_f32_16x16x32_bf16 v[148:151], v[64:67], a[4:7], 0\n"
  "v_mfma_f32_16x16x32_bf16 v[152:155], v[64:67], a[8:11], 0\n"
  "ds_read_b64_tr_b16 v[52:53], v20 offset:9216\n"
  "v_mfma_f32_16x16x32_bf16 v[156:159], v[64:67], a[12:15], 0\n"
  "ds_read_b64_tr_b16 v[54:55], v20 offset:13824\n"
  "v_mfma_f32_16x16x32_bf16 v[160:163], v[68:71], a[0:3], 0\n"
  "buffer_load_dwordx4 a[48:51], v1, s[36:39], s60 offen offset:3072 sc1\n"
  "v_mfma_f32_16x16x32_bf16 v[164:167], v[68:71], a[4:7], 0\n"
  "v_mfma_f32_16x16x32_bf16 v[168:171], v[68:71], a[8:11], 0\n"
  "ds_read_b64_tr_b16 v[56:57], v20 offset:9248\n"
  "v_mfma_f32_16x16x32_bf16 v[172:175], v[68:71], a[12:15], 0\n"
  "ds_read_b64_tr_b16 v[58:59], v20 offset:13856\n"
  "v_mfma_f32_16x16x32_bf16 v[176:179], v[72:75], a[0:3], 0\n"
  "v_mfma_f32_16x16x32_bf16 v[180:183], v[72:75], a[4:7], 0\n"
  "buffer_load_dwordx4 a[52:55], v1, s[36:39], s61 offen offset:3072 sc1\n"
  "v_mfma_f32_16x16x32_bf16 v[184:187], v[72:75], a[8:11], 0\n"
  "ds_read_b64_tr_b16 v[60:61], v20 offset:9280\n"
  "v_mfma_f32_16x16x32_bf16 v[188:191], v[72:75], a[12:15], 0\n"
  "s_waitcnt lgkmcnt(3)\n"
  "ds_read_b64_tr_b16 v[62:63], v20 offset:13888\n"
  "v_mfma_f32_16x16x32_bf16 v[192:195], v[76:79], a[0:3], 0\n"
  "v_mfma_f32_16x16x32_bf16 v[196:199], v[76:79], a[4:7], 0\n"
  "v_mfma_f32_16x16x32_bf16 v[200:203], v[76:79], a[8:11], 0\n"
  "ds_read_b64_tr_b16 v[64:65], v20 offset:9312\n"
  "v_mfma_f32_16x16x32_bf16 v[204:207], v[76:79], a[12:15], 0\n"
  "ds_read_b64_tr_b16 v[66:67], v20 offset:13920\n"
  "v_mfma_f32_16x16x32_bf16 v[208:211], v[48:51], a[0:3], 0\n"
  "buffer_load_dwordx4 a[56:59], v1, s[36:39], s62 offen offset:3072 sc1\n"
  "v_mfma_f32_16x16x32_bf16 v[212:215], v[48:51], a[4:7], 0\n"
  "v_mfma_f32_16x16x32_bf16 v[216:219], v[48:51], a[8:11], 0\n"
  "ds_read_b64_tr_b16 v[68:69], v20 offset:9344\n"
  "v_mfma_f32_16x16x32_bf16 v[220:223], v[48:51], a[12:15], 0\n"
  "s_waitcnt vmcnt(10)\n"
  "ds_read_b64_tr_b16 v[70:71], v20 offset:13952\n"
  "v_mfma_f32_16x16x32_bf16 v[80:83], v[52:55], a[16:19], v[80:83]\n"
  "s_waitcnt vmcnt(9)\n"
  "buffer_load_dwordx4 a[60:63], v1, s[36:39], s63 offen offset:3072 sc1\n"
  "v_mfma_f32_16x16x32_bf16 v[84:87], v[52:55], a[20:23], v[84:87]\n"
  "s_add_u32 m0, s67, 36864\n"
  "s_waitcnt vmcnt(9)\n"
  "v_mfma_f32_16x16x32_bf16 v[88:91], v[52:55], a[24:27], v[88:91]\n"
  "s_add_u32 s66, s64, 1179648\n"
  "ds_read_b64_tr_b16 v[72:73], v20 offset:9376\n"
  "s_waitcnt vmcnt(8)\n"
  "v_mfma_f32_16x16x32_bf16 v[92:95], v[52:55], a[28:31], v[92:95]\n"
  "s_waitcnt lgkmcnt(1)\n"
  "ds_read_b64_tr_b16 v[74:75], v20 offset:13984\n"
  "v_mfma_f32_16x16x32_bf16 v[96:99], v[56:59], a[16:19], v[96:99]\n"
  "buffer_load_dwordx4 v2, s[40:43], s66 offen offset:0 lds\n"
  "v_mfma_f32_16x16x32_bf16 v[100:103], v[56:59], a[20:23], v[100:103]\n"
  "v_mfma_f32_16x16x32_bf16 v[104:107], v[56:59], a[24:27], v[104:107]\n"
  "ds_read_b64_tr_b16 v[76:77], v20 offset:9408\n"
  "v_mfma_f32_16x16x32_bf16 v[108:111], v[56:59], a[28:31], v[108:111]\n"
  "ds_read_b64_tr_b16 v[78:79], v20 offset:14016\n"
  "v_mfma_f32_16x16x32_bf16 v[112:115], v[60:63], a[16:19], v[112:115]\n"
  "v_mfma_f32_16x16x32_bf16 v[116:119], v[60:63], a[20:23], v[116:119]\n"
  "buffer_load_dwordx4 v3, s[40:43], s66 offen offset:1024 lds\n"
  "v_mfma_f32_16x16x32_bf16 v[120:123], v[60:63], a[24:27], v[120:123]\n"
  "ds_read_b64_tr_b16 v[48:49], v20 offset:9440\n"
  "v_mfma_f32_16x16x32_bf16 v[124:127], v[60:63], a[28:31], v[124:127]\n"
  "ds_read_b64_tr_b16 v[50:51], v20 offset:14048\n"
  "v_mfma_f32_16x16x32_bf16 v[128:131], v[64:67], a[16:19], v[128:131]\n"
  "v_mfma_f32_16x16x32_bf16 v[132:135], v[64:67], a[20:23], v[132:135]\n"
  "v_mfma_f32_16x16x32_bf16 v[136:139], v[64:67], a[24:27], v[136:139]\n"
  "ds_read_b64_tr_b16 v[52:53], v21 offset:9216\n"
  "v_mfma_f32_16x16x32_bf16 v[140:143], v[64:67], a[28:31], v[140:143]\n"
  "ds_read_b64_tr_b16 v[54:55], v21 offset:13824\n"
  "v_mfma_f32_16x16x32_bf16 v[144:147], v[68:71], a[16:19], v[144:147]\n"
  "buffer_load_dwordx4 v4, s[40:43], s66 offen offset:2048 lds\n"
  "v_mfma_f32_16x16x32_bf16 v[148:151], v[68:71], a[20:23], v[148:151]\n"
  "v_mfma_f32_16x16x32_bf16 v[152:155], v[68:71], a[24:27], v[152:155]\n"
  "ds_read_b64_tr_b16 v[56:57], v20 offset:18432\n"
  "v_mfma_f32_16x16x32_bf16 v[156:159], v[68:71], a[28:31], v[156:159]\n"
  "s_waitcnt lgkmcnt(3)\n"
  "ds_read_b64_tr_b16 v[58:59], v20 offset:23040\n"
  "v_mfma_f32_16x16x32_bf16 v[160:163], v[72:75], a[16:19], v[160:163]\n"
  "buffer_load_dwordx4 v5, s[40:43], s66 offen offset:3072 lds\n"
  "v_mfma_f32_16x16x32_bf16 v[164:167], v[72:75], a[20:23], v[164:167]\n"
  "s_add_u32 m0, s67, 40960\n"
  "v_mfma_f32_16x16x32_bf16 v[168:171], v[72:75], a[24:27], v[168:171]\n"
  "ds_read_b64_tr_b16 v[60:61], v20 offset:18464\n"
  "v_mfma_f32_16x16x32_bf16 v[172:175], v[72:75], a[28:31], v[172:175]\n"
  "ds_read_b64_tr_b16 v[62:63], v20 offset:23072\n"
  "v_mfma_f32_16x16x32_bf16 v[176:179], v[76:79], a[16:19], v[176:179]\n"
  "buffer_load_dwordx4 v6, s[40:43], s66 offen offset:0 lds\n"
  "v_mfma_f32_16x16x32_bf16 v[180:183], v[76:79], a[20:23], v[180:183]\n"
  "v_mfma_f32_16x16x32_bf16 v[184:187], v[76:79], a[24:27], v[184:187]\n"
  "ds_read_b64_tr_b16 v[64:65], v20 offset:18496\n"
  "v_mfma_f32_16x16x32_bf16 v[188:191], v[76:79], a[28:31], v[188:191]\n"
  "ds_read_b64_tr_b16 v[66:67], v20 offset:23104\n"
  "v_mfma_f32_16x16x32_bf16 v[192:195], v[48:51], a[16:19], v[192:195]\n"
  "v_mfma_f32_16x16x32_bf16 v[196:199], v[48:51], a[20:23], v[196:199]\n"
  "buffer_load_dwordx4 v7, s[40:43], s66 offen offset:1024 lds\n"
  "v_mfma_f32_16x16x32_bf16 v[200:203], v[48:51], a[24:27], v[200:203]\n"
  "ds_read_b64_tr_b16 v[68:69], v20 offset:18528\n"
  "v_mfma_f32_16x16x32_bf16 v[204:207], v[48:51], a[28:31], v[204:207]\n"
  "s_waitcnt lgkmcnt(3)\n"
  "ds_read_b64_tr_b16 v[70:71], v20 offset:23136\n"
  "v_mfma_f32_16x16x32_bf16 v[208:211], v[52:55], a[16:19], v[208:211]\n"
  "v_mfma_f32_16x16x32_bf16 v[212:215], v[52:55], a[20:23], v[212:215]\n"
  "v_mfma_f32_16x16x32_bf16 v[216:219], v[52:55], a[24:27], v[216:219]\n"
  "ds_read_b64_tr_b16 v[72:73], v20 offset:18560\n"
  "v_mfma_f32_16x16x32_bf16 v[220:223], v[52:55], a[28:31], v[220:223]\n"
  "s_waitcnt vmcnt(13)\n"
  "ds_read_b64_tr_b16 v[74:75], v20 offset:23168\n"
  "v_mfma_f32_16x16x32_bf16 v[80:83], v[56:59], a[32:35], v[80:83]\n"
  "s_waitcnt vmcnt(12)\n"
  "buffer_load_dwordx4 v8, s[40:43], s66 offen offset:2048 lds\n"
  "v_mfma_f32_16x16x32_bf16 v[84:87], v[56:59], a[36:39], v[84:87]\n"
  "s_waitcnt vmcnt(12)\n"
  "v_mfma_f32_16x16x32_bf16 v[88:91], v[56:59], a[40:43], v[88:91]\n"
  "s_waitcnt vmcnt(11)\n"
  "ds_read_b64_tr_b16 v[76:77], v20 offset:18592\n"
  "v_mfma_f32_16x16x32_bf16 v[92:95], v[56:59], a[44:47], v[92:95]\n"
  "ds_read_b64_tr_b16 v[78:79], v20 offset:23200\n"
  "v_mfma_f32_16x16x32_bf16 v[96:99], v[60:63], a[32:35], v[96:99]\n"
  "buffer_load_dwordx4 v9, s[40:43], s66 offen offset:3072 lds\n"
  "v_mfma_f32_16x16x32_bf16 v[100:103], v[60:63], a[36:39], v[100:103]\n"
  "s_add_u32 m0, s67, 45056\n"
  "v_mfma_f32_16x16x32_bf16 v[104:107], v[60:63], a[40:43], v[104:107]\n"
  "ds_read_b64_tr_b16 v[48:49], v20 offset:18624\n"
  "v_mfma_f32_16x16x32_bf16 v[108:111], v[60:63], a[44:47], v[108:111]\n"
  "s_waitcnt lgkmcnt(3)\n"
  "ds_read_b64_tr_b16 v[50:51], v20 offset:23232\n"
  "v_mfma_f32_16x16x32_bf16 v[112:115], v[64:67], a[32:35], v[112:115]\n"
  "buffer_load_dwordx4 v10, s[40:43], s66 offen offset:0 lds\n"
  "v_mfma_f32_16x16x32_bf16 v[116:119], v[64:67], a[36:39], v[116:119]\n"
  "s_add_u32 s60, s60, 4096\n"
  "v_mfma_f32_16x16x32_bf16 v[120:123], v[64:67], a[40:43], v[120:123]\n"
  "ds_read_b64_tr_b16 v[52:53], v20 offset:18656\n"
  "v_mfma_f32_16x16x32_bf16 v[124:127], v[64:67], a[44:47], v[124:127]\n"
  "ds_read_b64_tr_b16 v[54:55], v20 offset:23264\n"
  "v_mfma_f32_16x16x32_bf16 v[128:131], v[68:71], a[32:35], v[128:131]\n"
  "v_mfma_f32_16x16x32_bf16 v[132:135], v[68:71], a[36:39], v[132:135]\n"
  "buffer_load_dwordx4 a[64:67], v1, s[36:39], s60 offen offset:0 sc1\n"
  "v_mfma_f32_16x16x32_bf16 v[136:139], v[68:71], a[40:43], v[136:139]\n"
  "s_add_u32 s61, s61, 4096\n"
  "ds_read_b64_tr_b16 v[56:57], v21 offset:18432\n"
  "v_mfma_f32_16x16x32_bf16 v[140:143], v[68:71], a[44:47], v[140:143]\n"
  "ds_read_b64_tr_b16 v[58:59], v21 offset:23040\n"
  "v_mfma_f32_16x16x32_bf16 v[144:147], v[72:75], a[32:35], v[144:147]\n"
  "v_mfma_f32_16x16x32_bf16 v[148:151], v[72:75], a[36:39], v[148:151]\n"
  "v_mfma_f32_16x16x32_bf16 v[152:155], v[72:75], a[40:43], v[152:155]\n"
  "ds_read_b64_tr_b16 v[60:61], v20 offset:27648\n"
  "v_mfma_f32_16x16x32_bf16 v[156:159], v[72:75], a[44:47], v[156:159]\n"
  "s_waitcnt lgkmcnt(3)\n"
  "ds_read_b64_tr_b16 v[62:63], v20 offset:32256\n"
  "v_mfma_f32_16x16x32_bf16 v[160:163], v[76:79], a[32:35], v[160:163]\n"
  "buffer_load_dwordx4 a[68:71], v1, s[36:39], s61 offen offset:0 sc1\n"
  "v_mfma_f32_16x16x32_bf16 v[164:167], v[76:79], a[36:39], v[164:167]\n"
  "s_add_u32 s62, s62, 4096\n"
  "v_mfma_f32_16x16x32_bf16 v[168:171], v[76:79], a[40:43], v[168:171]\n"
  "ds_read_b64_tr_b16 v[64:65], v20 offset:27680\n"
  "v_mfma_f32_16x16x32_bf16 v[172:175], v[76:79], a[44:47], v[172:175]\n"
  "ds_read_b64_tr_b16 v[66:67], v20 offset:32288\n"
  "v_mfma_f32_16x16x32_bf16 v[176:179], v[48:51], a[32:35], v[176:179]\n"
  "buffer_load_dwordx4 a[72:75], v1, s[36:39], s62 offen offset:0 sc1\n"
  "v_mfma_f32_16x16x32_bf16 v[180:183], v[48:51], a[36:39], v[180:183]\n"
  "s_add_u32 s63, s63, 4096\n"
  "v_mfma_f32_16x16x32_bf16 v[184:187], v[48:51], a[40:43], v[184:187]\n"
  "ds_read_b64_tr_b16 v[68:69], v20 offset:27712\n"
  "v_mfma_f32_16x16x32_bf16 v[188:191], v[48:51], a[44:47], v[188:191]\n"
  "ds_read_b64_tr_b16 v[70:71], v20 offset:32320\n"
  "v_mfma_f32_16x16x32_bf16 v[192:195], v[52:55], a[32:35], v[192:195]\n"
  "buffer_load_dwordx4 a[76:79], v1, s[36:39], s63 offen offset:0 sc1\n"
  "v_mfma_f32_16x16x32_bf16 v[196:199], v[52:55], a[36:39], v[196:199]\n"
  "v_mfma_f32_16x16x32_bf16 v[200:203], v[52:55], a[40:43], v[200:203]\n"
  "ds_read_b64_tr_b16 v[72:73], v20 offset:27744\n"
  "v_mfma_f32_16x16x32_bf16 v[204:207], v[52:55], a[44:47], v[204:207]\n"
  "s_waitcnt lgkmcnt(3)\n"
  "ds_read_b64_tr_b16 v[74:75], v20 offset:32352\n"
  "v_mfma_f32_16x16x32_bf16 v[208:211], v[56:59], a[32:35], v[208:211]\n"
  "v_mfma_f32_16x16x32_bf16 v[212:215], v[56:59], a[36:39], v[212:215]\n"
  "buffer_load_dwordx4 a[80:83], v1, s[36:39], s60 offen offset:1024 sc1\n"
  "v_mfma_f32_16x16x32_bf16 v[216:219], v[56:59], a[40:43], v[216:219]\n"
  "ds_read_b64_tr_b16 v[76:77], v20 offset:27776\n"
  "v_mfma_f32_16x16x32_bf16 v[220:223], v[56:59], a[44:47], v[220:223]\n"
  "s_waitcnt vmcnt(17)\n"
  "ds_read_b64_tr_b16 v[78:79], v20 offset:32384\n"
  "v_mfma_f32_16x16x32_bf16 v[80:83], v[60:63], a[48:51], v[80:83]\n"
  "s_waitcnt vmcnt(16)\n"
  "v_mfma_f32_16x16x32_bf16 v[84:87], v[60:63], a[52:55], v[84:87]\n"
  "s_waitcnt vmcnt(15)\n"
  "v_mfma_f32_16x16x32_bf16 v[88:91], v[60:63], a[56:59], v[88:91]\n"
  "s_waitcnt vmcnt(14)\n"
  "ds_read_b64_tr_b16 v[48:49], v20 offset:27808\n"
  "v_mfma_f32_16x16x32_bf16 v[92:95], v[60:63], a[60:63], v[92:95]\n"
  "ds_read_b64_tr_b16 v[50:51], v20 offset:32416\n"
  "v_mfma_f32_16x16x32_bf16 v[96:99], v[64:67], a[48:51], v[96:99]\n"
  "buffer_load_dwordx4 a[84:87], v1, s[36:39], s61 offen offset:1024 sc1\n"
  "v_mfma_f32_16x16x32_bf16 v[100:103], v[64:67], a[52:55], v[100:103]\n"
  "v_mfma_f32_16x16x32_bf16 v[104:107], v[64:67], a[56:59], v[104:107]\n"
  "ds_read_b64_tr_b16 v[52:53], v20 offset:27840\n"
  "v_mfma_f32_16x16x32_bf16 v[108:111], v[64:67], a[60:63], v[108:111]\n"
  "s_waitcnt lgkmcnt(3)\n"
  "ds_read_b64_tr_b16 v[54:55], v20 offset:32448\n"
  "v_mfma_f32_16x16x32_bf16 v[112:115], v[68:71], a[48:51], v[112:115]\n"
  "buffer_load_dwordx4 a[88:91], v1, s[36:39], s62 offen offset:1024 sc1\n"
  "v_mfma_f32_16x16x32_bf16 v[116:119], v[68:71], a[52:55], v[116:119]\n"
  "v_mfma_f32_16x16x32_bf16 v[120:123], v[68:71], a[56:59], v[120:123]\n"
  "ds_read_b64_tr_b16 v[56:57], v20 offset:27872\n"
  "v_mfma_f32_16x16x32_bf16 v[124:127], v[68:71], a[60:63], v[124:127]\n"
  "ds_read_b64_tr_b16 v[58:59], v20 offset:32480\n"
  "v_mfma_f32_16x16x32_bf16 v[128:131], v[72:75], a[48:51], v[128:131]\n"
  "buffer_load_dwordx4 a[92:95], v1, s[36:39], s63 offen offset:1024 sc1\n"
  "v_mfma_f32_16x16x32_bf16 v[132:135], v[72:75], a[52:55], v[132:135]\n"
  "v_mfma_f32_16x16x32_bf16 v[136:139], v[72:75], a[56:59], v[136:139]\n"
  "ds_read_b64_tr_b16 v[60:61], v21 offset:27648\n"
  "v_mfma_f32_16x16x32_bf16 v[140:143], v[72:75], a[60:63], v[140:143]\n"
  "ds_read_b64_tr_b16 v[62:63], v21 offset:32256\n"
  "v_mfma_f32_16x16x32_bf16 v[144:147], v[76:79], a[48:51], v[144:147]\n"
  "v_mfma_f32_16x16x32_bf16 v[148:151], v[76:79], a[52:55], v[148:151]\n"
  "buffer_load_dwordx4 a[96:99], v1, s[36:39], s60 offen offset:2048 sc1\n"
  "v_mfma_f32_16x16x32_bf16 v[152:155], v[76:79], a[56:59], v[152:155]\n"
  "s_waitcnt vmcnt(9)\n"
  "s_barrier\n"
  "ds_read_b64_tr_b16 v[64:65], v20 offset:36864\n"
  "v_mfma_f32_16x16x32_bf16 v[156:159], v[76:79], a[60:63], v[156:159]\n"
  "s_waitcnt lgkmcnt(3)\n"
  "ds_read_b64_tr_b16 v[66:67], v20 offset:41472\n"
  "v_mfma_f32_16x16x32_bf16 v[160:163], v[48:51], a[48:51], v[160:163]\n"
  "ds_read_b64_tr_b16 v[68:69], v20 offset:46080\n"
  "v_mfma_f32_16x16x32_bf16 v[164:167], v[48:51], a[52:55], v[164:167]\n"
  "ds_read_b64_tr_b16 v[70:71], v20 offset:50688\n"
  "v_mfma_f32_16x16x32_bf16 v[168:171], v[48:51], a[56:59], v[168:171]\n"
  "buffer_load_dwordx4 a[100:103], v1, s[36:39], s61 offen offset:2048 sc1\n"
  "v_mfma_f32_16x16x32_bf16 v[172:175], v[48:51], a[60:63], v[172:175]\n"
  "v_mfma_f32_16x16x32_bf16 v[176:179], v[52:55], a[48:51], v[176:179]\n"
  "v_mfma_f32_16x16x32_bf16 v[180:183], v[52:55], a[52:55], v[180:183]\n"
  "v_mfma_f32_16x16x32_bf16 v[184:187], v[52:55], a[56:59], v[184:187]\n"
  "ds_read_b64_tr_b16 v[72:73], v20 offset:36896\n"
  "v_mfma_f32_16x16x32_bf16 v[188:191], v[52:55], a[60:63], v[188:191]\n"
  "ds_read_b64_tr_b16 v[74:75], v20 offset:41504\n"
  "v_mfma_f32_16x16x32_bf16 v[192:195], v[56:59], a[48:51], v[192:195]\n"
  "ds_read_b64_tr_b16 v[76:77], v20 offset:46112\n"
  "v_mfma_f32_16x16x32_bf16 v[196:199], v[56:59], a[52:55], v[196:199]\n"
  "ds_read_b64_tr_b16 v[78:79], v20 offset:50720\n"
  "v_mfma_f32_16x16x32_bf16 v[200:203], v[56:59], a[56:59], v[200:203]\n"
  "buffer_load_dwordx4 a[104:107], v1, s[36:39], s62 offen offset:2048 sc1\n"
  "v_mfma_f32_16x16x32_bf16 v[204:207], v[56:59], a[60:63], v[204:207]\n"
  "s_waitcnt lgkmcnt(4)\n"
  "v_mfma_f32_16x16x32_bf16 v[208:211], v[60:63], a[48:51], v[208:211]\n"
  "buffer_load_dwordx4 a[108:111], v1, s[36:39], s63 offen offset:2048 sc1\n"
  "v_mfma_f32_16x16x32_bf16 v[212:215], v[60:63], a[52:55], v[212:215]\n"
  "v_mfma_f32_16x16x32_bf16 v[216:219], v[60:63], a[56:59], v[216:219]\n"
  "ds_read_b64_tr_b16 v[48:49], v20 offset:36928\n"
  "v_mfma_f32_16x16x32_bf16 v[220:223], v[60:63], a[60:63], v[220:223]\n"
  "ds_read_b64_tr_b16 v[50:51], v20 offset:41536\n"
  "s_barrier\n"
  "s_waitcnt vmcnt(11)\n"
  "v_mfma_f32_16x16x32_bf16 v[80:83], v[64:67], a[64:67], v[80:83]\n"
  "s_waitcnt vmcnt(7)\n"
  "ds_read_b64_tr_b16 v[52:53], v20 offset:46144\n"
  "v_mfma_f32_16x16x32_bf16 v[80:83], v[68:71], a[80:83], v[80:83]\n"
  "ds_read_b64_tr_b16 v[54:55], v20 offset:50752\n"
  "v_mfma_f32_16x16x32_bf16 v[84:87], v[64:67], a[68:71], v[84:87]\n"
  "s_waitcnt vmcnt(6)\n"
  "buffer_load_dwordx4 a[112:115], v1, s[36:39], s60 offen offset:3072 sc1\n"
  "v_mfma_f32_16x16x32_bf16 v[84:87], v[68:71], a[84:87], v[84:87]\n"
  "v_mfma_f32_16x16x32_bf16 v[88:91], v[64:67], a[72:75], v[88:91]\n"
  "s_waitcnt vmcnt(6)\n"
  "v_mfma_f32_16x16x32_bf16 v[88:91], v[68:71], a[88:91], v[88:91]\n"
  "v_mfma_f32_16x16x32_bf16 v[92:95], v[64:67], a[76:79], v[92:95]\n"
  "s_waitcnt vmcnt(5)\n"
  "ds_read_b64_tr_b16 v[56:57], v20 offset:36960\n"
  "v_mfma_f32_16x16x32_bf16 v[92:95], v[68:71], a[92:95], v[92:95]\n"
  "s_waitcnt lgkmcnt(2)\n"
  "ds_read_b64_tr_b16 v[58:59], v20 offset:41568\n"
  "v_mfma_f32_16x16x32_bf16 v[96:99], v[72:75], a[64:67], v[96:99]\n"
  "ds_read_b64_tr_b16 v[60:61], v20 offset:46176\n"
  "v_mfma_f32_16x16x32_bf16 v[96:99], v[76:79], a[80:83], v[96:99]\n"
  "ds_read_b64_tr_b16 v[62:63], v20 offset:50784\n"
  "v_mfma_f32_16x16x32_bf16 v[100:103], v[72:75], a[68:71], v[100:103]\n"
  "buffer_load_dwordx4 a[116:119], v1, s[36:39], s61 offen offset:3072 sc1\n"
  "v_mfma_f32_16x16x32_bf16 v[100:103], v[76:79], a[84:87], v[100:103]\n"
  "buffer_load_dwordx4 a[120:123], v1, s[36:39], s62 offen offset:3072 sc1\n"
  "v_mfma_f32_16x16x32_bf16 v[104:107], v[72:75], a[72:75], v[104:107]\n"
  "v_mfma_f32_16x16x32_bf16 v[104:107], v[76:79], a[88:91], v[104:107]\n"
  "v_mfma_f32_16x16x32_bf16 v[108:111], v[72:75], a[76:79], v[108:111]\n"
  "ds_read_b64_tr_b16 v[64:65], v20 offset:36992\n"
  "v_mfma_f32_16x16x32_bf16 v[108:111], v[76:79], a[92:95], v[108:111]\n"
  "ds_read_b64_tr_b16 v[66:67], v20 offset:41600\n"
  "v_mfma_f32_16x16x32_bf16 v[112:115], v[48:51], a[64:67], v[112:115]\n"
  "s_waitcnt lgkmcnt(3)\n"
  "ds_read_b64_tr_b16 v[68:69], v20 offset:46208\n"
  "v_mfma_f32_16x16x32_bf16 v[112:115], v[52:55], a[80:83], v[112:115]\n"
  "ds_read_b64_tr_b16 v[70:71], v20 offset:50816\n"
  "v_mfma_f32_16x16x32_bf16 v[116:119], v[48:51], a[68:71], v[116:119]\n"
  "buffer_load_dwordx4 a[124:127], v1, s[36:39], s63 offen offset:3072 sc1\n"
  "v_mfma_f32_16x16x32_bf16 v[116:119], v[52:55], a[84:87], v[116:119]\n"
  "s_add_u32 m0, s67, 73728\n"
  "v_mfma_f32_16x16x32_bf16 v[120:123], v[48:51], a[72:75], v[120:123]\n"
  "s_add_u32 s66, s64, 2359296\n"
  "v_mfma_f32_16x16x32_bf16 v[120:123], v[52:55], a[88:91], v[120:123]\n"
  "buffer_load_dwordx4 v2, s[40:43], s66 offen offset:0 lds\n"
  "v_mfma_f32_16x16x32_bf16 v[124:127], v[48:51], a[76:79], v[124:127]\n"
  "ds_read_b64_tr_b16 v[72:73], v20 offset:37024\n"
  "v_mfma_f32_16x16x32_bf16 v[124:127], v[52:55], a[92:95], v[124:127]\n"
  "ds_read_b64_tr_b16 v[74:75], v20 offset:41632\n"
  "v_mfma_f32_16x16x32_bf16 v[128:131], v[56:59], a[64:67], v[128:131]\n"
  "s_waitcnt lgkmcnt(2)\n"
  "ds_read_b64_tr_b16 v[76:77], v20 offset:46240\n"
  "v_mfma_f32_16x16x32_bf16 v[128:131], v[60:63], a[80:83], v[128:131]\n"
  "ds_read_b64_tr_b16 v[78:79], v20 offset:50848\n"
  "v_mfma_f32_16x16x32_bf16 v[132:135], v[56:59], a[68:71], v[132:135]\n"
  "buffer_load_dwordx4 v3, s[40:43], s66 offen offset:1024 lds\n"
  "v_mfma_f32_16x16x32_bf16 v[132:135], v[60:63], a[84:87], v[132:135]\n"
  "v_mfma_f32_16x16x32_bf16 v[136:139], v[56:59], a[72:75], v[136:139]\n"
  "v_mfma_f32_16x16x32_bf16 v[136:139], v[60:63], a[88:91], v[136:139]\n"
  "v_mfma_f32_16x16x32_bf16 v[140:143], v[56:59], a[76:79], v[140:143]\n"
  "ds_read_b64_tr_b16 v[48:49], v20 offset:37056\n"
  "v_mfma_f32_16x16x32_bf16 v[140:143], v[60:63], a[92:95], v[140:143]\n"
  "ds_read_b64_tr_b16 v[50:51], v20 offset:41664\n"
  "v_mfma_f32_16x16x32_bf16 v[144:147], v[64:67], a[64:67], v[144:147]\n"
  "ds_read_b64_tr_b16 v[52:53], v20 offset:46272\n"
  "v_mfma_f32_16x16x32_bf16 v[144:147], v[68:71], a[80:83], v[144:147]\n"
  "ds_read_b64_tr_b16 v[54:55], v20 offset:50880\n"
  "v_mfma_f32_16x16x32_bf16 v[148:151], v[64:67], a[68:71], v[148:151]\n"
  "buffer_load_dwordx4 v4, s[40:43], s66 offen offset:2048 lds\n"
  "v_mfma_f32_16x16x32_bf16 v[148:151], v[68:71], a[84:87], v[148:151]\n"
  "v_mfma_f32_16x16x32_bf16 v[152:155], v[64:67], a[72:75], v[152:155]\n"
  "buffer_load_dwordx4 v5, s[40:43], s66 offen offset:3072 lds\n"
  "v_mfma_f32_16x16x32_bf16 v[152:155], v[68:71], a[88:91], v[152:155]\n"
  "s_add_u32 m0, s67, 77824\n"
  "v_mfma_f32_16x16x32_bf16 v[156:159], v[64:67], a[76:79], v[156:159]\n"
  "ds_read_b64_tr_b16 v[56:57], v20 offset:37088\n"
  "v_mfma_f32_16x16x32_bf16 v[156:159], v[68:71], a[92:95], v[156:159]\n"
  "s_waitcnt lgkmcnt(2)\n"
  "ds_read_b64_tr_b16 v[58:59], v20 offset:41696\n"
  "v_mfma_f32_16x16x32_bf16 v[160:163], v[72:75], a[64:67], v[160:163]\n"
  "ds_read_b64_tr_b16 v[60:61], v20 offset:46304\n"
  "v_mfma_f32_16x16x32_bf16 v[160:163], v[76:79], a[80:83], v[160:163]\n"
  "ds_read_b64_tr_b16 v[62:63], v20 offset:50912\n"
  "v_mfma_f32_16x16x32_bf16 v[164:167], v[72:75], a[68:71], v[164:167]\n"
  "buffer_load_dwordx4 v6, s[40:43], s66 offen offset:0 lds\n"
  "v_mfma_f32_16x16x32_bf16 v[164:167], v[76:79], a[84:87], v[164:167]\n"
  "v_mfma_f32_16x16x32_bf16 v[168:171], v[72:75], a[72:75], v[168:171]\n"
  "v_mfma_f32_16x16x32_bf16 v[168:171], v[76:79], a[88:91], v[168:171]\n"
  "v_mfma_f32_16x16x32_bf16 v[172:175], v[72:75], a[76:79], v[172:175]\n"
  "ds_read_b64_tr_b16 v[64:65], v21 offset:36864\n"
  "v_mfma_f32_16x16x32_bf16 v[172:175], v[76:79], a[92:95], v[172:175]\n"
  "ds_read_b64_tr_b16 v[66:67], v21 offset:41472\n"
  "v_mfma_f32_16x16x32_bf16 v[176:179], v[48:51], a[64:67], v[176:179]\n"
  "s_waitcnt lgkmcnt(3)\n"
  "ds_read_b64_tr_b16 v[68:69], v21 offset:46080\n"
  "v_mfma_f32_16x16x32_bf16 v[176:179], v[52:55], a[80:83], v[176:179]\n"
  "ds_read_b64_tr_b16 v[70:71], v21 offset:50688\n"
  "v_mfma_f32_16x16x32_bf16 v[180:183], v[48:51], a[68:71], v[180:183]\n"
  "buffer_load_dwordx4 v7, s[40:43], s66 offen offset:1024 lds\n"
  "v_mfma_f32_16x16x32_bf16 v[180:183], v[52:55], a[84:87], v[180:183]\n"
  "buffer_load_dwordx4 v8, s[40:43], s66 offen offset:2048 lds\n"
  "v_mfma_f32_16x16x32_bf16 v[184:187], v[48:51], a[72:75], v[184:187]\n"
  "v_mfma_f32_16x16x32_bf16 v[184:187], v[52:55], a[88:91], v[184:187]\n"
  "v_mfma_f32_16x16x32_bf16 v[188:191], v[48:51], a[76:79], v[188:191]\n"
  "ds_read_b64_tr_b16 v[72:73], v20 offset:55296\n"
  "v_mfma_f32_16x16x32_bf16 v[188:191], v[52:55], a[92:95], v[188:191]\n"
  "ds_read_b64_tr_b16 v[74:75], v20 offset:59904\n"
  "v_add_u32 v20, 36864, v20\n"
  "v_mfma_f32_16x16x32_bf16 v[192:195], v[56:59], a[64:67], v[192:195]\n"
  "s_waitcnt lgkmcnt(2)\n"
  "ds_read_b64_tr_b16 v[76:77], v20 offset:27648\n"
  "v_mfma_f32_16x16x32_bf16 v[192:195], v[60:63], a[80:83], v[192:195]\n"
  "ds_read_b64_tr_b16 v[78:79], v20 offset:32256\n"
  "v_mfma_f32_16x16x32_bf16 v[196:199], v[56:59], a[68:71], v[196:199]\n"
  "buffer_load_dwordx4 v9, s[40:43], s66 offen offset:3072 lds\n"
  "v_mfma_f32_16x16x32_bf16 v[196:199], v[60:63], a[84:87], v[196:199]\n"
  "s_add_u32 m0, s67, 81920\n"
  "v_mfma_f32_16x16x32_bf16 v[200:203], v[56:59], a[72:75], v[200:203]\n"
  "v_mfma_f32_16x16x32_bf16 v[200:203], v[60:63], a[88:91], v[200:203]\n"
  "buffer_load_dwordx4 v10, s[40:43], s66 offen offset:0 lds\n"
  "v_mfma_f32_16x16x32_bf16 v[204:207], v[56:59], a[76:79], v[204:207]\n"
  "s_add_u32 s60, s60, 4096\n"
  "ds_read_b64_tr_b16 v[48:49], v20 offset:18464\n"
  "v_mfma_f32_16x16x32_bf16 v[204:207], v[60:63], a[92:95], v[204:207]\n"
  "ds_read_b64_tr_b16 v[50:51], v20 offset:23072\n"
  "v_mfma_f32_16x16x32_bf16 v[208:211], v[64:67], a[64:67], v[208:211]\n"
  "ds_read_b64_tr_b16 v[52:53], v20 offset:27680\n"
  "v_mfma_f32_16x16x32_bf16 v[208:211], v[68:71], a[80:83], v[208:211]\n"
  "ds_read_b64_tr_b16 v[54:55], v20 offset:32288\n"
  "v_mfma_f32_16x16x32_bf16 v[212:215], v[64:67], a[68:71], v[212:215]\n"
  "buffer_load_dwordx4 a[128:131], v1, s[36:39], s60 offen offset:0 sc1\n"
  "v_mfma_f32_16x16x32_bf16 v[212:215], v[68:71], a[84:87], v[212:215]\n"
  "s_add_u32 s61, s61, 4096\n"
  "v_mfma_f32_16x16x32_bf16 v[216:219], v[64:67], a[72:75], v[216:219]\n"
  "v_mfma_f32_16x16x32_bf16 v[216:219], v[68:71], a[88:91], v[216:219]\n"
  "v_mfma_f32_16x16x32_bf16 v[220:223], v[64:67], a[76:79], v[220:223]\n"
  "ds_read_b64_tr_b16 v[56:57], v20 offset:18496\n"
  "v_mfma_f32_16x16x32_bf16 v[220:223], v[68:71], a[92:95], v[220:223]\n"
  "s_waitcnt vmcnt(17) lgkmcnt(3)\n"
  "ds_read_b64_tr_b16 v[58:59], v20 offset:23104\n"
  "v_mfma_f32_16x16x32_bf16 v[80:83], v[72:75], a[96:99], v[80:83]\n"
  "s_waitcnt vmcnt(13)\n"
  "ds_read_b64_tr_b16 v[60:61], v20 offset:27712\n"
  "v_mfma_f32_16x16x32_bf16 v[80:83], v[76:79], a[112:115], v[80:83]\n"
  "ds_read_b64_tr_b16 v[62:63], v20 offset:32320\n"
  "v_mfma_f32_16x16x32_bf16 v[84:87], v[72:75], a[100:103], v[84:87]\n"
  "s_waitcnt vmcnt(12)\n"
  "buffer_load_dwordx4 a[132:135], v1, s[36:39], s61 offen offset:0 sc1\n"
  "v_mfma_f32_16x16x32_bf16 v[84:87], v[76:79], a[116:119], v[84:87]\n"
  "s_add_u32 s62, s62, 4096\n"
  "v_mfma_f32_16x16x32_bf16 v[88:91], v[72:75], a[104:107], v[88:91]\n"
  "s_waitcnt vmcnt(12)\n"
  "buffer_load_dwordx4 a[136:139], v1, s[36:39], s62 offen offset:0 sc1\n"
  "v_mfma_f32_16x16x32_bf16 v[88:91], v[76:79], a[120:123], v[88:91]\n"
  "s_add_u32 s63, s63, 4096\n"
  "v_mfma_f32_16x16x32_bf16 v[92:95], v[72:75], a[108:111], v[92:95]\n"
  "s_waitcnt vmcnt(12)\n"
  "ds_read_b64_tr_b16 v[64:65], v20 offset:18528\n"
  "v_mfma_f32_16x16x32_bf16 v[92:95], v[76:79], a[124:127], v[92:95]\n"
  "ds_read_b64_tr_b16 v[66:67], v20 offset:23136\n"
  "v_mfma_f32_16x16x32_bf16 v[96:99], v[48:51], a[96:99], v[96:99]\n"
  "s_waitcnt lgkmcnt(2)\n"
  "ds_read_b64_tr_b16 v[68:69], v20 offset:27744\n"
  "v_mfma_f32_16x16x32_bf16 v[96:99], v[52:55], a[112:115], v[96:99]\n"
  "ds_read_b64_tr_b16 v[70:71], v20 offset:32352\n"
  "v_mfma_f32_16x16x32_bf16 v[100:103], v[48:51], a[100:103], v[100:103]\n"
  "buffer_load_dwordx4 a[140:143], v1, s[36:39], s63 offen offset:0 sc1\n"
  "v_mfma_f32_16x16x32_bf16 v[100:103], v[52:55], a[116:119], v[100:103]\n"
  "v_mfma_f32_16x16x32_bf16 v[104:107], v[48:51], a[104:107], v[104:107]\n"
  "v_mfma_f32_16x16x32_bf16 v[104:107], v[52:55], a[120:123], v[104:107]\n"
  "v_mfma_f32_16x16x32_bf16 v[108:111], v[48:51], a[108:111], v[108:111]\n"
  "ds_read_b64_tr_b16 v[72:73], v20 offset:18560\n"
  "v_mfma_f32_16x16x32_bf16 v[108:111], v[52:55], a[124:127], v[108:111]\n"
  "ds_read_b64_tr_b16 v[74:75], v20 offset:23168\n"
  "v_mfma_f32_16x16x32_bf16 v[112:115], v[56:59], a[96:99], v[112:115]\n"
  "ds_read_b64_tr_b16 v[76:77], v20 offset:27776\n"
  "v_mfma_f32_16x16x32_bf16 v[112:115], v[60:63], a[112:115], v[112:115]\n"
  "ds_read_b64_tr_b16 v[78:79], v20 offset:32384\n"
  "v_mfma_f32_16x16x32_bf16 v[116:119], v[56:59], a[100:103], v[116:119]\n"
  "buffer_load_dwordx4 a[144:147], v1, s[36:39], s60 offen offset:1024 sc1\n"
  "v_mfma_f32_16x16x32_bf16 v[116:119], v[60:63], a[116:119], v[116:119]\n"
  "buffer_load_dwordx4 a[148:151], v1, s[36:39], s61 offen offset:1024 sc1\n"
  "v_mfma_f32_16x16x32_bf16 v[120:123], v[56:59], a[104:107], v[120:123]\n"
  "v_mfma_f32_16x16x32_bf16 v[120:123], v[60:63], a[120:123], v[120:123]\n"
  "v_mfma_f32_16x16x32_bf16 v[124:127], v[56:59], a[108:111], v[124:127]\n"
  "ds_read_b64_tr_b16 v[48:49], v20 offset:18592\n"
  "v_mfma_f32_16x16x32_bf16 v[124:127], v[60:63], a[124:127], v[124:127]\n"
  "s_waitcnt lgkmcnt(3)\n"
  "ds_read_b64_tr_b16 v[50:51], v20 offset:23200\n"
  "v_mfma_f32_16x16x32_bf16 v[128:131], v[64:67], a[96:99], v[128:131]\n"
  "ds_read_b64_tr_b16 v[52:53], v20 offset:27808\n"
  "v_mfma_f32_16x16x32_bf16 v[128:131], v[68:71], a[112:115], v[128:131]\n"
  "ds_read_b64_tr_b16 v[54:55], v20 offset:32416\n"
  "v_mfma_f32_16x16x32_bf16 v[132:135], v[64:67], a[100:103], v[132:135]\n"
  "buffer_load_dwordx4 a[152:155], v1, s[36:39], s62 offen offset:1024 sc1\n"
  "v_mfma_f32_16x16x32_bf16 v[132:135], v[68:71], a[116:119], v[132:135]\n"
  "v_mfma_f32_16x16x32_bf16 v[136:139], v[64:67], a[104:107], v[136:139]\n"
  "v_mfma_f32_16x16x32_bf16 v[136:139], v[68:71], a[120:123], v[136:139]\n"
  "buffer_load_dwordx4 a[156:159], v1, s[36:39], s63 offen offset:1024 sc1\n"
  "v_mfma_f32_16x16x32_bf16 v[140:143], v[64:67], a[108:111], v[140:143]\n"
  "ds_read_b64_tr_b16 v[56:57], v20 offset:18624\n"
  "v_mfma_f32_16x16x32_bf16 v[140:143], v[68:71], a[124:127], v[140:143]\n"
  "ds_read_b64_tr_b16 v[58:59], v20 offset:23232\n"
  "v_mfma_f32_16x16x32_bf16 v[144:147], v[72:75], a[96:99], v[144:147]\n"
  "s_waitcnt lgkmcnt(3)\n"
  "ds_read_b64_tr_b16 v[60:61], v20 offset:27840\n"
  "v_mfma_f32_16x16x32_bf16 v[144:147], v[76:79], a[112:115], v[144:147]\n"
  "ds_read_b64_tr_b16 v[62:63], v20 offset:32448\n"
  "v_mfma_f32_16x16x32_bf16 v[148:151], v[72:75], a[100:103], v[148:151]\n"
  "buffer_load_dwordx4 a[160:163], v1, s[36:39], s60 offen offset:2048 sc1\n"
  "v_mfma_f32_16x16x32_bf16 v[148:151], v[76:79], a[116:119], v[148:151]\n"
  "v_mfma_f32_16x16x32_bf16 v[152:155], v[72:75], a[104:107], v[152:155]\n"
  "v_mfma_f32_16x16x32_bf16 v[152:155], v[76:79], a[120:123], v[152:155]\n"
  "v_mfma_f32_16x16x32_bf16 v[156:159], v[72:75], a[108:111], v[156:159]\n"
  "ds_read_b64_tr_b16 v[64:65], v20 offset:18656\n"
  "v_mfma_f32_16x16x32_bf16 v[156:159], v[76:79], a[124:127], v[156:159]\n"
  "ds_read_b64_tr_b16 v[66:67], v20 offset:23264\n"
  "v_mfma_f32_16x16x32_bf16 v[160:163], v[48:51], a[96:99], v[160:163]\n"
  "s_waitcnt lgkmcnt(3)\n"
  "ds_read_b64_tr_b16 v[68:69], v20 offset:27872\n"
  "v_mfma_f32_16x16x32_bf16 v[160:163], v[52:55], a[112:115], v[160:163]\n"
  "ds_read_b64_tr_b16 v[70:71], v20 offset:32480\n"
  "v_mfma_f32_16x16x32_bf16 v[164:167], v[48:51], a[100:103], v[164:167]\n"
  "buffer_load_dwordx4 a[164:167], v1, s[36:39], s61 offen offset:2048 sc1\n"
  "v_mfma_f32_16x16x32_bf16 v[164:167], v[52:55], a[116:119], v[164:167]\n"
  "v_mfma_f32_16x16x32_bf16 v[168:171], v[48:51], a[104:107], v[168:171]\n"
  "buffer_load_dwordx4 a[168:171], v1, s[36:39], s62 offen offset:2048 sc1\n"
  "v_mfma_f32_16x16x32_bf16 v[168:171], v[52:55], a[120:123], v[168:171]\n"
  "v_mfma_f32_16x16x32_bf16 v[172:175], v[48:51], a[108:111], v[172:175]\n"
  "ds_read_b64_tr_b16 v[72:73], v21 offset:55296\n"
  "v_mfma_f32_16x16x32_bf16 v[172:175], v[52:55], a[124:127], v[172:175]\n"
  "ds_read_b64_tr_b16 v[74:75], v21 offset:59904\n"
  "v_add_u32 v21, 36864, v21\n"
  "v_mfma_f32_16x16x32_bf16 v[176:179], v[56:59], a[96:99], v[176:179]\n"
  "s_waitcnt lgkmcnt(2)\n"
  "ds_read_b64_tr_b16 v[76:77], v21 offset:27648\n"
  "v_mfma_f32_16x16x32_bf16 v[176:179], v[60:63], a[112:115], v[176:179]\n"
  "ds_read_b64_tr_b16 v[78:79], v21 offset:32256\n"
  "v_mfma_f32_16x16x32_bf16 v[180:183], v[56:59], a[100:103], v[180:183]\n"
  "buffer_load_dwordx4 a[172:175], v1, s[36:39], s63 offen offset:2048 sc1\n"
  "v_mfma_f32_16x16x32_bf16 v[180:183], v[60:63], a[116:119], v[180:183]\n"
  "v_mfma_f32_16x16x32_bf16 v[184:187], v[56:59], a[104:107], v[184:187]\n"
  "v_mfma_f32_16x16x32_bf16 v[184:187], v[60:63], a[120:123], v[184:187]\n"
  "v_mfma_f32_16x16x32_bf16 v[188:191], v[56:59], a[108:111], v[188:191]\n"
  "s_waitcnt vmcnt(12)\n"
  "s_barrier\n"
  "ds_read_b64_tr_b16 v[48:49], v20 offset:36864\n"
  "v_mfma_f32_16x16x32_bf16 v[188:191], v[60:63], a[124:127], v[188:191]\n"
  "ds_read_b64_tr_b16 v[50:51], v20 offset:41472\n"
  "v_mfma_f32_16x16x32_bf16 v[192:195], v[64:67], a[96:99], v[192:195]\n"
  "ds_read_b64_tr_b16 v[52:53], v20 offset:46080\n"
  "v_mfma_f32_16x16x32_bf16 v[192:195], v[68:71], a[112:115], v[192:195]\n"
  "ds_read_b64_tr_b16 v[54:55], v20 offset:50688\n"
  "v_mfma_f32_16x16x32_bf16 v[196:199], v[64:67], a[100:103], v[196:199]\n"
  "buffer_load_dwordx4 a[176:179], v1, s[36:39], s60 offen offset:3072 sc1\n"
  "v_mfma_f32_16x16x32_bf16 v[196:199], v[68:71], a[116:119], v[196:199]\n"
  "buffer_load_dwordx4 a[180:183], v1, s[36:39], s61 offen offset:3072 sc1\n"
  "v_mfma_f32_16x16x32_bf16 v[200:203], v[64:67], a[104:107], v[200:203]\n"
  "v_mfma_f32_16x16x32_bf16 v[200:203], v[68:71], a[120:123], v[200:203]\n"
  "v_mfma_f32_16x16x32_bf16 v[204:207], v[64:67], a[108:111], v[204:207]\n"
  "ds_read_b64_tr_b16 v[56:57], v20 offset:36896\n"
  "v_mfma_f32_16x16x32_bf16 v[204:207], v[68:71], a[124:127], v[204:207]\n"
  "s_waitcnt lgkmcnt(3)\n"
  "ds_read_b64_tr_b16 v[58:59], v20 offset:41504\n"
  "v_mfma_f32_16x16x32_bf16 v[208:211], v[72:75], a[96:99], v[208:211]\n"
  "ds_read_b64_tr_b16 v[60:61], v20 offset:46112\n"
  "v_mfma_f32_16x16x32_bf16 v[208:211], v[76:79], a[112:115], v[208:211]\n"
  "ds_read_b64_tr_b16 v[62:63], v20 offset:50720\n"
  "v_mfma_f32_16x16x32_bf16 v[212:215], v[72:75], a[100:103], v[212:215]\n"
  "buffer_load_dwordx4 a[184:187], v1, s[36:39], s62 offen offset:3072 sc1\n"
  "v_mfma_f32_16x16x32_bf16 v[212:215], v[76:79], a[116:119], v[212:215]\n"
  "v_mfma_f32_16x16x32_bf16 v[216:219], v[72:75], a[104:107], v[216:219]\n"
  "v_mfma_f32_16x16x32_bf16 v[216:219], v[76:79], a[120:123], v[216:219]\n"
  "buffer_load_dwordx4 a[188:191], v1, s[36:39], s63 offen offset:3072 sc1\n"
  "v_mfma_f32_16x16x32_bf16 v[220:223], v[72:75], a[108:111], v[220:223]\n"
  "s_add_u32 m0, s67, 110592\n"
  "ds_read_b64_tr_b16 v[64:65], v20 offset:36928\n"
  "v_mfma_f32_16x16x32_bf16 v[220:223], v[76:79], a[124:127], v[220:223]\n"
  "ds_read_b64_tr_b16 v[66:67], v20 offset:41536\n"
  "s_barrier\n"
  "s_waitcnt vmcnt(15)\n"
  "v_mfma_f32_16x16x32_bf16 v[80:83], v[48:51], a[128:131], v[80:83]\n"
  "s_add_u32 s66, s64, 3538944\n"
  "ds_read_b64_tr_b16 v[68:69], v20 offset:46144\n"
  "s_waitcnt vmcnt(11) lgkmcnt(3)\n"
  "v_mfma_f32_16x16x32_bf16 v[80:83], v[52:55], a[144:147], v[80:83]\n"
  "ds_read_b64_tr_b16 v[70:71], v20 offset:50752\n"
  "v_mfma_f32_16x16x32_bf16 v[84:87], v[48:51], a[132:135], v[84:87]\n"
  "s_waitcnt vmcnt(10)\n"
  "buffer_load_dwordx4 v2, s[40:43], s66 offen offset:0 lds\n"
  "v_mfma_f32_16x16x32_bf16 v[84:87], v[52:55], a[148:151], v[84:87]\n"
  "v_mfma_f32_16x16x32_bf16 v[88:91], v[48:51], a[136:139], v[88:91]\n"
  "s_waitcnt vmcnt(10)\n"
  "v_mfma_f32_16x16x32_bf16 v[88:91], v[52:55], a[152:155], v[88:91]\n"
  "v_mfma_f32_16x16x32_bf16 v[92:95], v[48:51], a[140:143], v[92:95]\n"
  "s_waitcnt vmcnt(9)\n"
  "ds_read_b64_tr_b16 v[72:73], v20 offset:36960\n"
  "v_mfma_f32_16x16x32_bf16 v[92:95], v[52:55], a[156:159], v[92:95]\n"
  "ds_read_b64_tr_b16 v[74:75], v20 offset:41568\n"
  "v_mfma_f32_16x16x32_bf16 v[96:99], v[56:59], a[128:131], v[96:99]\n"
  "ds_read_b64_tr_b16 v[76:77], v20 offset:46176\n"
  "v_mfma_f32_16x16x32_bf16 v[96:99], v[60:63], a[144:147], v[96:99]\n"
  "ds_read_b64_tr_b16 v[78:79], v20 offset:50784\n"
  "v_mfma_f32_16x16x32_bf16 v[100:103], v[56:59], a[132:135], v[100:103]\n"
  "buffer_load_dwordx4 v3, s[40:43], s66 offen offset:1024 lds\n"
  "v_mfma_f32_16x16x32_bf16 v[100:103], v[60:63], a[148:151], v[100:103]\n"
  "v_mfma_f32_16x16x32_bf16 v[104:107], v[56:59], a[136:139], v[104:107]\n"
  "buffer_load_dwordx4 v4, s[40:43], s66 offen offset:2048 lds\n"
  "v_mfma_f32_16x16x32_bf16 v[104:107], v[60:63], a[152:155], v[104:107]\n"
  "v_mfma_f32_16x16x32_bf16 v[108:111], v[56:59], a[140:143], v[108:111]\n"
  "ds_read_b64_tr_b16 v[48:49], v20 offset:36992\n"
  "v_mfma_f32_16x16x32_bf16 v[108:111], v[60:63], a[156:159], v[108:111]\n"
  "s_waitcnt lgkmcnt(3)\n"
  "ds_read_b64_tr_b16 v[50:51], v20 offset:41600\n"
  "v_mfma_f32_16x16x32_bf16 v[112:115], v[64:67], a[128:131], v[112:115]\n"
  "ds_read_b64_tr_b16 v[52:53], v20 offset:46208\n"
  "v_mfma_f32_16x16x32_bf16 v[112:115], v[68:71], a[144:147], v[112:115]\n"
  "ds_read_b64_tr_b16 v[54:55], v20 offset:50816\n"
  "v_mfma_f32_16x16x32_bf16 v[116:119], v[64:67], a[132:135], v[116:119]\n"
  "buffer_load_dwordx4 v5, s[40:43], s66 offen offset:3072 lds\n"
  "v_mfma_f32_16x16x32_bf16 v[116:119], v[68:71], a[148:151], v[116:119]\n"
  "s_add_u32 m0, s67, 114688\n"
  "v_mfma_f32_16x16x32_bf16 v[120:123], v[64:67], a[136:139], v[120:123]\n"
  "v_mfma_f32_16x16x32_bf16 v[120:123], v[68:71], a[152:155], v[120:123]\n"
  "v_mfma_f32_16x16x32_bf16 v[124:127], v[64:67], a[140:143], v[124:127]\n"
  "ds_read_b64_tr_b16 v[56:57], v20 offset:37024\n"
  "v_mfma_f32_16x16x32_bf16 v[124:127], v[68:71], a[156:159], v[124:127]\n"
  "ds_read_b64_tr_b16 v[58:59], v20 offset:41632\n"
  "v_mfma_f32_16x16x32_bf16 v[128:131], v[72:75], a[128:131], v[128:131]\n"
  "s_waitcnt lgkmcnt(3)\n"
  "ds_read_b64_tr_b16 v[60:61], v20 offset:46240\n"
  "v_mfma_f32_16x16x32_bf16 v[128:131], v[76:79], a[144:147], v[128:131]\n"
  "ds_read_b64_tr_b16 v[62:63], v20 offset:50848\n"
  "v_mfma_f32_16x16x32_bf16 v[132:135], v[72:75], a[132:135], v[132:135]\n"
  "buffer_load_dwordx4 v6, s[40:43], s66 offen offset:0 lds\n"
  "v_mfma_f32_16x16x32_bf16 v[132:135], v[76:79], a[148:151], v[132:135]\n"
  "buffer_load_dwordx4 v7, s[40:43], s66 offen offset:1024 lds\n"
  "v_mfma_f32_16x16x32_bf16 v[136:139], v[72:75], a[136:139], v[136:139]\n"
  "v_mfma_f32_16x16x32_bf16 v[136:139], v[76:79], a[152:155], v[136:139]\n"
  "v_mfma_f32_16x16x32_bf16 v[140:143], v[72:75], a[140:143], v[140:143]\n"
  "ds_read_b64_tr_b16 v[64:65], v20 offset:37056\n"
  "v_mfma_f32_16x16x32_bf16 v[140:143], v[76:79], a[156:159], v[140:143]\n"
  "ds_read_b64_tr_b16 v[66:67], v20 offset:41664\n"
  "v_mfma_f32_16x16x32_bf16 v[144:147], v[48:51], a[128:131], v[144:147]\n"
  "s_waitcnt lgkmcnt(3)\n"
  "ds_read_b64_tr_b16 v[68:69], v20 offset:46272\n"
  "v_mfma_f32_16x16x32_bf16 v[144:147], v[52:55], a[144:147], v[144:147]\n"
  "ds_read_b64_tr_b16 v[70:71], v20 offset:50880\n"
  "v_mfma_f32_16x16x32_bf16 v[148:151], v[48:51], a[132:135], v[148:151]\n"
  "buffer_load_dwordx4 v8, s[40:43], s66 offen offset:2048 lds\n"
  "v_mfma_f32_16x16x32_bf16 v[148:151], v[52:55], a[148:151], v[148:151]\n"
  "v_mfma_f32_16x16x32_bf16 v[152:155], v[48:51], a[136:139], v[152:155]\n"
  "v_mfma_f32_16x16x32_bf16 v[152:155], v[52:55], a[152:155], v[152:155]\n"
  "buffer_load_dwordx4 v9, s[40:43], s66 offen offset:3072 lds\n"
  "v_mfma_f32_16x16x32_bf16 v[156:159], v[48:51], a[140:143], v[156:159]\n"
  "s_add_u32 m0, s67, 118784\n"
  "ds_read_b64_tr_b16 v[72:73], v20 offset:37088\n"
  "v_mfma_f32_16x16x32_bf16 v[156:159], v[52:55], a[156:159], v[156:159]\n"
  "ds_read_b64_tr_b16 v[74:75], v20 offset:41696\n"
  "v_mfma_f32_16x16x32_bf16 v[160:163], v[56:59], a[128:131], v[160:163]\n"
  "s_waitcnt lgkmcnt(2)\n"
  "ds_read_b64_tr_b16 v[76:77], v20 offset:46304\n"
  "v_mfma_f32_16x16x32_bf16 v[160:163], v[60:63], a[144:147], v[160:163]\n"
  "ds_read_b64_tr_b16 v[78:79], v20 offset:50912\n"
  "v_mfma_f32_16x16x32_bf16 v[164:167], v[56:59], a[132:135], v[164:167]\n"
  "buffer_load_dwordx4 v10, s[40:43], s66 offen offset:0 lds\n"
  "v_mfma_f32_16x16x32_bf16 v[164:167], v[60:63], a[148:151], v[164:167]\n"
  "s_add_u32 s60, s60, 4096\n"
  "v_mfma_f32_16x16x32_bf16 v[168:171], v[56:59], a[136:139], v[168:171]\n"
  "v_mfma_f32_16x16x32_bf16 v[168:171], v[60:63], a[152:155], v[168:171]\n"
  "v_mfma_f32_16x16x32_bf16 v[172:175], v[56:59], a[140:143], v[172:175]\n"
  "ds_read_b64_tr_b16 v[48:49], v21 offset:36864\n"
  "v_mfma_f32_16x16x32_bf16 v[172:175], v[60:63], a[156:159], v[172:175]\n"
  "ds_read_b64_tr_b16 v[50:51], v21 offset:41472\n"
  "v_mfma_f32_16x16x32_bf16 v[176:179], v[64:67], a[128:131], v[176:179]\n"
  "ds_read_b64_tr_b16 v[52:53], v21 offset:46080\n"
  "v_mfma_f32_16x16x32_bf16 v[176:179], v[68:71], a[144:147], v[176:179]\n"
  "ds_read_b64_tr_b16 v[54:55], v21 offset:50688\n"
  "v_mfma_f32_16x16x32_bf16 v[180:183], v[64:67], a[132:135], v[180:183]\n"
  "buffer_load_dwordx4 a[192:195], v1, s[36:39], s60 offen offset:0 sc1\n"
  "v_mfma_f32_16x16x32_bf16 v[180:183], v[68:71], a[148:151], v[180:183]\n"
  "s_add_u32 s61, s61, 4096\n"
  "v_mfma_f32_16x16x32_bf16 v[184:187], v[64:67], a[136:139], v[184:187]\n"
  "buffer_load_dwordx4 a[196:199], v1, s[36:39], s61 offen offset:0 sc1\n"
  "v_mfma_f32_16x16x32_bf16 v[184:187], v[68:71], a[152:155], v[184:187]\n"
  "s_add_u32 s62, s62, 4096\n"
  "v_mfma_f32_16x16x32_bf16 v[188:191], v[64:67], a[140:143], v[188:191]\n"
  "ds_read_b64_tr_b16 v[56:57], v20 offset:55296\n"
  "v_mfma_f32_16x16x32_bf16 v[188:191], v[68:71], a[156:159], v[188:191]\n"
  "s_waitcnt lgkmcnt(1)\n"
  "ds_read_b64_tr_b16 v[58:59], v20 offset:59904\n"
  "v_add_u32 v20, 36864, v20\n"
  "v_mfma_f32_16x16x32_bf16 v[192:195], v[72:75], a[128:131], v[192:195]\n"
  "ds_read_b64_tr_b16 v[60:61], v20 offset:27648\n"
  "v_mfma_f32_16x16x32_bf16 v[192:195], v[76:79], a[144:147], v[192:195]\n"
  "ds_read_b64_tr_b16 v[62:63], v20 offset:32256\n"
  "v_mfma_f32_16x16x32_bf16 v[196:199], v[72:75], a[132:135], v[196:199]\n"
  "buffer_load_dwordx4 a[200:203], v1, s[36:39], s62 offen offset:0 sc1\n"
  "v_mfma_f32_16x16x32_bf16 v[196:199], v[76:79], a[148:151], v[196:199]\n"
  "s_add_u32 s63, s63, 4096\n"
  "v_mfma_f32_16x16x32_bf16 v[200:203], v[72:75], a[136:139], v[200:203]\n"
  "v_mfma_f32_16x16x32_bf16 v[200:203], v[76:79], a[152:155], v[200:203]\n"
  "v_mfma_f32_16x16x32_bf16 v[204:207], v[72:75], a[140:143], v[204:207]\n"
  "ds_read_b64_tr_b16 v[64:65], v20 offset:18464\n"
  "v_mfma_f32_16x16x32_bf16 v[204:207], v[76:79], a[156:159], v[204:207]\n"
  "ds_read_b64_tr_b16 v[66:67], v20 offset:23072\n"
  "v_mfma_f32_16x16x32_bf16 v[208:211], v[48:51], a[128:131], v[208:211]\n"
  "ds_read_b64_tr_b16 v[68:69], v20 offset:27680\n"
  "v_mfma_f32_16x16x32_bf16 v[208:211], v[52:55], a[144:147], v[208:211]\n"
  "ds_read_b64_tr_b16 v[70:71], v20 offset:32288\n"
  "v_mfma_f32_16x16x32_bf16 v[212:215], v[48:51], a[132:135], v[212:215]\n"
  "buffer_load_dwordx4 a[204:207], v1, s[36:39], s63 offen offset:0 sc1\n"
  "v_mfma_f32_16x16x32_bf16 v[212:215], v[52:55], a[148:151], v[212:215]\n"
  "buffer_load_dwordx4 a[208:211], v1, s[36:39], s60 offen offset:1024 sc1\n"
  "v_mfma_f32_16x16x32_bf16 v[216:219], v[48:51], a[136:139], v[216:219]\n"
  "v_mfma_f32_16x16x32_bf16 v[216:219], v[52:55], a[152:155], v[216:219]\n"
  "v_mfma_f32_16x16x32_bf16 v[220:223], v[48:51], a[140:143], v[220:223]\n"
  "ds_read_b64_tr_b16 v[72:73], v20 offset:18496\n"
  "v_mfma_f32_16x16x32_bf16 v[220:223], v[52:55], a[156:159], v[220:223]\n"
  "s_waitcnt vmcnt(21) lgkmcnt(3)\n"
  "ds_read_b64_tr_b16 v[74:75], v20 offset:23104\n"
  "v_mfma_f32_16x16x32_bf16 v[80:83], v[56:59], a[160:163], v[80:83]\n"
  "s_waitcnt vmcnt(17)\n"
  "ds_read_b64_tr_b16 v[76:77], v20 offset:27712\n"
  "v_mfma_f32_16x16x32_bf16 v[80:83], v[60:63], a[176:179], v[80:83]\n"
  "ds_read_b64_tr_b16 v[78:79], v20 offset:32320\n"
  "v_mfma_f32_16x16x32_bf16 v[84:87], v[56:59], a[164:167], v[84:87]\n"
  "s_waitcnt vmcnt(16)\n"
  "buffer_load_dwordx4 a[212:215], v1, s[36:39], s61 offen offset:1024 sc1\n"
  "v_mfma_f32_16x16x32_bf16 v[84:87], v[60:63], a[180:183], v[84:87]\n"
  "v_mfma_f32_16x16x32_bf16 v[88:91], v[56:59], a[168:171], v[88:91]\n"
  "s_waitcnt vmcnt(16)\n"
  "v_mfma_f32_16x16x32_bf16 v[88:91], v[60:63], a[184:187], v[88:91]\n"
  "buffer_load_dwordx4 a[216:219], v1, s[36:39], s62 offen offset:1024 sc1\n"
  "v_mfma_f32_16x16x32_bf16 v[92:95], v[56:59], a[172:175], v[92:95]\n"
  "s_waitcnt vmcnt(16)\n"
  "ds_read_b64_tr_b16 v[48:49], v20 offset:18528\n"
  "v_mfma_f32_16x16x32_bf16 v[92:95], v[60:63], a[188:191], v[92:95]\n"
  "ds_read_b64_tr_b16 v[50:51], v20 offset:23136\n"
  "v_mfma_f32_16x16x32_bf16 v[96:99], v[64:67], a[160:163], v[96:99]\n"
  "s_waitcnt lgkmcnt(2)\n"
  "ds_read_b64_tr_b16 v[52:53], v20 offset:27744\n"
  "v_mfma_f32_16x16x32_bf16 v[96:99], v[68:71], a[176:179], v[96:99]\n"
  "ds_read_b64_tr_b16 v[54:55], v20 offset:32352\n"
  "v_mfma_f32_16x16x32_bf16 v[100:103], v[64:67], a[164:167], v[100:103]\n"
  "buffer_load_dwordx4 a[220:223], v1, s[36:39], s63 offen offset:1024 sc1\n"
  "v_mfma_f32_16x16x32_bf16 v[100:103], v[68:71], a[180:183], v[100:103]\n"
  "v_mfma_f32_16x16x32_bf16 v[104:107], v[64:67], a[168:171], v[104:107]\n"
  "v_mfma_f32_16x16x32_bf16 v[104:107], v[68:71], a[184:187], v[104:107]\n"
  "v_mfma_f32_16x16x32_bf16 v[108:111], v[64:67], a[172:175], v[108:111]\n"
  "ds_read_b64_tr_b16 v[56:57], v20 offset:18560\n"
  "v_mfma_f32_16x16x32_bf16 v[108:111], v[68:71], a[188:191], v[108:111]\n"
  "ds_read_b64_tr_b16 v[58:59], v20 offset:23168\n"
  "v_mfma_f32_16x16x32_bf16 v[112:115], v[72:75], a[160:163], v[112:115]\n"
  "ds_read_b64_tr_b16 v[60:61], v20 offset:27776\n"
  "v_mfma_f32_16x16x32_bf16 v[112:115], v[76:79], a[176:179], v[112:115]\n"
  "ds_read_b64_tr_b16 v[62:63], v20 offset:32384\n"
  "v_mfma_f32_16x16x32_bf16 v[116:119], v[72:75], a[164:167], v[116:119]\n"
  "buffer_load_dwordx4 a[224:227], v1, s[36:39], s60 offen offset:2048 sc1\n"
  "v_mfma_f32_16x16x32_bf16 v[116:119], v[76:79], a[180:183], v[116:119]\n"
  "v_mfma_f32_16x16x32_bf16 v[120:123], v[72:75], a[168:171], v[120:123]\n"
  "buffer_load_dwordx4 a[228:231], v1, s[36:39], s61 offen offset:2048 sc1\n"
  "v_mfma_f32_16x16x32_bf16 v[120:123], v[76:79], a[184:187], v[120:123]\n"
  "v_mfma_f32_16x16x32_bf16 v[124:127], v[72:75], a[172:175], v[124:127]\n"
  "ds_read_b64_tr_b16 v[64:65], v20 offset:18592\n"
  "v_mfma_f32_16x16x32_bf16 v[124:127], v[76:79], a[188:191], v[124:127]\n"
  "s_waitcnt lgkmcnt(3)\n"
  "ds_read_b64_tr_b16 v[66:67], v20 offset:23200\n"
  "v_mfma_f32_16x16x32_bf16 v[128:131], v[48:51], a[160:163], v[128:131]\n"
  "ds_read_b64_tr_b16 v[68:69], v20 offset:27808\n"
  "v_mfma_f32_16x16x32_bf16 v[128:131], v[52:55], a[176:179], v[128:131]\n"
  "ds_read_b64_tr_b16 v[70:71], v20 offset:32416\n"
  "v_mfma_f32_16x16x32_bf16 v[132:135], v[48:51], a[164:167], v[132:135]\n"
  "buffer_load_dwordx4 a[232:235], v1, s[36:39], s62 offen offset:2048 sc1\n"
  "v_mfma_f32_16x16x32_bf16 v[132:135], v[52:55], a[180:183], v[132:135]\n"
  "v_mfma_f32_16x16x32_bf16 v[136:139], v[48:51], a[168:171], v[136:139]\n"
  "v_mfma_f32_16x16x32_bf16 v[136:139], v[52:55], a[184:187], v[136:139]\n"
  "v_mfma_f32_16x16x32_bf16 v[140:143], v[48:51], a[172:175], v[140:143]\n"
  "ds_read_b64_tr_b16 v[72:73], v20 offset:18624\n"
  "v_mfma_f32_16x16x32_bf16 v[140:143], v[52:55], a[188:191], v[140:143]\n"
  "ds_read_b64_tr_b16 v[74:75], v20 offset:23232\n"
  "v_mfma_f32_16x16x32_bf16 v[144:147], v[56:59], a[160:163], v[144:147]\n"
  "s_waitcnt lgkmcnt(3)\n"
  "ds_read_b64_tr_b16 v[76:77], v20 offset:27840\n"
  "v_mfma_f32_16x16x32_bf16 v[144:147], v[60:63], a[176:179], v[144:147]\n"
  "ds_read_b64_tr_b16 v[78:79], v20 offset:32448\n"
  "v_mfma_f32_16x16x32_bf16 v[148:151], v[56:59], a[164:167], v[148:151]\n"
  "buffer_load_dwordx4 a[236:239], v1, s[36:39], s63 offen offset:2048 sc1\n"
  "v_mfma_f32_16x16x32_bf16 v[148:151], v[60:63], a[180:183], v[148:151]\n"
  "buffer_load_dwordx4 a[240:243], v1, s[36:39], s60 offen offset:3072 sc1\n"
  "v_mfma_f32_16x16x32_bf16 v[152:155], v[56:59], a[168:171], v[152:155]\n"
  "v_mfma_f32_16x16x32_bf16 v[152:155], v[60:63], a[184:187], v[152:155]\n"
  "v_mfma_f32_16x16x32_bf16 v[156:159], v[56:59], a[172:175], v[156:159]\n"
  "ds_read_b64_tr_b16 v[48:49], v20 offset:18656\n"
  "v_mfma_f32_16x16x32_bf16 v[156:159], v[60:63], a[188:191], v[156:159]\n"
  "ds_read_b64_tr_b16 v[50:51], v20 offset:23264\n"
  "v_mfma_f32_16x16x32_bf16 v[160:163], v[64:67], a[160:163], v[160:163]\n"
  "s_waitcnt lgkmcnt(3)\n"
  "ds_read_b64_tr_b16 v[52:53], v20 offset:27872\n"
  "v_mfma_f32_16x16x32_bf16 v[160:163], v[68:71], a[176:179], v[160:163]\n"
  "ds_read_b64_tr_b16 v[54:55], v20 offset:32480\n"
  "v_mfma_f32_16x16x32_bf16 v[164:167], v[64:67], a[164:167], v[164:167]\n"
  "buffer_load_dwordx4 a[244:247], v1, s[36:39], s61 offen offset:3072 sc1\n"
  "v_mfma_f32_16x16x32_bf16 v[164:167], v[68:71], a[180:183], v[164:167]\n"
  "v_mfma_f32_16x16x32_bf16 v[168:171], v[64:67], a[168:171], v[168:171]\n"
  "v_mfma_f32_16x16x32_bf16 v[168:171], v[68:71], a[184:187], v[168:171]\n"
  "buffer_load_dwordx4 a[248:251], v1, s[36:39], s62 offen offset:3072 sc1\n"
  "v_mfma_f32_16x16x32_bf16 v[172:175], v[64:67], a[172:175], v[172:175]\n"
  "ds_read_b64_tr_b16 v[56:57], v21 offset:55296\n"
  "v_mfma_f32_16x16x32_bf16 v[172:175], v[68:71], a[188:191], v[172:175]\n"
  "ds_read_b64_tr_b16 v[58:59], v21 offset:59904\n"
  "v_add_u32 v21, 36864, v21\n"
  "v_mfma_f32_16x16x32_bf16 v[176:179], v[72:75], a[160:163], v[176:179]\n"
  "s_waitcnt lgkmcnt(2)\n"
  "ds_read_b64_tr_b16 v[60:61], v21 offset:27648\n"
  "v_mfma_f32_16x16x32_bf16 v[176:179], v[76:79], a[176:179], v[176:179]\n"
  "ds_read_b64_tr_b16 v[62:63], v21 offset:32256\n"
  "v_mfma_f32_16x16x32_bf16 v[180:183], v[72:75], a[164:167], v[180:183]\n"
  "buffer_load_dwordx4 a[252:255], v1, s[36:39], s63 offen offset:3072 sc1\n"
  "v_mfma_f32_16x16x32_bf16 v[180:183], v[76:79], a[180:183], v[180:183]\n"
  "s_add_u32 m0, s67, 0\n"
  "v_mfma_f32_16x16x32_bf16 v[184:187], v[72:75], a[168:171], v[184:187]\n"
  "s_add_u32 s79, s64, 4718592\n"
  "v_mfma_f32_16x16x32_bf16 v[184:187], v[76:79], a[184:187], v[184:187]\n"
  "s_add_u32 s80, s65, 4194304\n"
  "v_mfma_f32_16x16x32_bf16 v[188:191], v[72:75], a[172:175], v[188:191]\n"
  "s_waitcnt vmcnt(16)\n"
  "s_barrier\n"
  "ds_read_b64_tr_b16 v[64:65], v20 offset:36864\n"
  "v_mfma_f32_16x16x32_bf16 v[188:191], v[76:79], a[188:191], v[188:191]\n"
  "ds_read_b64_tr_b16 v[66:67], v20 offset:41472\n"
  "v_mfma_f32_16x16x32_bf16 v[192:195], v[48:51], a[160:163], v[192:195]\n"
  "ds_read_b64_tr_b16 v[68:69], v20 offset:46080\n"
  "v_mfma_f32_16x16x32_bf16 v[192:195], v[52:55], a[176:179], v[192:195]\n"
  "ds_read_b64_tr_b16 v[70:71], v20 offset:50688\n"
  "v_mfma_f32_16x16x32_bf16 v[196:199], v[48:51], a[164:167], v[196:199]\n"
  "s_cmp_lt_u32 s82, 2\n s_cselect_b32 s66, s79, s80\n s_cselect_b64 s[56:57], s[40:41], s[44:45]\n s_cselect_b64 s[58:59], s[42:43], s[46:47]\n s_cselect_b64 vcc, -1, 0\n"
  "v_mfma_f32_16x16x32_bf16 v[196:199], v[52:55], a[180:183], v[196:199]\n"
  "v_cndmask_b32 v2, v11, v2, vcc\n"
  "v_mfma_f32_16x16x32_bf16 v[200:203], v[48:51], a[168:171], v[200:203]\n"
  "buffer_load_dwordx4 v2, s[56:59], s66 offen offset:0 lds\n"
  "v_mfma_f32_16x16x32_bf16 v[200:203], v[52:55], a[184:187], v[200:203]\n"
  "v_cndmask_b32 v3, v12, v3, vcc\n"
  "v_mfma_f32_16x16x32_bf16 v[204:207], v[48:51], a[172:175], v[204:207]\n"
  "ds_read_b64_tr_b16 v[72:73], v20 offset:36896\n"
  "v_mfma_f32_16x16x32_bf16 v[204:207], v[52:55], a[188:191], v[204:207]\n"
  "s_waitcnt lgkmcnt(2)\n"
  "ds_read_b64_tr_b16 v[74:75], v20 offset:41504\n"
  "v_mfma_f32_16x16x32_bf16 v[208:211], v[56:59], a[160:163], v[208:211]\n"
  "ds_read_b64_tr_b16 v[76:77], v20 offset:46112\n"
  "v_mfma_f32_16x16x32_bf16 v[208:211], v[60:63], a[176:179], v[208:211]\n"
  "ds_read_b64_tr_b16 v[78:79], v20 offset:50720\n"
  "v_mfma_f32_16x16x32_bf16 v[212:215], v[56:59], a[164:167], v[212:215]\n"
  "buffer_load_dwordx4 v3, s[56:59], s66 offen offset:1024 lds\n"
  "v_mfma_f32_16x16x32_bf16 v[212:215], v[60:63], a[180:183], v[212:215]\n"
  "v_cndmask_b32 v4, v13, v4, vcc\n"
  "v_mfma_f32_16x16x32_bf16 v[216:219], v[56:59], a[168:171], v[216:219]\n"
  "buffer_load_dwordx4 v4, s[56:59], s66 offen offset:2048 lds\n"
  "v_mfma_f32_16x16x32_bf16 v[216:219], v[60:63], a[184:187], v[216:219]\n"
  "v_cndmask_b32 v5, v14, v5, vcc\n"
  "v_mfma_f32_16x16x32_bf16 v[220:223], v[56:59], a[172:175], v[220:223]\n"
  "ds_read_b64_tr_b16 v[48:49], v20 offset:36928\n"
  "v_mfma_f32_16x16x32_bf16 v[220:223], v[60:63], a[188:191], v[220:223]\n"
  "ds_read_b64_tr_b16 v[50:51], v20 offset:41536\n"
  "s_barrier\n"
  "s_waitcnt vmcnt(18)\n"
  "v_mfma_f32_16x16x32_bf16 v[80:83], v[64:67], a[192:195], v[80:83]\n"
  "s_waitcnt vmcnt(14) lgkmcnt(2)\n"
  "ds_read_b64_tr_b16 v[52:53], v20 offset:46144\n"
  "v_mfma_f32_16x16x32_bf16 v[80:83], v[68:71], a[208:211], v[80:83]\n"
  "ds_read_b64_tr_b16 v[54:55], v20 offset:50752\n"
  "v_mfma_f32_16x16x32_bf16 v[84:87], v[64:67], a[196:199], v[84:87]\n"
  "s_waitcnt vmcnt(13)\n"
  "buffer_load_dwordx4 v5, s[56:59], s66 offen offset:3072 lds\n"
  "v_mfma_f32_16x16x32_bf16 v[84:87], v[68:71], a[212:215], v[84:87]\n"
  "s_add_u32 m0, s67, 4096\n"
  "v_cndmask_b32 v6, v15, v6, vcc\n"
  "v_mfma_f32_16x16x32_bf16 v[88:91], v[64:67], a[200:203], v[88:91]\n"
  "s_waitcnt vmcnt(13)\n"
  "buffer_load_dwordx4 v6, s[56:59], s66 offen offset:0 lds\n"
  "v_mfma_f32_16x16x32_bf16 v[88:91], v[68:71], a[216:219], v[88:91]\n"
  "v_cndmask_b32 v7, v16, v7, vcc\n"
  "v_mfma_f32_16x16x32_bf16 v[92:95], v[64:67], a[204:207], v[92:95]\n"
  "s_waitcnt vmcnt(13)\n"
  "ds_read_b64_tr_b16 v[56:57], v20 offset:36960\n"
  "v_mfma_f32_16x16x32_bf16 v[92:95], v[68:71], a[220:223], v[92:95]\n"
  "ds_read_b64_tr_b16 v[58:59], v20 offset:41568\n"
  "v_mfma_f32_16x16x32_bf16 v[96:99], v[72:75], a[192:195], v[96:99]\n"
  "ds_read_b64_tr_b16 v[60:61], v20 offset:46176\n"
  "v_mfma_f32_16x16x32_bf16 v[96:99], v[76:79], a[208:211], v[96:99]\n"
  "ds_read_b64_tr_b16 v[62:63], v20 offset:50784\n"
  "v_mfma_f32_16x16x32_bf16 v[100:103], v[72:75], a[196:199], v[100:103]\n"
  "buffer_load_dwordx4 v7, s[56:59], s66 offen offset:1024 lds\n"
  "v_mfma_f32_16x16x32_bf16 v[100:103], v[76:79], a[212:215], v[100:103]\n"
  "v_cndmask_b32 v8, v17, v8, vcc\n"
  "v_mfma_f32_16x16x32_bf16 v[104:107], v[72:75], a[200:203], v[104:107]\n"
  "v_mfma_f32_16x16x32_bf16 v[104:107], v[76:79], a[216:219], v[104:107]\n"
  "buffer_load_dwordx4 v8, s[56:59], s66 offen offset:2048 lds\n"
  "v_mfma_f32_16x16x32_bf16 v[108:111], v[72:75], a[204:207], v[108:111]\n"
  "ds_read_b64_tr_b16 v[64:65], v20 offset:36992\n"
  "v_cndmask_b32 v9, v18, v9, vcc\n"
  "v_mfma_f32_16x16x32_bf16 v[108:111], v[76:79], a[220:223], v[108:111]\n"
  "s_waitcnt lgkmcnt(2)\n"
  "ds_read_b64_tr_b16 v[66:67], v20 offset:41600\n"
  "v_mfma_f32_16x16x32_bf16 v[112:115], v[48:51], a[192:195], v[112:115]\n"
  "ds_read_b64_tr_b16 v[68:69], v20 offset:46208\n"
  "v_mfma_f32_16x16x32_bf16 v[112:115], v[52:55], a[208:211], v[112:115]\n"
  "ds_read_b64_tr_b16 v[70:71], v20 offset:50816\n"
  "v_mfma_f32_16x16x32_bf16 v[116:119], v[48:51], a[196:199], v[116:119]\n"
  "buffer_load_dwordx4 v9, s[56:59], s66 offen offset:3072 lds\n"
  "v_mfma_f32_16x16x32_bf16 v[116:119], v[52:55], a[212:215], v[116:119]\n"
  "s_add_u32 m0, s67, 8192\n"
  "v_cndmask_b32 v10, v19, v10, vcc\n"
  "v_mfma_f32_16x16x32_bf16 v[120:123], v[48:51], a[200:203], v[120:123]\n"
  "v_mfma_f32_16x16x32_bf16 v[120:123], v[52:55], a[216:219], v[120:123]\n"
  "v_mfma_f32_16x16x32_bf16 v[124:127], v[48:51], a[204:207], v[124:127]\n"
  "ds_read_b64_tr_b16 v[72:73], v20 offset:37024\n"
  "v_mfma_f32_16x16x32_bf16 v[124:127], v[52:55], a[220:223], v[124:127]\n"
  "ds_read_b64_tr_b16 v[74:75], v20 offset:41632\n"
  "v_mfma_f32_16x16x32_bf16 v[128:131], v[56:59], a[192:195], v[128:131]\n"
  "s_waitcnt lgkmcnt(2)\n"
  "ds_read_b64_tr_b16 v[76:77], v20 offset:46240\n"
  "v_mfma_f32_16x16x32_bf16 v[128:131], v[60:63], a[208:211], v[128:131]\n"
  "ds_read_b64_tr_b16 v[78:79], v20 offset:50848\n"
  "v_mfma_f32_16x16x32_bf16 v[132:135], v[56:59], a[196:199], v[132:135]\n"
  "buffer_load_dwordx4 v10, s[56:59], s66 offen offset:0 lds\n"
  "v_mfma_f32_16x16x32_bf16 v[132:135], v[60:63], a[212:215], v[132:135]\n"
  "s_add_u32 s60, s60, 4096\n"
  "v_mfma_f32_16x16x32_bf16 v[136:139], v[56:59], a[200:203], v[136:139]\n"
  "buffer_load_dwordx4 v[224:227], v1, s[36:39], s60 offen offset:0 sc1\n"
  "v_mfma_f32_16x16x32_bf16 v[136:139], v[60:63], a[216:219], v[136:139]\n"
  "s_add_u32 s61, s61, 4096\n"
  "v_mfma_f32_16x16x32_bf16 v[140:143], v[56:59], a[204:207], v[140:143]\n"
  "ds_read_b64_tr_b16 v[48:49], v20 offset:37056\n"
  "v_mfma_f32_16x16x32_bf16 v[140:143], v[60:63], a[220:223], v[140:143]\n"
  "ds_read_b64_tr_b16 v[50:51], v20 offset:41664\n"
  "v_mfma_f32_16x16x32_bf16 v[144:147], v[64:67], a[192:195], v[144:147]\n"
  "ds_read_b64_tr_b16 v[52:53], v20 offset:46272\n"
  "v_mfma_f32_16x16x32_bf16 v[144:147], v[68:71], a[208:211], v[144:147]\n"
  "ds_read_b64_tr_b16 v[54:55], v20 offset:50880\n"
  "v_mfma_f32_16x16x32_bf16 v[148:151], v[64:67], a[196:199], v[148:151]\n"
  "buffer_load_dwordx4 v[228:231], v1, s[36:39], s61 offen offset:0 sc1\n"
  "v_mfma_f32_16x16x32_bf16 v[148:151], v[68:71], a[212:215], v[148:151]\n"
  "s_add_u32 s62, s62, 4096\n"
  "v_mfma_f32_16x16x32_bf16 v[152:155], v[64:67], a[200:203], v[152:155]\n"
  "v_mfma_f32_16x16x32_bf16 v[152:155], v[68:71], a[216:219], v[152:155]\n"
  "v_mfma_f32_16x16x32_bf16 v[156:159], v[64:67], a[204:207], v[156:159]\n"
  "ds_read_b64_tr_b16 v[56:57], v20 offset:37088\n"
  "v_mfma_f32_16x16x32_bf16 v[156:159], v[68:71], a[220:223], v[156:159]\n"
  "s_waitcnt lgkmcnt(3)\n"
  "ds_read_b64_tr_b16 v[58:59], v20 offset:41696\n"
  "v_mfma_f32_16x16x32_bf16 v[160:163], v[72:75], a[192:195], v[160:163]\n"
  "ds_read_b64_tr_b16 v[60:61], v20 offset:46304\n"
  "v_mfma_f32_16x16x32_bf16 v[160:163], v[76:79], a[208:211], v[160:163]\n"
  "ds_read_b64_tr_b16 v[62:63], v20 offset:50912\n"
  "v_mfma_f32_16x16x32_bf16 v[164:167], v[72:75], a[196:199], v[164:167]\n"
  "buffer_load_dwordx4 v[232:235], v1, s[36:39], s62 offen offset:0 sc1\n"
  "v_mfma_f32_16x16x32_bf16 v[164:167], v[76:79], a[212:215], v[164:167]\n"
  "s_add_u32 s63, s63, 4096\n"
  "v_mfma_f32_16x16x32_bf16 v[168:171], v[72:75], a[200:203], v[168:171]\n"
  "buffer_load_dwordx4 v[236:239], v1, s[36:39], s63 offen offset:0 sc1\n"
  "v_mfma_f32_16x16x32_bf16 v[168:171], v[76:79], a[216:219], v[168:171]\n"
  "v_mfma_f32_16x16x32_bf16 v[172:175], v[72:75], a[204:207], v[172:175]\n"
  "ds_read_b64_tr_b16 v[64:65], v21 offset:36864\n"
  "v_mfma_f32_16x16x32_bf16 v[172:175], v[76:79], a[220:223], v[172:175]\n"
  "ds_read_b64_tr_b16 v[66:67], v21 offset:41472\n"
  "v_mfma_f32_16x16x32_bf16 v[176:179], v[48:51], a[192:195], v[176:179]\n"
  "s_waitcnt lgkmcnt(2)\n"
  "ds_read_b64_tr_b16 v[68:69], v21 offset:46080\n"
  "v_mfma_f32_16x16x32_bf16 v[176:179], v[52:55], a[208:211], v[176:179]\n"
  "ds_read_b64_tr_b16 v[70:71], v21 offset:50688\n"
  "v_mfma_f32_16x16x32_bf16 v[180:183], v[48:51], a[196:199], v[180:183]\n"
  "buffer_load_dwordx4 v[240:243], v1, s[36:39], s60 offen offset:1024 sc1\n"
  "v_mfma_f32_16x16x32_bf16 v[180:183], v[52:55], a[212:215], v[180:183]\n"
  "v_mfma_f32_16x16x32_bf16 v[184:187], v[48:51], a[200:203], v[184:187]\n"
  "v_mfma_f32_16x16x32_bf16 v[184:187], v[52:55], a[216:219], v[184:187]\n"
  "buffer_load_dwordx4 v[244:247], v1, s[36:39], s61 offen offset:1024 sc1\n"
  "v_mfma_f32_16x16x32_bf16 v[188:191], v[48:51], a[204:207], v[188:191]\n"
  "ds_read_b64_tr_b16 v[72:73], v20 offset:55296\n"
  "v_mfma_f32_16x16x32_bf16 v[188:191], v[52:55], a[220:223], v[188:191]\n"
  "ds_read_b64_tr_b16 v[74:75], v20 offset:59904\n"
  "v_add_u32 v20, 36864, v20\n"
  "v_mfma_f32_16x16x32_bf16 v[192:195], v[56:59], a[192:195], v[192:195]\n"
  "ds_read_b64_tr_b16 v[76:77], v20 offset:27648\n"
  "v_mfma_f32_16x16x32_bf16 v[192:195], v[60:63], a[208:211], v[192:195]\n"
  "ds_read_b64_tr_b16 v[78:79], v20 offset:32256\n"
  "v_mfma_f32_16x16x32_bf16 v[196:199], v[56:59], a[196:199], v[196:199]\n"
  "buffer_load_dwordx4 v[248:251], v1, s[36:39], s62 offen offset:1024 sc1\n"
  "v_mfma_f32_16x16x32_bf16 v[196:199], v[60:63], a[212:215], v[196:199]\n"
  "v_mfma_f32_16x16x32_bf16 v[200:203], v[56:59], a[200:203], v[200:203]\n"
  "v_mfma_f32_16x16x32_bf16 v[200:203], v[60:63], a[216:219], v[200:203]\n"
  "v_mfma_f32_16x16x32_bf16 v[204:207], v[56:59], a[204:207], v[204:207]\n"
  "ds_read_b64_tr_b16 v[48:49], v20 offset:18464\n"
  "v_mfma_f32_16x16x32_bf16 v[204:207], v[60:63], a[220:223], v[204:207]\n"
  "s_waitcnt lgkmcnt(3)\n"
  "ds_read_b64_tr_b16 v[50:51], v20 offset:23072\n"
  "v_mfma_f32_16x16x32_bf16 v[208:211], v[64:67], a[192:195], v[208:211]\n"
  "ds_read_b64_tr_b16 v[52:53], v20 offset:27680\n"
  "v_mfma_f32_16x16x32_bf16 v[208:211], v[68:71], a[208:211], v[208:211]\n"
  "ds_read_b64_tr_b16 v[54:55], v20 offset:32288\n"
  "v_mfma_f32_16x16x32_bf16 v[212:215], v[64:67], a[196:199], v[212:215]\n"
  "buffer_load_dwordx4 v[252:255], v1, s[36:39], s63 offen offset:1024 sc1\n"
  "v_mfma_f32_16x16x32_bf16 v[212:215], v[68:71], a[212:215], v[212:215]\n"
  "s_add_u32 m0, s67, 36864\n"
  "v_mfma_f32_16x16x32_bf16 v[216:219], v[64:67], a[200:203], v[216:219]\n"
  "s_add_u32 s66, s65, 5242880\n"
  "v_mfma_f32_16x16x32_bf16 v[216:219], v[68:71], a[216:219], v[216:219]\n"
  "buffer_load_dwordx4 v11, s[44:47], s66 offen offset:0 lds\n"
  "v_mfma_f32_16x16x32_bf16 v[220:223], v[64:67], a[204:207], v[220:223]\n"
  "ds_read_b64_tr_b16 v[56:57], v20 offset:18496\n"
  "v_mfma_f32_16x16x32_bf16 v[220:223], v[68:71], a[220:223], v[220:223]\n"
  "s_waitcnt vmcnt(25)\n"
  "ds_read_b64_tr_b16 v[58:59], v20 offset:23104\n"
  "v_mfma_f32_16x16x32_bf16 v[80:83], v[72:75], a[224:227], v[80:83]\n"
  "s_waitcnt vmcnt(21) lgkmcnt(2)\n"
  "ds_read_b64_tr_b16 v[60:61], v20 offset:27712\n"
  "v_mfma_f32_16x16x32_bf16 v[80:83], v[76:79], a[240:243], v[80:83]\n"
  "ds_read_b64_tr_b16 v[62:63], v20 offset:32320\n"
  "v_mfma_f32_16x16x32_bf16 v[84:87], v[72:75], a[228:231], v[84:87]\n"
  "s_waitcnt vmcnt(20)\n"
  "buffer_load_dwordx4 v12, s[44:47], s66 offen offset:1024 lds\n"
  "v_mfma_f32_16x16x32_bf16 v[84:87], v[76:79], a[244:247], v[84:87]\n"
  "v_mfma_f32_16x16x32_bf16 v[88:91], v[72:75], a[232:235], v[88:91]\n"
  "s_waitcnt vmcnt(20)\n"
  "v_mfma_f32_16x16x32_bf16 v[88:91], v[76:79], a[248:251], v[88:91]\n"
  "v_mfma_f32_16x16x32_bf16 v[92:95], v[72:75], a[236:239], v[92:95]\n"
  "s_waitcnt vmcnt(19)\n"
  "ds_read_b64_tr_b16 v[64:65], v20 offset:18528\n"
  "v_mfma_f32_16x16x32_bf16 v[92:95], v[76:79], a[252:255], v[92:95]\n"
  "ds_read_b64_tr_b16 v[66:67], v20 offset:23136\n"
  "v_mfma_f32_16x16x32_bf16 v[96:99], v[48:51], a[224:227], v[96:99]\n"
  "ds_read_b64_tr_b16 v[68:69], v20 offset:27744\n"
  "v_mfma_f32_16x16x32_bf16 v[96:99], v[52:55], a[240:243], v[96:99]\n"
  "ds_read_b64_tr_b16 v[70:71], v20 offset:32352\n"
  "v_mfma_f32_16x16x32_bf16 v[100:103], v[48:51], a[228:231], v[100:103]\n"
  "buffer_load_dwordx4 v13, s[44:47], s66 offen offset:2048 lds\n"
  "v_mfma_f32_16x16x32_bf16 v[100:103], v[52:55], a[244:247], v[100:103]\n"
  "buffer_load_dwordx4 v14, s[44:47], s66 offen offset:3072 lds\n"
  "v_mfma_f32_16x16x32_bf16 v[104:107], v[48:51], a[232:235], v[104:107]\n"
  "s_add_u32 m0, s67, 40960\n"
  "v_mfma_f32_16x16x32_bf16 v[104:107], v[52:55], a[248:251], v[104:107]\n"
  "v_mfma_f32_16x16x32_bf16 v[108:111], v[48:51], a[236:239], v[108:111]\n"
  "ds_read_b64_tr_b16 v[72:73], v20 offset:18560\n"
  "v_mfma_f32_16x16x32_bf16 v[108:111], v[52:55], a[252:255], v[108:111]\n"
  "s_waitcnt lgkmcnt(2)\n"
  "ds_read_b64_tr_b16 v[74:75], v20 offset:23168\n"
  "v_mfma_f32_16x16x32_bf16 v[112:115], v[56:59], a[224:227], v[112:115]\n"
  "ds_read_b64_tr_b16 v[76:77], v20 offset:27776\n"
  "v_mfma_f32_16x16x32_bf16 v[112:115], v[60:63], a[240:243], v[112:115]\n"
  "ds_read_b64_tr_b16 v[78:79], v20 offset:32384\n"
  "v_mfma_f32_16x16x32_bf16 v[116:119], v[56:59], a[228:231], v[116:119]\n"
  "buffer_load_dwordx4 v15, s[44:47], s66 offen offset:0 lds\n"
  "v_mfma_f32_16x16x32_bf16 v[116:119], v[60:63], a[244:247], v[116:119]\n"
  "v_mfma_f32_16x16x32_bf16 v[120:123], v[56:59], a[232:235], v[120:123]\n"
  "v_mfma_f32_16x16x32_bf16 v[120:123], v[60:63], a[248:251], v[120:123]\n"
  "buffer_load_dwordx4 v16, s[44:47], s66 offen offset:1024 lds\n"
  "v_mfma_f32_16x16x32_bf16 v[124:127], v[56:59], a[236:239], v[124:127]\n"
  "ds_read_b64_tr_b16 v[48:49], v20 offset:18592\n"
  "v_mfma_f32_16x16x32_bf16 v[124:127], v[60:63], a[252:255], v[124:127]\n"
  "ds_read_b64_tr_b16 v[50:51], v20 offset:23200\n"
  "v_mfma_f32_16x16x32_bf16 v[128:131], v[64:67], a[224:227], v[128:131]\n"
  "s_waitcnt lgkmcnt(3)\n"
  "ds_read_b64_tr_b16 v[52:53], v20 offset:27808\n"
  "v_mfma_f32_16x16x32_bf16 v[128:131], v[68:71], a[240:243], v[128:131]\n"
  "ds_read_b64_tr_b16 v[54:55], v20 offset:32416\n"
  "v_mfma_f32_16x16x32_bf16 v[132:135], v[64:67], a[228:231], v[132:135]\n"
  "buffer_load_dwordx4 v17, s[44:47], s66 offen offset:2048 lds\n"
  "v_mfma_f32_16x16x32_bf16 v[132:135], v[68:71], a[244:247], v[132:135]\n"
  "v_mfma_f32_16x16x32_bf16 v[136:139], v[64:67], a[232:235], v[136:139]\n"
  "v_mfma_f32_16x16x32_bf16 v[136:139], v[68:71], a[248:251], v[136:139]\n"
  "v_mfma_f32_16x16x32_bf16 v[140:143], v[64:67], a[236:239], v[140:143]\n"
  "ds_read_b64_tr_b16 v[56:57], v20 offset:18624\n"
  "v_mfma_f32_16x16x32_bf16 v[140:143], v[68:71], a[252:255], v[140:143]\n"
  "ds_read_b64_tr_b16 v[58:59], v20 offset:23232\n"
  "v_mfma_f32_16x16x32_bf16 v[144:147], v[72:75], a[224:227], v[144:147]\n"
  "s_waitcnt lgkmcnt(3)\n"
  "ds_read_b64_tr_b16 v[60:61], v20 offset:27840\n"
  "v_mfma_f32_16x16x32_bf16 v[144:147], v[76:79], a[240:243], v[144:147]\n"
  "ds_read_b64_tr_b16 v[62:63], v20 offset:32448\n"
  "v_mfma_f32_16x16x32_bf16 v[148:151], v[72:75], a[228:231], v[148:151]\n"
  "buffer_load_dwordx4 v18, s[44:47], s66 offen offset:3072 lds\n"
  "v_mfma_f32_16x16x32_bf16 v[148:151], v[76:79], a[244:247], v[148:151]\n"
  "s_add_u32 m0, s67, 73728\n"
  "v_mfma_f32_16x16x32_bf16 v[152:155], v[72:75], a[232:235], v[152:155]\n"
  "s_add_u32 s66, s65, 6291456\n"
  "v_mfma_f32_16x16x32_bf16 v[152:155], v[76:79], a[248:251], v[152:155]\n"
  "buffer_load_dwordx4 v11, s[44:47], s66 offen offset:0 lds\n"
  "v_mfma_f32_16x16x32_bf16 v[156:159], v[72:75], a[236:239], v[156:159]\n"
  "ds_read_b64_tr_b16 v[64:65], v20 offset:18656\n"
  "v_mfma_f32_16x16x32_bf16 v[156:159], v[76:79], a[252:255], v[156:159]\n"
  "ds_read_b64_tr_b16 v[66:67], v20 offset:23264\n"
  "v_mfma_f32_16x16x32_bf16 v[160:163], v[48:51], a[224:227], v[160:163]\n"
  "s_waitcnt lgkmcnt(2)\n"
  "ds_read_b64_tr_b16 v[68:69], v20 offset:27872\n"
  "v_mfma_f32_16x16x32_bf16 v[160:163], v[52:55], a[240:243], v[160:163]\n"
  "ds_read_b64_tr_b16 v[70:71], v20 offset:32480\n"
  "v_mfma_f32_16x16x32_bf16 v[164:167], v[48:51], a[228:231], v[164:167]\n"
  "buffer_load_dwordx4 v12, s[44:47], s66 offen offset:1024 lds\n"
  "v_mfma_f32_16x16x32_bf16 v[164:167], v[52:55], a[244:247], v[164:167]\n"
  "v_mfma_f32_16x16x32_bf16 v[168:171], v[48:51], a[232:235], v[168:171]\n"
  "v_mfma_f32_16x16x32_bf16 v[168:171], v[52:55], a[248:251], v[168:171]\n"
  "v_mfma_f32_16x16x32_bf16 v[172:175], v[48:51], a[236:239], v[172:175]\n"
  "ds_read_b64_tr_b16 v[72:73], v21 offset:55296\n"
  "v_mfma_f32_16x16x32_bf16 v[172:175], v[52:55], a[252:255], v[172:175]\n"
  "ds_read_b64_tr_b16 v[74:75], v21 offset:59904\n"
  "v_add_u32 v21, 36864, v21\n"
  "v_mfma_f32_16x16x32_bf16 v[176:179], v[56:59], a[224:227], v[176:179]\n"
  "ds_read_b64_tr_b16 v[76:77], v21 offset:27648\n"
  "v_mfma_f32_16x16x32_bf16 v[176:179], v[60:63], a[240:243], v[176:179]\n"
  "ds_read_b64_tr_b16 v[78:79], v21 offset:32256\n"
  "v_add_u32 v20, 0xfffe5000, v20\n"
  "v_mfma_f32_16x16x32_bf16 v[180:183], v[56:59], a[228:231], v[180:183]\n"
  "buffer_load_dwordx4 v13, s[44:47], s66 offen offset:2048 lds\n"
  "v_mfma_f32_16x16x32_bf16 v[180:183], v[60:63], a[244:247], v[180:183]\n"
  "buffer_load_dwordx4 v14, s[44:47], s66 offen offset:3072 lds\n"
  "v_mfma_f32_16x16x32_bf16 v[184:187], v[56:59], a[232:235], v[184:187]\n"
  "s_add_u32 m0, s67, 77824\n"
  "v_mfma_f32_16x16x32_bf16 v[184:187], v[60:63], a[248:251], v[184:187]\n"
  "v_mfma_f32_16x16x32_bf16 v[188:191], v[56:59], a[236:239], v[188:191]\n"
  "s_waitcnt vmcnt(20)\n"
  "s_barrier\n"
  "ds_read_b64_tr_b16 v[48:49], v20 offset:0\n"
  "v_mfma_f32_16x16x32_bf16 v[188:191], v[60:63], a[252:255], v[188:191]\n"
  "s_waitcnt lgkmcnt(1)\n"
  "ds_read_b64_tr_b16 v[50:51], v20 offset:4608\n"
  "v_mfma_f32_16x16x32_bf16 v[192:195], v[64:67], a[224:227], v[192:195]\n"
  "ds_read_b64_tr_b16 v[52:53], v20 offset:9216\n"
  "v_mfma_f32_16x16x32_bf16 v[192:195], v[68:71], a[240:243], v[192:195]\n"
  "ds_read_b64_tr_b16 v[54:55], v20 offset:13824\n"
  "v_mfma_f32_16x16x32_bf16 v[196:199], v[64:67], a[228:231], v[196:199]\n"
  "buffer_load_dwordx4 v15, s[44:47], s66 offen offset:0 lds\n"
  "v_mfma_f32_16x16x32_bf16 v[196:199], v[68:71], a[244:247], v[196:199]\n"
  "v_mfma_f32_16x16x32_bf16 v[200:203], v[64:67], a[232:235], v[200:203]\n"
  "v_mfma_f32_16x16x32_bf16 v[200:203], v[68:71], a[248:251], v[200:203]\n"
  "buffer_load_dwordx4 v16, s[44:47], s66 offen offset:1024 lds\n"
  "v_mfma_f32_16x16x32_bf16 v[204:207], v[64:67], a[236:239], v[204:207]\n"
  "ds_read_b64_tr_b16 v[56:57], v20 offset:32\n"
  "v_mfma_f32_16x16x32_bf16 v[204:207], v[68:71], a[252:255], v[204:207]\n"
  "ds_read_b64_tr_b16 v[58:59], v20 offset:4640\n"
  "v_mfma_f32_16x16x32_bf16 v[208:211], v[72:75], a[224:227], v[208:211]\n"
  "ds_read_b64_tr_b16 v[60:61], v20 offset:9248\n"
  "v_mfma_f32_16x16x32_bf16 v[208:211], v[76:79], a[240:243], v[208:211]\n"
  "ds_read_b64_tr_b16 v[62:63], v20 offset:13856\n"
  "v_mfma_f32_16x16x32_bf16 v[212:215], v[72:75], a[228:231], v[212:215]\n"
  "buffer_load_dwordx4 v17, s[44:47], s66 offen offset:2048 lds\n"
  "v_mfma_f32_16x16x32_bf16 v[212:215], v[76:79], a[244:247], v[212:215]\n"
  "v_mfma_f32_16x16x32_bf16 v[216:219], v[72:75], a[232:235], v[216:219]\n"
  "v_mfma_f32_16x16x32_bf16 v[216:219], v[76:79], a[248:251], v[216:219]\n"
  "v_mfma_f32_16x16x32_bf16 v[220:223], v[72:75], a[236:239], v[220:223]\n"
  "ds_read_b64_tr_b16 v[64:65], v20 offset:64\n"
  "v_mfma_f32_16x16x32_bf16 v[220:223], v[76:79], a[252:255], v[220:223]\n"
  "ds_read_b64_tr_b16 v[66:67], v20 offset:4672\n"
  "s_barrier\n"
  "s_waitcnt vmcnt(22) lgkmcnt(4)\n"
  "v_mfma_f32_16x16x32_bf16 v[80:83], v[48:51], v[224:227], v[80:83]\n"
  "s_waitcnt vmcnt(18)\n"
  "ds_read_b64_tr_b16 v[68:69], v20 offset:9280\n"
  "v_mfma_f32_16x16x32_bf16 v[80:83], v[52:55], v[240:243], v[80:83]\n"
  "ds_read_b64_tr_b16 v[70:71], v20 offset:13888\n"
  "v_mfma_f32_16x16x32_bf16 v[84:87], v[48:51], v[228:231], v[84:87]\n"
  "s_waitcnt vmcnt(17)\n"
  "buffer_load_dwordx4 v18, s[44:47], s66 offen offset:3072 lds\n"
  "v_mfma_f32_16x16x32_bf16 v[84:87], v[52:55], v[244:247], v[84:87]\n"
  "v_mfma_f32_16x16x32_bf16 v[88:91], v[48:51], v[232:235], v[88:91]\n"
  "s_waitcnt vmcnt(17)\n"
  "v_mfma_f32_16x16x32_bf16 v[88:91], v[52:55], v[248:251], v[88:91]\n"
  "v_mfma_f32_16x16x32_bf16 v[92:95], v[48:51], v[236:239], v[92:95]\n"
  "s_waitcnt vmcnt(16)\n"
  "ds_read_b64_tr_b16 v[72:73], v20 offset:96\n"
  "v_mfma_f32_16x16x32_bf16 v[92:95], v[52:55], v[252:255], v[92:95]\n"
  "ds_read_b64_tr_b16 v[74:75], v20 offset:4704\n"
  "v_mfma_f32_16x16x32_bf16 v[96:99], v[56:59], v[224:227], v[96:99]\n"
  "s_waitcnt lgkmcnt(2)\n"
  "ds_read_b64_tr_b16 v[76:77], v20 offset:9312\n"
  "v_mfma_f32_16x16x32_bf16 v[96:99], v[60:63], v[240:243], v[96:99]\n"
  "ds_read_b64_tr_b16 v[78:79], v20 offset:13920\n"
  "v_mfma_f32_16x16x32_bf16 v[100:103], v[56:59], v[228:231], v[100:103]\n"
  "v_mfma_f32_16x16x32_bf16 v[100:103], v[60:63], v[244:247], v[100:103]\n"
  "v_mfma_f32_16x16x32_bf16 v[104:107], v[56:59], v[232:235], v[104:107]\n"
  "s_nop 3\n"
  "v_cvt_pk_bf16_f32 v99, v98, v99\n"
  "v_cvt_pk_bf16_f32 v98, v96, v97\n"
  "v_mfma_f32_16x16x32_bf16 v[104:107], v[60:63], v[248:251], v[104:107]\n"
  "v_cvt_pk_bf16_f32 v96, v80, v81\n"
  "v_cvt_pk_bf16_f32 v97, v82, v83\n"
  "v_mfma_f32_16x16x32_bf16 v[108:111], v[56:59], v[236:239], v[108:111]\n"
  "ds_read_b64_tr_b16 v[48:49], v20 offset:128\n"
  "v_pk_max_i16 v96, v96, 0\n"
  "v_mfma_f32_16x16x32_bf16 v[108:111], v[60:63], v[252:255], v[108:111]\n"
  "ds_read_b64_tr_b16 v[50:51], v20 offset:4736\n"
  "v_pk_max_i16 v97, v97, 0\n"
  "v_mfma_f32_16x16x32_bf16 v[112:115], v[64:67], v[224:227], v[112:115]\n"
  "ds_read_b64_tr_b16 v[52:53], v20 offset:9344\n"
  "v_pk_max_i16 v98, v98, 0\n"
  "v_mfma_f32_16x16x32_bf16 v[112:115], v[68:71], v[240:243], v[112:115]\n"
  "ds_read_b64_tr_b16 v[54:55], v20 offset:13952\n"
  "v_pk_max_i16 v99, v99, 0\n"
  "v_mfma_f32_16x16x32_bf16 v[116:119], v[64:67], v[228:231], v[116:119]\n"
  "buffer_store_dwordx4 v[96:99], v1, s[36:39], s68 offen offset:0 sc1\n"
  "v_cvt_pk_bf16_f32 v103, v102, v103\n"
  "v_cvt_pk_bf16_f32 v102, v100, v101\n"
  "v_mfma_f32_16x16x32_bf16 v[116:119], v[68:71], v[244:247], v[116:119]\n"
  "v_cvt_pk_bf16_f32 v100, v84, v85\n"
  "v_cvt_pk_bf16_f32 v101, v86, v87\n"
  "v_mfma_f32_16x16x32_bf16 v[120:123], v[64:67], v[232:235], v[120:123]\n"
  "v_pk_max_i16 v100, v100, 0\n"
  "v_pk_max_i16 v101, v101, 0\n"
  "v_mfma_f32_16x16x32_bf16 v[120:123], v[68:71], v[248:251], v[120:123]\n"
  "v_pk_max_i16 v102, v102, 0\n"
  "v_pk_max_i16 v103, v103, 0\n"
  "v_mfma_f32_16x16x32_bf16 v[124:127], v[64:67], v[236:239], v[124:127]\n"
  "ds_read_b64_tr_b16 v[56:57], v20 offset:160\n"
  "v_cvt_pk_bf16_f32 v107, v106, v107\n"
  "v_mfma_f32_16x16x32_bf16 v[124:127], v[68:71], v[252:255], v[124:127]\n"
  "s_waitcnt lgkmcnt(1)\n"
  "ds_read_b64_tr_b16 v[58:59], v20 offset:4768\n"
  "v_cvt_pk_bf16_f32 v106, v104, v105\n"
  "v_mfma_f32_16x16x32_bf16 v[128:131], v[72:75], v[224:227], v[128:131]\n"
  "ds_read_b64_tr_b16 v[60:61], v20 offset:9376\n"
  "v_cvt_pk_bf16_f32 v104, v88, v89\n"
  "v_mfma_f32_16x16x32_bf16 v[128:131], v[76:79], v[240:243], v[128:131]\n"
  "ds_read_b64_tr_b16 v[62:63], v20 offset:13984\n"
  "v_cvt_pk_bf16_f32 v105, v90, v91\n"
  "v_mfma_f32_16x16x32_bf16 v[132:135], v[72:75], v[228:231], v[132:135]\n"
  "v_pk_max_i16 v104, v104, 0\n"
  "v_pk_max_i16 v105, v105, 0\n"
  "v_mfma_f32_16x16x32_bf16 v[132:135], v[76:79], v[244:247], v[132:135]\n"
  "v_pk_max_i16 v106, v106, 0\n"
  "v_pk_max_i16 v107, v107, 0\n"
  "v_mfma_f32_16x16x32_bf16 v[136:139], v[72:75], v[232:235], v[136:139]\n"
  "v_cvt_pk_bf16_f32 v111, v110, v111\n"
  "v_cvt_pk_bf16_f32 v110, v108, v109\n"
  "v_mfma_f32_16x16x32_bf16 v[136:139], v[76:79], v[248:251], v[136:139]\n"
  "v_cvt_pk_bf16_f32 v108, v92, v93\n"
  "v_cvt_pk_bf16_f32 v109, v94, v95\n"
  "v_mfma_f32_16x16x32_bf16 v[140:143], v[72:75], v[236:239], v[140:143]\n"
  "ds_read_b64_tr_b16 v[64:65], v20 offset:192\n"
  "v_pk_max_i16 v108, v108, 0\n"
  "v_mfma_f32_16x16x32_bf16 v[140:143], v[76:79], v[252:255], v[140:143]\n"
  "ds_read_b64_tr_b16 v[66:67], v20 offset:4800\n"
  "v_pk_max_i16 v109, v109, 0\n"
  "v_mfma_f32_16x16x32_bf16 v[144:147], v[48:51], v[224:227], v[144:147]\n"
  "ds_read_b64_tr_b16 v[68:69], v20 offset:9408\n"
  "buffer_store_dwordx4 v[100:103], v1, s[36:39], s69 offen offset:0 sc1\n"
  "v_pk_max_i16 v110, v110, 0\n"
  "v_mfma_f32_16x16x32_bf16 v[144:147], v[52:55], v[240:243], v[144:147]\n"
  "ds_read_b64_tr_b16 v[70:71], v20 offset:14016\n"
  "v_pk_max_i16 v111, v111, 0\n"
  "v_mfma_f32_16x16x32_bf16 v[148:151], v[48:51], v[228:231], v[148:151]\n"
  "v_cvt_pk_bf16_f32 v131, v130, v131\n"
  "v_cvt_pk_bf16_f32 v130, v128, v129\n"
  "v_mfma_f32_16x16x32_bf16 v[148:151], v[52:55], v[244:247], v[148:151]\n"
  "v_cvt_pk_bf16_f32 v128, v112, v113\n"
  "v_cvt_pk_bf16_f32 v129, v114, v115\n"
  "v_mfma_f32_16x16x32_bf16 v[152:155], v[48:51], v[232:235], v[152:155]\n"
  "v_pk_max_i16 v128, v128, 0\n"
  "v_pk_max_i16 v129, v129, 0\n"
  "v_mfma_f32_16x16x32_bf16 v[152:155], v[52:55], v[248:251], v[152:155]\n"
  "v_pk_max_i16 v130, v130, 0\n"
  "v_pk_max_i16 v131, v131, 0\n"
  "v_mfma_f32_16x16x32_bf16 v[156:159], v[48:51], v[236:239], v[156:159]\n"
  "ds_read_b64_tr_b16 v[72:73], v20 offset:224\n"
  "v_cvt_pk_bf16_f32 v135, v134, v135\n"
  "v_mfma_f32_16x16x32_bf16 v[156:159], v[52:55], v[252:255], v[156:159]\n"
  "s_waitcnt lgkmcnt(1)\n"
  "ds_read_b64_tr_b16 v[74:75], v20 offset:4832\n"
  "v_cvt_pk_bf16_f32 v134, v132, v133\n"
  "v_mfma_f32_16x16x32_bf16 v[160:163], v[56:59], v[224:227], v[160:163]\n"
  "ds_read_b64_tr_b16 v[76:77], v20 offset:9440\n"
  "v_cvt_pk_bf16_f32 v132, v116, v117\n"
  "v_mfma_f32_16x16x32_bf16 v[160:163], v[60:63], v[240:243], v[160:163]\n"
  "ds_read_b64_tr_b16 v[78:79], v20 offset:14048\n"
  "v_add_u32 v21, 0xfffe5000, v21\n"
  "v_mfma_f32_16x16x32_bf16 v[164:167], v[56:59], v[228:231], v[164:167]\n"
  "v_cvt_pk_bf16_f32 v133, v118, v119\n"
  "v_pk_max_i16 v132, v132, 0\n"
  "v_mfma_f32_16x16x32_bf16 v[164:167], v[60:63], v[244:247], v[164:167]\n"
  "v_pk_max_i16 v133, v133, 0\n"
  "v_pk_max_i16 v134, v134, 0\n"
  "v_mfma_f32_16x16x32_bf16 v[168:171], v[56:59], v[232:235], v[168:171]\n"
  "v_pk_max_i16 v135, v135, 0\n"
  "v_cvt_pk_bf16_f32 v139, v138, v139\n"
  "v_mfma_f32_16x16x32_bf16 v[168:171], v[60:63], v[248:251], v[168:171]\n"
  "v_cvt_pk_bf16_f32 v138, v136, v137\n"
  "v_cvt_pk_bf16_f32 v136, v120, v121\n"
  "v_mfma_f32_16x16x32_bf16 v[172:175], v[56:59], v[236:239], v[172:175]\n"
  "ds_read_b64_tr_b16 v[48:49], v21 offset:0\n"
  "buffer_store_dwordx4 v[104:107], v1, s[36:39], s70 offen offset:0 sc1\n"
  "v_cvt_pk_bf16_f32 v137, v122, v123\n"
  "v_mfma_f32_16x16x32_bf16 v[172:175], v[60:63], v[252:255], v[172:175]\n"
  "ds_read_b64_tr_b16 v[50:51], v21 offset:4608\n"
  "v_pk_max_i16 v136, v136, 0\n"
  "v_mfma_f32_16x16x32_bf16 v[176:179], v[64:67], v[224:227], v[176:179]\n"
  "ds_read_b64_tr_b16 v[52:53], v21 offset:9216\n"
  "v_pk_max_i16 v137, v137, 0\n"
  "v_mfma_f32_16x16x32_bf16 v[176:179], v[68:71], v[240:243], v[176:179]\n"
  "ds_read_b64_tr_b16 v[54:55], v21 offset:13824\n"
  "v_pk_max_i16 v138, v138, 0\n"
  "v_mfma_f32_16x16x32_bf16 v[180:183], v[64:67], v[228:231], v[180:183]\n"
  "v_pk_max_i16 v139, v139, 0\n"
  "v_cvt_pk_bf16_f32 v143, v142, v143\n"
  "v_mfma_f32_16x16x32_bf16 v[180:183], v[68:71], v[244:247], v[180:183]\n"
  "v_cvt_pk_bf16_f32 v142, v140, v141\n"
  "v_cvt_pk_bf16_f32 v140, v124, v125\n"
  "v_mfma_f32_16x16x32_bf16 v[184:187], v[64:67], v[232:235], v[184:187]\n"
  "v_cvt_pk_bf16_f32 v141, v126, v127\n"
  "v_pk_max_i16 v140, v140, 0\n"
  "v_mfma_f32_16x16x32_bf16 v[184:187], v[68:71], v[248:251], v[184:187]\n"
  "v_pk_max_i16 v141, v141, 0\n"
  "v_pk_max_i16 v142, v142, 0\n"
  "v_mfma_f32_16x16x32_bf16 v[188:191], v[64:67], v[236:239], v[188:191]\n"
  "ds_read_b64_tr_b16 v[56:57], v44 offset:18432\n"
  "v_pk_max_i16 v143, v143, 0\n"
  "v_mfma_f32_16x16x32_bf16 v[188:191], v[68:71], v[252:255], v[188:191]\n"
  "s_waitcnt lgkmcnt(1)\n"
  "v_cvt_pk_bf16_f32 v163, v162, v163\n"
  "v_cvt_pk_bf16_f32 v162, v160, v161\n"
  "v_mfma_f32_16x16x32_bf16 v[192:195], v[72:75], v[224:227], v[192:195]\n"
  "ds_read_b64_tr_b16 v[58:59], v44 offset:22528\n"
  "v_cvt_pk_bf16_f32 v160, v144, v145\n"
  "v_mfma_f32_16x16x32_bf16 v[192:195], v[76:79], v[240:243], v[192:195]\n"
  "v_cvt_pk_bf16_f32 v161, v146, v147\n"
  "v_pk_max_i16 v160, v160, 0\n"
  "v_mfma_f32_16x16x32_bf16 v[196:199], v[72:75], v[228:231], v[196:199]\n"
  "ds_read_b64_tr_b16 v[60:61], v44 offset:27648\n"
  "v_pk_max_i16 v161, v161, 0\n"
  "v_mfma_f32_16x16x32_bf16 v[196:199], v[76:79], v[244:247], v[196:199]\n"
  "v_pk_max_i16 v162, v162, 0\n"
  "v_pk_max_i16 v163, v163, 0\n"
  "v_mfma_f32_16x16x32_bf16 v[200:203], v[72:75], v[232:235], v[200:203]\n"
  "ds_read_b64_tr_b16 v[62:63], v44 offset:31744\n"
  "buffer_store_dwordx4 v[108:111], v1, s[36:39], s71 offen offset:0 sc1\n"
  "v_cvt_pk_bf16_f32 v167, v166, v167\n"
  "v_mfma_f32_16x16x32_bf16 v[200:203], v[76:79], v[248:251], v[200:203]\n"
  "v_cvt_pk_bf16_f32 v166, v164, v165\n"
  "v_cvt_pk_bf16_f32 v164, v148, v149\n"
  "v_mfma_f32_16x16x32_bf16 v[204:207], v[72:75], v[236:239], v[204:207]\n"
  "s_waitcnt vmcnt(12)\n"
  "s_barrier\n"
  "ds_read_b64_tr_b16 v[64:65], v44 offset:36864\n"
  "v_cvt_pk_bf16_f32 v165, v150, v151\n"
  "v_mfma_f32_16x16x32_bf16 v[204:207], v[76:79], v[252:255], v[204:207]\n"
  "v_pk_max_i16 v164, v164, 0\n"
  "v_pk_max_i16 v165, v165, 0\n"
  "v_mfma_f32_16x16x32_bf16 v[208:211], v[48:51], v[224:227], v[208:211]\n"
  "ds_read_b64_tr_b16 v[66:67], v44 offset:40960\n"
  "v_pk_max_i16 v166, v166, 0\n"
  "v_mfma_f32_16x16x32_bf16 v[208:211], v[52:55], v[240:243], v[208:211]\n"
  "v_pk_max_i16 v167, v167, 0\n"
  "v_cvt_pk_bf16_f32 v171, v170, v171\n"
  "v_mfma_f32_16x16x32_bf16 v[212:215], v[48:51], v[228:231], v[212:215]\n"
  "ds_read_b64_tr_b16 v[68:69], v44 offset:46080\n"
  "v_cvt_pk_bf16_f32 v170, v168, v169\n"
  "v_mfma_f32_16x16x32_bf16 v[212:215], v[52:55], v[244:247], v[212:215]\n"
  "v_cvt_pk_bf16_f32 v168, v152, v153\n"
  "v_cvt_pk_bf16_f32 v169, v154, v155\n"
  "v_mfma_f32_16x16x32_bf16 v[216:219], v[48:51], v[232:235], v[216:219]\n"
  "ds_read_b64_tr_b16 v[70:71], v44 offset:50176\n"
  "v_pk_max_i16 v168, v168, 0\n"
  "v_mfma_f32_16x16x32_bf16 v[216:219], v[52:55], v[248:251], v[216:219]\n"
  "v_pk_max_i16 v169, v169, 0\n"
  "v_pk_max_i16 v170, v170, 0\n"
  "v_mfma_f32_16x16x32_bf16 v[220:223], v[48:51], v[236:239], v[220:223]\n"
  "ds_read_b64_tr_b16 v[72:73], v44 offset:55296\n"
  "v_pk_max_i16 v171, v171, 0\n"
  "v_mfma_f32_16x16x32_bf16 v[220:223], v[52:55], v[252:255], v[220:223]\n"
  "s_waitcnt lgkmcnt(2)\n"
  "v_cvt_pk_bf16_f32 v175, v174, v175\n"
  "v_cvt_pk_bf16_f32 v174, v172, v173\n"
  "v_mfma_f32_16x16x32_bf16 v[80:83], v[56:59], a[0:3], 0\n"
  "ds_read_b64_tr_b16 v[74:75], v44 offset:59392\n"
  "v_add_u32 v44, 36864, v44\n"
  "v_mfma_f32_16x16x32_bf16 v[80:83], v[60:63], a[16:19], v[80:83]\n"
  "v_cvt_pk_bf16_f32 v172, v156, v157\n"
  "v_cvt_pk_bf16_f32 v173, v158, v159\n"
  "v_mfma_f32_16x16x32_bf16 v[84:87], v[56:59], a[4:7], 0\n"
  "ds_read_b64_tr_b16 v[76:77], v44 offset:27648\n"
  "v_pk_max_i16 v172, v172, 0\n"
  "v_mfma_f32_16x16x32_bf16 v[84:87], v[60:63], a[20:23], v[84:87]\n"
  "v_pk_max_i16 v173, v173, 0\n"
  "v_pk_max_i16 v174, v174, 0\n"
  "v_mfma_f32_16x16x32_bf16 v[88:91], v[56:59], a[8:11], 0\n"
  "ds_read_b64_tr_b16 v[78:79], v44 offset:31744\n"
  "v_add_u32 v44, 0xffff7000, v44\n"
  "v_mfma_f32_16x16x32_bf16 v[88:91], v[60:63], a[24:27], v[88:91]\n"
  "v_pk_max_i16 v175, v175, 0\n"
  "v_cvt_pk_bf16_f32 v195, v194, v195\n"
  "v_mfma_f32_16x16x32_bf16 v[92:95], v[56:59], a[12:15], 0\n"
  "ds_read_b64_tr_b16 v[48:49], v44 offset:18440\n"
  "v_cvt_pk_bf16_f32 v194, v192, v193\n"
  "v_mfma_f32_16x16x32_bf16 v[92:95], v[60:63], a[28:31], v[92:95]\n"
  "v_cvt_pk_bf16_f32 v192, v176, v177\n"
  "v_cvt_pk_bf16_f32 v193, v178, v179\n"
  "v_mfma_f32_16x16x32_bf16 v[80:83], v[64:67], a[32:35], v[80:83]\n"
  "s_waitcnt lgkmcnt(1)\n"
  "ds_read_b64_tr_b16 v[50:51], v44 offset:22536\n"
  "v_pk_max_i16 v192, v192, 0\n"
  "v_mfma_f32_16x16x32_bf16 v[80:83], v[68:71], a[48:51], v[80:83]\n"
  "v_pk_max_i16 v193, v193, 0\n"
  "v_pk_max_i16 v194, v194, 0\n"
  "v_mfma_f32_16x16x32_bf16 v[84:87], v[64:67], a[36:39], v[84:87]\n"
  "ds_read_b64_tr_b16 v[52:53], v44 offset:27656\n"
  "v_pk_max_i16 v195, v195, 0\n"
  "v_mfma_f32_16x16x32_bf16 v[84:87], v[68:71], a[52:55], v[84:87]\n"
  "v_cvt_pk_bf16_f32 v199, v198, v199\n"
  "v_cvt_pk_bf16_f32 v198, v196, v197\n"
  "v_mfma_f32_16x16x32_bf16 v[88:91], v[64:67], a[40:43], v[88:91]\n"
  "ds_read_b64_tr_b16 v[54:55], v44 offset:31752\n"
  "v_cvt_pk_bf16_f32 v196, v180, v181\n"
  "v_mfma_f32_16x16x32_bf16 v[88:91], v[68:71], a[56:59], v[88:91]\n"
  "v_cvt_pk_bf16_f32 v197, v182, v183\n"
  "v_pk_max_i16 v196, v196, 0\n"
  "v_mfma_f32_16x16x32_bf16 v[92:95], v[64:67], a[44:47], v[92:95]\n"
  "ds_read_b64_tr_b16 v[56:57], v44 offset:36872\n"
  "v_pk_max_i16 v197, v197, 0\n"
  "v_mfma_f32_16x16x32_bf16 v[92:95], v[68:71], a[60:63], v[92:95]\n"
  "v_pk_max_i16 v198, v198, 0\n"
  "v_pk_max_i16 v199, v199, 0\n"
  "v_mfma_f32_16x16x32_bf16 v[80:83], v[72:75], a[64:67], v[80:83]\n"
  "ds_read_b64_tr_b16 v[58:59], v44 offset:40968\n"
  "v_cvt_pk_bf16_f32 v203, v202, v203\n"
  "v_mfma_f32_16x16x32_bf16 v[80:83], v[76:79], a[80:83], v[80:83]\n"
  "v_cvt_pk_bf16_f32 v202, v200, v201\n"
  "v_cvt_pk_bf16_f32 v200, v184, v185\n"
  "v_mfma_f32_16x16x32_bf16 v[84:87], v[72:75], a[68:71], v[84:87]\n"
  "ds_read_b64_tr_b16 v[60:61], v44 offset:46088\n"
  "v_cvt_pk_bf16_f32 v201, v186, v187\n"
  "v_mfma_f32_16x16x32_bf16 v[84:87], v[76:79], a[84:87], v[84:87]\n"
  "v_pk_max_i16 v200, v200, 0\n"
  "v_pk_max_i16 v201, v201, 0\n"
  "v_mfma_f32_16x16x32_bf16 v[88:91], v[72:75], a[72:75], v[88:91]\n"
  "ds_read_b64_tr_b16 v[62:63], v44 offset:50184\n"
  "v_pk_max_i16 v202, v202, 0\n"
  "v_mfma_f32_16x16x32_bf16 v[88:91], v[76:79], a[88:91], v[88:91]\n"
  "v_pk_max_i16 v203, v203, 0\n"
  "v_cvt_pk_bf16_f32 v207, v206, v207\n"
  "v_mfma_f32_16x16x32_bf16 v[92:95], v[72:75], a[76:79], v[92:95]\n"
  "ds_read_b64_tr_b16 v[64:65], v44 offset:55304\n"
  "v_cvt_pk_bf16_f32 v206, v204, v205\n"
  "v_mfma_f32_16x16x32_bf16 v[92:95], v[76:79], a[92:95], v[92:95]\n"
  "s_waitcnt lgkmcnt(2)\n"
  "v_cvt_pk_bf16_f32 v204, v188, v189\n"
  "v_cvt_pk_bf16_f32 v205, v190, v191\n"
  "v_mfma_f32_16x16x32_bf16 v[96:99], v[48:51], a[0:3], 0\n"
  "ds_read_b64_tr_b16 v[66:67], v44 offset:59400\n"
  "v_add_u32 v44, 36864, v44\n"
  "v_mfma_f32_16x16x32_bf16 v[96:99], v[52:55], a[16:19], v[96:99]\n"
  "v_pk_max_i16 v204, v204, 0\n"
  "v_pk_max_i16 v205, v205, 0\n"
  "v_mfma_f32_16x16x32_bf16 v[100:103], v[48:51], a[4:7], 0\n"
  "ds_read_b64_tr_b16 v[68:69], v44 offset:27656\n"
  "v_pk_max_i16 v206, v206, 0\n"
  "v_mfma_f32_16x16x32_bf16 v[100:103], v[52:55], a[20:23], v[100:103]\n"
  "v_pk_max_i16 v207, v207, 0\n"
  "v_cvt_pk_bf16_f32 v208, v208, v209\n"
  "v_mfma_f32_16x16x32_bf16 v[104:107], v[48:51], a[8:11], 0\n"
  "ds_read_b64_tr_b16 v[70:71], v44 offset:31752\n"
  "v_cvt_pk_bf16_f32 v209, v210, v211\n"
  "v_mfma_f32_16x16x32_bf16 v[104:107], v[52:55], a[24:27], v[104:107]\n"
  "v_pk_max_i16 v208, v208, 0\n"
  "v_pk_max_i16 v209, v209, 0\n"
  "v_mfma_f32_16x16x32_bf16 v[108:111], v[48:51], a[12:15], 0\n"
  "ds_read_b64_tr_b16 v[72:73], v45 offset:18432\n"
  "v_cvt_pk_bf16_f32 v212, v212, v213\n"
  "v_mfma_f32_16x16x32_bf16 v[108:111], v[52:55], a[28:31], v[108:111]\n"
  "v_cvt_pk_bf16_f32 v213, v214, v215\n"
  "v_pk_max_i16 v212, v212, 0\n"
  "v_mfma_f32_16x16x32_bf16 v[96:99], v[56:59], a[32:35], v[96:99]\n"
  "s_waitcnt lgkmcnt(1)\n"
  "ds_read_b64_tr_b16 v[74:75], v45 offset:22528\n"
  "v_pk_max_i16 v213, v213, 0\n"
  "v_mfma_f32_16x16x32_bf16 v[96:99], v[60:63], a[48:51], v[96:99]\n"
  "v_cvt_pk_bf16_f32 v216, v216, v217\n"
  "v_cvt_pk_bf16_f32 v217, v218, v219\n"
  "v_mfma_f32_16x16x32_bf16 v[100:103], v[56:59], a[36:39], v[100:103]\n"
  "ds_read_b64_tr_b16 v[76:77], v45 offset:27648\n"
  "v_pk_max_i16 v216, v216, 0\n"
  "v_mfma_f32_16x16x32_bf16 v[100:103], v[60:63], a[52:55], v[100:103]\n"
  "v_pk_max_i16 v217, v217, 0\n"
  "v_cvt_pk_bf16_f32 v220, v220, v221\n"
  "v_mfma_f32_16x16x32_bf16 v[104:107], v[56:59], a[40:43], v[104:107]\n"
  "ds_read_b64_tr_b16 v[78:79], v45 offset:31744\n"
  "v_cvt_pk_bf16_f32 v221, v222, v223\n"
  "v_mfma_f32_16x16x32_bf16 v[104:107], v[60:63], a[56:59], v[104:107]\n"
  "v_pk_max_i16 v220, v220, 0\n"
  "v_pk_max_i16 v221, v221, 0\n"
  "v_mfma_f32_16x16x32_bf16 v[108:111], v[56:59], a[44:47], v[108:111]\n"
  "ds_read_b64_tr_b16 v[48:49], v45 offset:36864\n"
  "v_mfma_f32_16x16x32_bf16 v[108:111], v[60:63], a[60:63], v[108:111]\n"
  "buffer_store_dwordx4 v[128:131], v1, s[36:39], s68 offen offset:1024 sc1\n"
  "v_mfma_f32_16x16x32_bf16 v[96:99], v[64:67], a[64:67], v[96:99]\n"
  "ds_read_b64_tr_b16 v[50:51], v45 offset:40960\n"
  "v_mfma_f32_16x16x32_bf16 v[96:99], v[68:71], a[80:83], v[96:99]\n"
  "v_mfma_f32_16x16x32_bf16 v[100:103], v[64:67], a[68:71], v[100:103]\n"
  "ds_read_b64_tr_b16 v[52:53], v45 offset:46080\n"
  "v_mfma_f32_16x16x32_bf16 v[100:103], v[68:71], a[84:87], v[100:103]\n"
  "v_mfma_f32_16x16x32_bf16 v[104:107], v[64:67], a[72:75], v[104:107]\n"
  "ds_read_b64_tr_b16 v[54:55], v45 offset:50176\n"
  "v_mfma_f32_16x16x32_bf16 v[104:107], v[68:71], a[88:91], v[104:107]\n"
  "v_mfma_f32_16x16x32_bf16 v[108:111], v[64:67], a[76:79], v[108:111]\n"
  "ds_read_b64_tr_b16 v[56:57], v45 offset:55296\n"
  "v_mfma_f32_16x16x32_bf16 v[108:111], v[68:71], a[92:95], v[108:111]\n"
  "s_waitcnt lgkmcnt(4)\n"
  "v_mfma_f32_16x16x32_bf16 v[112:115], v[72:75], a[0:3], 0\n"
  "ds_read_b64_tr_b16 v[58:59], v45 offset:59392\n"
  "v_add_u32 v45, 36864, v45\n"
  "v_mfma_f32_16x16x32_bf16 v[112:115], v[76:79], a[16:19], v[112:115]\n"
  "v_mfma_f32_16x16x32_bf16 v[116:119], v[72:75], a[4:7], 0\n"
  "ds_read_b64_tr_b16 v[60:61], v45 offset:27648\n"
  "v_mfma_f32_16x16x32_bf16 v[116:119], v[76:79], a[20:23], v[116:119]\n"
  "v_mfma_f32_16x16x32_bf16 v[120:123], v[72:75], a[8:11], 0\n"
  "ds_read_b64_tr_b16 v[62:63], v45 offset:31744\n"
  "v_add_u32 v45, 0xffff7000, v45\n"
  "v_mfma_f32_16x16x32_bf16 v[120:123], v[76:79], a[24:27], v[120:123]\n"
  "buffer_store_dwordx4 v[132:135], v1, s[36:39], s69 offen offset:1024 sc1\n"
  "v_mfma_f32_16x16x32_bf16 v[124:127], v[72:75], a[12:15], 0\n"
  "ds_read_b64_tr_b16 v[64:65], v45 offset:18440\n"
  "v_mfma_f32_16x16x32_bf16 v[124:127], v[76:79], a[28:31], v[124:127]\n"
  "s_waitcnt lgkmcnt(3)\n"
  "v_mfma_f32_16x16x32_bf16 v[112:115], v[48:51], a[32:35], v[112:115]\n"
  "ds_read_b64_tr_b16 v[66:67], v45 offset:22536\n"
  "v_mfma_f32_16x16x32_bf16 v[112:115], v[52:55], a[48:51], v[112:115]\n"
  "v_mfma_f32_16x16x32_bf16 v[116:119], v[48:51], a[36:39], v[116:119]\n"
  "ds_read_b64_tr_b16 v[68:69], v45 offset:27656\n"
  "v_mfma_f32_16x16x32_bf16 v[116:119], v[52:55], a[52:55], v[116:119]\n"
  "v_mfma_f32_16x16x32_bf16 v[120:123], v[48:51], a[40:43], v[120:123]\n"
  "ds_read_b64_tr_b16 v[70:71], v45 offset:31752\n"
  "v_mfma_f32_16x16x32_bf16 v[120:123], v[52:55], a[56:59], v[120:123]\n"
  "v_mfma_f32_16x16x32_bf16 v[124:127], v[48:51], a[44:47], v[124:127]\n"
  "ds_read_b64_tr_b16 v[72:73], v45 offset:36872\n"
  "v_mfma_f32_16x16x32_bf16 v[124:127], v[52:55], a[60:63], v[124:127]\n"
  "v_mfma_f32_16x16x32_bf16 v[112:115], v[56:59], a[64:67], v[112:115]\n"
  "s_waitcnt lgkmcnt(4)\n"
  "ds_read_b64_tr_b16 v[74:75], v45 offset:40968\n"
  "v_mfma_f32_16x16x32_bf16 v[112:115], v[60:63], a[80:83], v[112:115]\n"
  "v_mfma_f32_16x16x32_bf16 v[116:119], v[56:59], a[68:71], v[116:119]\n"
  "ds_read_b64_tr_b16 v[76:77], v45 offset:46088\n"
  "v_mfma_f32_16x16x32_bf16 v[116:119], v[60:63], a[84:87], v[116:119]\n"
  "buffer_store_dwordx4 v[136:139], v1, s[36:39], s70 offen offset:1024 sc1\n"
  "v_mfma_f32_16x16x32_bf16 v[120:123], v[56:59], a[72:75], v[120:123]\n"
  "ds_read_b64_tr_b16 v[78:79], v45 offset:50184\n"
  "v_mfma_f32_16x16x32_bf16 v[120:123], v[60:63], a[88:91], v[120:123]\n"
  "v_mfma_f32_16x16x32_bf16 v[124:127], v[56:59], a[76:79], v[124:127]\n"
  "ds_read_b64_tr_b16 v[48:49], v45 offset:55304\n"
  "v_mfma_f32_16x16x32_bf16 v[124:127], v[60:63], a[92:95], v[124:127]\n"
  "s_waitcnt lgkmcnt(4)\n"
  "v_mfma_f32_16x16x32_bf16 v[128:131], v[64:67], a[0:3], 0\n"
  "ds_read_b64_tr_b16 v[50:51], v45 offset:59400\n"
  "v_add_u32 v45, 36864, v45\n"
  "v_mfma_f32_16x16x32_bf16 v[128:131], v[68:71], a[16:19], v[128:131]\n"
  "v_mfma_f32_16x16x32_bf16 v[132:135], v[64:67], a[4:7], 0\n"
  "ds_read_b64_tr_b16 v[52:53], v45 offset:27656\n"
  "v_mfma_f32_16x16x32_bf16 v[132:135], v[68:71], a[20:23], v[132:135]\n"
  "v_mfma_f32_16x16x32_bf16 v[136:139], v[64:67], a[8:11], 0\n"
  "ds_read_b64_tr_b16 v[54:55], v45 offset:31752\n"
  "buffer_store_dwordx4 v[140:143], v1, s[36:39], s71 offen offset:1024 sc1\n"
  "v_mfma_f32_16x16x32_bf16 v[136:139], v[68:71], a[24:27], v[136:139]\n"
  "s_nop 0\n"
  "v_mfma_f32_16x16x32_bf16 v[140:143], v[64:67], a[12:15], 0\n"
  "ds_read_b64_tr_b16 v[56:57], v46 offset:18432\n"
  "v_mfma_f32_16x16x32_bf16 v[140:143], v[68:71], a[28:31], v[140:143]\n"
  "s_waitcnt lgkmcnt(4)\n"
  "v_mfma_f32_16x16x32_bf16 v[128:131], v[72:75], a[32:35], v[128:131]\n"
  "ds_read_b64_tr_b16 v[58:59], v46 offset:22528\n"
  "v_mfma_f32_16x16x32_bf16 v[128:131], v[76:79], a[48:51], v[128:131]\n"
  "v_mfma_f32_16x16x32_bf16 v[132:135], v[72:75], a[36:39], v[132:135]\n"
  "ds_read_b64_tr_b16 v[60:61], v46 offset:27648\n"
  "v_mfma_f32_16x16x32_bf16 v[132:135], v[76:79], a[52:55], v[132:135]\n"
  "v_mfma_f32_16x16x32_bf16 v[136:139], v[72:75], a[40:43], v[136:139]\n"
  "ds_read_b64_tr_b16 v[62:63], v46 offset:31744\n"
  "v_mfma_f32_16x16x32_bf16 v[136:139], v[76:79], a[56:59], v[136:139]\n"
  "v_mfma_f32_16x16x32_bf16 v[140:143], v[72:75], a[44:47], v[140:143]\n"
  "ds_read_b64_tr_b16 v[64:65], v46 offset:36864\n"
  "v_mfma_f32_16x16x32_bf16 v[140:143], v[76:79], a[60:63], v[140:143]\n"
  "s_waitcnt lgkmcnt(4)\n"
  "v_mfma_f32_16x16x32_bf16 v[128:131], v[48:51], a[64:67], v[128:131]\n"
  "ds_read_b64_tr_b16 v[66:67], v46 offset:40960\n"
  "v_mfma_f32_16x16x32_bf16 v[128:131], v[52:55], a[80:83], v[128:131]\n"
  "v_mfma_f32_16x16x32_bf16 v[132:135], v[48:51], a[68:71], v[132:135]\n"
  "ds_read_b64_tr_b16 v[68:69], v46 offset:46080\n"
  "v_mfma_f32_16x16x32_bf16 v[132:135], v[52:55], a[84:87], v[132:135]\n"
  "buffer_store_dwordx4 v[160:163], v1, s[36:39], s68 offen offset:2048 sc1\n"
  "v_mfma_f32_16x16x32_bf16 v[136:139], v[48:51], a[72:75], v[136:139]\n"
  "ds_read_b64_tr_b16 v[70:71], v46 offset:50176\n"
  "v_mfma_f32_16x16x32_bf16 v[136:139], v[52:55], a[88:91], v[136:139]\n"
  "v_mfma_f32_16x16x32_bf16 v[140:143], v[48:51], a[76:79], v[140:143]\n"
  "ds_read_b64_tr_b16 v[72:73], v46 offset:55296\n"
  "v_mfma_f32_16x16x32_bf16 v[140:143], v[52:55], a[92:95], v[140:143]\n"
  "s_waitcnt lgkmcnt(4)\n"
  "v_mfma_f32_16x16x32_bf16 v[144:147], v[56:59], a[0:3], 0\n"
  "ds_read_b64_tr_b16 v[74:75], v46 offset:59392\n"
  "v_add_u32 v46, 36864, v46\n"
  "v_mfma_f32_16x16x32_bf16 v[144:147], v[60:63], a[16:19], v[144:147]\n"
  "v_mfma_f32_16x16x32_bf16 v[148:151], v[56:59], a[4:7], 0\n"
  "ds_read_b64_tr_b16 v[76:77], v46 offset:27648\n"
  "v_mfma_f32_16x16x32_bf16 v[148:151], v[60:63], a[20:23], v[148:151]\n"
  "v_mfma_f32_16x16x32_bf16 v[152:155], v[56:59], a[8:11], 0\n"
  "ds_read_b64_tr_b16 v[78:79], v46 offset:31744\n"
  "v_add_u32 v46, 0xffff7000, v46\n"
  "v_mfma_f32_16x16x32_bf16 v[152:155], v[60:63], a[24:27], v[152:155]\n"
  "v_mfma_f32_16x16x32_bf16 v[156:159], v[56:59], a[12:15], 0\n"
  "ds_read_b64_tr_b16 v[48:49], v46 offset:18440\n"
  "v_mfma_f32_16x16x32_bf16 v[156:159], v[60:63], a[28:31], v[156:159]\n"
  "s_waitcnt lgkmcnt(4)\n"
  "v_mfma_f32_16x16x32_bf16 v[144:147], v[64:67], a[32:35], v[144:147]\n"
  "ds_read_b64_tr_b16 v[50:51], v46 offset:22536\n"
  "v_mfma_f32_16x16x32_bf16 v[144:147], v[68:71], a[48:51], v[144:147]\n"
  "buffer_store_dwordx4 v[164:167], v1, s[36:39], s69 offen offset:2048 sc1\n"
  "v_mfma_f32_16x16x32_bf16 v[148:151], v[64:67], a[36:39], v[148:151]\n"
  "ds_read_b64_tr_b16 v[52:53], v46 offset:27656\n"
  "v_mfma_f32_16x16x32_bf16 v[148:151], v[68:71], a[52:55], v[148:151]\n"
  "v_mfma_f32_16x16x32_bf16 v[152:155], v[64:67], a[40:43], v[152:155]\n"
  "ds_read_b64_tr_b16 v[54:55], v46 offset:31752\n"
  "v_mfma_f32_16x16x32_bf16 v[152:155], v[68:71], a[56:59], v[152:155]\n"
  "v_mfma_f32_16x16x32_bf16 v[156:159], v[64:67], a[44:47], v[156:159]\n"
  "ds_read_b64_tr_b16 v[56:57], v46 offset:36872\n"
  "v_mfma_f32_16x16x32_bf16 v[156:159], v[68:71], a[60:63], v[156:159]\n"
  "s_waitcnt lgkmcnt(4)\n"
  "v_mfma_f32_16x16x32_bf16 v[144:147], v[72:75], a[64:67], v[144:147]\n"
  "ds_read_b64_tr_b16 v[58:59], v46 offset:40968\n"
  "v_mfma_f32_16x16x32_bf16 v[144:147], v[76:79], a[80:83], v[144:147]\n"
  "v_mfma_f32_16x16x32_bf16 v[148:151], v[72:75], a[68:71], v[148:151]\n"
  "ds_read_b64_tr_b16 v[60:61], v46 offset:46088\n"
  "v_mfma_f32_16x16x32_bf16 v[148:151], v[76:79], a[84:87], v[148:151]\n"
  "v_mfma_f32_16x16x32_bf16 v[152:155], v[72:75], a[72:75], v[152:155]\n"
  "ds_read_b64_tr_b16 v[62:63], v46 offset:50184\n"
  "v_mfma_f32_16x16x32_bf16 v[152:155], v[76:79], a[88:91], v[152:155]\n"
  "v_mfma_f32_16x16x32_bf16 v[156:159], v[72:75], a[76:79], v[156:159]\n"
  "ds_read_b64_tr_b16 v[64:65], v46 offset:55304\n"
  "v_mfma_f32_16x16x32_bf16 v[156:159], v[76:79], a[92:95], v[156:159]\n"
  "s_waitcnt lgkmcnt(4)\n"
  "buffer_store_dwordx4 v[168:171], v1, s[36:39], s70 offen offset:2048 sc1\n"
  "v_mfma_f32_16x16x32_bf16 v[160:163], v[48:51], a[0:3], 0\n"
  "ds_read_b64_tr_b16 v[66:67], v46 offset:59400\n"
  "v_add_u32 v46, 36864, v46\n"
  "v_mfma_f32_16x16x32_bf16 v[160:163], v[52:55], a[16:19], v[160:163]\n"
  "v_mfma_f32_16x16x32_bf16 v[164:167], v[48:51], a[4:7], 0\n"
  "ds_read_b64_tr_b16 v[68:69], v46 offset:27656\n"
  "v_mfma_f32_16x16x32_bf16 v[164:167], v[52:55], a[20:23], v[164:167]\n"
  "v_mfma_f32_16x16x32_bf16 v[168:171], v[48:51], a[8:11], 0\n"
  "ds_read_b64_tr_b16 v[70:71], v46 offset:31752\n"
  "buffer_store_dwordx4 v[172:175], v1, s[36:39], s71 offen offset:2048 sc1\n"
  "v_mfma_f32_16x16x32_bf16 v[168:171], v[52:55], a[24:27], v[168:171]\n"
  "s_nop 0\n"
  "v_mfma_f32_16x16x32_bf16 v[172:175], v[48:51], a[12:15], 0\n"
  "ds_read_b64_tr_b16 v[72:73], v47 offset:18432\n"
  "v_mfma_f32_16x16x32_bf16 v[172:175], v[52:55], a[28:31], v[172:175]\n"
  "s_waitcnt lgkmcnt(4)\n"
  "v_mfma_f32_16x16x32_bf16 v[160:163], v[56:59], a[32:35], v[160:163]\n"
  "ds_read_b64_tr_b16 v[74:75], v47 offset:22528\n"
  "v_mfma_f32_16x16x32_bf16 v[160:163], v[60:63], a[48:51], v[160:163]\n"
  "v_mfma_f32_16x16x32_bf16 v[164:167], v[56:59], a[36:39], v[164:167]\n"
  "ds_read_b64_tr_b16 v[76:77], v47 offset:27648\n"
  "v_mfma_f32_16x16x32_bf16 v[164:167], v[60:63], a[52:55], v[164:167]\n"
  "v_mfma_f32_16x16x32_bf16 v[168:171], v[56:59], a[40:43], v[168:171]\n"
  "ds_read_b64_tr_b16 v[78:79], v47 offset:31744\n"
  "v_mfma_f32_16x16x32_bf16 v[168:171], v[60:63], a[56:59], v[168:171]\n"
  "v_mfma_f32_16x16x32_bf16 v[172:175], v[56:59], a[44:47], v[172:175]\n"
  "ds_read_b64_tr_b16 v[48:49], v47 offset:36864\n"
  "v_mfma_f32_16x16x32_bf16 v[172:175], v[60:63], a[60:63], v[172:175]\n"
  "s_waitcnt lgkmcnt(4)\n"
  "v_mfma_f32_16x16x32_bf16 v[160:163], v[64:67], a[64:67], v[160:163]\n"
  "ds_read_b64_tr_b16 v[50:51], v47 offset:40960\n"
  "v_mfma_f32_16x16x32_bf16 v[160:163], v[68:71], a[80:83], v[160:163]\n"
  "v_mfma_f32_16x16x32_bf16 v[164:167], v[64:67], a[68:71], v[164:167]\n"
  "ds_read_b64_tr_b16 v[52:53], v47 offset:46080\n"
  "v_mfma_f32_16x16x32_bf16 v[164:167], v[68:71], a[84:87], v[164:167]\n"
  "buffer_store_dwordx4 v[192:195], v1, s[36:39], s68 offen offset:3072 sc1\n"
  "v_mfma_f32_16x16x32_bf16 v[168:171], v[64:67], a[72:75], v[168:171]\n"
  "ds_read_b64_tr_b16 v[54:55], v47 offset:50176\n"
  "v_mfma_f32_16x16x32_bf16 v[168:171], v[68:71], a[88:91], v[168:171]\n"
  "v_mfma_f32_16x16x32_bf16 v[172:175], v[64:67], a[76:79], v[172:175]\n"
  "ds_read_b64_tr_b16 v[56:57], v47 offset:55296\n"
  "v_mfma_f32_16x16x32_bf16 v[172:175], v[68:71], a[92:95], v[172:175]\n"
  "s_waitcnt lgkmcnt(4)\n"
  "v_mfma_f32_16x16x32_bf16 v[176:179], v[72:75], a[0:3], 0\n"
  "ds_read_b64_tr_b16 v[58:59], v47 offset:59392\n"
  "v_add_u32 v47, 36864, v47\n"
  "v_mfma_f32_16x16x32_bf16 v[176:179], v[76:79], a[16:19], v[176:179]\n"
  "v_mfma_f32_16x16x32_bf16 v[180:183], v[72:75], a[4:7], 0\n"
  "ds_read_b64_tr_b16 v[60:61], v47 offset:27648\n"
  "v_mfma_f32_16x16x32_bf16 v[180:183], v[76:79], a[20:23], v[180:183]\n"
  "v_mfma_f32_16x16x32_bf16 v[184:187], v[72:75], a[8:11], 0\n"
  "ds_read_b64_tr_b16 v[62:63], v47 offset:31744\n"
  "v_add_u32 v47, 0xffff7000, v47\n"
  "v_mfma_f32_16x16x32_bf16 v[184:187], v[76:79], a[24:27], v[184:187]\n"
  "v_mfma_f32_16x16x32_bf16 v[188:191], v[72:75], a[12:15], 0\n"
  "ds_read_b64_tr_b16 v[64:65], v47 offset:18440\n"
  "v_mfma_f32_16x16x32_bf16 v[188:191], v[76:79], a[28:31], v[188:191]\n"
  "s_waitcnt lgkmcnt(4)\n"
  "v_mfma_f32_16x16x32_bf16 v[176:179], v[48:51], a[32:35], v[176:179]\n"
  "ds_read_b64_tr_b16 v[66:67], v47 offset:22536\n"
  "v_mfma_f32_16x16x32_bf16 v[176:179], v[52:55], a[48:51], v[176:179]\n"
  "buffer_store_dwordx4 v[196:199], v1, s[36:39], s69 offen offset:3072 sc1\n"
  "v_mfma_f32_16x16x32_bf16 v[180:183], v[48:51], a[36:39], v[180:183]\n"
  "ds_read_b64_tr_b16 v[68:69], v47 offset:27656\n"
  "v_mfma_f32_16x16x32_bf16 v[180:183], v[52:55], a[52:55], v[180:183]\n"
  "v_mfma_f32_16x16x32_bf16 v[184:187], v[48:51], a[40:43], v[184:187]\n"
  "ds_read_b64_tr_b16 v[70:71], v47 offset:31752\n"
  "v_mfma_f32_16x16x32_bf16 v[184:187], v[52:55], a[56:59], v[184:187]\n"
  "v_mfma_f32_16x16x32_bf16 v[188:191], v[48:51], a[44:47], v[188:191]\n"
  "ds_read_b64_tr_b16 v[72:73], v47 offset:36872\n"
  "v_mfma_f32_16x16x32_bf16 v[188:191], v[52:55], a[60:63], v[188:191]\n"
  "s_waitcnt lgkmcnt(4)\n"
  "v_mfma_f32_16x16x32_bf16 v[176:179], v[56:59], a[64:67], v[176:179]\n"
  "ds_read_b64_tr_b16 v[74:75], v47 offset:40968\n"
  "v_mfma_f32_16x16x32_bf16 v[176:179], v[60:63], a[80:83], v[176:179]\n"
  "v_mfma_f32_16x16x32_bf16 v[180:183], v[56:59], a[68:71], v[180:183]\n"
  "ds_read_b64_tr_b16 v[76:77], v47 offset:46088\n"
  "v_mfma_f32_16x16x32_bf16 v[180:183], v[60:63], a[84:87], v[180:183]\n"
  "v_mfma_f32_16x16x32_bf16 v[184:187], v[56:59], a[72:75], v[184:187]\n"
  "ds_read_b64_tr_b16 v[78:79], v47 offset:50184\n"
  "v_mfma_f32_16x16x32_bf16 v[184:187], v[60:63], a[88:91], v[184:187]\n"
  "v_mfma_f32_16x16x32_bf16 v[188:191], v[56:59], a[76:79], v[188:191]\n"
  "ds_read_b64_tr_b16 v[48:49], v47 offset:55304\n"
  "v_mfma_f32_16x16x32_bf16 v[188:191], v[60:63], a[92:95], v[188:191]\n"
  "s_waitcnt lgkmcnt(4)\n"
  "buffer_store_dwordx4 v[200:203], v1, s[36:39], s70 offen offset:3072 sc1\n"
  "v_mfma_f32_16x16x32_bf16 v[192:195], v[64:67], a[0:3], 0\n"
  "ds_read_b64_tr_b16 v[50:51], v47 offset:59400\n"
  "v_add_u32 v47, 36864, v47\n"
  "v_mfma_f32_16x16x32_bf16 v[192:195], v[68:71], a[16:19], v[192:195]\n"
  "v_mfma_f32_16x16x32_bf16 v[196:199], v[64:67], a[4:7], 0\n"
  "ds_read_b64_tr_b16 v[52:53], v47 offset:27656\n"
  "v_mfma_f32_16x16x32_bf16 v[196:199], v[68:71], a[20:23], v[196:199]\n"
  "v_mfma_f32_16x16x32_bf16 v[200:203], v[64:67], a[8:11], 0\n"
  "ds_read_b64_tr_b16 v[54:55], v47 offset:31752\n"
  "buffer_store_dwordx4 v[204:207], v1, s[36:39], s71 offen offset:3072 sc1\n"
  "v_mfma_f32_16x16x32_bf16 v[200:203], v[68:71], a[24:27], v[200:203]\n"
  "s_nop 0\n"
  "v_mfma_f32_16x16x32_bf16 v[204:207], v[64:67], a[12:15], 0\n"
  "s_waitcnt vmcnt(13)\n"
  "s_barrier\n"
  "ds_read_b64_tr_b16 v[56:57], v44 offset:36864\n"
  "v_mfma_f32_16x16x32_bf16 v[204:207], v[68:71], a[28:31], v[204:207]\n"
  "s_waitcnt lgkmcnt(3)\n"
  "v_mfma_f32_16x16x32_bf16 v[192:195], v[72:75], a[32:35], v[192:195]\n"
  "ds_read_b64_tr_b16 v[58:59], v44 offset:40960\n"
  "v_mfma_f32_16x16x32_bf16 v[192:195], v[76:79], a[48:51], v[192:195]\n"
  "v_mfma_f32_16x16x32_bf16 v[196:199], v[72:75], a[36:39], v[196:199]\n"
  "ds_read_b64_tr_b16 v[60:61], v44 offset:46080\n"
  "v_mfma_f32_16x16x32_bf16 v[196:199], v[76:79], a[52:55], v[196:199]\n"
  "v_mfma_f32_16x16x32_bf16 v[200:203], v[72:75], a[40:43], v[200:203]\n"
  "ds_read_b64_tr_b16 v[62:63], v44 offset:50176\n"
  "v_mfma_f32_16x16x32_bf16 v[200:203], v[76:79], a[56:59], v[200:203]\n"
  "v_mfma_f32_16x16x32_bf16 v[204:207], v[72:75], a[44:47], v[204:207]\n"
  "s_add_u32 s83, s68, s72\n"
  "ds_read_b64_tr_b16 v[64:65], v44 offset:55296\n"
  "v_mfma_f32_16x16x32_bf16 v[204:207], v[76:79], a[60:63], v[204:207]\n"
  "v_mfma_f32_16x16x32_bf16 v[192:195], v[48:51], a[64:67], v[192:195]\n"
  "s_waitcnt lgkmcnt(3)\n"
  "ds_read_b64_tr_b16 v[66:67], v44 offset:59392\n"
  "v_add_u32 v44, 36864, v44\n"
  "v_mfma_f32_16x16x32_bf16 v[192:195], v[52:55], a[80:83], v[192:195]\n"
  "v_mfma_f32_16x16x32_bf16 v[196:199], v[48:51], a[68:71], v[196:199]\n"
  "s_add_u32 m0, s67, 110592\n"
  "ds_read_b64_tr_b16 v[68:69], v44 offset:27648\n"
  "v_mfma_f32_16x16x32_bf16 v[196:199], v[52:55], a[84:87], v[196:199]\n"
  "s_add_u32 s66, s65, 7340032\n"
  "buffer_store_dwordx2 v[208:209], v1, s[36:39], s83 offen offset:0 sc1\n"
  "v_mfma_f32_16x16x32_bf16 v[200:203], v[48:51], a[72:75], v[200:203]\n"
  "ds_read_b64_tr_b16 v[70:71], v44 offset:31744\n"
  "v_mfma_f32_16x16x32_bf16 v[200:203], v[52:55], a[88:91], v[200:203]\n"
  "v_mfma_f32_16x16x32_bf16 v[204:207], v[48:51], a[76:79], v[204:207]\n"
  "ds_read_b64_tr_b16 v[72:73], v44 offset:8\n"
  "v_mfma_f32_16x16x32_bf16 v[204:207], v[52:55], a[92:95], v[204:207]\n"
  "s_barrier\n"
  "v_mfma_f32_16x16x32_bf16 v[80:83], v[56:59], a[96:99], v[80:83]\n"
  "s_waitcnt lgkmcnt(3)\n"
  "ds_read_b64_tr_b16 v[74:75], v44 offset:4104\n"
  "v_mfma_f32_16x16x32_bf16 v[80:83], v[60:63], a[112:115], v[80:83]\n"
  "buffer_load_dwordx4 v11, s[44:47], s66 offen offset:0 lds\n"
  "v_mfma_f32_16x16x32_bf16 v[80:83], v[64:67], a[128:131], v[80:83]\n"
  "s_waitcnt lgkmcnt(2)\n"
  "ds_read_b64_tr_b16 v[76:77], v44 offset:9224\n"
  "v_mfma_f32_16x16x32_bf16 v[80:83], v[68:71], a[144:147], v[80:83]\n"
  "v_mfma_f32_16x16x32_bf16 v[84:87], v[56:59], a[100:103], v[84:87]\n"
  "ds_read_b64_tr_b16 v[78:79], v44 offset:13320\n"
  "v_mfma_f32_16x16x32_bf16 v[84:87], v[60:63], a[116:119], v[84:87]\n"
  "s_add_u32 s83, s69, s72\n"
  "buffer_load_dwordx4 v12, s[44:47], s66 offen offset:1024 lds\n"
  "v_mfma_f32_16x16x32_bf16 v[84:87], v[64:67], a[132:135], v[84:87]\n"
  "ds_read_b64_tr_b16 v[48:49], v44 offset:18440\n"
  "v_mfma_f32_16x16x32_bf16 v[84:87], v[68:71], a[148:151], v[84:87]\n"
  "v_mfma_f32_16x16x32_bf16 v[88:91], v[56:59], a[104:107], v[88:91]\n"
  "ds_read_b64_tr_b16 v[50:51], v44 offset:22536\n"
  "v_mfma_f32_16x16x32_bf16 v[88:91], v[60:63], a[120:123], v[88:91]\n"
  "buffer_store_dwordx2 v[212:213], v1, s[36:39], s83 offen offset:0 sc1\n"
  "v_mfma_f32_16x16x32_bf16 v[88:91], v[64:67], a[136:139], v[88:91]\n"
  "ds_read_b64_tr_b16 v[52:53], v44 offset:27656\n"
  "v_mfma_f32_16x16x32_bf16 v[88:91], v[68:71], a[152:155], v[88:91]\n"
  "buffer_load_dwordx4 v13, s[44:47], s66 offen offset:2048 lds\n"
  "v_mfma_f32_16x16x32_bf16 v[92:95], v[56:59], a[108:111], v[92:95]\n"
  "ds_read_b64_tr_b16 v[54:55], v44 offset:31752\n"
  "v_mfma_f32_16x16x32_bf16 v[92:95], v[60:63], a[124:127], v[92:95]\n"
  "v_mfma_f32_16x16x32_bf16 v[92:95], v[64:67], a[140:143], v[92:95]\n"
  "ds_read_b64_tr_b16 v[56:57], v45 offset:36864\n"
  "v_mfma_f32_16x16x32_bf16 v[92:95], v[68:71], a[156:159], v[92:95]\n"
  "s_waitcnt lgkmcnt(3)\n"
  "buffer_load_dwordx4 v14, s[44:47], s66 offen offset:3072 lds\n"
  "v_mfma_f32_16x16x32_bf16 v[96:99], v[72:75], a[96:99], v[96:99]\n"
  "s_add_u32 m0, s67, 114688\n"
  "ds_read_b64_tr_b16 v[58:59], v45 offset:40960\n"
  "v_mfma_f32_16x16x32_bf16 v[96:99], v[76:79], a[112:115], v[96:99]\n"
  "v_mfma_f32_16x16x32_bf16 v[96:99], v[48:51], a[128:131], v[96:99]\n"
  "s_waitcnt lgkmcnt(2)\n"
  "ds_read_b64_tr_b16 v[60:61], v45 offset:46080\n"
  "v_mfma_f32_16x16x32_bf16 v[96:99], v[52:55], a[144:147], v[96:99]\n"
  "s_add_u32 s83, s70, s72\n"
  "v_mfma_f32_16x16x32_bf16 v[100:103], v[72:75], a[100:103], v[100:103]\n"
  "ds_read_b64_tr_b16 v[62:63], v45 offset:50176\n"
  "v_mfma_f32_16x16x32_bf16 v[100:103], v[76:79], a[116:119], v[100:103]\n"
  "buffer_load_dwordx4 v15, s[44:47], s66 offen offset:0 lds\n"
  "v_mfma_f32_16x16x32_bf16 v[100:103], v[48:51], a[132:135], v[100:103]\n"
  "ds_read_b64_tr_b16 v[64:65], v45 offset:55296\n"
  "v_mfma_f32_16x16x32_bf16 v[100:103], v[52:55], a[148:151], v[100:103]\n"
  "buffer_store_dwordx2 v[216:217], v1, s[36:39], s83 offen offset:0 sc1\n"
  "v_mfma_f32_16x16x32_bf16 v[104:107], v[72:75], a[104:107], v[104:107]\n"
  "ds_read_b64_tr_b16 v[66:67], v45 offset:59392\n"
  "v_add_u32 v45, 36864, v45\n"
  "v_mfma_f32_16x16x32_bf16 v[104:107], v[76:79], a[120:123], v[104:107]\n"
  "buffer_load_dwordx4 v16, s[44:47], s66 offen offset:1024 lds\n"
  "v_mfma_f32_16x16x32_bf16 v[104:107], v[48:51], a[136:139], v[104:107]\n"
  "ds_read_b64_tr_b16 v[68:69], v45 offset:27648\n"
  "v_mfma_f32_16x16x32_bf16 v[104:107], v[52:55], a[152:155], v[104:107]\n"
  "v_mfma_f32_16x16x32_bf16 v[108:111], v[72:75], a[108:111], v[108:111]\n"
  "ds_read_b64_tr_b16 v[70:71], v45 offset:31744\n"
  "v_mfma_f32_16x16x32_bf16 v[108:111], v[76:79], a[124:127], v[108:111]\n"
  "v_mfma_f32_16x16x32_bf16 v[108:111], v[48:51], a[140:143], v[108:111]\n"
  "ds_read_b64_tr_b16 v[72:73], v45 offset:8\n"
  "v_mfma_f32_16x16x32_bf16 v[108:111], v[52:55], a[156:159], v[108:111]\n"
  "s_waitcnt lgkmcnt(3)\n"
  "buffer_load_dwordx4 v17, s[44:47], s66 offen offset:2048 lds\n"
  "v_mfma_f32_16x16x32_bf16 v[112:115], v[56:59], a[96:99], v[112:115]\n"
  "ds_read_b64_tr_b16 v[74:75], v45 offset:4104\n"
  "v_mfma_f32_16x16x32_bf16 v[112:115], v[60:63], a[112:115], v[112:115]\n"
  "s_add_u32 s83, s71, s72\n"
  "v_mfma_f32_16x16x32_bf16 v[112:115], v[64:67], a[128:131], v[112:115]\n"
  "s_waitcnt lgkmcnt(2)\n"
  "ds_read_b64_tr_b16 v[76:77], v45 offset:9224\n"
  "v_mfma_f32_16x16x32_bf16 v[112:115], v[68:71], a[144:147], v[112:115]\n"
  "buffer_load_dwordx4 v18, s[44:47], s66 offen offset:3072 lds\n"
  "v_mfma_f32_16x16x32_bf16 v[116:119], v[56:59], a[100:103], v[116:119]\n"
  "s_add_u32 m0, s67, 0\n"
  "ds_read_b64_tr_b16 v[78:79], v45 offset:13320\n"
  "v_mfma_f32_16x16x32_bf16 v[116:119], v[60:63], a[116:119], v[116:119]\n"
  "s_add_u32 s66, s65, 8388608\n"
  "buffer_store_dwordx2 v[220:221], v1, s[36:39], s83 offen offset:0 sc1\n"
  "v_mfma_f32_16x16x32_bf16 v[116:119], v[64:67], a[132:135], v[116:119]\n"
  "ds_read_b64_tr_b16 v[48:49], v45 offset:18440\n"
  "v_mfma_f32_16x16x32_bf16 v[116:119], v[68:71], a[148:151], v[116:119]\n"
  "v_mfma_f32_16x16x32_bf16 v[120:123], v[56:59], a[104:107], v[120:123]\n"
  "ds_read_b64_tr_b16 v[50:51], v45 offset:22536\n"
  "v_mfma_f32_16x16x32_bf16 v[120:123], v[60:63], a[120:123], v[120:123]\n"
  "buffer_load_dwordx4 v11, s[44:47], s66 offen offset:0 lds\n"
  "v_mfma_f32_16x16x32_bf16 v[120:123], v[64:67], a[136:139], v[120:123]\n"
  "ds_read_b64_tr_b16 v[52:53], v45 offset:27656\n"
  "v_mfma_f32_16x16x32_bf16 v[120:123], v[68:71], a[152:155], v[120:123]\n"
  "v_mfma_f32_16x16x32_bf16 v[124:127], v[56:59], a[108:111], v[124:127]\n"
  "ds_read_b64_tr_b16 v[54:55], v45 offset:31752\n"
  "v_mfma_f32_16x16x32_bf16 v[124:127], v[60:63], a[124:127], v[124:127]\n"
  "buffer_load_dwordx4 v12, s[44:47], s66 offen offset:1024 lds\n"
  "v_mfma_f32_16x16x32_bf16 v[124:127], v[64:67], a[140:143], v[124:127]\n"
  "ds_read_b64_tr_b16 v[56:57], v46 offset:36864\n"
  "v_mfma_f32_16x16x32_bf16 v[124:127], v[68:71], a[156:159], v[124:127]\n"
  "s_waitcnt lgkmcnt(4)\n"
  "v_mfma_f32_16x16x32_bf16 v[128:131], v[72:75], a[96:99], v[128:131]\n"
  "ds_read_b64_tr_b16 v[58:59], v46 offset:40960\n"
  "v_mfma_f32_16x16x32_bf16 v[128:131], v[76:79], a[112:115], v[128:131]\n"
  "s_waitcnt lgkmcnt(4)\n"
  "v_mfma_f32_16x16x32_bf16 v[128:131], v[48:51], a[128:131], v[128:131]\n"
  "s_waitcnt lgkmcnt(2)\n"
  "ds_read_b64_tr_b16 v[60:61], v46 offset:46080\n"
  "v_mfma_f32_16x16x32_bf16 v[128:131], v[52:55], a[144:147], v[128:131]\n"
  "buffer_load_dwordx4 v13, s[44:47], s66 offen offset:2048 lds\n"
  "v_mfma_f32_16x16x32_bf16 v[132:135], v[72:75], a[100:103], v[132:135]\n"
  "ds_read_b64_tr_b16 v[62:63], v46 offset:50176\n"
  "v_mfma_f32_16x16x32_bf16 v[132:135], v[76:79], a[116:119], v[132:135]\n"
  "v_mfma_f32_16x16x32_bf16 v[132:135], v[48:51], a[132:135], v[132:135]\n"
  "ds_read_b64_tr_b16 v[64:65], v46 offset:55296\n"
  "v_mfma_f32_16x16x32_bf16 v[132:135], v[52:55], a[148:151], v[132:135]\n"
  "buffer_load_dwordx4 v14, s[44:47], s66 offen offset:3072 lds\n"
  "v_mfma_f32_16x16x32_bf16 v[136:139], v[72:75], a[104:107], v[136:139]\n"
  "s_add_u32 m0, s67, 4096\n"
  "ds_read_b64_tr_b16 v[66:67], v46 offset:59392\n"
  "v_add_u32 v46, 36864, v46\n"
  "v_mfma_f32_16x16x32_bf16 v[136:139], v[76:79], a[120:123], v[136:139]\n"
  "v_mfma_f32_16x16x32_bf16 v[136:139], v[48:51], a[136:139], v[136:139]\n"
  "ds_read_b64_tr_b16 v[68:69], v46 offset:27648\n"
  "v_mfma_f32_16x16x32_bf16 v[136:139], v[52:55], a[152:155], v[136:139]\n"
  "v_mfma_f32_16x16x32_bf16 v[140:143], v[72:75], a[108:111], v[140:143]\n"
  "ds_read_b64_tr_b16 v[70:71], v46 offset:31744\n"
  "v_mfma_f32_16x16x32_bf16 v[140:143], v[76:79], a[124:127], v[140:143]\n"
  "buffer_load_dwordx4 v15, s[44:47], s66 offen offset:0 lds\n"
  "v_mfma_f32_16x16x32_bf16 v[140:143], v[48:51], a[140:143], v[140:143]\n"
  "ds_read_b64_tr_b16 v[72:73], v46 offset:8\n"
  "v_mfma_f32_16x16x32_bf16 v[140:143], v[52:55], a[156:159], v[140:143]\n"
  "s_waitcnt lgkmcnt(4)\n"
  "v_mfma_f32_16x16x32_bf16 v[144:147], v[56:59], a[96:99], v[144:147]\n"
  "ds_read_b64_tr_b16 v[74:75], v46 offset:4104\n"
  "v_mfma_f32_16x16x32_bf16 v[144:147], v[60:63], a[112:115], v[144:147]\n"
  "s_waitcnt lgkmcnt(3)\n"
  "buffer_load_dwordx4 v16, s[44:47], s66 offen offset:1024 lds\n"
  "v_mfma_f32_16x16x32_bf16 v[144:147], v[64:67], a[128:131], v[144:147]\n"
  "s_waitcnt lgkmcnt(2)\n"
  "ds_read_b64_tr_b16 v[76:77], v46 offset:9224\n"
  "v_mfma_f32_16x16x32_bf16 v[144:147], v[68:71], a[144:147], v[144:147]\n"
  "v_mfma_f32_16x16x32_bf16 v[148:151], v[56:59], a[100:103], v[148:151]\n"
  "ds_read_b64_tr_b16 v[78:79], v46 offset:13320\n"
  "v_mfma_f32_16x16x32_bf16 v[148:151], v[60:63], a[116:119], v[148:151]\n"
  "v_mfma_f32_16x16x32_bf16 v[148:151], v[64:67], a[132:135], v[148:151]\n"
  "ds_read_b64_tr_b16 v[48:49], v46 offset:18440\n"
  "v_mfma_f32_16x16x32_bf16 v[148:151], v[68:71], a[148:151], v[148:151]\n"
  "buffer_load_dwordx4 v17, s[44:47], s66 offen offset:2048 lds\n"
  "v_mfma_f32_16x16x32_bf16 v[152:155], v[56:59], a[104:107], v[152:155]\n"
  "ds_read_b64_tr_b16 v[50:51], v46 offset:22536\n"
  "v_mfma_f32_16x16x32_bf16 v[152:155], v[60:63], a[120:123], v[152:155]\n"
  "v_mfma_f32_16x16x32_bf16 v[152:155], v[64:67], a[136:139], v[152:155]\n"
  "ds_read_b64_tr_b16 v[52:53], v46 offset:27656\n"
  "v_mfma_f32_16x16x32_bf16 v[152:155], v[68:71], a[152:155], v[152:155]\n"
  "buffer_load_dwordx4 v18, s[44:47], s66 offen offset:3072 lds\n"
  "v_mfma_f32_16x16x32_bf16 v[156:159], v[56:59], a[108:111], v[156:159]\n"
  "s_add_u32 m0, s78, 147456\n"
  "ds_read_b64_tr_b16 v[54:55], v46 offset:31752\n"
  "v_mfma_f32_16x16x32_bf16 v[156:159], v[60:63], a[124:127], v[156:159]\n"
  "s_add_u32 s79, s77, 0\n"
  "v_mfma_f32_16x16x32_bf16 v[156:159], v[64:67], a[140:143], v[156:159]\n"
  "ds_read_b64_tr_b16 v[56:57], v47 offset:36864\n"
  "v_mfma_f32_16x16x32_bf16 v[156:159], v[68:71], a[156:159], v[156:159]\n"
  "s_waitcnt lgkmcnt(3)\n"
  "v_mfma_f32_16x16x32_bf16 v[160:163], v[72:75], a[96:99], v[160:163]\n"
  "ds_read_b64_tr_b16 v[58:59], v47 offset:40960\n"
  "v_mfma_f32_16x16x32_bf16 v[160:163], v[76:79], a[112:115], v[160:163]\n"
  "buffer_load_dwordx4 v22, s[48:51], s79 offen offset:0 lds\n"
  "v_mfma_f32_16x16x32_bf16 v[160:163], v[48:51], a[128:131], v[160:163]\n"
  "s_waitcnt lgkmcnt(2)\n"
  "ds_read_b64_tr_b16 v[60:61], v47 offset:46080\n"
  "v_mfma_f32_16x16x32_bf16 v[160:163], v[52:55], a[144:147], v[160:163]\n"
  "v_mfma_f32_16x16x32_bf16 v[164:167], v[72:75], a[100:103], v[164:167]\n"
  "ds_read_b64_tr_b16 v[62:63], v47 offset:50176\n"
  "v_mfma_f32_16x16x32_bf16 v[164:167], v[76:79], a[116:119], v[164:167]\n"
  "buffer_load_dwordx4 v23, s[48:51], s79 offen offset:1024 lds\n"
  "v_mfma_f32_16x16x32_bf16 v[164:167], v[48:51], a[132:135], v[164:167]\n"
  "ds_read_b64_tr_b16 v[64:65], v47 offset:55296\n"
  "v_mfma_f32_16x16x32_bf16 v[164:167], v[52:55], a[148:151], v[164:167]\n"
  "v_mfma_f32_16x16x32_bf16 v[168:171], v[72:75], a[104:107], v[168:171]\n"
  "ds_read_b64_tr_b16 v[66:67], v47 offset:59392\n"
  "v_add_u32 v47, 36864, v47\n"
  "v_mfma_f32_16x16x32_bf16 v[168:171], v[76:79], a[120:123], v[168:171]\n"
  "v_mfma_f32_16x16x32_bf16 v[168:171], v[48:51], a[136:139], v[168:171]\n"
  "ds_read_b64_tr_b16 v[68:69], v47 offset:27648\n"
  "v_mfma_f32_16x16x32_bf16 v[168:171], v[52:55], a[152:155], v[168:171]\n"
  "buffer_load_dwordx4 v24, s[48:51], s79 offen offset:2048 lds\n"
  "v_mfma_f32_16x16x32_bf16 v[172:175], v[72:75], a[108:111], v[172:175]\n"
  "ds_read_b64_tr_b16 v[70:71], v47 offset:31744\n"
  "v_mfma_f32_16x16x32_bf16 v[172:175], v[76:79], a[124:127], v[172:175]\n"
  "v_mfma_f32_16x16x32_bf16 v[172:175], v[48:51], a[140:143], v[172:175]\n"
  "ds_read_b64_tr_b16 v[72:73], v47 offset:8\n"
  "v_mfma_f32_16x16x32_bf16 v[172:175], v[52:55], a[156:159], v[172:175]\n"
  "s_waitcnt lgkmcnt(3)\n"
  "buffer_load_dwordx4 v25, s[48:51], s79 offen offset:3072 lds\n"
  "v_mfma_f32_16x16x32_bf16 v[176:179], v[56:59], a[96:99], v[176:179]\n"
  "s_add_u32 m0, s78, 49152\n"
  "ds_read_b64_tr_b16 v[74:75], v47 offset:4104\n"
  "v_mfma_f32_16x16x32_bf16 v[176:179], v[60:63], a[112:115], v[176:179]\n"
  "s_add_u32 s79, s77, 0\n"
  "v_mfma_f32_16x16x32_bf16 v[176:179], v[64:67], a[128:131], v[176:179]\n"
  "s_waitcnt lgkmcnt(2)\n"
  "ds_read_b64_tr_b16 v[76:77], v47 offset:9224\n"
  "v_mfma_f32_16x16x32_bf16 v[176:179], v[68:71], a[144:147], v[176:179]\n"
  "v_mfma_f32_16x16x32_bf16 v[180:183], v[56:59], a[100:103], v[180:183]\n"
  "ds_read_b64_tr_b16 v[78:79], v47 offset:13320\n"
  "v_mfma_f32_16x16x32_bf16 v[180:183], v[60:63], a[116:119], v[180:183]\n"
  "buffer_load_dwordx4 v26, s[52:55], s79 offen offset:0 lds\n"
  "v_mfma_f32_16x16x32_bf16 v[180:183], v[64:67], a[132:135], v[180:183]\n"
  "ds_read_b64_tr_b16 v[48:49], v47 offset:18440\n"
  "v_mfma_f32_16x16x32_bf16 v[180:183], v[68:71], a[148:151], v[180:183]\n"
  "v_mfma_f32_16x16x32_bf16 v[184:187], v[56:59], a[104:107], v[184:187]\n"
  "ds_read_b64_tr_b16 v[50:51], v47 offset:22536\n"
  "v_mfma_f32_16x16x32_bf16 v[184:187], v[60:63], a[120:123], v[184:187]\n"
  "buffer_load_dwordx4 v27, s[52:55], s79 offen offset:1024 lds\n"
  "v_mfma_f32_16x16x32_bf16 v[184:187], v[64:67], a[136:139], v[184:187]\n"
  "ds_read_b64_tr_b16 v[52:53], v47 offset:27656\n"
  "v_mfma_f32_16x16x32_bf16 v[184:187], v[68:71], a[152:155], v[184:187]\n"
  "v_mfma_f32_16x16x32_bf16 v[188:191], v[56:59], a[108:111], v[188:191]\n"
  "ds_read_b64_tr_b16 v[54:55], v47 offset:31752\n"
  "v_mfma_f32_16x16x32_bf16 v[188:191], v[60:63], a[124:127], v[188:191]\n"
  "v_mfma_f32_16x16x32_bf16 v[188:191], v[64:67], a[140:143], v[188:191]\n"
  "s_waitcnt vmcnt(15)\n"
  "s_barrier\n"
  "ds_read_b64_tr_b16 v[56:57], v44 offset:36864\n"
  "v_mfma_f32_16x16x32_bf16 v[188:191], v[68:71], a[156:159], v[188:191]\n"
  "s_waitcnt lgkmcnt(3)\n"
  "buffer_load_dwordx4 v26, s[52:55], s79 offen offset:2048 lds\n"
  "v_mfma_f32_16x16x32_bf16 v[192:195], v[72:75], a[96:99], v[192:195]\n"
  "ds_read_b64_tr_b16 v[58:59], v44 offset:40960\n"
  "v_mfma_f32_16x16x32_bf16 v[192:195], v[76:79], a[112:115], v[192:195]\n"
  "v_mfma_f32_16x16x32_bf16 v[192:195], v[48:51], a[128:131], v[192:195]\n"
  "s_waitcnt lgkmcnt(2)\n"
  "ds_read_b64_tr_b16 v[60:61], v44 offset:46080\n"
  "v_mfma_f32_16x16x32_bf16 v[192:195], v[52:55], a[144:147], v[192:195]\n"
  "buffer_load_dwordx4 v27, s[52:55], s79 offen offset:3072 lds\n"
  "v_mfma_f32_16x16x32_bf16 v[196:199], v[72:75], a[100:103], v[196:199]\n"
  "ds_read_b64_tr_b16 v[62:63], v44 offset:50176\n"
  "v_mfma_f32_16x16x32_bf16 v[196:199], v[76:79], a[116:119], v[196:199]\n"
  "v_mfma_f32_16x16x32_bf16 v[196:199], v[48:51], a[132:135], v[196:199]\n"
  "ds_read_b64_tr_b16 v[64:65], v44 offset:55296\n"
  "v_mfma_f32_16x16x32_bf16 v[196:199], v[52:55], a[148:151], v[196:199]\n"
  "v_mfma_f32_16x16x32_bf16 v[200:203], v[72:75], a[104:107], v[200:203]\n"
  "ds_read_b64_tr_b16 v[66:67], v44 offset:59392\n"
  "v_add_u32 v44, 36864, v44\n"
  "v_mfma_f32_16x16x32_bf16 v[200:203], v[76:79], a[120:123], v[200:203]\n"
  "v_mfma_f32_16x16x32_bf16 v[200:203], v[48:51], a[136:139], v[200:203]\n"
  "s_add_u32 m0, s78, 65536\n"
  "ds_read_b64_tr_b16 v[68:69], v44 offset:27648\n"
  "v_mfma_f32_16x16x32_bf16 v[200:203], v[52:55], a[152:155], v[200:203]\n"
  "s_add_u32 s79, s77, 16384\n"
  "v_mfma_f32_16x16x32_bf16 v[204:207], v[72:75], a[108:111], v[204:207]\n"
  "ds_read_b64_tr_b16 v[70:71], v44 offset:31744\n"
  "v_mfma_f32_16x16x32_bf16 v[204:207], v[76:79], a[124:127], v[204:207]\n"
  "v_mfma_f32_16x16x32_bf16 v[204:207], v[48:51], a[140:143], v[204:207]\n"
  "ds_read_b64_tr_b16 v[72:73], v44 offset:8\n"
  "v_mfma_f32_16x16x32_bf16 v[204:207], v[52:55], a[156:159], v[204:207]\n"
  "s_barrier\n"
  "s_waitcnt lgkmcnt(3)\n"
  "v_mfma_f32_16x16x32_bf16 v[80:83], v[56:59], a[160:163], v[80:83]\n"
  "ds_read_b64_tr_b16 v[74:75], v44 offset:4104\n"
  "v_mfma_f32_16x16x32_bf16 v[80:83], v[60:63], a[176:179], v[80:83]\n"
  "buffer_load_dwordx4 v22, s[48:51], s79 offen offset:0 lds\n"
  "v_mfma_f32_16x16x32_bf16 v[80:83], v[64:67], a[192:195], v[80:83]\n"
  "s_waitcnt lgkmcnt(2)\n"
  "ds_read_b64_tr_b16 v[76:77], v44 offset:9224\n"
  "v_mfma_f32_16x16x32_bf16 v[80:83], v[68:71], a[208:211], v[80:83]\n"
  "v_mfma_f32_16x16x32_bf16 v[84:87], v[56:59], a[164:167], v[84:87]\n"
  "ds_read_b64_tr_b16 v[78:79], v44 offset:13320\n"
  "v_mfma_f32_16x16x32_bf16 v[84:87], v[60:63], a[180:183], v[84:87]\n"
  "buffer_load_dwordx4 v23, s[48:51], s79 offen offset:1024 lds\n"
  "v_mfma_f32_16x16x32_bf16 v[84:87], v[64:67], a[196:199], v[84:87]\n"
  "ds_read_b64_tr_b16 v[48:49], v44 offset:18440\n"
  "v_mfma_f32_16x16x32_bf16 v[84:87], v[68:71], a[212:215], v[84:87]\n"
  "v_mfma_f32_16x16x32_bf16 v[88:91], v[56:59], a[168:171], v[88:91]\n"
  "ds_read_b64_tr_b16 v[50:51], v44 offset:22536\n"
  "v_mfma_f32_16x16x32_bf16 v[88:91], v[60:63], a[184:187], v[88:91]\n"
  "v_mfma_f32_16x16x32_bf16 v[88:91], v[64:67], a[200:203], v[88:91]\n"
  "ds_read_b64_tr_b16 v[52:53], v44 offset:27656\n"
  "v_mfma_f32_16x16x32_bf16 v[88:91], v[68:71], a[216:219], v[88:91]\n"
  "buffer_load_dwordx4 v24, s[48:51], s79 offen offset:2048 lds\n"
  "v_mfma_f32_16x16x32_bf16 v[92:95], v[56:59], a[172:175], v[92:95]\n"
  "ds_read_b64_tr_b16 v[54:55], v44 offset:31752\n"
  "v_mfma_f32_16x16x32_bf16 v[92:95], v[60:63], a[188:191], v[92:95]\n"
  "v_mfma_f32_16x16x32_bf16 v[92:95], v[64:67], a[204:207], v[92:95]\n"
  "ds_read_b64_tr_b16 v[56:57], v45 offset:36864\n"
  "v_mfma_f32_16x16x32_bf16 v[92:95], v[68:71], a[220:223], v[92:95]\n"
  "s_waitcnt lgkmcnt(4)\n"
  "buffer_load_dwordx4 v25, s[48:51], s79 offen offset:3072 lds\n"
  "v_mfma_f32_16x16x32_bf16 v[96:99], v[72:75], a[160:163], v[96:99]\n"
  "s_add_u32 m0, s78, 81920\n"
  "ds_read_b64_tr_b16 v[58:59], v45 offset:40960\n"
  "v_mfma_f32_16x16x32_bf16 v[96:99], v[76:79], a[176:179], v[96:99]\n"
  "s_add_u32 s79, s77, 16384\n"
  "s_waitcnt lgkmcnt(3)\n"
  "v_mfma_f32_16x16x32_bf16 v[96:99], v[48:51], a[192:195], v[96:99]\n"
  "s_waitcnt lgkmcnt(2)\n"
  "ds_read_b64_tr_b16 v[60:61], v45 offset:46080\n"
  "v_mfma_f32_16x16x32_bf16 v[96:99], v[52:55], a[208:211], v[96:99]\n"
  "v_mfma_f32_16x16x32_bf16 v[100:103], v[72:75], a[164:167], v[100:103]\n"
  "ds_read_b64_tr_b16 v[62:63], v45 offset:50176\n"
  "v_mfma_f32_16x16x32_bf16 v[100:103], v[76:79], a[180:183], v[100:103]\n"
  "buffer_load_dwordx4 v26, s[52:55], s79 offen offset:0 lds\n"
  "v_mfma_f32_16x16x32_bf16 v[100:103], v[48:51], a[196:199], v[100:103]\n"
  "ds_read_b64_tr_b16 v[64:65], v45 offset:55296\n"
  "v_mfma_f32_16x16x32_bf16 v[100:103], v[52:55], a[212:215], v[100:103]\n"
  "v_mfma_f32_16x16x32_bf16 v[104:107], v[72:75], a[168:171], v[104:107]\n"
  "ds_read_b64_tr_b16 v[66:67], v45 offset:59392\n"
  "v_add_u32 v45, 36864, v45\n"
  "v_mfma_f32_16x16x32_bf16 v[104:107], v[76:79], a[184:187], v[104:107]\n"
  "buffer_load_dwordx4 v27, s[52:55], s79 offen offset:1024 lds\n"
  "v_mfma_f32_16x16x32_bf16 v[104:107], v[48:51], a[200:203], v[104:107]\n"
  "ds_read_b64_tr_b16 v[68:69], v45 offset:27648\n"
  "v_mfma_f32_16x16x32_bf16 v[104:107], v[52:55], a[216:219], v[104:107]\n"
  "v_mfma_f32_16x16x32_bf16 v[108:111], v[72:75], a[172:175], v[108:111]\n"
  "ds_read_b64_tr_b16 v[70:71], v45 offset:31744\n"
  "v_mfma_f32_16x16x32_bf16 v[108:111], v[76:79], a[188:191], v[108:111]\n"
  "v_mfma_f32_16x16x32_bf16 v[108:111], v[48:51], a[204:207], v[108:111]\n"
  "ds_read_b64_tr_b16 v[72:73], v45 offset:8\n"
  "v_mfma_f32_16x16x32_bf16 v[108:111], v[52:55], a[220:223], v[108:111]\n"
  "s_waitcnt lgkmcnt(3)\n"
  "buffer_load_dwordx4 v26, s[52:55], s79 offen offset:2048 lds\n"
  "v_mfma_f32_16x16x32_bf16 v[112:115], v[56:59], a[160:163], v[112:115]\n"
  "ds_read_b64_tr_b16 v[74:75], v45 offset:4104\n"
  "v_mfma_f32_16x16x32_bf16 v[112:115], v[60:63], a[176:179], v[112:115]\n"
  "v_mfma_f32_16x16x32_bf16 v[112:115], v[64:67], a[192:195], v[112:115]\n"
  "s_waitcnt lgkmcnt(2)\n"
  "ds_read_b64_tr_b16 v[76:77], v45 offset:9224\n"
  "v_mfma_f32_16x16x32_bf16 v[112:115], v[68:71], a[208:211], v[112:115]\n"
  "buffer_load_dwordx4 v27, s[52:55], s79 offen offset:3072 lds\n"
  "v_mfma_f32_16x16x32_bf16 v[116:119], v[56:59], a[164:167], v[116:119]\n"
  "ds_read_b64_tr_b16 v[78:79], v45 offset:13320\n"
  "v_mfma_f32_16x16x32_bf16 v[116:119], v[60:63], a[180:183], v[116:119]\n"
  "v_mfma_f32_16x16x32_bf16 v[116:119], v[64:67], a[196:199], v[116:119]\n"
  "ds_read_b64_tr_b16 v[48:49], v45 offset:18440\n"
  "v_mfma_f32_16x16x32_bf16 v[116:119], v[68:71], a[212:215], v[116:119]\n"
  "v_mfma_f32_16x16x32_bf16 v[120:123], v[56:59], a[168:171], v[120:123]\n"
  "ds_read_b64_tr_b16 v[50:51], v45 offset:22536\n"
  "v_mfma_f32_16x16x32_bf16 v[120:123], v[60:63], a[184:187], v[120:123]\n"
  "v_mfma_f32_16x16x32_bf16 v[120:123], v[64:67], a[200:203], v[120:123]\n"
  "ds_read_b64_tr_b16 v[52:53], v45 offset:27656\n"
  "v_mfma_f32_16x16x32_bf16 v[120:123], v[68:71], a[216:219], v[120:123]\n"
  "v_mfma_f32_16x16x32_bf16 v[124:127], v[56:59], a[172:175], v[124:127]\n"
  "ds_read_b64_tr_b16 v[54:55], v45 offset:31752\n"
  "v_mfma_f32_16x16x32_bf16 v[124:127], v[60:63], a[188:191], v[124:127]\n"
  "v_mfma_f32_16x16x32_bf16 v[124:127], v[64:67], a[204:207], v[124:127]\n"
  "ds_read_b64_tr_b16 v[56:57], v46 offset:36864\n"
  "v_mfma_f32_16x16x32_bf16 v[124:127], v[68:71], a[220:223], v[124:127]\n"
  "s_waitcnt lgkmcnt(4)\n"
  "v_mfma_f32_16x16x32_bf16 v[128:131], v[72:75], a[160:163], v[128:131]\n"
  "ds_read_b64_tr_b16 v[58:59], v46 offset:40960\n"
  "v_mfma_f32_16x16x32_bf16 v[128:131], v[76:79], a[176:179], v[128:131]\n"
  "s_waitcnt lgkmcnt(4)\n"
  "v_mfma_f32_16x16x32_bf16 v[128:131], v[48:51], a[192:195], v[128:131]\n"
  "s_waitcnt lgkmcnt(2)\n"
  "ds_read_b64_tr_b16 v[60:61], v46 offset:46080\n"
  "v_mfma_f32_16x16x32_bf16 v[128:131], v[52:55], a[208:211], v[128:131]\n"
  "v_mfma_f32_16x16x32_bf16 v[132:135], v[72:75], a[164:167], v[132:135]\n"
  "ds_read_b64_tr_b16 v[62:63], v46 offset:50176\n"
  "v_mfma_f32_16x16x32_bf16 v[132:135], v[76:79], a[180:183], v[132:135]\n"
  "v_mfma_f32_16x16x32_bf16 v[132:135], v[48:51], a[196:199], v[132:135]\n"
  "ds_read_b64_tr_b16 v[64:65], v46 offset:55296\n"
  "v_mfma_f32_16x16x32_bf16 v[132:135], v[52:55], a[212:215], v[132:135]\n"
  "v_mfma_f32_16x16x32_bf16 v[136:139], v[72:75], a[168:171], v[136:139]\n"
  "ds_read_b64_tr_b16 v[66:67], v46 offset:59392\n"
  "v_add_u32 v46, 36864, v46\n"
  "v_mfma_f32_16x16x32_bf16 v[136:139], v[76:79], a[184:187], v[136:139]\n"
  "v_mfma_f32_16x16x32_bf16 v[136:139], v[48:51], a[200:203], v[136:139]\n"
  "ds_read_b64_tr_b16 v[68:69], v46 offset:27648\n"
  "v_mfma_f32_16x16x32_bf16 v[136:139], v[52:55], a[216:219], v[136:139]\n"
  "v_mfma_f32_16x16x32_bf16 v[140:143], v[72:75], a[172:175], v[140:143]\n"
  "ds_read_b64_tr_b16 v[70:71], v46 offset:31744\n"
  "v_mfma_f32_16x16x32_bf16 v[140:143], v[76:79], a[188:191], v[140:143]\n"
  "v_mfma_f32_16x16x32_bf16 v[140:143], v[48:51], a[204:207], v[140:143]\n"
  "ds_read_b64_tr_b16 v[72:73], v46 offset:8\n"
  "v_mfma_f32_16x16x32_bf16 v[140:143], v[52:55], a[220:223], v[140:143]\n"
  "s_waitcnt lgkmcnt(4)\n"
  "v_mfma_f32_16x16x32_bf16 v[144:147], v[56:59], a[160:163], v[144:147]\n"
  "ds_read_b64_tr_b16 v[74:75], v46 offset:4104\n"
  "v_mfma_f32_16x16x32_bf16 v[144:147], v[60:63], a[176:179], v[144:147]\n"
  "s_waitcnt lgkmcnt(4)\n"
  "v_mfma_f32_16x16x32_bf16 v[144:147], v[64:67], a[192:195], v[144:147]\n"
  "s_waitcnt lgkmcnt(2)\n"
  "ds_read_b64_tr_b16 v[76:77], v46 offset:9224\n"
  "v_mfma_f32_16x16x32_bf16 v[144:147], v[68:71], a[208:211], v[144:147]\n"
  "v_mfma_f32_16x16x32_bf16 v[148:151], v[56:59], a[164:167], v[148:151]\n"
  "ds_read_b64_tr_b16 v[78:79], v46 offset:13320\n"
  "v_mfma_f32_16x16x32_bf16 v[148:151], v[60:63], a[180:183], v[148:151]\n"
  "v_mfma_f32_16x16x32_bf16 v[148:151], v[64:67], a[196:199], v[148:151]\n"
  "ds_read_b64_tr_b16 v[48:49], v46 offset:18440\n"
  "v_mfma_f32_16x16x32_bf16 v[148:151], v[68:71], a[212:215], v[148:151]\n"
  "v_mfma_f32_16x16x32_bf16 v[152:155], v[56:59], a[168:171], v[152:155]\n"
  "ds_read_b64_tr_b16 v[50:51], v46 offset:22536\n"
  "v_mfma_f32_16x16x32_bf16 v[152:155], v[60:63], a[184:187], v[152:155]\n"
  "v_mfma_f32_16x16x32_bf16 v[152:155], v[64:67], a[200:203], v[152:155]\n"
  "ds_read_b64_tr_b16 v[52:53], v46 offset:27656\n"
  "v_mfma_f32_16x16x32_bf16 v[152:155], v[68:71], a[216:219], v[152:155]\n"
  "v_mfma_f32_16x16x32_bf16 v[156:159], v[56:59], a[172:175], v[156:159]\n"
  "ds_read_b64_tr_b16 v[54:55], v46 offset:31752\n"
  "v_mfma_f32_16x16x32_bf16 v[156:159], v[60:63], a[188:191], v[156:159]\n"
  "v_mfma_f32_16x16x32_bf16 v[156:159], v[64:67], a[204:207], v[156:159]\n"
  "ds_read_b64_tr_b16 v[56:57], v47 offset:36864\n"
  "v_mfma_f32_16x16x32_bf16 v[156:159], v[68:71], a[220:223], v[156:159]\n"
  "s_waitcnt lgkmcnt(4)\n"
  "v_mfma_f32_16x16x32_bf16 v[160:163], v[72:75], a[160:163], v[160:163]\n"
  "ds_read_b64_tr_b16 v[58:59], v47 offset:40960\n"
  "v_mfma_f32_16x16x32_bf16 v[160:163], v[76:79], a[176:179], v[160:163]\n"
  "s_waitcnt lgkmcnt(4)\n"
  "v_mfma_f32_16x16x32_bf16 v[160:163], v[48:51], a[192:195], v[160:163]\n"
  "s_waitcnt lgkmcnt(2)\n"
  "ds_read_b64_tr_b16 v[60:61], v47 offset:46080\n"
  "v_mfma_f32_16x16x32_bf16 v[160:163], v[52:55], a[208:211], v[160:163]\n"
  "v_mfma_f32_16x16x32_bf16 v[164:167], v[72:75], a[164:167], v[164:167]\n"
  "ds_read_b64_tr_b16 v[62:63], v47 offset:50176\n"
  "v_mfma_f32_16x16x32_bf16 v[164:167], v[76:79], a[180:183], v[164:167]\n"
  "v_mfma_f32_16x16x32_bf16 v[164:167], v[48:51], a[196:199], v[164:167]\n"
  "ds_read_b64_tr_b16 v[64:65], v47 offset:55296\n"
  "v_mfma_f32_16x16x32_bf16 v[164:167], v[52:55], a[212:215], v[164:167]\n"
  "v_mfma_f32_16x16x32_bf16 v[168:171], v[72:75], a[168:171], v[168:171]\n"
  "ds_read_b64_tr_b16 v[66:67], v47 offset:59392\n"
  "v_add_u32 v47, 36864, v47\n"
  "v_mfma_f32_16x16x32_bf16 v[168:171], v[76:79], a[184:187], v[168:171]\n"
  "v_mfma_f32_16x16x32_bf16 v[168:171], v[48:51], a[200:203], v[168:171]\n"
  "ds_read_b64_tr_b16 v[68:69], v47 offset:27648\n"
  "v_mfma_f32_16x16x32_bf16 v[168:171], v[52:55], a[216:219], v[168:171]\n"
  "v_mfma_f32_16x16x32_bf16 v[172:175], v[72:75], a[172:175], v[172:175]\n"
  "ds_read_b64_tr_b16 v[70:71], v47 offset:31744\n"
  "v_mfma_f32_16x16x32_bf16 v[172:175], v[76:79], a[188:191], v[172:175]\n"
  "v_mfma_f32_16x16x32_bf16 v[172:175], v[48:51], a[204:207], v[172:175]\n"
  "ds_read_b64_tr_b16 v[72:73], v47 offset:8\n"
  "v_mfma_f32_16x16x32_bf16 v[172:175], v[52:55], a[220:223], v[172:175]\n"
  "s_waitcnt lgkmcnt(4)\n"
  "v_mfma_f32_16x16x32_bf16 v[176:179], v[56:59], a[160:163], v[176:179]\n"
  "ds_read_b64_tr_b16 v[74:75], v47 offset:4104\n"
  "v_mfma_f32_16x16x32_bf16 v[176:179], v[60:63], a[176:179], v[176:179]\n"
  "s_waitcnt lgkmcnt(4)\n"
  "v_mfma_f32_16x16x32_bf16 v[176:179], v[64:67], a[192:195], v[176:179]\n"
  "s_waitcnt lgkmcnt(2)\n"
  "ds_read_b64_tr_b16 v[76:77], v47 offset:9224\n"
  "v_mfma_f32_16x16x32_bf16 v[176:179], v[68:71], a[208:211], v[176:179]\n"
  "v_mfma_f32_16x16x32_bf16 v[180:183], v[56:59], a[164:167], v[180:183]\n"
  "ds_read_b64_tr_b16 v[78:79], v47 offset:13320\n"
  "v_mfma_f32_16x16x32_bf16 v[180:183], v[60:63], a[180:183], v[180:183]\n"
  "v_mfma_f32_16x16x32_bf16 v[180:183], v[64:67], a[196:199], v[180:183]\n"
  "ds_read_b64_tr_b16 v[48:49], v47 offset:18440\n"
  "v_mfma_f32_16x16x32_bf16 v[180:183], v[68:71], a[212:215], v[180:183]\n"
  "v_mfma_f32_16x16x32_bf16 v[184:187], v[56:59], a[168:171], v[184:187]\n"
  "ds_read_b64_tr_b16 v[50:51], v47 offset:22536\n"
  "v_mfma_f32_16x16x32_bf16 v[184:187], v[60:63], a[184:187], v[184:187]\n"
  "v_mfma_f32_16x16x32_bf16 v[184:187], v[64:67], a[200:203], v[184:187]\n"
  "ds_read_b64_tr_b16 v[52:53], v47 offset:27656\n"
  "v_mfma_f32_16x16x32_bf16 v[184:187], v[68:71], a[216:219], v[184:187]\n"
  "v_mfma_f32_16x16x32_bf16 v[188:191], v[56:59], a[172:175], v[188:191]\n"
  "ds_read_b64_tr_b16 v[54:55], v47 offset:31752\n"
  "v_add_u32 v44, 0xfffe5000, v44\n"
  "v_mfma_f32_16x16x32_bf16 v[188:191], v[60:63], a[188:191], v[188:191]\n"
  "v_mfma_f32_16x16x32_bf16 v[188:191], v[64:67], a[204:207], v[188:191]\n"
  "s_waitcnt vmcnt(12)\n"
  "s_barrier\n"
  "ds_read_b64_tr_b16 v[56:57], v44 offset:0\n"
  "v_mfma_f32_16x16x32_bf16 v[188:191], v[68:71], a[220:223], v[188:191]\n"
  "s_waitcnt lgkmcnt(3)\n"
  "v_mfma_f32_16x16x32_bf16 v[192:195], v[72:75], a[160:163], v[192:195]\n"
  "ds_read_b64_tr_b16 v[58:59], v44 offset:4096\n"
  "v_mfma_f32_16x16x32_bf16 v[192:195], v[76:79], a[176:179], v[192:195]\n"
  "v_mfma_f32_16x16x32_bf16 v[192:195], v[48:51], a[192:195], v[192:195]\n"
  "s_waitcnt lgkmcnt(2)\n"
  "ds_read_b64_tr_b16 v[60:61], v44 offset:9216\n"
  "v_mfma_f32_16x16x32_bf16 v[192:195], v[52:55], a[208:211], v[192:195]\n"
  "v_mfma_f32_16x16x32_bf16 v[196:199], v[72:75], a[164:167], v[196:199]\n"
  "ds_read_b64_tr_b16 v[62:63], v44 offset:13312\n"
  "v_mfma_f32_16x16x32_bf16 v[196:199], v[76:79], a[180:183], v[196:199]\n"
  "v_mfma_f32_16x16x32_bf16 v[196:199], v[48:51], a[196:199], v[196:199]\n"
  "ds_read_b64_tr_b16 v[64:65], v44 offset:18432\n"
  "v_mfma_f32_16x16x32_bf16 v[196:199], v[52:55], a[212:215], v[196:199]\n"
  "v_mfma_f32_16x16x32_bf16 v[200:203], v[72:75], a[168:171], v[200:203]\n"
  "ds_read_b64_tr_b16 v[66:67], v44 offset:22528\n"
  "v_mfma_f32_16x16x32_bf16 v[200:203], v[76:79], a[184:187], v[200:203]\n"
  "v_mfma_f32_16x16x32_bf16 v[200:203], v[48:51], a[200:203], v[200:203]\n"
  "ds_read_b64_tr_b16 v[68:69], v44 offset:27648\n"
  "v_mfma_f32_16x16x32_bf16 v[200:203], v[52:55], a[216:219], v[200:203]\n"
  "v_mfma_f32_16x16x32_bf16 v[204:207], v[72:75], a[172:175], v[204:207]\n"
  "ds_read_b64_tr_b16 v[70:71], v44 offset:31744\n"
  "v_mfma_f32_16x16x32_bf16 v[204:207], v[76:79], a[188:191], v[204:207]\n"
  "v_mfma_f32_16x16x32_bf16 v[204:207], v[48:51], a[204:207], v[204:207]\n"
  "ds_read_b64_tr_b16 v[72:73], v44 offset:8\n"
  "v_mfma_f32_16x16x32_bf16 v[204:207], v[52:55], a[220:223], v[204:207]\n"
  "s_barrier\n"
  "s_waitcnt lgkmcnt(4)\n"
  "v_mfma_f32_16x16x32_bf16 v[80:83], v[56:59], a[224:227], v[80:83]\n"
  "ds_read_b64_tr_b16 v[74:75], v44 offset:4104\n"
  "v_mfma_f32_16x16x32_bf16 v[80:83], v[60:63], a[240:243], v[80:83]\n"
  "s_waitcnt lgkmcnt(4)\n"
  "v_mfma_f32_16x16x32_bf16 v[80:83], v[64:67], v[224:227], v[80:83]\n"
  "s_waitcnt lgkmcnt(2)\n"
  "ds_read_b64_tr_b16 v[76:77], v44 offset:9224\n"
  "v_mfma_f32_16x16x32_bf16 v[80:83], v[68:71], v[240:243], v[80:83]\n"
  "v_mfma_f32_16x16x32_bf16 v[84:87], v[56:59], a[228:231], v[84:87]\n"
  "ds_read_b64_tr_b16 v[78:79], v44 offset:13320\n"
  "v_mfma_f32_16x16x32_bf16 v[84:87], v[60:63], a[244:247], v[84:87]\n"
  "v_mfma_f32_16x16x32_bf16 v[84:87], v[64:67], v[228:231], v[84:87]\n"
  "ds_read_b64_tr_b16 v[48:49], v44 offset:18440\n"
  "v_mfma_f32_16x16x32_bf16 v[84:87], v[68:71], v[244:247], v[84:87]\n"
  "v_mfma_f32_16x16x32_bf16 v[88:91], v[56:59], a[232:235], v[88:91]\n"
  "ds_read_b64_tr_b16 v[50:51], v44 offset:22536\n"
  "v_mfma_f32_16x16x32_bf16 v[88:91], v[60:63], a[248:251], v[88:91]\n"
  "v_mfma_f32_16x16x32_bf16 v[88:91], v[64:67], v[232:235], v[88:91]\n"
  "ds_read_b64_tr_b16 v[52:53], v44 offset:27656\n"
  "v_mfma_f32_16x16x32_bf16 v[88:91], v[68:71], v[248:251], v[88:91]\n"
  "v_mfma_f32_16x16x32_bf16 v[92:95], v[56:59], a[236:239], v[92:95]\n"
  "ds_read_b64_tr_b16 v[54:55], v44 offset:31752\n"
  "v_add_u32 v45, 0xfffe5000, v45\n"
  "v_mfma_f32_16x16x32_bf16 v[92:95], v[60:63], a[252:255], v[92:95]\n"
  "v_mfma_f32_16x16x32_bf16 v[92:95], v[64:67], v[236:239], v[92:95]\n"
  "ds_read_b64_tr_b16 v[56:57], v45 offset:0\n"
  "v_mfma_f32_16x16x32_bf16 v[92:95], v[68:71], v[252:255], v[92:95]\n"
  "s_waitcnt lgkmcnt(4)\n"
  "v_mfma_f32_16x16x32_bf16 v[96:99], v[72:75], a[224:227], v[96:99]\n"
  "ds_read_b64_tr_b16 v[58:59], v45 offset:4096\n"
  "v_mfma_f32_16x16x32_bf16 v[96:99], v[76:79], a[240:243], v[96:99]\n"
  "s_waitcnt lgkmcnt(3)\n"
  "ds_read_b128 a[128:131], v32 offset:16384\n"
  "v_mfma_f32_16x16x32_bf16 v[96:99], v[48:51], v[224:227], v[96:99]\n"
  "s_waitcnt lgkmcnt(3)\n"
  "ds_read_b64_tr_b16 v[60:61], v45 offset:9216\n"
  "v_mfma_f32_16x16x32_bf16 v[96:99], v[52:55], v[240:243], v[96:99]\n"
  "v_mfma_f32_16x16x32_bf16 v[100:103], v[72:75], a[228:231], v[100:103]\n"
  "ds_read_b64_tr_b16 v[62:63], v45 offset:13312\n"
  "v_mfma_f32_16x16x32_bf16 v[100:103], v[76:79], a[244:247], v[100:103]\n"
  "ds_read_b128 a[132:135], v33 offset:16384\n"
  "v_mfma_f32_16x16x32_bf16 v[100:103], v[48:51], v[228:231], v[100:103]\n"
  "ds_read_b64_tr_b16 v[64:65], v45 offset:18432\n"
  "v_mul_f32 v80, 0x3e0293ee, v80\n"
  "v_mfma_f32_16x16x32_bf16 v[100:103], v[52:55], v[244:247], v[100:103]\n"
  "v_mul_f32 v81, 0x3e0293ee, v81\n"
  "v_mul_f32 v82, 0x3e0293ee, v82\n"
  "v_mfma_f32_16x16x32_bf16 v[104:107], v[72:75], a[232:235], v[104:107]\n"
  "ds_read_b64_tr_b16 v[66:67], v45 offset:22528\n"
  "v_mul_f32 v83, 0x3e0293ee, v83\n"
  "v_mfma_f32_16x16x32_bf16 v[104:107], v[76:79], a[248:251], v[104:107]\n"
  "v_mul_f32 v96, 0x3e0293ee, v96\n"
  "v_mul_f32 v97, 0x3e0293ee, v97\n"
  "v_mfma_f32_16x16x32_bf16 v[104:107], v[48:51], v[232:235], v[104:107]\n"
  "ds_read_b64_tr_b16 v[68:69], v45 offset:27648\n"
  "v_mul_f32 v98, 0x3e0293ee, v98\n"
  "v_mfma_f32_16x16x32_bf16 v[104:107], v[52:55], v[248:251], v[104:107]\n"
  "ds_read_b128 a[136:139], v34 offset:16384\n"
  "v_mul_f32 v99, 0x3e0293ee, v99\n"
  "v_cvt_pk_bf16_f32 v208, v80, v81\n"
  "v_mfma_f32_16x16x32_bf16 v[108:111], v[72:75], a[236:239], v[108:111]\n"
  "ds_read_b64_tr_b16 v[70:71], v45 offset:31744\n"
  "v_cvt_pk_bf16_f32 v209, v82, v83\n"
  "v_mfma_f32_16x16x32_bf16 v[108:111], v[76:79], a[252:255], v[108:111]\n"
  "v_cvt_pk_bf16_f32 v210, v96, v97\n"
  "v_cvt_pk_bf16_f32 v211, v98, v99\n"
  "v_mfma_f32_16x16x32_bf16 v[108:111], v[48:51], v[236:239], v[108:111]\n"
  "ds_read_b64_tr_b16 v[72:73], v45 offset:8\n"
  "v_mul_f32 v84, 0x3e0293ee, v84\n"
  "v_mfma_f32_16x16x32_bf16 v[108:111], v[52:55], v[252:255], v[108:111]\n"
  "s_waitcnt lgkmcnt(2)\n"
  "ds_read_b128 a[140:143], v35 offset:16384\n"
  "v_mul_f32 v85, 0x3e0293ee, v85\n"
  "v_mul_f32 v86, 0x3e0293ee, v86\n"
  "v_mfma_f32_16x16x32_bf16 v[112:115], v[56:59], a[224:227], v[112:115]\n"
  "ds_read_b64_tr_b16 v[74:75], v45 offset:4104\n"
  "v_mul_f32 v87, 0x3e0293ee, v87\n"
  "v_mfma_f32_16x16x32_bf16 v[112:115], v[60:63], a[240:243], v[112:115]\n"
  "v_mul_f32 v100, 0x3e0293ee, v100\n"
  "v_mul_f32 v101, 0x3e0293ee, v101\n"
  "v_mfma_f32_16x16x32_bf16 v[112:115], v[64:67], v[224:227], v[112:115]\n"
  "s_waitcnt lgkmcnt(2)\n"
  "ds_read_b64_tr_b16 v[76:77], v45 offset:9224\n"
  "v_mul_f32 v102, 0x3e0293ee, v102\n"
  "v_mfma_f32_16x16x32_bf16 v[112:115], v[68:71], v[240:243], v[112:115]\n"
  "v_mul_f32 v103, 0x3e0293ee, v103\n"
  "v_cvt_pk_bf16_f32 v212, v84, v85\n"
  "v_mfma_f32_16x16x32_bf16 v[116:119], v[56:59], a[228:231], v[116:119]\n"
  "ds_read_b64_tr_b16 v[78:79], v45 offset:13320\n"
  "v_cvt_pk_bf16_f32 v213, v86, v87\n"
  "v_mfma_f32_16x16x32_bf16 v[116:119], v[60:63], a[244:247], v[116:119]\n"
  "ds_read_b128 a[144:147], v32 offset:20480\n"
  "v_cvt_pk_bf16_f32 v214, v100, v101\n"
  "v_cvt_pk_bf16_f32 v215, v102, v103\n"
  "v_mfma_f32_16x16x32_bf16 v[116:119], v[64:67], v[228:231], v[116:119]\n"
  "ds_read_b64_tr_b16 v[48:49], v45 offset:18440\n"
  "v_mul_f32 v88, 0x3e0293ee, v88\n"
  "v_mfma_f32_16x16x32_bf16 v[116:119], v[68:71], v[244:247], v[116:119]\n"
  "v_mul_f32 v89, 0x3e0293ee, v89\n"
  "v_mul_f32 v90, 0x3e0293ee, v90\n"
  "v_mfma_f32_16x16x32_bf16 v[120:123], v[56:59], a[232:235], v[120:123]\n"
  "ds_read_b64_tr_b16 v[50:51], v45 offset:22536\n"
  "v_mul_f32 v91, 0x3e0293ee, v91\n"
  "v_mfma_f32_16x16x32_bf16 v[120:123], v[60:63], a[248:251], v[120:123]\n"
  "ds_read_b128 a[148:151], v33 offset:20480\n"
  "v_mul_f32 v104, 0x3e0293ee, v104\n"
  "v_mul_f32 v105, 0x3e0293ee, v105\n"
  "v_mfma_f32_16x16x32_bf16 v[120:123], v[64:67], v[232:235], v[120:123]\n"
  "ds_read_b64_tr_b16 v[52:53], v45 offset:27656\n"
  "v_mul_f32 v106, 0x3e0293ee, v106\n"
  "v_mfma_f32_16x16x32_bf16 v[120:123], v[68:71], v[248:251], v[120:123]\n"
  "v_mul_f32 v107, 0x3e0293ee, v107\n"
  "v_cvt_pk_bf16_f32 v216, v88, v89\n"
  "v_mfma_f32_16x16x32_bf16 v[124:127], v[56:59], a[236:239], v[124:127]\n"
  "ds_read_b64_tr_b16 v[54:55], v45 offset:31752\n"
  "v_add_u32 v46, 0xfffe5000, v46\n"
  "v_mfma_f32_16x16x32_bf16 v[124:127], v[60:63], a[252:255], v[124:127]\n"
  "v_cvt_pk_bf16_f32 v217, v90, v91\n"
  "v_cvt_pk_bf16_f32 v218, v104, v105\n"
  "v_mfma_f32_16x16x32_bf16 v[124:127], v[64:67], v[236:239], v[124:127]\n"
  "ds_read_b64_tr_b16 v[56:57], v46 offset:0\n"
  "v_cvt_pk_bf16_f32 v219, v106, v107\n"
  "v_mfma_f32_16x16x32_bf16 v[124:127], v[68:71], v[252:255], v[124:127]\n"
  "s_waitcnt lgkmcnt(2)\n"
  "ds_read_b128 a[152:155], v34 offset:20480\n"
  "v_mul_f32 v92, 0x3e0293ee, v92\n"
  "v_mul_f32 v93, 0x3e0293ee, v93\n"
  "v_mfma_f32_16x16x32_bf16 v[128:131], v[72:75], a[224:227], v[128:131]\n"
  "ds_read_b64_tr_b16 v[58:59], v46 offset:4096\n"
  "v_mul_f32 v94, 0x3e0293ee, v94\n"
  "v_mfma_f32_16x16x32_bf16 v[128:131], v[76:79], a[240:243], v[128:131]\n"
  "v_mul_f32 v95, 0x3e0293ee, v95\n"
  "v_mul_f32 v108, 0x3e0293ee, v108\n"
  "v_mfma_f32_16x16x32_bf16 v[128:131], v[48:51], v[224:227], v[128:131]\n"
  "s_waitcnt lgkmcnt(2)\n"
  "ds_read_b64_tr_b16 v[60:61], v46 offset:9216\n"
  "v_mul_f32 v109, 0x3e0293ee, v109\n"
  "v_mfma_f32_16x16x32_bf16 v[128:131], v[52:55], v[240:243], v[128:131]\n"
  "ds_read_b128 a[156:159], v35 offset:20480\n"
  "v_mul_f32 v110, 0x3e0293ee, v110\n"
  "v_mul_f32 v111, 0x3e0293ee, v111\n"
  "v_mfma_f32_16x16x32_bf16 v[132:135], v[72:75], a[228:231], v[132:135]\n"
  "ds_read_b64_tr_b16 v[62:63], v46 offset:13312\n"
  "v_cvt_pk_bf16_f32 v220, v92, v93\n"
  "v_mfma_f32_16x16x32_bf16 v[132:135], v[76:79], a[244:247], v[132:135]\n"
  "v_cvt_pk_bf16_f32 v221, v94, v95\n"
  "v_cvt_pk_bf16_f32 v222, v108, v109\n"
  "v_mfma_f32_16x16x32_bf16 v[132:135], v[48:51], v[228:231], v[132:135]\n"
  "ds_read_b64_tr_b16 v[64:65], v46 offset:18432\n"
  "v_cvt_pk_bf16_f32 v223, v110, v111\n"
  "v_mfma_f32_16x16x32_bf16 v[132:135], v[52:55], v[244:247], v[132:135]\n"
  "v_mul_f32 v112, 0x3e0293ee, v112\n"
  "v_mul_f32 v113, 0x3e0293ee, v113\n"
  "v_mfma_f32_16x16x32_bf16 v[136:139], v[72:75], a[232:235], v[136:139]\n"
  "ds_read_b64_tr_b16 v[66:67], v46 offset:22528\n"
  "v_mul_f32 v114, 0x3e0293ee, v114\n"
  "v_mfma_f32_16x16x32_bf16 v[136:139], v[76:79], a[248:251], v[136:139]\n"
  "ds_read_b128 a[160:163], v32 offset:24576\n"
  "v_mul_f32 v115, 0x3e0293ee, v115\n"
  "v_mul_f32 v128, 0x3e0293ee, v128\n"
  "v_mfma_f32_16x16x32_bf16 v[136:139], v[48:51], v[232:235], v[136:139]\n"
  "ds_read_b64_tr_b16 v[68:69], v46 offset:27648\n"
  "v_mul_f32 v129, 0x3e0293ee, v129\n"
  "v_mfma_f32_16x16x32_bf16 v[136:139], v[52:55], v[248:251], v[136:139]\n"
  "v_mul_f32 v130, 0x3e0293ee, v130\n"
  "v_mul_f32 v131, 0x3e0293ee, v131\n"
  "v_mfma_f32_16x16x32_bf16 v[140:143], v[72:75], a[236:239], v[140:143]\n"
  "ds_read_b64_tr_b16 v[70:71], v46 offset:31744\n"
  "v_cvt_pk_bf16_f32 v80, v112, v113\n"
  "v_mfma_f32_16x16x32_bf16 v[140:143], v[76:79], a[252:255], v[140:143]\n"
  "ds_read_b128 a[164:167], v33 offset:24576\n"
  "v_cvt_pk_bf16_f32 v81, v114, v115\n"
  "v_cvt_pk_bf16_f32 v82, v128, v129\n"
  "v_mfma_f32_16x16x32_bf16 v[140:143], v[48:51], v[236:239], v[140:143]\n"
  "ds_read_b64_tr_b16 v[72:73], v46 offset:8\n"
  "v_cvt_pk_bf16_f32 v83, v130, v131\n"
  "v_mfma_f32_16x16x32_bf16 v[140:143], v[52:55], v[252:255], v[140:143]\n"
  "s_waitcnt lgkmcnt(3)\n"
  "v_mul_f32 v116, 0x3e0293ee, v116\n"
  "v_mul_f32 v117, 0x3e0293ee, v117\n"
  "v_mfma_f32_16x16x32_bf16 v[144:147], v[56:59], a[224:227], v[144:147]\n"
  "ds_read_b64_tr_b16 v[74:75], v46 offset:4104\n"
  "v_mul_f32 v118, 0x3e0293ee, v118\n"
  "v_mfma_f32_16x16x32_bf16 v[144:147], v[60:63], a[240:243], v[144:147]\n"
  "v_mul_f32 v119, 0x3e0293ee, v119\n"
  "v_mul_f32 v132, 0x3e0293ee, v132\n"
  "v_mfma_f32_16x16x32_bf16 v[144:147], v[64:67], v[224:227], v[144:147]\n"
  "s_waitcnt lgkmcnt(1)\n"
  "ds_read_b64_tr_b16 v[76:77], v46 offset:9224\n"
  "v_mul_f32 v133, 0x3e0293ee, v133\n"
  "v_mfma_f32_16x16x32_bf16 v[144:147], v[68:71], v[240:243], v[144:147]\n"
  "ds_read_b128 a[168:171], v34 offset:24576\n"
  "v_mul_f32 v134, 0x3e0293ee, v134\n"
  "v_mul_f32 v135, 0x3e0293ee, v135\n"
  "v_mfma_f32_16x16x32_bf16 v[148:151], v[56:59], a[228:231], v[148:151]\n"
  "ds_read_b64_tr_b16 v[78:79], v46 offset:13320\n"
  "v_cvt_pk_bf16_f32 v84, v116, v117\n"
  "v_mfma_f32_16x16x32_bf16 v[148:151], v[60:63], a[244:247], v[148:151]\n"
  "v_cvt_pk_bf16_f32 v85, v118, v119\n"
  "v_cvt_pk_bf16_f32 v86, v132, v133\n"
  "v_mfma_f32_16x16x32_bf16 v[148:151], v[64:67], v[228:231], v[148:151]\n"
  "ds_read_b64_tr_b16 v[48:49], v46 offset:18440\n"
  "v_cvt_pk_bf16_f32 v87, v134, v135\n"
  "v_mfma_f32_16x16x32_bf16 v[148:151], v[68:71], v[244:247], v[148:151]\n"
  "ds_read_b128 a[172:175], v35 offset:24576\n"
  "v_mul_f32 v120, 0x3e0293ee, v120\n"
  "v_mul_f32 v121, 0x3e0293ee, v121\n"
  "v_mfma_f32_16x16x32_bf16 v[152:155], v[56:59], a[232:235], v[152:155]\n"
  "ds_read_b64_tr_b16 v[50:51], v46 offset:22536\n"
  "v_mul_f32 v122, 0x3e0293ee, v122\n"
  "v_mfma_f32_16x16x32_bf16 v[152:155], v[60:63], a[248:251], v[152:155]\n"
  "v_mul_f32 v123, 0x3e0293ee, v123\n"
  "v_mul_f32 v136, 0x3e0293ee, v136\n"
  "v_mfma_f32_16x16x32_bf16 v[152:155], v[64:67], v[232:235], v[152:155]\n"
  "ds_read_b64_tr_b16 v[52:53], v46 offset:27656\n"
  "v_mul_f32 v137, 0x3e0293ee, v137\n"
  "v_mfma_f32_16x16x32_bf16 v[152:155], v[68:71], v[248:251], v[152:155]\n"
  "v_mul_f32 v138, 0x3e0293ee, v138\n"
  "v_mul_f32 v139, 0x3e0293ee, v139\n"
  "v_mfma_f32_16x16x32_bf16 v[156:159], v[56:59], a[236:239], v[156:159]\n"
  "ds_read_b64_tr_b16 v[54:55], v46 offset:31752\n"
  "v_add_u32 v47, 0xfffe5000, v47\n"
  "v_mfma_f32_16x16x32_bf16 v[156:159], v[60:63], a[252:255], v[156:159]\n"
  "ds_read_b128 a[176:179], v32 offset:28672\n"
  "v_cvt_pk_bf16_f32 v88, v120, v121\n"
  "v_cvt_pk_bf16_f32 v89, v122, v123\n"
  "v_mfma_f32_16x16x32_bf16 v[156:159], v[64:67], v[236:239], v[156:159]\n"
  "ds_read_b64_tr_b16 v[56:57], v47 offset:0\n"
  "v_cvt_pk_bf16_f32 v90, v136, v137\n"
  "v_mfma_f32_16x16x32_bf16 v[156:159], v[68:71], v[252:255], v[156:159]\n"
  "s_waitcnt lgkmcnt(3)\n"
  "v_cvt_pk_bf16_f32 v91, v138, v139\n"
  "v_mul_f32 v124, 0x3e0293ee, v124\n"
  "v_mfma_f32_16x16x32_bf16 v[160:163], v[72:75], a[224:227], v[160:163]\n"
  "ds_read_b64_tr_b16 v[58:59], v47 offset:4096\n"
  "v_mul_f32 v125, 0x3e0293ee, v125\n"
  "v_mfma_f32_16x16x32_bf16 v[160:163], v[76:79], a[240:243], v[160:163]\n"
  "ds_read_b128 a[180:183], v33 offset:28672\n"
  "v_mul_f32 v126, 0x3e0293ee, v126\n"
  "v_mul_f32 v127, 0x3e0293ee, v127\n"
  "v_mfma_f32_16x16x32_bf16 v[160:163], v[48:51], v[224:227], v[160:163]\n"
  "s_waitcnt lgkmcnt(2)\n"
  "ds_read_b64_tr_b16 v[60:61], v47 offset:9216\n"
  "v_mul_f32 v140, 0x3e0293ee, v140\n"
  "v_mfma_f32_16x16x32_bf16 v[160:163], v[52:55], v[240:243], v[160:163]\n"
  "v_mul_f32 v141, 0x3e0293ee, v141\n"
  "v_mul_f32 v142, 0x3e0293ee, v142\n"
  "v_mfma_f32_16x16x32_bf16 v[164:167], v[72:75], a[228:231], v[164:167]\n"
  "ds_read_b64_tr_b16 v[62:63], v47 offset:13312\n"
  "v_mul_f32 v143, 0x3e0293ee, v143\n"
  "v_mfma_f32_16x16x32_bf16 v[164:167], v[76:79], a[244:247], v[164:167]\n"
  "v_cvt_pk_bf16_f32 v92, v124, v125\n"
  "v_cvt_pk_bf16_f32 v93, v126, v127\n"
  "v_mfma_f32_16x16x32_bf16 v[164:167], v[48:51], v[228:231], v[164:167]\n"
  "ds_read_b64_tr_b16 v[64:65], v47 offset:18432\n"
  "v_cvt_pk_bf16_f32 v94, v140, v141\n"
  "v_mfma_f32_16x16x32_bf16 v[164:167], v[52:55], v[244:247], v[164:167]\n"
  "ds_read_b128 a[184:187], v34 offset:28672\n"
  "v_cvt_pk_bf16_f32 v95, v142, v143\n"
  "v_mul_f32 v144, 0x3e0293ee, v144\n"
  "v_mfma_f32_16x16x32_bf16 v[168:171], v[72:75], a[232:235], v[168:171]\n"
  "ds_read_b64_tr_b16 v[66:67], v47 offset:22528\n"
  "v_mul_f32 v145, 0x3e0293ee, v145\n"
  "v_mfma_f32_16x16x32_bf16 v[168:171], v[76:79], a[248:251], v[168:171]\n"
  "v_mul_f32 v146, 0x3e0293ee, v146\n"
  "v_mul_f32 v147, 0x3e0293ee, v147\n"
  "v_mfma_f32_16x16x32_bf16 v[168:171], v[48:51], v[232:235], v[168:171]\n"
  "ds_read_b64_tr_b16 v[68:69], v47 offset:27648\n"
  "v_mul_f32 v160, 0x3e0293ee, v160\n"
  "v_mfma_f32_16x16x32_bf16 v[168:171], v[52:55], v[248:251], v[168:171]\n"
  "ds_read_b128 a[188:191], v35 offset:28672\n"
  "v_mul_f32 v161, 0x3e0293ee, v161\n"
  "v_mul_f32 v162, 0x3e0293ee, v162\n"
  "v_mfma_f32_16x16x32_bf16 v[172:175], v[72:75], a[236:239], v[172:175]\n"
  "ds_read_b64_tr_b16 v[70:71], v47 offset:31744\n"
  "v_mul_f32 v163, 0x3e0293ee, v163\n"
  "v_mfma_f32_16x16x32_bf16 v[172:175], v[76:79], a[252:255], v[172:175]\n"
  "v_cvt_pk_bf16_f32 v96, v144, v145\n"
  "v_cvt_pk_bf16_f32 v97, v146, v147\n"
  "v_mfma_f32_16x16x32_bf16 v[172:175], v[48:51], v[236:239], v[172:175]\n"
  "ds_read_b64_tr_b16 v[72:73], v47 offset:8\n"
  "v_cvt_pk_bf16_f32 v98, v160, v161\n"
  "v_mfma_f32_16x16x32_bf16 v[172:175], v[52:55], v[252:255], v[172:175]\n"
  "s_waitcnt lgkmcnt(2)\n"
  "v_cvt_pk_bf16_f32 v99, v162, v163\n"
  "v_mul_f32 v148, 0x3e0293ee, v148\n"
  "v_mfma_f32_16x16x32_bf16 v[176:179], v[56:59], a[224:227], v[176:179]\n"
  "ds_read_b64_tr_b16 v[74:75], v47 offset:4104\n"
  "v_mul_f32 v149, 0x3e0293ee, v149\n"
  "v_mfma_f32_16x16x32_bf16 v[176:179], v[60:63], a[240:243], v[176:179]\n"
  "v_mul_f32 v150, 0x3e0293ee, v150\n"
  "v_mul_f32 v151, 0x3e0293ee, v151\n"
  "v_mfma_f32_16x16x32_bf16 v[176:179], v[64:67], v[224:227], v[176:179]\n"
  "s_waitcnt lgkmcnt(1)\n"
  "ds_read_b64_tr_b16 v[76:77], v47 offset:9224\n"
  "v_mul_f32 v164, 0x3e0293ee, v164\n"
  "v_mfma_f32_16x16x32_bf16 v[176:179], v[68:71], v[240:243], v[176:179]\n"
  "v_mul_f32 v165, 0x3e0293ee, v165\n"
  "v_mul_f32 v166, 0x3e0293ee, v166\n"
  "v_mfma_f32_16x16x32_bf16 v[180:183], v[56:59], a[228:231], v[180:183]\n"
  "ds_read_b64_tr_b16 v[78:79], v47 offset:13320\n"
  "v_mul_f32 v167, 0x3e0293ee, v167\n"
  "v_mfma_f32_16x16x32_bf16 v[180:183], v[60:63], a[244:247], v[180:183]\n"
  "v_cvt_pk_bf16_f32 v100, v148, v149\n"
  "v_cvt_pk_bf16_f32 v101, v150, v151\n"
  "v_mfma_f32_16x16x32_bf16 v[180:183], v[64:67], v[228:231], v[180:183]\n"
  "ds_read_b64_tr_b16 v[48:49], v47 offset:18440\n"
  "v_cvt_pk_bf16_f32 v102, v164, v165\n"
  "v_mfma_f32_16x16x32_bf16 v[180:183], v[68:71], v[244:247], v[180:183]\n"
  "v_cvt_pk_bf16_f32 v103, v166, v167\n"
  "v_mul_f32 v152, 0x3e0293ee, v152\n"
  "v_mfma_f32_16x16x32_bf16 v[184:187], v[56:59], a[232:235], v[184:187]\n"
  "ds_read_b64_tr_b16 v[50:51], v47 offset:22536\n"
  "v_mul_f32 v153, 0x3e0293ee, v153\n"
  "v_mfma_f32_16x16x32_bf16 v[184:187], v[60:63], a[248:251], v[184:187]\n"
  "v_mul_f32 v154, 0x3e0293ee, v154\n"
  "v_mul_f32 v155, 0x3e0293ee, v155\n"
  "v_mfma_f32_16x16x32_bf16 v[184:187], v[64:67], v[232:235], v[184:187]\n"
  "ds_read_b64_tr_b16 v[52:53], v47 offset:27656\n"
  "v_mul_f32 v168, 0x3e0293ee, v168\n"
  "v_mfma_f32_16x16x32_bf16 v[184:187], v[68:71], v[248:251], v[184:187]\n"
  "v_mul_f32 v169, 0x3e0293ee, v169\n"
  "v_mul_f32 v170, 0x3e0293ee, v170\n"
  "v_mfma_f32_16x16x32_bf16 v[188:191], v[56:59], a[236:239], v[188:191]\n"
  "ds_read_b64_tr_b16 v[54:55], v47 offset:31752\n"
  "v_mul_f32 v171, 0x3e0293ee, v171\n"
  "v_mfma_f32_16x16x32_bf16 v[188:191], v[60:63], a[252:255], v[188:191]\n"
  "v_cvt_pk_bf16_f32 v104, v152, v153\n"
  "v_cvt_pk_bf16_f32 v105, v154, v155\n"
  "v_mfma_f32_16x16x32_bf16 v[188:191], v[64:67], v[236:239], v[188:191]\n"
  "v_cvt_pk_bf16_f32 v106, v168, v169\n"
  "v_cvt_pk_bf16_f32 v107, v170, v171\n"
  "v_mfma_f32_16x16x32_bf16 v[188:191], v[68:71], v[252:255], v[188:191]\n"
  "s_waitcnt lgkmcnt(1)\n"
  "v_mul_f32 v156, 0x3e0293ee, v156\n"
  "v_mul_f32 v157, 0x3e0293ee, v157\n"
  "v_mfma_f32_16x16x32_bf16 v[192:195], v[72:75], a[224:227], v[192:195]\n"
  "v_mul_f32 v158, 0x3e0293ee, v158\n"
  "v_mul_f32 v159, 0x3e0293ee, v159\n"
  "v_mfma_f32_16x16x32_bf16 v[192:195], v[76:79], a[240:243], v[192:195]\n"
  "v_mul_f32 v172, 0x3e0293ee, v172\n"
  "v_mul_f32 v173, 0x3e0293ee, v173\n"
  "v_mfma_f32_16x16x32_bf16 v[192:195], v[48:51], v[224:227], v[192:195]\n"
  "s_waitcnt lgkmcnt(0)\n"
  "v_mul_f32 v174, 0x3e0293ee, v174\n"
  "v_mul_f32 v175, 0x3e0293ee, v175\n"
  "v_mfma_f32_16x16x32_bf16 v[192:195], v[52:55], v[240:243], v[192:195]\n"
  "v_cvt_pk_bf16_f32 v108, v156, v157\n"
  "v_cvt_pk_bf16_f32 v109, v158, v159\n"
  "v_mfma_f32_16x16x32_bf16 v[196:199], v[72:75], a[228:231], v[196:199]\n"
  "v_cvt_pk_bf16_f32 v110, v172, v173\n"
  "v_cvt_pk_bf16_f32 v111, v174, v175\n"
  "v_mfma_f32_16x16x32_bf16 v[196:199], v[76:79], a[244:247], v[196:199]\n"
  "v_mfma_f32_16x16x32_bf16 v[196:199], v[48:51], v[228:231], v[196:199]\n"
  "v_mfma_f32_16x16x32_bf16 v[196:199], v[52:55], v[244:247], v[196:199]\n"
  "v_mfma_f32_16x16x32_bf16 v[200:203], v[72:75], a[232:235], v[200:203]\n"
  "v_mfma_f32_16x16x32_bf16 v[200:203], v[76:79], a[248:251], v[200:203]\n"
  "v_mfma_f32_16x16x32_bf16 v[200:203], v[48:51], v[232:235], v[200:203]\n"
  "v_mfma_f32_16x16x32_bf16 v[200:203], v[52:55], v[248:251], v[200:203]\n"
  "v_mfma_f32_16x16x32_bf16 v[204:207], v[72:75], a[236:239], v[204:207]\n"
  "v_mfma_f32_16x16x32_bf16 v[204:207], v[76:79], a[252:255], v[204:207]\n"
  "v_mfma_f32_16x16x32_bf16 v[204:207], v[48:51], v[236:239], v[204:207]\n"
  "v_mfma_f32_16x16x32_bf16 v[204:207], v[52:55], v[252:255], v[204:207]\n"
  "v_mov_b32 v68, 0xff800000\n"
  "v_mov_b32 v69, 0xff800000\n"
  "v_mov_b32 v70, 0xff800000\n"
  "v_mov_b32 v71, 0xff800000\n"
  "v_mul_f32 v176, 0x3e0293ee, v176\n"
  "v_mul_f32 v177, 0x3e0293ee, v177\n"
  "v_mul_f32 v178, 0x3e0293ee, v178\n"
  "v_mul_f32 v179, 0x3e0293ee, v179\n"
  "v_mul_f32 v192, 0x3e0293ee, v192\n"
  "v_mul_f32 v193, 0x3e0293ee, v193\n"
  "v_mul_f32 v194, 0x3e0293ee, v194\n"
  "v_mul_f32 v195, 0x3e0293ee, v195\n"
  "v_cvt_pk_bf16_f32 v112, v176, v177\n"
  "v_cvt_pk_bf16_f32 v113, v178, v179\n"
  "v_cvt_pk_bf16_f32 v114, v192, v193\n"
  "v_cvt_pk_bf16_f32 v115, v194, v195\n"
  "v_mul_f32 v180, 0x3e0293ee, v180\n"
  "v_mul_f32 v181, 0x3e0293ee, v181\n"
  "v_mul_f32 v182, 0x3e0293ee, v182\n"
  "v_mul_f32 v183, 0x3e0293ee, v183\n"
  "v_mul_f32 v196, 0x3e0293ee, v196\n"
  "v_mul_f32 v197, 0x3e0293ee, v197\n"
  "v_mul_f32 v198, 0x3e0293ee, v198\n"
  "v_mul_f32 v199, 0x3e0293ee, v199\n"
  "v_cvt_pk_bf16_f32 v116, v180, v181\n"
  "v_cvt_pk_bf16_f32 v117, v182, v183\n"
  "v_cvt_pk_bf16_f32 v118, v196, v197\n"
  "v_cvt_pk_bf16_f32 v119, v198, v199\n"
  "v_mul_f32 v184, 0x3e0293ee, v184\n"
  "v_mul_f32 v185, 0x3e0293ee, v185\n"
  "v_mul_f32 v186, 0x3e0293ee, v186\n"
  "v_mul_f32 v187, 0x3e0293ee, v187\n"
  "v_mul_f32 v200, 0x3e0293ee, v200\n"
  "v_mul_f32 v201, 0x3e0293ee, v201\n"
  "v_mul_f32 v202, 0x3e0293ee, v202\n"
  "v_mul_f32 v203, 0x3e0293ee, v203\n"
  "v_cvt_pk_bf16_f32 v120, v184, v185\n"
  "v_cvt_pk_bf16_f32 v121, v186, v187\n"
  "v_cvt_pk_bf16_f32 v122, v200, v201\n"
  "v_cvt_pk_bf16_f32 v123, v202, v203\n"
  "v_mul_f32 v188, 0x3e0293ee, v188\n"
  "v_mul_f32 v189, 0x3e0293ee, v189\n"
  "v_mul_f32 v190, 0x3e0293ee, v190\n"
  "v_mul_f32 v191, 0x3e0293ee, v191\n"
  "v_mul_f32 v204, 0x3e0293ee, v204\n"
  "v_mul_f32 v205, 0x3e0293ee, v205\n"
  "v_mul_f32 v206, 0x3e0293ee, v206\n"
  "v_mul_f32 v207, 0x3e0293ee, v207\n"
  "v_cvt_pk_bf16_f32 v124, v188, v189\n"
  "v_cvt_pk_bf16_f32 v125, v190, v191\n"
  "v_cvt_pk_bf16_f32 v126, v204, v205\n"
  "v_cvt_pk_bf16_f32 v127, v206, v207\n"
  "s_waitcnt vmcnt(0)\n"
  "s_barrier\n"
  "Lattn_%=:\n"
  "v_mov_b32 v64, 0x3f803f80\n"
  "v_mov_b32 v65, 0x3f803f80\n"
  "v_mov_b32 v66, 0x3f803f80\n"
  "v_mov_b32 v67, 0x3f803f80\n"
  "v_mfma_f32_16x16x32_bf16 v[128:131], a[128:131], v[208:211], 0\n"
  "s_mov_b32 s40, s84\n"
  "s_and_b32 s41, s85, 0xffff\n"
  "s_mov_b32 s42, s86\n"
  "s_add_u32 m0, s67, 0\n"
  "s_add_u32 s80, s87, 0\n"
  "ds_read_b64_tr_b16 a[192:193], v36 offset:49152\n"
  "v_mfma_f32_16x16x32_bf16 v[128:131], a[132:135], v[80:83], v[128:131]\n"
  "ds_read_b64_tr_b16 a[194:195], v36 offset:53248\n"
  "buffer_load_dwordx4 v1, s[40:43], s80 offen offset:0 nt lds\n"
  "v_mfma_f32_16x16x32_bf16 v[128:131], a[136:139], v[96:99], v[128:131]\n"
  "ds_read_b64_tr_b16 a[196:197], v36 offset:57344\n"
  "v_add_u32 v32, 0xffff0000, v32\n"
  "v_add_u32 v33, 0xffff0000, v33\n"
  "v_mfma_f32_16x16x32_bf16 v[128:131], a[140:143], v[112:115], v[128:131]\n"
  "ds_read_b64_tr_b16 a[198:199], v36 offset:61440\n"
  "v_add_u32 v34, 0xffff0000, v34\n"
  "v_add_u32 v35, 0xffff0000, v35\n"
  "v_mfma_f32_16x16x32_bf16 v[144:147], a[128:131], v[212:215], 0\n"
  "ds_read_b64_tr_b16 a[200:201], v37 offset:49152\n"
  "v_mfma_f32_16x16x32_bf16 v[144:147], a[132:135], v[84:87], v[144:147]\n"
  "ds_read_b64_tr_b16 a[202:203], v37 offset:53248\n"
  "buffer_load_dwordx4 v1, s[40:43], s80 offen offset:1024 nt lds\n"
  "v_mfma_f32_16x16x32_bf16 v[144:147], a[136:139], v[100:103], v[144:147]\n"
  "ds_read_b64_tr_b16 a[204:205], v37 offset:57344\n"
  "v_max3_f32 v72, v128, v129, v130\n"
  "v_max_f32 v72, v72, v131\n"
  "v_mfma_f32_16x16x32_bf16 v[144:147], a[140:143], v[116:119], v[144:147]\n"
  "ds_read_b64_tr_b16 a[206:207], v37 offset:61440\n"
  "v_mov_b32 v73, v72\n"
  "s_nop 1\n"
  "v_permlane32_swap_b32 v72, v73\n"
  "v_mfma_f32_16x16x32_bf16 v[160:163], a[128:131], v[216:219], 0\n"
  "ds_read_b64_tr_b16 a[208:209], v38 offset:49152\n"
  "v_max_f32 v72, v72, v73\n"
  "v_mov_b32 v73, v72\n"
  "v_mfma_f32_16x16x32_bf16 v[160:163], a[132:135], v[88:91], v[160:163]\n"
  "ds_read_b64_tr_b16 a[210:211], v38 offset:53248\n"
  "buffer_load_dwordx4 v1, s[40:43], s80 offen offset:2048 nt lds\n"
  "v_permlane16_swap_b32 v72, v73\n"
  "v_max3_f32 v72, v72, v73, v68\n"
  "v_mfma_f32_16x16x32_bf16 v[160:163], a[136:139], v[104:107], v[160:163]\n"
  "ds_read_b64_tr_b16 a[212:213], v38 offset:57344\n"
  "v_sub_f32 v192, 0, v72\n"
  "v_sub_f32 v193, 0, v72\n"
  "v_mfma_f32_16x16x32_bf16 v[160:163], a[140:143], v[120:123], v[160:163]\n"
  "ds_read_b64_tr_b16 a[214:215], v38 offset:61440\n"
  "v_sub_f32 v194, 0, v72\n"
  "v_sub_f32 v195, 0, v72\n"
  "v_mfma_f32_16x16x32_bf16 v[176:179], a[128:131], v[220:223], 0\n"
  "s_add_u32 m0, s67, 0\n"
  "s_add_u32 s80, s87, 0\n"
  "ds_read_b64_tr_b16 a[216:217], v39 offset:49152\n"
  "v_sub_f32 v128, v128, v72\n"
  "v_sub_f32 v129, v129, v72\n"
  "v_mfma_f32_16x16x32_bf16 v[176:179], a[132:135], v[92:95], v[176:179]\n"
  "ds_read_b64_tr_b16 a[218:219], v39 offset:53248\n"
  "buffer_load_dwordx4 v1, s[40:43], s80 offen offset:3072 nt lds\n"
  "v_sub_f32 v130, v130, v72\n"
  "v_sub_f32 v131, v131, v72\n"
  "v_mfma_f32_16x16x32_bf16 v[176:179], a[136:139], v[108:111], v[176:179]\n"
  "ds_read_b64_tr_b16 a[220:221], v39 offset:57344\n"
  "v_mfma_f32_16x16x32_bf16 v[176:179], a[140:143], v[124:127], v[176:179]\n"
  "ds_read_b64_tr_b16 a[222:223], v39 offset:61440\n"
  "v_exp_f32 v128, v128\n"
  "v_mfma_f32_16x16x32_bf16 v[132:135], a[144:147], v[208:211], v[192:195]\n"
  "s_add_u32 m0, s67, 4096\n"
  "s_add_u32 s80, s87, 4096\n"
  "ds_read_b64_tr_b16 a[224:225], v40 offset:49152\n"
  "v_max3_f32 v74, v144, v145, v146\n"
  "v_max_f32 v74, v74, v147\n"
  "v_mfma_f32_16x16x32_bf16 v[132:135], a[148:151], v[80:83], v[132:135]\n"
  "ds_read_b64_tr_b16 a[226:227], v40 offset:53248\n"
  "buffer_load_dwordx4 v1, s[40:43], s80 offen offset:0 nt lds\n"
  "v_exp_f32 v129, v129\n"
  "v_mfma_f32_16x16x32_bf16 v[132:135], a[152:155], v[96:99], v[132:135]\n"
  "ds_read_b64_tr_b16 a[228:229], v40 offset:57344\n"
  "v_mov_b32 v75, v74\n"
  "s_nop 1\n"
  "v_permlane32_swap_b32 v74, v75\n"
  "v_mfma_f32_16x16x32_bf16 v[132:135], a[156:159], v[112:115], v[132:135]\n"
  "ds_read_b64_tr_b16 a[230:231], v40 offset:61440\n"
  "v_exp_f32 v130, v130\n"
  "v_mfma_f32_16x16x32_bf16 v[136:139], a[160:163], v[208:211], v[192:195]\n"
  "ds_read_b64_tr_b16 a[232:233], v41 offset:49152\n"
  "v_max_f32 v74, v74, v75\n"
  "v_mov_b32 v75, v74\n"
  "v_mfma_f32_16x16x32_bf16 v[136:139], a[164:167], v[80:83], v[136:139]\n"
  "ds_read_b64_tr_b16 a[234:235], v41 offset:53248\n"
  "buffer_load_dwordx4 v1, s[40:43], s80 offen offset:1024 nt lds\n"
  "v_exp_f32 v131, v131\n"
  "v_mfma_f32_16x16x32_bf16 v[136:139], a[168:171], v[96:99], v[136:139]\n"
  "ds_read_b64_tr_b16 a[236:237], v41 offset:57344\n"
  "v_permlane16_swap_b32 v74, v75\n"
  "v_max3_f32 v74, v74, v75, v69\n"
  "v_mfma_f32_16x16x32_bf16 v[136:139], a[172:175], v[112:115], v[136:139]\n"
  "ds_read_b64_tr_b16 a[238:239], v41 offset:61440\n"
  "v_exp_f32 v132, v132\n"
  "v_mfma_f32_16x16x32_bf16 v[140:143], a[176:179], v[208:211], v[192:195]\n"
  "s_add_u32 m0, s67, 4096\n"
  "s_add_u32 s80, s87, 4096\n"
  "ds_read_b64_tr_b16 a[240:241], v42 offset:49152\n"
  "v_sub_f32 v196, 0, v74\n"
  "v_sub_f32 v197, 0, v74\n"
  "v_mfma_f32_16x16x32_bf16 v[140:143], a[180:183], v[80:83], v[140:143]\n"
  "ds_read_b64_tr_b16 a[242:243], v42 offset:53248\n"
  "buffer_load_dwordx4 v1, s[40:43], s80 offen offset:2048 nt lds\n"
  "v_exp_f32 v133, v133\n"
  "v_mfma_f32_16x16x32_bf16 v[140:143], a[184:187], v[96:99], v[140:143]\n"
  "ds_read_b64_tr_b16 a[244:245], v42 offset:57344\n"
  "v_sub_f32 v198, 0, v74\n"
  "v_sub_f32 v199, 0, v74\n"
  "v_mfma_f32_16x16x32_bf16 v[140:143], a[188:191], v[112:115], v[140:143]\n"
  "ds_read_b64_tr_b16 a[246:247], v42 offset:61440\n"
  "v_exp_f32 v134, v134\n"
  "v_mfma_f32_16x16x32_bf16 v[148:151], a[144:147], v[212:215], v[196:199]\n"
  "ds_read_b64_tr_b16 a[248:249], v43 offset:49152\n"
  "v_sub_f32 v144, v144, v74\n"
  "v_sub_f32 v145, v145, v74\n"
  "v_mfma_f32_16x16x32_bf16 v[148:151], a[148:151], v[84:87], v[148:151]\n"
  "ds_read_b64_tr_b16 a[250:251], v43 offset:53248\n"
  "buffer_load_dwordx4 v1, s[40:43], s80 offen offset:3072 nt lds\n"
  "v_exp_f32 v135, v135\n"
  "v_mfma_f32_16x16x32_bf16 v[148:151], a[152:155], v[100:103], v[148:151]\n"
  "ds_read_b64_tr_b16 a[252:253], v43 offset:57344\n"
  "v_sub_f32 v146, v146, v74\n"
  "v_sub_f32 v147, v147, v74\n"
  "v_mfma_f32_16x16x32_bf16 v[148:151], a[156:159], v[116:119], v[148:151]\n"
  "ds_read_b64_tr_b16 a[254:255], v43 offset:61440\n"
  "v_exp_f32 v136, v136\n"
  "v_mfma_f32_16x16x32_bf16 v[152:155], a[160:163], v[212:215], v[196:199]\n"
  "s_add_u32 m0, s67, 8192\n"
  "s_add_u32 s80, s87, 8192\n"
  "v_max3_f32 v72, v160, v161, v162\n"
  "v_max_f32 v72, v72, v163\n"
  "v_cvt_pk_bf16_f32 v224, v128, v129\n"
  "v_mfma_f32_16x16x32_bf16 v[152:155], a[164:167], v[84:87], v[152:155]\n"
  "buffer_load_dwordx4 v1, s[40:43], s80 offen offset:0 nt lds\n"
  "v_exp_f32 v137, v137\n"
  "v_cvt_pk_bf16_f32 v225, v130, v131\n"
  "v_mfma_f32_16x16x32_bf16 v[152:155], a[168:171], v[100:103], v[152:155]\n"
  "v_mov_b32 v73, v72\n"
  "s_nop 1\n"
  "v_permlane32_swap_b32 v72, v73\n"
  "v_cvt_pk_bf16_f32 v226, v132, v133\n"
  "v_mfma_f32_16x16x32_bf16 v[152:155], a[172:175], v[116:119], v[152:155]\n"
  "v_exp_f32 v138, v138\n"
  "v_cvt_pk_bf16_f32 v227, v134, v135\n"
  "v_mfma_f32_16x16x32_bf16 v[156:159], a[176:179], v[212:215], v[196:199]\n"
  "s_add_u32 m0, s78, 98304\n"
  "v_max_f32 v72, v72, v73\n"
  "v_mov_b32 v73, v72\n"
  "v_cvt_pk_bf16_f32 v228, v136, v137\n"
  "v_mfma_f32_16x16x32_bf16 v[156:159], a[180:183], v[84:87], v[156:159]\n"
  "s_add_u32 s79, s77, 32768\n"
  "v_exp_f32 v139, v139\n"
  "v_mfma_f32_16x16x32_bf16 v[156:159], a[184:187], v[100:103], v[156:159]\n"
  "v_permlane16_swap_b32 v72, v73\n"
  "v_max3_f32 v72, v72, v73, v70\n"
  "v_cvt_pk_bf16_f32 v229, v138, v139\n"
  "v_mfma_f32_16x16x32_bf16 v[156:159], a[188:191], v[116:119], v[156:159]\n"
  "v_sub_f32 v200, 0, v72\n"
  "v_sub_f32 v201, 0, v72\n"
  "v_sub_f32 v202, 0, v72\n"
  "v_sub_f32 v203, 0, v72\n"
  "s_nop 1\n"
  "v_mfma_f32_16x16x32_bf16 v[164:167], a[144:147], v[216:219], v[200:203]\n"
  "v_sub_f32 v160, v160, v72\n"
  "v_sub_f32 v161, v161, v72\n"
  "v_mfma_f32_16x16x32_bf16 v[164:167], a[148:151], v[88:91], v[164:167]\n"
  "v_exp_f32 v140, v140\n"
  "v_mfma_f32_16x16x32_bf16 v[164:167], a[152:155], v[104:107], v[164:167]\n"
  "v_sub_f32 v162, v162, v72\n"
  "v_sub_f32 v163, v163, v72\n"
  "v_mfma_f32_16x16x32_bf16 v[164:167], a[156:159], v[120:123], v[164:167]\n"
  "v_exp_f32 v141, v141\n"
  "v_mfma_f32_16x16x32_bf16 v[168:171], a[160:163], v[216:219], v[200:203]\n"
  "v_max3_f32 v74, v176, v177, v178\n"
  "v_max_f32 v74, v74, v179\n"
  "v_cvt_pk_bf16_f32 v230, v140, v141\n"
  "v_mfma_f32_16x16x32_bf16 v[168:171], a[164:167], v[88:91], v[168:171]\n"
  "v_exp_f32 v142, v142\n"
  "v_mfma_f32_16x16x32_bf16 v[168:171], a[168:171], v[104:107], v[168:171]\n"
  "v_mov_b32 v75, v74\n"
  "s_nop 1\n"
  "v_permlane32_swap_b32 v74, v75\n"
  "v_mfma_f32_16x16x32_bf16 v[168:171], a[172:175], v[120:123], v[168:171]\n"
  "v_exp_f32 v143, v143\n"
  "v_mfma_f32_16x16x32_bf16 v[172:175], a[176:179], v[216:219], v[200:203]\n"
  "v_max_f32 v74, v74, v75\n"
  "v_mov_b32 v75, v74\n"
  "v_cvt_pk_bf16_f32 v231, v142, v143\n"
  "v_mfma_f32_16x16x32_bf16 v[172:175], a[180:183], v[88:91], v[172:175]\n"
  "v_exp_f32 v144, v144\n"
  "v_mfma_f32_16x16x32_bf16 v[172:175], a[184:187], v[104:107], v[172:175]\n"
  "v_permlane16_swap_b32 v74, v75\n"
  "v_max3_f32 v74, v74, v75, v71\n"
  "v_mfma_f32_16x16x32_bf16 v[172:175], a[188:191], v[120:123], v[172:175]\n"
  "v_sub_f32 v204, 0, v74\n"
  "v_sub_f32 v205, 0, v74\n"
  "v_sub_f32 v206, 0, v74\n"
  "v_sub_f32 v207, 0, v74\n"
  "s_nop 1\n"
  "v_mfma_f32_16x16x32_bf16 v[180:183], a[144:147], v[220:223], v[204:207]\n"
  "v_sub_f32 v176, v176, v74\n"
  "v_sub_f32 v177, v177, v74\n"
  "v_mfma_f32_16x16x32_bf16 v[180:183], a[148:151], v[92:95], v[180:183]\n"
  "v_exp_f32 v145, v145\n"
  "v_mfma_f32_16x16x32_bf16 v[180:183], a[152:155], v[108:111], v[180:183]\n"
  "v_sub_f32 v178, v178, v74\n"
  "v_sub_f32 v179, v179, v74\n"
  "v_cvt_pk_bf16_f32 v232, v144, v145\n"
  "v_mfma_f32_16x16x32_bf16 v[180:183], a[156:159], v[124:127], v[180:183]\n"
  "v_exp_f32 v146, v146\n"
  "v_mfma_f32_16x16x32_bf16 v[184:187], a[160:163], v[220:223], v[204:207]\n"
  "v_exp_f32 v147, v147\n"
  "v_mfma_f32_16x16x32_bf16 v[184:187], a[164:167], v[92:95], v[184:187]\n"
  "v_exp_f32 v148, v148\n"
  "v_cvt_pk_bf16_f32 v233, v146, v147\n"
  "v_mfma_f32_16x16x32_bf16 v[184:187], a[168:171], v[108:111], v[184:187]\n"
  "v_exp_f32 v149, v149\n"
  "v_mfma_f32_16x16x32_bf16 v[184:187], a[172:175], v[124:127], v[184:187]\n"
  "v_exp_f32 v150, v150\n"
  "v_cvt_pk_bf16_f32 v234, v148, v149\n"
  "v_mfma_f32_16x16x32_bf16 v[188:191], a[176:179], v[220:223], v[204:207]\n"
  "v_exp_f32 v151, v151\n"
  "v_mfma_f32_16x16x32_bf16 v[188:191], a[180:183], v[92:95], v[188:191]\n"
  "v_exp_f32 v152, v152\n"
  "v_cvt_pk_bf16_f32 v235, v150, v151\n"
  "v_mfma_f32_16x16x32_bf16 v[188:191], a[184:187], v[108:111], v[188:191]\n"
  "v_exp_f32 v153, v153\n"
  "v_mfma_f32_16x16x32_bf16 v[188:191], a[188:191], v[124:127], v[188:191]\n"
  "s_waitcnt lgkmcnt(0)\n"
  "s_barrier\n"
  "v_exp_f32 v154, v154\n"
  "v_cvt_pk_bf16_f32 v236, v152, v153\n"
  "v_mfma_f32_16x16x32_bf16 v[48:51], v[64:67], v[224:227], 0\n"
  "v_exp_f32 v155, v155\n"
  "v_mfma_f32_16x16x32_bf16 v[48:51], v[64:67], v[228:231], v[48:51]\n"
  "v_exp_f32 v156, v156\n"
  "v_cvt_pk_bf16_f32 v237, v154, v155\n"
  "v_mfma_f32_16x16x32_bf16 a[0:3], a[192:195], v[224:227], 0\n"
  "v_exp_f32 v157, v157\n"
  "v_mfma_f32_16x16x32_bf16 a[0:3], a[196:199], v[228:231], a[0:3]\n"
  "v_exp_f32 v158, v158\n"
  "v_cvt_pk_bf16_f32 v238, v156, v157\n"
  "v_mfma_f32_16x16x32_bf16 a[16:19], a[200:203], v[224:227], 0\n"
  "v_exp_f32 v159, v159\n"
  "v_mfma_f32_16x16x32_bf16 a[16:19], a[204:207], v[228:231], a[16:19]\n"
  "v_exp_f32 v160, v160\n"
  "v_cvt_pk_bf16_f32 v239, v158, v159\n"
  "v_mfma_f32_16x16x32_bf16 a[32:35], a[208:211], v[224:227], 0\n"
  "v_exp_f32 v161, v161\n"
  "v_mfma_f32_16x16x32_bf16 a[32:35], a[212:215], v[228:231], a[32:35]\n"
  "v_exp_f32 v162, v162\n"
  "v_cvt_pk_bf16_f32 v240, v160, v161\n"
  "v_mfma_f32_16x16x32_bf16 a[48:51], a[216:219], v[224:227], 0\n"
  "v_exp_f32 v163, v163\n"
  "v_mfma_f32_16x16x32_bf16 a[48:51], a[220:223], v[228:231], a[48:51]\n"
  "v_exp_f32 v164, v164\n"
  "v_cvt_pk_bf16_f32 v241, v162, v163\n"
  "v_mfma_f32_16x16x32_bf16 a[64:67], a[224:227], v[224:227], 0\n"
  "v_exp_f32 v165, v165\n"
  "v_mfma_f32_16x16x32_bf16 a[64:67], a[228:231], v[228:231], a[64:67]\n"
  "v_exp_f32 v166, v166\n"
  "v_cvt_pk_bf16_f32 v242, v164, v165\n"
  "v_mfma_f32_16x16x32_bf16 a[80:83], a[232:235], v[224:227], 0\n"
  "v_exp_f32 v167, v167\n"
  "v_mfma_f32_16x16x32_bf16 a[80:83], a[236:239], v[228:231], a[80:83]\n"
  "v_exp_f32 v168, v168\n"
  "v_cvt_pk_bf16_f32 v243, v166, v167\n"
  "v_mfma_f32_16x16x32_bf16 a[96:99], a[240:243], v[224:227], 0\n"
  "v_exp_f32 v169, v169\n"
  "v_mfma_f32_16x16x32_bf16 a[96:99], a[244:247], v[228:231], a[96:99]\n"
  "v_exp_f32 v170, v170\n"
  "v_cvt_pk_bf16_f32 v244, v168, v169\n"
  "v_mfma_f32_16x16x32_bf16 a[112:115], a[248:251], v[224:227], 0\n"
  "v_exp_f32 v171, v171\n"
  "v_mfma_f32_16x16x32_bf16 a[112:115], a[252:255], v[228:231], a[112:115]\n"
  "v_exp_f32 v172, v172\n"
  "v_cvt_pk_bf16_f32 v245, v170, v171\n"
  "v_mfma_f32_16x16x32_bf16 v[52:55], v[64:67], v[232:235], 0\n"
  "v_exp_f32 v173, v173\n"
  "v_mfma_f32_16x16x32_bf16 v[52:55], v[64:67], v[236:239], v[52:55]\n"
  "v_exp_f32 v174, v174\n"
  "v_cvt_pk_bf16_f32 v246, v172, v173\n"
  "v_mfma_f32_16x16x32_bf16 a[4:7], a[192:195], v[232:235], 0\n"
  "s_mul_i32 s28, s93, 7282\n"
  "s_lshr_b32 s28, s28, 16\n"
  "v_exp_f32 v175, v175\n"
  "v_mfma_f32_16x16x32_bf16 a[4:7], a[196:199], v[236:239], a[4:7]\n"
  "s_mul_i32 s29, s28, 9\n"
  "v_exp_f32 v176, v176\n"
  "v_cvt_pk_bf16_f32 v247, v174, v175\n"
  "v_mfma_f32_16x16x32_bf16 a[20:23], a[200:203], v[232:235], 0\n"
  "s_sub_u32 s29, s93, s29\n"
  "s_mul_i32 s62, s28, 36864\n"
  "v_exp_f32 v177, v177\n"
  "v_mfma_f32_16x16x32_bf16 a[20:23], a[204:207], v[236:239], a[20:23]\n"
  "s_add_u32 s62, s62, 4718592\n"
  "v_exp_f32 v178, v178\n"
  "v_cvt_pk_bf16_f32 v248, v176, v177\n"
  "v_mfma_f32_16x16x32_bf16 a[36:39], a[208:211], v[232:235], 0\n"
  "s_lshl_b32 s63, s29, 5\n"
  "s_add_u32 s62, s62, s63\n"
  "v_exp_f32 v179, v179\n"
  "v_mfma_f32_16x16x32_bf16 a[36:39], a[212:215], v[236:239], a[36:39]\n"
  "s_mul_i32 s63, s82, 288\n"
  "v_exp_f32 v180, v180\n"
  "v_cvt_pk_bf16_f32 v249, v178, v179\n"
  "v_mfma_f32_16x16x32_bf16 a[52:55], a[216:219], v[232:235], 0\n"
  "s_add_u32 s62, s62, s63\n"
  "v_exp_f32 v181, v181\n"
  "v_lshrrev_b32 v74, 1, v0\n"
  "v_mfma_f32_16x16x32_bf16 a[52:55], a[220:223], v[236:239], a[52:55]\n"
  "v_exp_f32 v182, v182\n"
  "v_cvt_pk_bf16_f32 v250, v180, v181\n"
  "v_mfma_f32_16x16x32_bf16 a[68:71], a[224:227], v[232:235], 0\n"
  "v_exp_f32 v183, v183\n"
  "v_mul_u32_u24 v74, 1152, v74\n"
  "v_mfma_f32_16x16x32_bf16 a[68:71], a[228:231], v[236:239], a[68:71]\n"
  "v_exp_f32 v184, v184\n"
  "v_cvt_pk_bf16_f32 v251, v182, v183\n"
  "v_mfma_f32_16x16x32_bf16 a[84:87], a[232:235], v[232:235], 0\n"
  "v_exp_f32 v185, v185\n"
  "v_and_b32 v75, 1, v0\n"
  "v_mfma_f32_16x16x32_bf16 a[84:87], a[236:239], v[236:239], a[84:87]\n"
  "v_exp_f32 v186, v186\n"
  "v_cvt_pk_bf16_f32 v252, v184, v185\n"
  "v_mfma_f32_16x16x32_bf16 a[100:103], a[240:243], v[232:235], 0\n"
  "v_exp_f32 v187, v187\n"
  "v_lshl_add_u32 v74, v75, 4, v74\n"
  "v_mfma_f32_16x16x32_bf16 a[100:103], a[244:247], v[236:239], a[100:103]\n"
  "v_exp_f32 v188, v188\n"
  "v_cvt_pk_bf16_f32 v253, v186, v187\n"
  "v_mfma_f32_16x16x32_bf16 a[116:119], a[248:251], v[232:235], 0\n"
  "s_lshr_b32 s63, s78, 2\n"
  "v_exp_f32 v189, v189\n"
  "v_lshlrev_b32 v75, 3, v0\n"
  "v_mfma_f32_16x16x32_bf16 a[116:119], a[252:255], v[236:239], a[116:119]\n"
  "s_add_u32 s63, s63, 36864\n"
  "v_exp_f32 v190, v190\n"
  "v_cvt_pk_bf16_f32 v254, v188, v189\n"
  "v_mfma_f32_16x16x32_bf16 v[56:59], v[64:67], v[240:243], 0\n"
  "ds_read_b128 a[128:131], v32 offset:0\n"
  "v_exp_f32 v191, v191\n"
  "v_mfma_f32_16x16x32_bf16 v[56:59], v[64:67], v[244:247], v[56:59]\n"
  "s_mul_i32 s28, s28, 9216\n"
  "s_lshl_b32 s29, s29, 10\n"
  "v_cvt_pk_bf16_f32 v255, v190, v191\n"
  "v_add_u32 v75, s63, v75\n"
  "v_mfma_f32_16x16x32_bf16 a[8:11], a[192:195], v[240:243], 0\n"
  "s_add_u32 s28, s28, s29\n"
  "s_mul_i32 s29, s82, 2506752\n"
  "s_add_u32 s28, s28, s29\n"
  "ds_read_b128 a[132:135], v33 offset:0\n"
  "v_mfma_f32_16x16x32_bf16 a[8:11], a[196:199], v[244:247], a[8:11]\n"
  "s_add_u32 s28, s28, 40370176\n"
  "s_cmp_eq_u32 s25, 0\n s_cselect_b32 s28, 0x7ff00000, s28\n"
  "v_mfma_f32_16x16x32_bf16 a[24:27], a[200:203], v[240:243], 0\n"
  "ds_read_b128 a[136:139], v34 offset:0\n"
  "buffer_load_dwordx4 v22, s[48:51], s79 offen offset:0 lds\n"
  "v_mfma_f32_16x16x32_bf16 a[24:27], a[204:207], v[244:247], a[24:27]\n"
  "v_mfma_f32_16x16x32_bf16 a[40:43], a[208:211], v[240:243], 0\n"
  "ds_read_b128 a[140:143], v35 offset:0\n"
  "v_mfma_f32_16x16x32_bf16 a[40:43], a[212:215], v[244:247], a[40:43]\n"
  "v_mfma_f32_16x16x32_bf16 a[56:59], a[216:219], v[240:243], 0\n"
  "ds_read_b128 a[144:147], v32 offset:4096\n"
  "buffer_load_dwordx4 v23, s[48:51], s79 offen offset:1024 lds\n"
  "v_mfma_f32_16x16x32_bf16 a[56:59], a[220:223], v[244:247], a[56:59]\n"
  "v_mfma_f32_16x16x32_bf16 a[72:75], a[224:227], v[240:243], 0\n"
  "ds_read_b128 a[148:151], v33 offset:4096\n"
  "v_mfma_f32_16x16x32_bf16 a[72:75], a[228:231], v[244:247], a[72:75]\n"
  "v_mfma_f32_16x16x32_bf16 a[88:91], a[232:235], v[240:243], 0\n"
  "ds_read_b128 a[152:155], v34 offset:4096\n"
  "buffer_load_dwordx4 v24, s[48:51], s79 offen offset:2048 lds\n"
  "v_mfma_f32_16x16x32_bf16 a[88:91], a[236:239], v[244:247], a[88:91]\n"
  "v_mfma_f32_16x16x32_bf16 a[104:107], a[240:243], v[240:243], 0\n"
  "ds_read_b128 a[156:159], v35 offset:4096\n"
  "v_mfma_f32_16x16x32_bf16 a[104:107], a[244:247], v[244:247], a[104:107]\n"
  "v_mfma_f32_16x16x32_bf16 a[120:123], a[248:251], v[240:243], 0\n"
  "ds_read_b128 a[160:163], v32 offset:8192\n"
  "buffer_load_dwordx4 v25, s[48:51], s79 offen offset:3072 lds\n"
  "v_mfma_f32_16x16x32_bf16 a[120:123], a[252:255], v[244:247], a[120:123]\n"
  "s_add_u32 m0, s78, 114688\n"
  "s_add_u32 s79, s77, 32768\n"
  "v_mfma_f32_16x16x32_bf16 v[60:63], v[64:67], v[248:251], 0\n"
  "ds_read_b128 a[164:167], v33 offset:8192\n"
  "v_mfma_f32_16x16x32_bf16 v[60:63], v[64:67], v[252:255], v[60:63]\n"
  "v_mfma_f32_16x16x32_bf16 a[12:15], a[192:195], v[248:251], 0\n"
  "ds_read_b128 a[168:171], v34 offset:8192\n"
  "buffer_load_dwordx4 v26, s[52:55], s79 offen offset:0 lds\n"
  "v_mfma_f32_16x16x32_bf16 a[12:15], a[196:199], v[252:255], a[12:15]\n"
  "v_mfma_f32_16x16x32_bf16 a[28:31], a[200:203], v[248:251], 0\n"
  "ds_read_b128 a[172:175], v35 offset:8192\n"
  "v_mfma_f32_16x16x32_bf16 a[28:31], a[204:207], v[252:255], a[28:31]\n"
  "v_mfma_f32_16x16x32_bf16 a[44:47], a[208:211], v[248:251], 0\n"
  "ds_read_b128 a[176:179], v32 offset:12288\n"
  "buffer_load_dwordx4 v27, s[52:55], s79 offen offset:1024 lds\n"
  "v_mfma_f32_16x16x32_bf16 a[44:47], a[212:215], v[252:255], a[44:47]\n"
  "v_mfma_f32_16x16x32_bf16 a[60:63], a[216:219], v[248:251], 0\n"
  "ds_read_b128 a[180:183], v33 offset:12288\n"
  "v_add_u32 v36, 65536, v36\n"
  "v_mfma_f32_16x16x32_bf16 a[60:63], a[220:223], v[252:255], a[60:63]\n"
  "v_add_u32 v37, 65536, v37\n"
  "v_add_u32 v38, 65536, v38\n"
  "v_mfma_f32_16x16x32_bf16 a[76:79], a[224:227], v[248:251], 0\n"
  "ds_read_b128 a[184:187], v34 offset:12288\n"
  "v_add_u32 v39, 65536, v39\n"
  "v_mfma_f32_16x16x32_bf16 a[76:79], a[228:231], v[252:255], a[76:79]\n"
  "v_add_u32 v40, 65536, v40\n"
  "v_add_u32 v41, 65536, v41\n"
  "v_mfma_f32_16x16x32_bf16 a[92:95], a[232:235], v[248:251], 0\n"
  "ds_read_b128 a[188:191], v35 offset:12288\n"
  "v_add_u32 v42, 65536, v42\n"
  "v_mfma_f32_16x16x32_bf16 a[92:95], a[236:239], v[252:255], a[92:95]\n"
  "v_add_u32 v43, 65536, v43\n"
  "v_mfma_f32_16x16x32_bf16 a[108:111], a[240:243], v[248:251], 0\n"
  "buffer_load_dwordx4 v26, s[52:55], s79 offen offset:2048 lds\n"
  "v_mfma_f32_16x16x32_bf16 a[108:111], a[244:247], v[252:255], a[108:111]\n"
  "v_mfma_f32_16x16x32_bf16 a[124:127], a[248:251], v[248:251], 0\n"
  "v_mfma_f32_16x16x32_bf16 a[124:127], a[252:255], v[252:255], a[124:127]\n"
  "s_waitcnt lgkmcnt(1)\n"
  "v_mfma_f32_16x16x32_bf16 v[128:131], a[128:131], v[208:211], v[192:195]\n"
  "ds_read_b64_tr_b16 a[192:193], v36 offset:16384\n"
  "buffer_load_dwordx4 v27, s[52:55], s79 offen offset:3072 lds\n"
  "v_mfma_f32_16x16x32_bf16 v[128:131], a[132:135], v[80:83], v[128:131]\n"
  "s_mov_b32 s40, s22\n"
  "s_and_b32 s41, s23, 0xffff\n"
  "ds_read_b64_tr_b16 a[194:195], v36 offset:20480\n"
  "v_mfma_f32_16x16x32_bf16 v[128:131], a[136:139], v[96:99], v[128:131]\n"
  "s_mov_b32 s42, s25\n"
  "s_mov_b32 m0, s63\n"
  "ds_read_b64_tr_b16 a[196:197], v36 offset:24576\n"
  "v_mfma_f32_16x16x32_bf16 v[128:131], a[140:143], v[112:115], v[128:131]\n"
  "ds_read_b64_tr_b16 a[198:199], v36 offset:28672\n"
  "v_mfma_f32_16x16x32_bf16 v[132:135], a[144:147], v[208:211], v[192:195]\n"
  "ds_read_b64_tr_b16 a[200:201], v37 offset:16384\n"
  "buffer_load_dwordx4 v74, s[40:43], s62 offen offset:0 nt lds\n"
  "v_mfma_f32_16x16x32_bf16 v[132:135], a[148:151], v[80:83], v[132:135]\n"
  "ds_read_b64_tr_b16 a[202:203], v37 offset:20480\n"
  "v_mov_b32 v76, s24\n"
  "v_mfma_f32_16x16x32_bf16 v[132:135], a[152:155], v[96:99], v[132:135]\n"
  "ds_read_b64_tr_b16 a[204:205], v37 offset:24576\n"
  "v_mfma_f32_16x16x32_bf16 v[132:135], a[156:159], v[112:115], v[132:135]\n"
  "v_exp_f32 v128, v128\n"
  "v_mfma_f32_16x16x32_bf16 v[136:139], a[160:163], v[208:211], v[192:195]\n"
  "ds_read_b64_tr_b16 a[206:207], v37 offset:28672\n"
  "buffer_store_dword v76, v30, s[36:39], s94 offen offset:0 sc1\n"
  "v_mfma_f32_16x16x32_bf16 v[136:139], a[164:167], v[80:83], v[136:139]\n"
  "s_add_u32 m0, s78, 131072\n"
  "v_exp_f32 v129, v129\n"
  "v_mfma_f32_16x16x32_bf16 v[136:139], a[168:171], v[96:99], v[136:139]\n"
  "s_add_u32 s79, s77, 49152\n"
  "ds_read_b64_tr_b16 a[208:209], v38 offset:16384\n"
  "v_cvt_pk_bf16_f32 v224, v128, v129\n"
  "v_mfma_f32_16x16x32_bf16 v[136:139], a[172:175], v[112:115], v[136:139]\n"
  "v_exp_f32 v130, v130\n"
  "v_mfma_f32_16x16x32_bf16 v[140:143], a[176:179], v[208:211], v[192:195]\n"
  "ds_read_b64_tr_b16 a[210:211], v38 offset:20480\n"
  "buffer_load_dwordx4 v22, s[48:51], s79 offen offset:0 lds\n"
  "v_mfma_f32_16x16x32_bf16 v[140:143], a[180:183], v[80:83], v[140:143]\n"
  "v_exp_f32 v131, v131\n"
  "v_mfma_f32_16x16x32_bf16 v[140:143], a[184:187], v[96:99], v[140:143]\n"
  "s_waitcnt lgkmcnt(2)\n"
  "ds_read_b64_tr_b16 a[212:213], v38 offset:24576\n"
  "v_cvt_pk_bf16_f32 v225, v130, v131\n"
  "v_mfma_f32_16x16x32_bf16 v[140:143], a[188:191], v[112:115], v[140:143]\n"
  "v_exp_f32 v132, v132\n"
  "v_mfma_f32_16x16x32_bf16 v[144:147], a[128:131], v[212:215], v[196:199]\n"
  "ds_read_b64_tr_b16 a[214:215], v38 offset:28672\n"
  "buffer_load_dwordx4 v23, s[48:51], s79 offen offset:1024 lds\n"
  "v_mfma_f32_16x16x32_bf16 v[144:147], a[132:135], v[84:87], v[144:147]\n"
  "v_exp_f32 v133, v133\n"
  "v_mfma_f32_16x16x32_bf16 v[144:147], a[136:139], v[100:103], v[144:147]\n"
  "ds_read_b64_tr_b16 a[216:217], v39 offset:16384\n"
  "v_cvt_pk_bf16_f32 v226, v132, v133\n"
  "v_mfma_f32_16x16x32_bf16 v[144:147], a[140:143], v[116:119], v[144:147]\n"
  "v_exp_f32 v134, v134\n"
  "v_mfma_f32_16x16x32_bf16 v[148:151], a[144:147], v[212:215], v[196:199]\n"
  "ds_read_b64_tr_b16 a[218:219], v39 offset:20480\n"
  "buffer_load_dwordx4 v24, s[48:51], s79 offen offset:2048 lds\n"
  "v_mfma_f32_16x16x32_bf16 v[148:151], a[148:151], v[84:87], v[148:151]\n"
  "v_exp_f32 v135, v135\n"
  "v_mfma_f32_16x16x32_bf16 v[148:151], a[152:155], v[100:103], v[148:151]\n"
  "ds_read_b64_tr_b16 a[220:221], v39 offset:24576\n"
  "v_cvt_pk_bf16_f32 v227, v134, v135\n"
  "v_mfma_f32_16x16x32_bf16 v[148:151], a[156:159], v[116:119], v[148:151]\n"
  "v_exp_f32 v136, v136\n"
  "v_mfma_f32_16x16x32_bf16 v[152:155], a[160:163], v[212:215], v[196:199]\n"
  "ds_read_b64_tr_b16 a[222:223], v39 offset:28672\n"
  "buffer_load_dwordx4 v25, s[48:51], s79 offen offset:3072 lds\n"
  "v_mfma_f32_16x16x32_bf16 v[152:155], a[164:167], v[84:87], v[152:155]\n"
  "s_add_u32 m0, s78, 147456\n"
  "v_exp_f32 v137, v137\n"
  "v_mfma_f32_16x16x32_bf16 v[152:155], a[168:171], v[100:103], v[152:155]\n"
  "s_add_u32 s79, s77, 49152\n"
  "ds_read_b64_tr_b16 a[224:225], v40 offset:16384\n"
  "v_cvt_pk_bf16_f32 v228, v136, v137\n"
  "v_mfma_f32_16x16x32_bf16 v[152:155], a[172:175], v[116:119], v[152:155]\n"
  "v_exp_f32 v138, v138\n"
  "v_mfma_f32_16x16x32_bf16 v[156:159], a[176:179], v[212:215], v[196:199]\n"
  "ds_read_b64_tr_b16 a[226:227], v40 offset:20480\n"
  "buffer_load_dwordx4 v26, s[52:55], s79 offen offset:0 lds\n"
  "v_mfma_f32_16x16x32_bf16 v[156:159], a[180:183], v[84:87], v[156:159]\n"
  "v_exp_f32 v139, v139\n"
  "v_mfma_f32_16x16x32_bf16 v[156:159], a[184:187], v[100:103], v[156:159]\n"
  "ds_read_b64_tr_b16 a[228:229], v40 offset:24576\n"
  "v_cvt_pk_bf16_f32 v229, v138, v139\n"
  "v_mfma_f32_16x16x32_bf16 v[156:159], a[188:191], v[116:119], v[156:159]\n"
  "v_exp_f32 v140, v140\n"
  "v_mfma_f32_16x16x32_bf16 v[160:163], a[128:131], v[216:219], v[200:203]\n"
  "ds_read_b64_tr_b16 a[230:231], v40 offset:28672\n"
  "buffer_load_dwordx4 v27, s[52:55], s79 offen offset:1024 lds\n"
  "v_mfma_f32_16x16x32_bf16 v[160:163], a[132:135], v[88:91], v[160:163]\n"
  "v_exp_f32 v141, v141\n"
  "v_mfma_f32_16x16x32_bf16 v[160:163], a[136:139], v[104:107], v[160:163]\n"
  "ds_read_b64_tr_b16 a[232:233], v41 offset:16384\n"
  "v_cvt_pk_bf16_f32 v230, v140, v141\n"
  "v_mfma_f32_16x16x32_bf16 v[160:163], a[140:143], v[120:123], v[160:163]\n"
  "v_exp_f32 v142, v142\n"
  "v_mfma_f32_16x16x32_bf16 v[164:167], a[144:147], v[216:219], v[200:203]\n"
  "ds_read_b64_tr_b16 a[234:235], v41 offset:20480\n"
  "buffer_load_dwordx4 v26, s[52:55], s79 offen offset:2048 lds\n"
  "v_mfma_f32_16x16x32_bf16 v[164:167], a[148:151], v[88:91], v[164:167]\n"
  "v_exp_f32 v143, v143\n"
  "v_mfma_f32_16x16x32_bf16 v[164:167], a[152:155], v[104:107], v[164:167]\n"
  "ds_read_b64_tr_b16 a[236:237], v41 offset:24576\n"
  "v_cvt_pk_bf16_f32 v231, v142, v143\n"
  "v_mfma_f32_16x16x32_bf16 v[164:167], a[156:159], v[120:123], v[164:167]\n"
  "v_exp_f32 v144, v144\n"
  "v_mfma_f32_16x16x32_bf16 v[168:171], a[160:163], v[216:219], v[200:203]\n"
  "ds_read_b64_tr_b16 a[238:239], v41 offset:28672\n"
  "buffer_load_dwordx4 v27, s[52:55], s79 offen offset:3072 lds\n"
  "v_mfma_f32_16x16x32_bf16 v[168:171], a[164:167], v[88:91], v[168:171]\n"
  "s_add_u32 m0, s78, 49152\n"
  "v_exp_f32 v145, v145\n"
  "v_mfma_f32_16x16x32_bf16 v[168:171], a[168:171], v[104:107], v[168:171]\n"
  "s_add_u32 s79, s77, 65536\n"
  "ds_read_b64_tr_b16 a[240:241], v42 offset:16384\n"
  "v_cvt_pk_bf16_f32 v232, v144, v145\n"
  "v_mfma_f32_16x16x32_bf16 v[168:171], a[172:175], v[120:123], v[168:171]\n"
  "v_exp_f32 v146, v146\n"
  "v_mfma_f32_16x16x32_bf16 v[172:175], a[176:179], v[216:219], v[200:203]\n"
  "ds_read_b64_tr_b16 a[242:243], v42 offset:20480\n"
  "buffer_load_dwordx4 v22, s[48:51], s79 offen offset:0 lds\n"
  "v_mfma_f32_16x16x32_bf16 v[172:175], a[180:183], v[88:91], v[172:175]\n"
  "v_exp_f32 v147, v147\n"
  "v_mfma_f32_16x16x32_bf16 v[172:175], a[184:187], v[104:107], v[172:175]\n"
  "ds_read_b64_tr_b16 a[244:245], v42 offset:24576\n"
  "v_cvt_pk_bf16_f32 v233, v146, v147\n"
  "v_mfma_f32_16x16x32_bf16 v[172:175], a[188:191], v[120:123], v[172:175]\n"
  "v_exp_f32 v148, v148\n"
  "v_mfma_f32_16x16x32_bf16 v[176:179], a[128:131], v[220:223], v[204:207]\n"
  "ds_read_b64_tr_b16 a[246:247], v42 offset:28672\n"
  "buffer_load_dwordx4 v23, s[48:51], s79 offen offset:1024 lds\n"
  "v_mfma_f32_16x16x32_bf16 v[176:179], a[132:135], v[92:95], v[176:179]\n"
  "v_exp_f32 v149, v149\n"
  "v_mfma_f32_16x16x32_bf16 v[176:179], a[136:139], v[108:111], v[176:179]\n"
  "ds_read_b64_tr_b16 a[248:249], v43 offset:16384\n"
  "v_cvt_pk_bf16_f32 v234, v148, v149\n"
  "v_mfma_f32_16x16x32_bf16 v[176:179], a[140:143], v[124:127], v[176:179]\n"
  "v_exp_f32 v150, v150\n"
  "v_mfma_f32_16x16x32_bf16 v[180:183], a[144:147], v[220:223], v[204:207]\n"
  "ds_read_b64_tr_b16 a[250:251], v43 offset:20480\n"
  "buffer_load_dwordx4 v24, s[48:51], s79 offen offset:2048 lds\n"
  "v_mfma_f32_16x16x32_bf16 v[180:183], a[148:151], v[92:95], v[180:183]\n"
  "v_exp_f32 v151, v151\n"
  "v_mfma_f32_16x16x32_bf16 v[180:183], a[152:155], v[108:111], v[180:183]\n"
  "ds_read_b64_tr_b16 a[252:253], v43 offset:24576\n"
  "v_cvt_pk_bf16_f32 v235, v150, v151\n"
  "v_mfma_f32_16x16x32_bf16 v[180:183], a[156:159], v[124:127], v[180:183]\n"
  "v_exp_f32 v152, v152\n"
  "v_mfma_f32_16x16x32_bf16 v[184:187], a[160:163], v[220:223], v[204:207]\n"
  "ds_read_b64_tr_b16 a[254:255], v43 offset:28672\n"
  "buffer_load_dwordx4 v25, s[48:51], s79 offen offset:3072 lds\n"
  "v_mfma_f32_16x16x32_bf16 v[184:187], a[164:167], v[92:95], v[184:187]\n"
  "v_exp_f32 v153, v153\n"
  "v_mfma_f32_16x16x32_bf16 v[184:187], a[168:171], v[108:111], v[184:187]\n"
  "v_exp_f32 v154, v154\n"
  "v_mfma_f32_16x16x32_bf16 v[184:187], a[172:175], v[124:127], v[184:187]\n"
  "v_exp_f32 v155, v155\n"
  "v_mfma_f32_16x16x32_bf16 v[188:191], a[176:179], v[220:223], v[204:207]\n"
  "v_exp_f32 v156, v156\n"
  "v_mfma_f32_16x16x32_bf16 v[188:191], a[180:183], v[92:95], v[188:191]\n"
  "v_cvt_pk_bf16_f32 v236, v152, v153\n"
  "v_cvt_pk_bf16_f32 v237, v154, v155\n"
  "v_mfma_f32_16x16x32_bf16 v[188:191], a[184:187], v[108:111], v[188:191]\n"
  "v_exp_f32 v157, v157\n"
  "v_mfma_f32_16x16x32_bf16 v[188:191], a[188:191], v[124:127], v[188:191]\n"
  "s_waitcnt vmcnt(14) lgkmcnt(0)\n"
  "s_barrier\n"
  "v_exp_f32 v158, v158\n"
  "v_mfma_f32_16x16x32_bf16 v[48:51], v[64:67], v[224:227], v[48:51]\n"
  "s_barrier\n"
  "v_exp_f32 v159, v159\n"
  "v_mfma_f32_16x16x32_bf16 v[48:51], v[64:67], v[228:231], v[48:51]\n"
  "ds_read_b128 a[128:131], v32 offset:32768\n"
  "v_cvt_pk_bf16_f32 v238, v156, v157\n"
  "v_mfma_f32_16x16x32_bf16 a[0:3], a[192:195], v[224:227], a[0:3]\n"
  "ds_read_b64_tr_b16 v[8:9], v28 offset:0\n"
  "v_mfma_f32_16x16x32_bf16 a[0:3], a[196:199], v[228:231], a[0:3]\n"
  "ds_read_b64_tr_b16 v[10:11], v28 offset:18432\n"
  "v_exp_f32 v160, v160\n"
  "v_mfma_f32_16x16x32_bf16 a[16:19], a[200:203], v[224:227], a[16:19]\n"
  "v_exp_f32 v161, v161\n"
  "v_mfma_f32_16x16x32_bf16 a[16:19], a[204:207], v[228:231], a[16:19]\n"
  "v_exp_f32 v162, v162\n"
  "v_mfma_f32_16x16x32_bf16 a[32:35], a[208:211], v[224:227], a[32:35]\n"
  "s_add_u32 s81, s88, 0\n"
  "v_cvt_pk_bf16_f32 v239, v158, v159\n"
  "v_cvt_pk_bf16_f32 v240, v160, v161\n"
  "v_mfma_f32_16x16x32_bf16 a[32:35], a[212:215], v[228:231], a[32:35]\n"
  "v_exp_f32 v163, v163\n"
  "v_mfma_f32_16x16x32_bf16 a[48:51], a[216:219], v[224:227], a[48:51]\n"
  "v_exp_f32 v164, v164\n"
  "v_mfma_f32_16x16x32_bf16 a[48:51], a[220:223], v[228:231], a[48:51]\n"
  "v_exp_f32 v165, v165\n"
  "v_mfma_f32_16x16x32_bf16 a[64:67], a[224:227], v[224:227], a[64:67]\n"
  "ds_read_b128 a[132:135], v33 offset:32768\n"
  "v_cvt_pk_bf16_f32 v241, v162, v163\n"
  "v_mfma_f32_16x16x32_bf16 a[64:67], a[228:231], v[228:231], a[64:67]\n"
  "s_waitcnt lgkmcnt(1)\n"
  "buffer_store_dwordx4 v[8:11], v1, s[36:39], s81 offen offset:0 sc1\n"
  "v_mfma_f32_16x16x32_bf16 a[80:83], a[232:235], v[224:227], a[80:83]\n"
  "ds_read_b64_tr_b16 v[16:17], v28 offset:32\n"
  "v_exp_f32 v166, v166\n"
  "v_mfma_f32_16x16x32_bf16 a[80:83], a[236:239], v[228:231], a[80:83]\n"
  "ds_read_b64_tr_b16 v[18:19], v28 offset:18464\n"
  "v_exp_f32 v167, v167\n"
  "v_mfma_f32_16x16x32_bf16 a[96:99], a[240:243], v[224:227], a[96:99]\n"
  "v_exp_f32 v168, v168\n"
  "v_mfma_f32_16x16x32_bf16 a[96:99], a[244:247], v[228:231], a[96:99]\n"
  "v_cvt_pk_bf16_f32 v242, v164, v165\n"
  "v_cvt_pk_bf16_f32 v243, v166, v167\n"
  "v_mfma_f32_16x16x32_bf16 a[112:115], a[248:251], v[224:227], a[112:115]\n"
  "v_exp_f32 v169, v169\n"
  "v_mfma_f32_16x16x32_bf16 a[112:115], a[252:255], v[228:231], a[112:115]\n"
  "v_exp_f32 v170, v170\n"
  "v_mfma_f32_16x16x32_bf16 v[52:55], v[64:67], v[232:235], v[52:55]\n"
  "v_exp_f32 v171, v171\n"
  "v_mfma_f32_16x16x32_bf16 v[52:55], v[64:67], v[236:239], v[52:55]\n"
  "ds_read_b128 a[136:139], v34 offset:32768\n"
  "v_cvt_pk_bf16_f32 v244, v168, v169\n"
  "v_mfma_f32_16x16x32_bf16 a[4:7], a[192:195], v[232:235], a[4:7]\n"
  "v_mfma_f32_16x16x32_bf16 a[4:7], a[196:199], v[236:239], a[4:7]\n"
  "v_exp_f32 v172, v172\n"
  "v_mfma_f32_16x16x32_bf16 a[20:23], a[200:203], v[232:235], a[20:23]\n"
  "v_exp_f32 v173, v173\n"
  "v_mfma_f32_16x16x32_bf16 a[20:23], a[204:207], v[236:239], a[20:23]\n"
  "v_exp_f32 v174, v174\n"
  "v_mfma_f32_16x16x32_bf16 a[36:39], a[208:211], v[232:235], a[36:39]\n"
  "v_cvt_pk_bf16_f32 v245, v170, v171\n"
  "v_cvt_pk_bf16_f32 v246, v172, v173\n"
  "v_mfma_f32_16x16x32_bf16 a[36:39], a[212:215], v[236:239], a[36:39]\n"
  "v_exp_f32 v175, v175\n"
  "v_mfma_f32_16x16x32_bf16 a[52:55], a[216:219], v[232:235], a[52:55]\n"
  "v_exp_f32 v176, v176\n"
  "v_mfma_f32_16x16x32_bf16 a[52:55], a[220:223], v[236:239], a[52:55]\n"
  "v_exp_f32 v177, v177\n"
  "v_mfma_f32_16x16x32_bf16 a[68:71], a[224:227], v[232:235], a[68:71]\n"
  "ds_read_b128 a[140:143], v35 offset:32768\n"
  "v_cvt_pk_bf16_f32 v247, v174, v175\n"
  "v_mfma_f32_16x16x32_bf16 a[68:71], a[228:231], v[236:239], a[68:71]\n"
  "s_waitcnt lgkmcnt(1)\n"
  "buffer_store_dwordx4 v[16:19], v1, s[36:39], s81 offen offset:1024 sc1\n"
  "v_mfma_f32_16x16x32_bf16 a[84:87], a[232:235], v[232:235], a[84:87]\n"
  "ds_read_b64_tr_b16 v[44:45], v28 offset:64\n"
  "v_exp_f32 v178, v178\n"
  "v_mfma_f32_16x16x32_bf16 a[84:87], a[236:239], v[236:239], a[84:87]\n"
  "ds_read_b64_tr_b16 v[46:47], v28 offset:18496\n"
  "v_exp_f32 v179, v179\n"
  "v_mfma_f32_16x16x32_bf16 a[100:103], a[240:243], v[232:235], a[100:103]\n"
  "v_exp_f32 v180, v180\n"
  "v_mfma_f32_16x16x32_bf16 a[100:103], a[244:247], v[236:239], a[100:103]\n"
  "v_cvt_pk_bf16_f32 v248, v176, v177\n"
  "v_cvt_pk_bf16_f32 v249, v178, v179\n"
  "v_mfma_f32_16x16x32_bf16 a[116:119], a[248:251], v[232:235], a[116:119]\n"
  "v_exp_f32 v181, v181\n"
  "v_mfma_f32_16x16x32_bf16 a[116:119], a[252:255], v[236:239], a[116:119]\n"
  "v_exp_f32 v182, v182\n"
  "v_mfma_f32_16x16x32_bf16 v[56:59], v[64:67], v[240:243], v[56:59]\n"
  "v_exp_f32 v183, v183\n"
  "v_mfma_f32_16x16x32_bf16 v[56:59], v[64:67], v[244:247], v[56:59]\n"
  "ds_read_b128 a[144:147], v32 offset:36864\n"
  "v_cvt_pk_bf16_f32 v250, v180, v181\n"
  "v_mfma_f32_16x16x32_bf16 a[8:11], a[192:195], v[240:243], a[8:11]\n"
  "v_mfma_f32_16x16x32_bf16 a[8:11], a[196:199], v[244:247], a[8:11]\n"
  "v_exp_f32 v184, v184\n"
  "v_mfma_f32_16x16x32_bf16 a[24:27], a[200:203], v[240:243], a[24:27]\n"
  "v_exp_f32 v185, v185\n"
  "v_mfma_f32_16x16x32_bf16 a[24:27], a[204:207], v[244:247], a[24:27]\n"
  "v_exp_f32 v186, v186\n"
  "v_mfma_f32_16x16x32_bf16 a[40:43], a[208:211], v[240:243], a[40:43]\n"
  "v_cvt_pk_bf16_f32 v251, v182, v183\n"
  "v_cvt_pk_bf16_f32 v252, v184, v185\n"
  "v_mfma_f32_16x16x32_bf16 a[40:43], a[212:215], v[244:247], a[40:43]\n"
  "v_exp_f32 v187, v187\n"
  "v_mfma_f32_16x16x32_bf16 a[56:59], a[216:219], v[240:243], a[56:59]\n"
  "v_exp_f32 v188, v188\n"
  "v_mfma_f32_16x16x32_bf16 a[56:59], a[220:223], v[244:247], a[56:59]\n"
  "v_exp_f32 v189, v189\n"
  "v_mfma_f32_16x16x32_bf16 a[72:75], a[224:227], v[240:243], a[72:75]\n"
  "ds_read_b128 a[148:151], v33 offset:36864\n"
  "v_cvt_pk_bf16_f32 v253, v186, v187\n"
  "v_mfma_f32_16x16x32_bf16 a[72:75], a[228:231], v[244:247], a[72:75]\n"
  "s_waitcnt lgkmcnt(1)\n"
  "buffer_store_dwordx4 v[44:47], v1, s[36:39], s81 offen offset:2048 sc1\n"
  "v_mfma_f32_16x16x32_bf16 a[88:91], a[232:235], v[240:243], a[88:91]\n"
  "v_exp_f32 v190, v190\n"
  "v_mfma_f32_16x16x32_bf16 a[88:91], a[236:239], v[244:247], a[88:91]\n"
  "v_exp_f32 v191, v191\n"
  "v_mfma_f32_16x16x32_bf16 a[104:107], a[240:243], v[240:243], a[104:107]\n"
  "v_cvt_pk_bf16_f32 v254, v188, v189\n"
  "v_cvt_pk_bf16_f32 v255, v190, v191\n"
  "v_mfma_f32_16x16x32_bf16 a[104:107], a[244:247], v[244:247], a[104:107]\n"
  "ds_read_b128 a[152:155], v34 offset:36864\n"
  "v_mfma_f32_16x16x32_bf16 a[120:123], a[248:251], v[240:243], a[120:123]\n"
  "v_mfma_f32_16x16x32_bf16 a[120:123], a[252:255], v[244:247], a[120:123]\n"
  "ds_read_b128 a[156:159], v35 offset:36864\n"
  "v_mfma_f32_16x16x32_bf16 v[60:63], v[64:67], v[248:251], v[60:63]\n"
  "v_mfma_f32_16x16x32_bf16 v[60:63], v[64:67], v[252:255], v[60:63]\n"
  "ds_read_b128 a[160:163], v32 offset:40960\n"
  "v_mfma_f32_16x16x32_bf16 a[12:15], a[192:195], v[248:251], a[12:15]\n"
  "v_mfma_f32_16x16x32_bf16 a[12:15], a[196:199], v[252:255], a[12:15]\n"
  "ds_read_b128 a[164:167], v33 offset:40960\n"
  "v_mfma_f32_16x16x32_bf16 a[28:31], a[200:203], v[248:251], a[28:31]\n"
  "v_mfma_f32_16x16x32_bf16 a[28:31], a[204:207], v[252:255], a[28:31]\n"
  "s_add_u32 m0, s78, 65536\n"
  "s_add_u32 s79, s77, 65536\n"
  "ds_read_b128 a[168:171], v34 offset:40960\n"
  "v_mfma_f32_16x16x32_bf16 a[44:47], a[208:211], v[248:251], a[44:47]\n"
  "buffer_load_dwordx4 v26, s[52:55], s79 offen offset:0 lds\n"
  "v_mfma_f32_16x16x32_bf16 a[44:47], a[212:215], v[252:255], a[44:47]\n"
  "ds_read_b128 a[172:175], v35 offset:40960\n"
  "v_mfma_f32_16x16x32_bf16 a[60:63], a[216:219], v[248:251], a[60:63]\n"
  "v_mfma_f32_16x16x32_bf16 a[60:63], a[220:223], v[252:255], a[60:63]\n"
  "ds_read_b128 a[176:179], v32 offset:45056\n"
  "v_mfma_f32_16x16x32_bf16 a[76:79], a[224:227], v[248:251], a[76:79]\n"
  "buffer_load_dwordx4 v27, s[52:55], s79 offen offset:1024 lds\n"
  "v_mfma_f32_16x16x32_bf16 a[76:79], a[228:231], v[252:255], a[76:79]\n"
  "ds_read_b128 a[180:183], v33 offset:45056\n"
  "v_mfma_f32_16x16x32_bf16 a[92:95], a[232:235], v[248:251], a[92:95]\n"
  "ds_read_b64_tr_b16 v[8:9], v28 offset:96\n"
  "v_mfma_f32_16x16x32_bf16 a[92:95], a[236:239], v[252:255], a[92:95]\n"
  "ds_read_b128 a[184:187], v34 offset:45056\n"
  "v_mfma_f32_16x16x32_bf16 a[108:111], a[240:243], v[248:251], a[108:111]\n"
  "buffer_load_dwordx4 v26, s[52:55], s79 offen offset:2048 lds\n"
  "v_mfma_f32_16x16x32_bf16 a[108:111], a[244:247], v[252:255], a[108:111]\n"
  "ds_read_b128 a[188:191], v35 offset:45056\n"
  "v_mfma_f32_16x16x32_bf16 a[124:127], a[248:251], v[248:251], a[124:127]\n"
  "ds_read_b64_tr_b16 v[10:11], v28 offset:18528\n"
  "v_mfma_f32_16x16x32_bf16 a[124:127], a[252:255], v[252:255], a[124:127]\n"
  "v_mfma_f32_16x16x32_bf16 v[128:131], a[128:131], v[208:211], v[192:195]\n"
  "ds_read_b64_tr_b16 a[192:193], v36 offset:49152\n"
  "buffer_load_dwordx4 v27, s[52:55], s79 offen offset:3072 lds\n"
  "v_mfma_f32_16x16x32_bf16 v[128:131], a[132:135], v[80:83], v[128:131]\n"
  "ds_read_b64_tr_b16 a[194:195], v36 offset:53248\n"
  "v_mfma_f32_16x16x32_bf16 v[128:131], a[136:139], v[96:99], v[128:131]\n"
  "ds_read_b64_tr_b16 a[196:197], v36 offset:57344\n"
  "v_add_u32 v32, 65536, v32\n"
  "v_mfma_f32_16x16x32_bf16 v[128:131], a[140:143], v[112:115], v[128:131]\n"
  "ds_read_b64_tr_b16 a[198:199], v36 offset:61440\n"
  "v_add_u32 v33, 65536, v33\n"
  "v_mfma_f32_16x16x32_bf16 v[132:135], a[144:147], v[208:211], v[192:195]\n"
  "s_waitcnt lgkmcnt(4)\n"
  "ds_read_b64_tr_b16 a[200:201], v37 offset:49152\n"
  "v_add_u32 v34, 65536, v34\n"
  "v_mfma_f32_16x16x32_bf16 v[132:135], a[148:151], v[80:83], v[132:135]\n"
  "ds_read_b64_tr_b16 a[202:203], v37 offset:53248\n"
  "v_add_u32 v35, 65536, v35\n"
  "v_mfma_f32_16x16x32_bf16 v[132:135], a[152:155], v[96:99], v[132:135]\n"
  "ds_read_b64_tr_b16 a[204:205], v37 offset:57344\n"
  "buffer_store_dwordx4 v[8:11], v1, s[36:39], s81 offen offset:3072 sc1\n"
  "v_mfma_f32_16x16x32_bf16 v[132:135], a[156:159], v[112:115], v[132:135]\n"
  "ds_read_b64_tr_b16 v[16:17], v28 offset:128\n"
  "v_exp_f32 v128, v128\n"
  "v_mfma_f32_16x16x32_bf16 v[136:139], a[160:163], v[208:211], v[192:195]\n"
  "ds_read_b64_tr_b16 a[206:207], v37 offset:61440\n"
  "v_mfma_f32_16x16x32_bf16 v[136:139], a[164:167], v[80:83], v[136:139]\n"
  "ds_read_b64_tr_b16 v[18:19], v28 offset:18560\n"
  "v_exp_f32 v129, v129\n"
  "v_mfma_f32_16x16x32_bf16 v[136:139], a[168:171], v[96:99], v[136:139]\n"
  "ds_read_b64_tr_b16 a[208:209], v38 offset:49152\n"
  "v_cvt_pk_bf16_f32 v224, v128, v129\n"
  "v_mfma_f32_16x16x32_bf16 v[136:139], a[172:175], v[112:115], v[136:139]\n"
  "v_exp_f32 v130, v130\n"
  "v_mfma_f32_16x16x32_bf16 v[140:143], a[176:179], v[208:211], v[192:195]\n"
  "ds_read_b64_tr_b16 a[210:211], v38 offset:53248\n"
  "v_mfma_f32_16x16x32_bf16 v[140:143], a[180:183], v[80:83], v[140:143]\n"
  "v_exp_f32 v131, v131\n"
  "v_mfma_f32_16x16x32_bf16 v[140:143], a[184:187], v[96:99], v[140:143]\n"
  "ds_read_b64_tr_b16 a[212:213], v38 offset:57344\n"
  "v_cvt_pk_bf16_f32 v225, v130, v131\n"
  "v_mfma_f32_16x16x32_bf16 v[140:143], a[188:191], v[112:115], v[140:143]\n"
  "v_exp_f32 v132, v132\n"
  "v_mfma_f32_16x16x32_bf16 v[144:147], a[128:131], v[212:215], v[196:199]\n"
  "ds_read_b64_tr_b16 a[214:215], v38 offset:61440\n"
  "v_mfma_f32_16x16x32_bf16 v[144:147], a[132:135], v[84:87], v[144:147]\n"
  "v_exp_f32 v133, v133\n"
  "v_mfma_f32_16x16x32_bf16 v[144:147], a[136:139], v[100:103], v[144:147]\n"
  "s_add_u32 s81, s88, 4096\n"
  "ds_read_b64_tr_b16 a[216:217], v39 offset:49152\n"
  "v_cvt_pk_bf16_f32 v226, v132, v133\n"
  "v_mfma_f32_16x16x32_bf16 v[144:147], a[140:143], v[116:119], v[144:147]\n"
  "v_exp_f32 v134, v134\n"
  "v_mfma_f32_16x16x32_bf16 v[148:151], a[144:147], v[212:215], v[196:199]\n"
  "ds_read_b64_tr_b16 a[218:219], v39 offset:53248\n"
  "s_waitcnt lgkmcnt(3)\n"
  "buffer_store_dwordx4 v[16:19], v1, s[36:39], s81 offen offset:0 sc1\n"
  "v_mfma_f32_16x16x32_bf16 v[148:151], a[148:151], v[84:87], v[148:151]\n"
  "ds_read_b64_tr_b16 v[44:45], v28 offset:160\n"
  "v_exp_f32 v135, v135\n"
  "v_mfma_f32_16x16x32_bf16 v[148:151], a[152:155], v[100:103], v[148:151]\n"
  "ds_read_b64_tr_b16 a[220:221], v39 offset:57344\n"
  "v_cvt_pk_bf16_f32 v227, v134, v135\n"
  "v_mfma_f32_16x16x32_bf16 v[148:151], a[156:159], v[116:119], v[148:151]\n"
  "ds_read_b64_tr_b16 v[46:47], v28 offset:18592\n"
  "v_exp_f32 v136, v136\n"
  "v_mfma_f32_16x16x32_bf16 v[152:155], a[160:163], v[212:215], v[196:199]\n"
  "ds_read_b64_tr_b16 a[222:223], v39 offset:61440\n"
  "v_mfma_f32_16x16x32_bf16 v[152:155], a[164:167], v[84:87], v[152:155]\n"
  "v_exp_f32 v137, v137\n"
  "v_mfma_f32_16x16x32_bf16 v[152:155], a[168:171], v[100:103], v[152:155]\n"
  "ds_read_b64_tr_b16 a[224:225], v40 offset:49152\n"
  "v_cvt_pk_bf16_f32 v228, v136, v137\n"
  "v_mfma_f32_16x16x32_bf16 v[152:155], a[172:175], v[116:119], v[152:155]\n"
  "v_exp_f32 v138, v138\n"
  "v_mfma_f32_16x16x32_bf16 v[156:159], a[176:179], v[212:215], v[196:199]\n"
  "ds_read_b64_tr_b16 a[226:227], v40 offset:53248\n"
  "v_mfma_f32_16x16x32_bf16 v[156:159], a[180:183], v[84:87], v[156:159]\n"
  "v_exp_f32 v139, v139\n"
  "v_mfma_f32_16x16x32_bf16 v[156:159], a[184:187], v[100:103], v[156:159]\n"
  "ds_read_b64_tr_b16 a[228:229], v40 offset:57344\n"
  "v_cvt_pk_bf16_f32 v229, v138, v139\n"
  "v_mfma_f32_16x16x32_bf16 v[156:159], a[188:191], v[116:119], v[156:159]\n"
  "v_exp_f32 v140, v140\n"
  "v_mfma_f32_16x16x32_bf16 v[160:163], a[128:131], v[216:219], v[200:203]\n"
  "ds_read_b64_tr_b16 a[230:231], v40 offset:61440\n"
  "s_waitcnt lgkmcnt(3)\n"
  "buffer_store_dwordx4 v[44:47], v1, s[36:39], s81 offen offset:1024 sc1\n"
  "v_mfma_f32_16x16x32_bf16 v[160:163], a[132:135], v[88:91], v[160:163]\n"
  "v_exp_f32 v141, v141\n"
  "v_mfma_f32_16x16x32_bf16 v[160:163], a[136:139], v[104:107], v[160:163]\n"
  "ds_read_b64_tr_b16 a[232:233], v41 offset:49152\n"
  "v_cvt_pk_bf16_f32 v230, v140, v141\n"
  "v_mfma_f32_16x16x32_bf16 v[160:163], a[140:143], v[120:123], v[160:163]\n"
  "v_exp_f32 v142, v142\n"
  "v_mfma_f32_16x16x32_bf16 v[164:167], a[144:147], v[216:219], v[200:203]\n"
  "ds_read_b64_tr_b16 a[234:235], v41 offset:53248\n"
  "v_mfma_f32_16x16x32_bf16 v[164:167], a[148:151], v[88:91], v[164:167]\n"
  "v_exp_f32 v143, v143\n"
  "v_mfma_f32_16x16x32_bf16 v[164:167], a[152:155], v[104:107], v[164:167]\n"
  "ds_read_b64_tr_b16 a[236:237], v41 offset:57344\n"
  "v_cvt_pk_bf16_f32 v231, v142, v143\n"
  "v_mfma_f32_16x16x32_bf16 v[164:167], a[156:159], v[120:123], v[164:167]\n"
  "v_exp_f32 v144, v144\n"
  "v_mfma_f32_16x16x32_bf16 v[168:171], a[160:163], v[216:219], v[200:203]\n"
  "ds_read_b64_tr_b16 a[238:239], v41 offset:61440\n"
  "v_mfma_f32_16x16x32_bf16 v[168:171], a[164:167], v[88:91], v[168:171]\n"
  "v_exp_f32 v145, v145\n"
  "v_mfma_f32_16x16x32_bf16 v[168:171], a[168:171], v[104:107], v[168:171]\n"
  "ds_read_b64_tr_b16 a[240:241], v42 offset:49152\n"
  "v_cvt_pk_bf16_f32 v232, v144, v145\n"
  "v_mfma_f32_16x16x32_bf16 v[168:171], a[172:175], v[120:123], v[168:171]\n"
  "v_exp_f32 v146, v146\n"
  "v_mfma_f32_16x16x32_bf16 v[172:175], a[176:179], v[216:219], v[200:203]\n"
  "s_add_u32 m0, s78, 81920\n"
  "s_add_u32 s79, s77, 81920\n"
  "ds_read_b64_tr_b16 a[242:243], v42 offset:53248\n"
  "v_mfma_f32_16x16x32_bf16 v[172:175], a[180:183], v[88:91], v[172:175]\n"
  "v_exp_f32 v147, v147\n"
  "v_mfma_f32_16x16x32_bf16 v[172:175], a[184:187], v[104:107], v[172:175]\n"
  "ds_read_b64_tr_b16 a[244:245], v42 offset:57344\n"
  "v_cvt_pk_bf16_f32 v233, v146, v147\n"
  "v_mfma_f32_16x16x32_bf16 v[172:175], a[188:191], v[120:123], v[172:175]\n"
  "v_exp_f32 v148, v148\n"
  "v_mfma_f32_16x16x32_bf16 v[176:179], a[128:131], v[220:223], v[204:207]\n"
  "ds_read_b64_tr_b16 a[246:247], v42 offset:61440\n"
  "buffer_load_dwordx4 v22, s[48:51], s79 offen offset:0 lds\n"
  "v_mfma_f32_16x16x32_bf16 v[176:179], a[132:135], v[92:95], v[176:179]\n"
  "v_exp_f32 v149, v149\n"
  "v_mfma_f32_16x16x32_bf16 v[176:179], a[136:139], v[108:111], v[176:179]\n"
  "ds_read_b64_tr_b16 a[248:249], v43 offset:49152\n"
  "v_cvt_pk_bf16_f32 v234, v148, v149\n"
  "v_mfma_f32_16x16x32_bf16 v[176:179], a[140:143], v[124:127], v[176:179]\n"
  "v_exp_f32 v150, v150\n"
  "v_mfma_f32_16x16x32_bf16 v[180:183], a[144:147], v[220:223], v[204:207]\n"
  "ds_read_b64_tr_b16 a[250:251], v43 offset:53248\n"
  "buffer_load_dwordx4 v23, s[48:51], s79 offen offset:1024 lds\n"
  "v_mfma_f32_16x16x32_bf16 v[180:183], a[148:151], v[92:95], v[180:183]\n"
  "v_exp_f32 v151, v151\n"
  "v_mfma_f32_16x16x32_bf16 v[180:183], a[152:155], v[108:111], v[180:183]\n"
  "ds_read_b64_tr_b16 a[252:253], v43 offset:57344\n"
  "v_cvt_pk_bf16_f32 v235, v150, v151\n"
  "v_mfma_f32_16x16x32_bf16 v[180:183], a[156:159], v[124:127], v[180:183]\n"
  "v_exp_f32 v152, v152\n"
  "v_mfma_f32_16x16x32_bf16 v[184:187], a[160:163], v[220:223], v[204:207]\n"
  "ds_read_b64_tr_b16 a[254:255], v43 offset:61440\n"
  "buffer_load_dwordx4 v24, s[48:51], s79 offen offset:2048 lds\n"
  "v_mfma_f32_16x16x32_bf16 v[184:187], a[164:167], v[92:95], v[184:187]\n"
  "v_exp_f32 v153, v153\n"
  "v_mfma_f32_16x16x32_bf16 v[184:187], a[168:171], v[108:111], v[184:187]\n"
  "ds_read_b64_tr_b16 v[8:9], v28 offset:192\n"
  "v_exp_f32 v154, v154\n"
  "v_mfma_f32_16x16x32_bf16 v[184:187], a[172:175], v[124:127], v[184:187]\n"
  "ds_read_b64_tr_b16 v[10:11], v28 offset:18624\n"
  "v_exp_f32 v155, v155\n"
  "v_mfma_f32_16x16x32_bf16 v[188:191], a[176:179], v[220:223], v[204:207]\n"
  "v_exp_f32 v156, v156\n"
  "v_mfma_f32_16x16x32_bf16 v[188:191], a[180:183], v[92:95], v[188:191]\n"
  "v_cvt_pk_bf16_f32 v236, v152, v153\n"
  "v_cvt_pk_bf16_f32 v237, v154, v155\n"
  "v_mfma_f32_16x16x32_bf16 v[188:191], a[184:187], v[108:111], v[188:191]\n"
  "v_exp_f32 v157, v157\n"
  "v_mfma_f32_16x16x32_bf16 v[188:191], a[188:191], v[124:127], v[188:191]\n"
  "s_waitcnt vmcnt(17) lgkmcnt(0)\n"
  "s_barrier\n"
  "v_exp_f32 v158, v158\n"
  "v_mfma_f32_16x16x32_bf16 v[48:51], v[64:67], v[224:227], v[48:51]\n"
  "v_exp_f32 v159, v159\n"
  "v_mfma_f32_16x16x32_bf16 v[48:51], v[64:67], v[228:231], v[48:51]\n"
  "ds_read_b128 a[128:131], v32 offset:0\n"
  "v_cvt_pk_bf16_f32 v238, v156, v157\n"
  "v_mfma_f32_16x16x32_bf16 a[0:3], a[192:195], v[224:227], a[0:3]\n"
  "buffer_load_dwordx4 v25, s[48:51], s79 offen offset:3072 lds\n"
  "v_mfma_f32_16x16x32_bf16 a[0:3], a[196:199], v[228:231], a[0:3]\n"
  "v_exp_f32 v160, v160\n"
  "v_mfma_f32_16x16x32_bf16 a[16:19], a[200:203], v[224:227], a[16:19]\n"
  "v_exp_f32 v161, v161\n"
  "v_mfma_f32_16x16x32_bf16 a[16:19], a[204:207], v[228:231], a[16:19]\n"
  "v_exp_f32 v162, v162\n"
  "v_mfma_f32_16x16x32_bf16 a[32:35], a[208:211], v[224:227], a[32:35]\n"
  "v_cvt_pk_bf16_f32 v239, v158, v159\n"
  "v_cvt_pk_bf16_f32 v240, v160, v161\n"
  "v_mfma_f32_16x16x32_bf16 a[32:35], a[212:215], v[228:231], a[32:35]\n"
  "v_exp_f32 v163, v163\n"
  "v_mfma_f32_16x16x32_bf16 a[48:51], a[216:219], v[224:227], a[48:51]\n"
  "v_exp_f32 v164, v164\n"
  "v_mfma_f32_16x16x32_bf16 a[48:51], a[220:223], v[228:231], a[48:51]\n"
  "v_exp_f32 v165, v165\n"
  "v_mfma_f32_16x16x32_bf16 a[64:67], a[224:227], v[224:227], a[64:67]\n"
  "ds_read_b128 a[132:135], v33 offset:0\n"
  "v_cvt_pk_bf16_f32 v241, v162, v163\n"
  "v_mfma_f32_16x16x32_bf16 a[64:67], a[228:231], v[228:231], a[64:67]\n"
  "buffer_store_dwordx4 v[8:11], v1, s[36:39], s81 offen offset:2048 sc1\n"
  "v_mfma_f32_16x16x32_bf16 a[80:83], a[232:235], v[224:227], a[80:83]\n"
  "ds_read_b64_tr_b16 v[16:17], v28 offset:224\n"
  "v_exp_f32 v166, v166\n"
  "v_mfma_f32_16x16x32_bf16 a[80:83], a[236:239], v[228:231], a[80:83]\n"
  "ds_read_b64_tr_b16 v[18:19], v28 offset:18656\n"
  "v_exp_f32 v167, v167\n"
  "v_mfma_f32_16x16x32_bf16 a[96:99], a[240:243], v[224:227], a[96:99]\n"
  "v_exp_f32 v168, v168\n"
  "v_mfma_f32_16x16x32_bf16 a[96:99], a[244:247], v[228:231], a[96:99]\n"
  "v_cvt_pk_bf16_f32 v242, v164, v165\n"
  "v_cvt_pk_bf16_f32 v243, v166, v167\n"
  "v_mfma_f32_16x16x32_bf16 a[112:115], a[248:251], v[224:227], a[112:115]\n"
  "v_exp_f32 v169, v169\n"
  "v_mfma_f32_16x16x32_bf16 a[112:115], a[252:255], v[228:231], a[112:115]\n"
  "v_exp_f32 v170, v170\n"
  "v_mfma_f32_16x16x32_bf16 v[52:55], v[64:67], v[232:235], v[52:55]\n"
  "v_exp_f32 v171, v171\n"
  "v_mfma_f32_16x16x32_bf16 v[52:55], v[64:67], v[236:239], v[52:55]\n"
  "ds_read_b128 a[136:139], v34 offset:0\n"
  "v_cvt_pk_bf16_f32 v244, v168, v169\n"
  "v_mfma_f32_16x16x32_bf16 a[4:7], a[192:195], v[232:235], a[4:7]\n"
  "v_mfma_f32_16x16x32_bf16 a[4:7], a[196:199], v[236:239], a[4:7]\n"
  "v_exp_f32 v172, v172\n"
  "v_mfma_f32_16x16x32_bf16 a[20:23], a[200:203], v[232:235], a[20:23]\n"
  "v_exp_f32 v173, v173\n"
  "v_mfma_f32_16x16x32_bf16 a[20:23], a[204:207], v[236:239], a[20:23]\n"
  "v_exp_f32 v174, v174\n"
  "v_mfma_f32_16x16x32_bf16 a[36:39], a[208:211], v[232:235], a[36:39]\n"
  "v_cvt_pk_bf16_f32 v245, v170, v171\n"
  "v_cvt_pk_bf16_f32 v246, v172, v173\n"
  "v_mfma_f32_16x16x32_bf16 a[36:39], a[212:215], v[236:239], a[36:39]\n"
  "v_exp_f32 v175, v175\n"
  "v_mfma_f32_16x16x32_bf16 a[52:55], a[216:219], v[232:235], a[52:55]\n"
  "v_exp_f32 v176, v176\n"
  "v_mfma_f32_16x16x32_bf16 a[52:55], a[220:223], v[236:239], a[52:55]\n"
  "v_exp_f32 v177, v177\n"
  "v_mfma_f32_16x16x32_bf16 a[68:71], a[224:227], v[232:235], a[68:71]\n"
  "ds_read_b128 a[140:143], v35 offset:0\n"
  "v_cvt_pk_bf16_f32 v247, v174, v175\n"
  "v_mfma_f32_16x16x32_bf16 a[68:71], a[228:231], v[236:239], a[68:71]\n"
  "s_waitcnt lgkmcnt(1)\n"
  "buffer_store_dwordx4 v[16:19], v1, s[36:39], s81 offen offset:3072 sc1\n"
  "v_mfma_f32_16x16x32_bf16 a[84:87], a[232:235], v[232:235], a[84:87]\n"
  "ds_read_b64_tr_b16 v[44:45], v28 offset:256\n"
  "v_exp_f32 v178, v178\n"
  "v_mfma_f32_16x16x32_bf16 a[84:87], a[236:239], v[236:239], a[84:87]\n"
  "ds_read_b64_tr_b16 v[46:47], v28 offset:18688\n"
  "v_exp_f32 v179, v179\n"
  "v_mfma_f32_16x16x32_bf16 a[100:103], a[240:243], v[232:235], a[100:103]\n"
  "v_exp_f32 v180, v180\n"
  "v_mfma_f32_16x16x32_bf16 a[100:103], a[244:247], v[236:239], a[100:103]\n"
  "v_cvt_pk_bf16_f32 v248, v176, v177\n"
  "v_cvt_pk_bf16_f32 v249, v178, v179\n"
  "v_mfma_f32_16x16x32_bf16 a[116:119], a[248:251], v[232:235], a[116:119]\n"
  "v_exp_f32 v181, v181\n"
  "v_mfma_f32_16x16x32_bf16 a[116:119], a[252:255], v[236:239], a[116:119]\n"
  "v_exp_f32 v182, v182\n"
  "v_mfma_f32_16x16x32_bf16 v[56:59], v[64:67], v[240:243], v[56:59]\n"
  "v_exp_f32 v183, v183\n"
  "v_mfma_f32_16x16x32_bf16 v[56:59], v[64:67], v[244:247], v[56:59]\n"
  "ds_read_b128 a[144:147], v32 offset:4096\n"
  "v_cvt_pk_bf16_f32 v250, v180, v181\n"
  "v_mfma_f32_16x16x32_bf16 a[8:11], a[192:195], v[240:243], a[8:11]\n"
  "v_mfma_f32_16x16x32_bf16 a[8:11], a[196:199], v[244:247], a[8:11]\n"
  "v_exp_f32 v184, v184\n"
  "v_mfma_f32_16x16x32_bf16 a[24:27], a[200:203], v[240:243], a[24:27]\n"
  "v_exp_f32 v185, v185\n"
  "v_mfma_f32_16x16x32_bf16 a[24:27], a[204:207], v[244:247], a[24:27]\n"
  "s_add_u32 s81, s88, 8192\n"
  "v_exp_f32 v186, v186\n"
  "v_mfma_f32_16x16x32_bf16 a[40:43], a[208:211], v[240:243], a[40:43]\n"
  "v_cvt_pk_bf16_f32 v251, v182, v183\n"
  "v_cvt_pk_bf16_f32 v252, v184, v185\n"
  "v_mfma_f32_16x16x32_bf16 a[40:43], a[212:215], v[244:247], a[40:43]\n"
  "v_exp_f32 v187, v187\n"
  "v_mfma_f32_16x16x32_bf16 a[56:59], a[216:219], v[240:243], a[56:59]\n"
  "v_exp_f32 v188, v188\n"
  "v_mfma_f32_16x16x32_bf16 a[56:59], a[220:223], v[244:247], a[56:59]\n"
  "v_exp_f32 v189, v189\n"
  "v_mfma_f32_16x16x32_bf16 a[72:75], a[224:227], v[240:243], a[72:75]\n"
  "ds_read_b128 a[148:151], v33 offset:4096\n"
  "v_cvt_pk_bf16_f32 v253, v186, v187\n"
  "v_mfma_f32_16x16x32_bf16 a[72:75], a[228:231], v[244:247], a[72:75]\n"
  "s_waitcnt lgkmcnt(1)\n"
  "buffer_store_dwordx4 v[44:47], v1, s[36:39], s81 offen offset:0 sc1\n"
  "v_mfma_f32_16x16x32_bf16 a[88:91], a[232:235], v[240:243], a[88:91]\n"
  "ds_read_b64_tr_b16 v[8:9], v75 offset:0\n"
  "v_exp_f32 v190, v190\n"
  "v_mfma_f32_16x16x32_bf16 a[88:91], a[236:239], v[244:247], a[88:91]\n"
  "ds_read_b64_tr_b16 v[10:11], v75 offset:512\n"
  "v_exp_f32 v191, v191\n"
  "v_mfma_f32_16x16x32_bf16 a[104:107], a[240:243], v[240:243], a[104:107]\n"
  "v_cvt_pk_bf16_f32 v254, v188, v189\n"
  "v_cvt_pk_bf16_f32 v255, v190, v191\n"
  "v_mfma_f32_16x16x32_bf16 a[104:107], a[244:247], v[244:247], a[104:107]\n"
  "ds_read_b128 a[152:155], v34 offset:4096\n"
  "v_mov_b32 v73, s24\n"
  "v_mfma_f32_16x16x32_bf16 a[120:123], a[248:251], v[240:243], a[120:123]\n"
  "v_mov_b32 v76, -1\n"
  "v_mov_b32 v77, -1\n"
  "v_mfma_f32_16x16x32_bf16 a[120:123], a[252:255], v[244:247], a[120:123]\n"
  "ds_read_b128 a[156:159], v35 offset:4096\n"
  "v_mov_b32 v78, -1\n"
  "v_mfma_f32_16x16x32_bf16 v[60:63], v[64:67], v[248:251], v[60:63]\n"
  "v_mov_b32 v79, -1\n"
  "v_mfma_f32_16x16x32_bf16 v[60:63], v[64:67], v[252:255], v[60:63]\n"
  "ds_read_b128 a[160:163], v32 offset:8192\n"
  "v_mfma_f32_16x16x32_bf16 a[12:15], a[192:195], v[248:251], a[12:15]\n"
  "v_mfma_f32_16x16x32_bf16 a[12:15], a[196:199], v[252:255], a[12:15]\n"
  "ds_read_b128 a[164:167], v33 offset:8192\n"
  "v_mfma_f32_16x16x32_bf16 a[28:31], a[200:203], v[248:251], a[28:31]\n"
  "v_mfma_f32_16x16x32_bf16 a[28:31], a[204:207], v[252:255], a[28:31]\n"
  "ds_read_b128 a[168:171], v34 offset:8192\n"
  "s_waitcnt lgkmcnt(4)\n"
  "buffer_store_dwordx4 v[8:11], v1, s[36:39], s28 offen offset:0 sc1\n"
  "v_mfma_f32_16x16x32_bf16 a[44:47], a[208:211], v[248:251], a[44:47]\n"
  "v_mfma_f32_16x16x32_bf16 a[44:47], a[212:215], v[252:255], a[44:47]\n"
  "s_bfe_u32 s68, s93, 131073\n"
  "ds_read_b128 a[172:175], v35 offset:8192\n"
  "v_mfma_f32_16x16x32_bf16 a[60:63], a[216:219], v[248:251], a[60:63]\n"
  "s_and_b32 s28, s93, 1\n"
  "v_add_u32 v36, 65536, v36\n"
  "v_add_u32 v37, 65536, v37\n"
  "v_mfma_f32_16x16x32_bf16 a[60:63], a[220:223], v[252:255], a[60:63]\n"
  "s_lshl_b32 s28, s28, 3\n"
  "ds_read_b128 a[176:179], v32 offset:12288\n"
  "v_add_u32 v38, 65536, v38\n"
  "v_mfma_f32_16x16x32_bf16 a[76:79], a[224:227], v[248:251], a[76:79]\n"
  "s_lshr_b32 s62, s93, 5\n"
  "v_add_u32 v39, 65536, v39\n"
  "v_add_u32 v40, 65536, v40\n"
  "v_mfma_f32_16x16x32_bf16 a[76:79], a[228:231], v[252:255], a[76:79]\n"
  "s_add_u32 s28, s28, s62\n"
  "ds_read_b128 a[180:183], v33 offset:12288\n"
  "v_add_u32 v41, 65536, v41\n"
  "v_mfma_f32_16x16x32_bf16 a[92:95], a[232:235], v[248:251], a[92:95]\n"
  "s_bfe_u32 s29, s93, 131075\n"
  "v_add_u32 v42, 65536, v42\n"
  "v_add_u32 v43, 65536, v43\n"
  "v_mfma_f32_16x16x32_bf16 a[92:95], a[236:239], v[252:255], a[92:95]\n"
  "s_mul_i32 s64, s29, 2506752\n"
  "ds_read_b128 a[184:187], v34 offset:12288\n"
  "v_mfma_f32_16x16x32_bf16 a[108:111], a[240:243], v[248:251], a[108:111]\n"
  "s_add_u32 s64, s64, 39190528\n"
  "v_mfma_f32_16x16x32_bf16 a[108:111], a[244:247], v[252:255], a[108:111]\n"
  "s_add_u32 s64, s64, s67\n"
  "ds_read_b128 a[188:191], v35 offset:12288\n"
  "v_mfma_f32_16x16x32_bf16 a[124:127], a[248:251], v[248:251], a[124:127]\n"
  "s_mul_i32 s62, s68, 331776\n"
  "v_mfma_f32_16x16x32_bf16 a[124:127], a[252:255], v[252:255], a[124:127]\n"
  "s_add_u32 s64, s64, s62\n"
  "s_add_u32 m0, s78, 98304\n"
  "s_add_u32 s79, s77, 81920\n"
  "v_mfma_f32_16x16x32_bf16 v[128:131], a[128:131], v[208:211], v[192:195]\n"
  "s_lshl_b32 s62, s29, 10\n"
  "ds_read_b64_tr_b16 a[192:193], v36 offset:16384\n"
  "buffer_load_dwordx4 v26, s[52:55], s79 offen offset:0 lds\n"
  "v_mfma_f32_16x16x32_bf16 v[128:131], a[132:135], v[80:83], v[128:131]\n"
  "s_add_u32 s69, s62, 73211904\n"
  "ds_read_b64_tr_b16 a[194:195], v36 offset:20480\n"
  "v_mfma_f32_16x16x32_bf16 v[128:131], a[136:139], v[96:99], v[128:131]\n"
  "s_lshl_b32 s62, s28, 2\n"
  "ds_read_b64_tr_b16 a[196:197], v36 offset:24576\n"
  "v_add_u32 v32, 0xfffe0000, v32\n"
  "v_mfma_f32_16x16x32_bf16 v[128:131], a[140:143], v[112:115], v[128:131]\n"
  "s_add_u32 s62, s62, s82\n"
  "ds_read_b64_tr_b16 a[198:199], v36 offset:28672\n"
  "v_add_u32 v33, 0xfffe0000, v33\n"
  "v_mfma_f32_16x16x32_bf16 v[132:135], a[144:147], v[208:211], v[192:195]\n"
  "s_lshl_b32 s62, s62, 1\n"
  "ds_read_b64_tr_b16 a[200:201], v37 offset:16384\n"
  "v_add_u32 v34, 0xfffe0000, v34\n"
  "v_mfma_f32_16x16x32_bf16 v[132:135], a[148:151], v[80:83], v[132:135]\n"
  "s_mul_i32 s60, s62, 147456\n"
  "ds_read_b64_tr_b16 a[202:203], v37 offset:20480\n"
  "v_add_u32 v35, 0xfffe0000, v35\n"
  "v_mfma_f32_16x16x32_bf16 v[132:135], a[152:155], v[96:99], v[132:135]\n"
  "s_mul_i32 s63, s68, 36864\n"
  "ds_read_b64_tr_b16 a[204:205], v37 offset:24576\n"
  "buffer_load_dwordx4 v27, s[52:55], s79 offen offset:1024 lds\n"
  "s_waitcnt lgkmcnt(3)\n"
  "v_mfma_f32_16x16x32_bf16 v[132:135], a[156:159], v[112:115], v[132:135]\n"
  "s_add_u32 s60, s60, s63\n"
  "v_exp_f32 v128, v128\n"
  "v_mfma_f32_16x16x32_bf16 v[136:139], a[160:163], v[208:211], v[192:195]\n"
  "s_add_u32 s60, s60, 2359296\n"
  "ds_read_b64_tr_b16 a[206:207], v37 offset:28672\n"
  "v_mfma_f32_16x16x32_bf16 v[136:139], a[164:167], v[80:83], v[136:139]\n"
  "s_add_u32 s61, s60, 147456\n"
  "v_exp_f32 v129, v129\n"
  "v_mfma_f32_16x16x32_bf16 v[136:139], a[168:171], v[96:99], v[136:139]\n"
  "s_mul_i32 s95, s62, 18432\n"
  "ds_read_b64_tr_b16 a[208:209], v38 offset:16384\n"
  "v_cvt_pk_bf16_f32 v224, v128, v129\n"
  "v_mfma_f32_16x16x32_bf16 v[136:139], a[172:175], v[112:115], v[136:139]\n"
  "s_mul_i32 s63, s29, 288\n"
  "v_exp_f32 v130, v130\n"
  "v_mfma_f32_16x16x32_bf16 v[140:143], a[176:179], v[208:211], v[192:195]\n"
  "s_add_u32 s95, s95, s63\n"
  "ds_read_b64_tr_b16 a[210:211], v38 offset:20480\n"
  "buffer_load_dwordx4 v26, s[52:55], s79 offen offset:2048 lds\n"
  "v_mfma_f32_16x16x32_bf16 v[140:143], a[180:183], v[80:83], v[140:143]\n"
  "s_lshl_b32 s63, s28, 2\n"
  "v_exp_f32 v131, v131\n"
  "v_mfma_f32_16x16x32_bf16 v[140:143], a[184:187], v[96:99], v[140:143]\n"
  "s_add_u32 s63, s63, s29\n"
  "ds_read_b64_tr_b16 a[212:213], v38 offset:24576\n"
  "v_cvt_pk_bf16_f32 v225, v130, v131\n"
  "v_mfma_f32_16x16x32_bf16 v[140:143], a[188:191], v[112:115], v[140:143]\n"
  "s_lshl_b32 s63, s63, 6\n"
  "v_exp_f32 v132, v132\n"
  "v_mfma_f32_16x16x32_bf16 v[144:147], a[128:131], v[212:215], v[196:199]\n"
  "s_add_u32 s63, s63, s82\n"
  "ds_read_b64_tr_b16 a[214:215], v38 offset:28672\n"
  "buffer_load_dwordx4 v27, s[52:55], s79 offen offset:3072 lds\n"
  "v_mfma_f32_16x16x32_bf16 v[144:147], a[132:135], v[84:87], v[144:147]\n"
  "s_lshl_b32 s58, s68, 4\n"
  "v_exp_f32 v133, v133\n"
  "v_mfma_f32_16x16x32_bf16 v[144:147], a[136:139], v[100:103], v[144:147]\n"
  "s_add_u32 s58, s58, s63\n"
  "ds_read_b64_tr_b16 a[216:217], v39 offset:16384\n"
  "v_cvt_pk_bf16_f32 v226, v132, v133\n"
  "v_mfma_f32_16x16x32_bf16 v[144:147], a[140:143], v[116:119], v[144:147]\n"
  "s_mul_i32 s58, s58, 3072\n"
  "v_exp_f32 v134, v134\n"
  "v_mfma_f32_16x16x32_bf16 v[148:151], a[144:147], v[212:215], v[196:199]\n"
  "s_add_u32 s58, s58, 60620800\n"
  "s_add_u32 m0, s78, 114688\n"
  "ds_read_b64_tr_b16 a[218:219], v39 offset:20480\n"
  "v_mfma_f32_16x16x32_bf16 v[148:151], a[148:151], v[84:87], v[148:151]\n"
  "s_lshl_b32 s59, s29, 10\n"
  "v_exp_f32 v135, v135\n"
  "v_mfma_f32_16x16x32_bf16 v[148:151], a[152:155], v[100:103], v[148:151]\n"
  "s_lshl_b32 s62, s28, 3\n"
  "ds_read_b64_tr_b16 a[220:221], v39 offset:24576\n"
  "v_cvt_pk_bf16_f32 v227, v134, v135\n"
  "v_mfma_f32_16x16x32_bf16 v[148:151], a[156:159], v[116:119], v[148:151]\n"
  "s_add_u32 s62, s62, s68\n"
  "v_exp_f32 v136, v136\n"
  "v_mfma_f32_16x16x32_bf16 v[152:155], a[160:163], v[212:215], v[196:199]\n"
  "s_lshl_b32 s62, s62, 6\n"
  "s_add_u32 s79, s77, 98304\n"
  "ds_read_b64_tr_b16 a[222:223], v39 offset:28672\n"
  "v_mfma_f32_16x16x32_bf16 v[152:155], a[164:167], v[84:87], v[152:155]\n"
  "s_add_u32 s65, s62, 73203712\n"
  "v_exp_f32 v137, v137\n"
  "v_mfma_f32_16x16x32_bf16 v[152:155], a[168:171], v[100:103], v[152:155]\n"
  "s_add_u32 s62, s68, 0\n"
  "ds_read_b64_tr_b16 a[224:225], v40 offset:16384\n"
  "v_cvt_pk_bf16_f32 v228, v136, v137\n"
  "v_mfma_f32_16x16x32_bf16 v[152:155], a[172:175], v[116:119], v[152:155]\n"
  "s_and_b32 s62, s62, 3\n"
  "v_exp_f32 v138, v138\n"
  "v_mfma_f32_16x16x32_bf16 v[156:159], a[176:179], v[212:215], v[196:199]\n"
  "s_lshl_b32 s62, s62, 11\n"
  "ds_read_b64_tr_b16 a[226:227], v40 offset:20480\n"
  "buffer_load_dwordx4 v22, s[48:51], s79 offen offset:0 lds\n"
  "v_mfma_f32_16x16x32_bf16 v[156:159], a[180:183], v[84:87], v[156:159]\n"
  "s_add_u32 s70, s62, s64\n"
  "v_exp_f32 v139, v139\n"
  "v_mfma_f32_16x16x32_bf16 v[156:159], a[184:187], v[100:103], v[156:159]\n"
  "s_add_u32 s62, s68, 1\n"
  "ds_read_b64_tr_b16 a[228:229], v40 offset:24576\n"
  "v_cvt_pk_bf16_f32 v229, v138, v139\n"
  "v_mfma_f32_16x16x32_bf16 v[156:159], a[188:191], v[116:119], v[156:159]\n"
  "s_and_b32 s62, s62, 3\n"
  "v_exp_f32 v140, v140\n"
  "v_mfma_f32_16x16x32_bf16 v[160:163], a[128:131], v[216:219], v[200:203]\n"
  "s_lshl_b32 s62, s62, 11\n"
  "ds_read_b64_tr_b16 a[230:231], v40 offset:28672\n"
  "buffer_load_dwordx4 v23, s[48:51], s79 offen offset:1024 lds\n"
  "v_mfma_f32_16x16x32_bf16 v[160:163], a[132:135], v[88:91], v[160:163]\n"
  "s_add_u32 s71, s62, s64\n"
  "v_exp_f32 v141, v141\n"
  "v_mfma_f32_16x16x32_bf16 v[160:163], a[136:139], v[104:107], v[160:163]\n"
  "s_add_u32 s62, s68, 2\n"
  "ds_read_b64_tr_b16 a[232:233], v41 offset:16384\n"
  "v_cvt_pk_bf16_f32 v230, v140, v141\n"
  "v_mfma_f32_16x16x32_bf16 v[160:163], a[140:143], v[120:123], v[160:163]\n"
  "s_and_b32 s62, s62, 3\n"
  "v_exp_f32 v142, v142\n"
  "v_mfma_f32_16x16x32_bf16 v[164:167], a[144:147], v[216:219], v[200:203]\n"
  "s_lshl_b32 s62, s62, 11\n"
  "ds_read_b64_tr_b16 a[234:235], v41 offset:20480\n"
  "buffer_load_dwordx4 v24, s[48:51], s79 offen offset:2048 lds\n"
  "v_mfma_f32_16x16x32_bf16 v[164:167], a[148:151], v[88:91], v[164:167]\n"
  "s_add_u32 s72, s62, s64\n"
  "v_exp_f32 v143, v143\n"
  "v_mfma_f32_16x16x32_bf16 v[164:167], a[152:155], v[104:107], v[164:167]\n"
  "s_add_u32 s62, s68, 3\n"
  "ds_read_b64_tr_b16 a[236:237], v41 offset:24576\n"
  "v_cvt_pk_bf16_f32 v231, v142, v143\n"
  "v_mfma_f32_16x16x32_bf16 v[164:167], a[156:159], v[120:123], v[164:167]\n"
  "s_and_b32 s62, s62, 3\n"
  "v_exp_f32 v144, v144\n"
  "v_mfma_f32_16x16x32_bf16 v[168:171], a[160:163], v[216:219], v[200:203]\n"
  "s_lshl_b32 s62, s62, 11\n"
  "ds_read_b64_tr_b16 a[238:239], v41 offset:28672\n"
  "buffer_load_dwordx4 v25, s[48:51], s79 offen offset:3072 lds\n"
  "v_mfma_f32_16x16x32_bf16 v[168:171], a[164:167], v[88:91], v[168:171]\n"
  "s_add_u32 s35, s62, s64\n"
  "v_exp_f32 v145, v145\n"
  "v_mfma_f32_16x16x32_bf16 v[168:171], a[168:171], v[104:107], v[168:171]\n"
  "s_mov_b32 s44, s89\n"
  "ds_read_b64_tr_b16 a[240:241], v42 offset:16384\n"
  "v_cvt_pk_bf16_f32 v232, v144, v145\n"
  "v_mfma_f32_16x16x32_bf16 v[168:171], a[172:175], v[120:123], v[168:171]\n"
  "s_and_b32 s45, s90, 0xffff\n"
  "s_waitcnt vmcnt(8)\n"
  "v_exp_f32 v146, v146\n"
  "v_mfma_f32_16x16x32_bf16 v[172:175], a[176:179], v[216:219], v[200:203]\n"
  "s_mov_b32 s46, 2359296\n"
  "ds_read_b64_tr_b16 a[242:243], v42 offset:20480\n"
  "v_mfma_f32_16x16x32_bf16 v[172:175], a[180:183], v[88:91], v[172:175]\n"
  "s_mov_b32 s47, 0x20000\n"
  "v_exp_f32 v147, v147\n"
  "v_mfma_f32_16x16x32_bf16 v[172:175], a[184:187], v[104:107], v[172:175]\n"
  "ds_read_b64_tr_b16 a[244:245], v42 offset:24576\n"
  "v_cvt_pk_bf16_f32 v233, v146, v147\n"
  "v_mfma_f32_16x16x32_bf16 v[172:175], a[188:191], v[120:123], v[172:175]\n"
  "v_exp_f32 v148, v148\n"
  "v_mfma_f32_16x16x32_bf16 v[176:179], a[128:131], v[220:223], v[204:207]\n"
  "ds_read_b64_tr_b16 a[246:247], v42 offset:28672\n"
  "buffer_store_dword v73, v30, s[36:39], s96 offen offset:0 sc1\n"
  "v_mfma_f32_16x16x32_bf16 v[176:179], a[132:135], v[92:95], v[176:179]\n"
  "v_exp_f32 v149, v149\n"
  "v_mfma_f32_16x16x32_bf16 v[176:179], a[136:139], v[108:111], v[176:179]\n"
  "ds_read_b64_tr_b16 a[248:249], v43 offset:16384\n"
  "v_cvt_pk_bf16_f32 v234, v148, v149\n"
  "v_mfma_f32_16x16x32_bf16 v[176:179], a[140:143], v[124:127], v[176:179]\n"
  "v_exp_f32 v150, v150\n"
  "v_mfma_f32_16x16x32_bf16 v[180:183], a[144:147], v[220:223], v[204:207]\n"
  "ds_read_b64_tr_b16 a[250:251], v43 offset:20480\n"
  "v_mfma_f32_16x16x32_bf16 v[180:183], a[148:151], v[92:95], v[180:183]\n"
  "v_exp_f32 v151, v151\n"
  "v_mfma_f32_16x16x32_bf16 v[180:183], a[152:155], v[108:111], v[180:183]\n"
  "ds_read_b64_tr_b16 a[252:253], v43 offset:24576\n"
  "v_cvt_pk_bf16_f32 v235, v150, v151\n"
  "v_mfma_f32_16x16x32_bf16 v[180:183], a[156:159], v[124:127], v[180:183]\n"
  "v_exp_f32 v152, v152\n"
  "v_mfma_f32_16x16x32_bf16 v[184:187], a[160:163], v[220:223], v[204:207]\n"
  "ds_read_b64_tr_b16 a[254:255], v43 offset:28672\n"
  "v_mfma_f32_16x16x32_bf16 v[184:187], a[164:167], v[92:95], v[184:187]\n"
  "v_exp_f32 v153, v153\n"
  "v_mfma_f32_16x16x32_bf16 v[184:187], a[168:171], v[108:111], v[184:187]\n"
  "s_add_u32 m0, s78, 131072\n"
  "v_exp_f32 v154, v154\n"
  "v_mfma_f32_16x16x32_bf16 v[184:187], a[172:175], v[124:127], v[184:187]\n"
  "s_add_u32 s79, s77, 98304\n"
  "v_exp_f32 v155, v155\n"
  "v_mfma_f32_16x16x32_bf16 v[188:191], a[176:179], v[220:223], v[204:207]\n"
  "v_exp_f32 v156, v156\n"
  "v_mfma_f32_16x16x32_bf16 v[188:191], a[180:183], v[92:95], v[188:191]\n"
  "v_cvt_pk_bf16_f32 v236, v152, v153\n"
  "v_cvt_pk_bf16_f32 v237, v154, v155\n"
  "v_mfma_f32_16x16x32_bf16 v[188:191], a[184:187], v[108:111], v[188:191]\n"
  "v_exp_f32 v157, v157\n"
  "v_mfma_f32_16x16x32_bf16 v[188:191], a[188:191], v[124:127], v[188:191]\n"
  "s_waitcnt lgkmcnt(0)\n"
  "s_barrier\n"
  "v_exp_f32 v158, v158\n"
  "v_mfma_f32_16x16x32_bf16 v[48:51], v[64:67], v[224:227], v[48:51]\n"
  "v_exp_f32 v159, v159\n"
  "v_mfma_f32_16x16x32_bf16 v[48:51], v[64:67], v[228:231], v[48:51]\n"
  "ds_read_b128 a[128:131], v32 offset:49152\n"
  "v_cvt_pk_bf16_f32 v238, v156, v157\n"
  "v_mfma_f32_16x16x32_bf16 a[0:3], a[192:195], v[224:227], a[0:3]\n"
  "buffer_load_dwordx4 v26, s[52:55], s79 offen offset:0 lds\n"
  "v_mfma_f32_16x16x32_bf16 a[0:3], a[196:199], v[228:231], a[0:3]\n"
  "v_exp_f32 v160, v160\n"
  "v_mfma_f32_16x16x32_bf16 a[16:19], a[200:203], v[224:227], a[16:19]\n"
  "v_exp_f32 v161, v161\n"
  "v_mfma_f32_16x16x32_bf16 a[16:19], a[204:207], v[228:231], a[16:19]\n"
  "v_exp_f32 v162, v162\n"
  "v_mfma_f32_16x16x32_bf16 a[32:35], a[208:211], v[224:227], a[32:35]\n"
  "v_cvt_pk_bf16_f32 v239, v158, v159\n"
  "v_cvt_pk_bf16_f32 v240, v160, v161\n"
  "v_mfma_f32_16x16x32_bf16 a[32:35], a[212:215], v[228:231], a[32:35]\n"
  "v_exp_f32 v163, v163\n"
  "v_mfma_f32_16x16x32_bf16 a[48:51], a[216:219], v[224:227], a[48:51]\n"
  "v_exp_f32 v164, v164\n"
  "v_mfma_f32_16x16x32_bf16 a[48:51], a[220:223], v[228:231], a[48:51]\n"
  "v_exp_f32 v165, v165\n"
  "v_mfma_f32_16x16x32_bf16 a[64:67], a[224:227], v[224:227], a[64:67]\n"
  "ds_read_b128 a[132:135], v33 offset:49152\n"
  "v_cvt_pk_bf16_f32 v241, v162, v163\n"
  "v_mfma_f32_16x16x32_bf16 a[64:67], a[228:231], v[228:231], a[64:67]\n"
  "buffer_load_dwordx4 v27, s[52:55], s79 offen offset:1024 lds\n"
  "v_mfma_f32_16x16x32_bf16 a[80:83], a[232:235], v[224:227], a[80:83]\n"
  "v_exp_f32 v166, v166\n"
  "v_mfma_f32_16x16x32_bf16 a[80:83], a[236:239], v[228:231], a[80:83]\n"
  "v_exp_f32 v167, v167\n"
  "v_mfma_f32_16x16x32_bf16 a[96:99], a[240:243], v[224:227], a[96:99]\n"
  "v_exp_f32 v168, v168\n"
  "v_mfma_f32_16x16x32_bf16 a[96:99], a[244:247], v[228:231], a[96:99]\n"
  "v_cvt_pk_bf16_f32 v242, v164, v165\n"
  "v_cvt_pk_bf16_f32 v243, v166, v167\n"
  "v_mfma_f32_16x16x32_bf16 a[112:115], a[248:251], v[224:227], a[112:115]\n"
  "v_exp_f32 v169, v169\n"
  "v_mfma_f32_16x16x32_bf16 a[112:115], a[252:255], v[228:231], a[112:115]\n"
  "v_exp_f32 v170, v170\n"
  "v_mfma_f32_16x16x32_bf16 v[52:55], v[64:67], v[232:235], v[52:55]\n"
  "v_exp_f32 v171, v171\n"
  "v_mfma_f32_16x16x32_bf16 v[52:55], v[64:67], v[236:239], v[52:55]\n"
  "ds_read_b128 a[136:139], v34 offset:49152\n"
  "v_cvt_pk_bf16_f32 v244, v168, v169\n"
  "v_mfma_f32_16x16x32_bf16 a[4:7], a[192:195], v[232:235], a[4:7]\n"
  "buffer_load_dwordx4 v26, s[52:55], s79 offen offset:2048 lds\n"
  "v_mfma_f32_16x16x32_bf16 a[4:7], a[196:199], v[236:239], a[4:7]\n"
  "v_exp_f32 v172, v172\n"
  "v_mfma_f32_16x16x32_bf16 a[20:23], a[200:203], v[232:235], a[20:23]\n"
  "v_exp_f32 v173, v173\n"
  "v_mfma_f32_16x16x32_bf16 a[20:23], a[204:207], v[236:239], a[20:23]\n"
  "v_exp_f32 v174, v174\n"
  "v_mfma_f32_16x16x32_bf16 a[36:39], a[208:211], v[232:235], a[36:39]\n"
  "v_cvt_pk_bf16_f32 v245, v170, v171\n"
  "v_cvt_pk_bf16_f32 v246, v172, v173\n"
  "v_mfma_f32_16x16x32_bf16 a[36:39], a[212:215], v[236:239], a[36:39]\n"
  "v_exp_f32 v175, v175\n"
  "v_mfma_f32_16x16x32_bf16 a[52:55], a[216:219], v[232:235], a[52:55]\n"
  "v_exp_f32 v176, v176\n"
  "v_mfma_f32_16x16x32_bf16 a[52:55], a[220:223], v[236:239], a[52:55]\n"
  "v_exp_f32 v177, v177\n"
  "v_mfma_f32_16x16x32_bf16 a[68:71], a[224:227], v[232:235], a[68:71]\n"
  "ds_read_b128 a[140:143], v35 offset:49152\n"
  "v_cvt_pk_bf16_f32 v247, v174, v175\n"
  "v_mfma_f32_16x16x32_bf16 a[68:71], a[228:231], v[236:239], a[68:71]\n"
  "buffer_load_dwordx4 v27, s[52:55], s79 offen offset:3072 lds\n"
  "v_mfma_f32_16x16x32_bf16 a[84:87], a[232:235], v[232:235], a[84:87]\n"
  "s_add_u32 m0, s78, 147456\n"
  "v_exp_f32 v178, v178\n"
  "v_mfma_f32_16x16x32_bf16 a[84:87], a[236:239], v[236:239], a[84:87]\n"
  "s_add_u32 s79, s77, 114688\n"
  "v_exp_f32 v179, v179\n"
  "v_mfma_f32_16x16x32_bf16 a[100:103], a[240:243], v[232:235], a[100:103]\n"
  "v_exp_f32 v180, v180\n"
  "v_mfma_f32_16x16x32_bf16 a[100:103], a[244:247], v[236:239], a[100:103]\n"
  "v_cvt_pk_bf16_f32 v248, v176, v177\n"
  "v_cvt_pk_bf16_f32 v249, v178, v179\n"
  "v_mfma_f32_16x16x32_bf16 a[116:119], a[248:251], v[232:235], a[116:119]\n"
  "v_exp_f32 v181, v181\n"
  "v_mfma_f32_16x16x32_bf16 a[116:119], a[252:255], v[236:239], a[116:119]\n"
  "v_exp_f32 v182, v182\n"
  "v_mfma_f32_16x16x32_bf16 v[56:59], v[64:67], v[240:243], v[56:59]\n"
  "v_exp_f32 v183, v183\n"
  "v_mfma_f32_16x16x32_bf16 v[56:59], v[64:67], v[244:247], v[56:59]\n"
  "ds_read_b128 a[144:147], v32 offset:53248\n"
  "v_cvt_pk_bf16_f32 v250, v180, v181\n"
  "v_mfma_f32_16x16x32_bf16 a[8:11], a[192:195], v[240:243], a[8:11]\n"
  "buffer_load_dwordx4 v26, s[52:55], s79 offen offset:0 lds\n"
  "v_mfma_f32_16x16x32_bf16 a[8:11], a[196:199], v[244:247], a[8:11]\n"
  "v_exp_f32 v184, v184\n"
  "v_mfma_f32_16x16x32_bf16 a[24:27], a[200:203], v[240:243], a[24:27]\n"
  "v_exp_f32 v185, v185\n"
  "v_mfma_f32_16x16x32_bf16 a[24:27], a[204:207], v[244:247], a[24:27]\n"
  "v_exp_f32 v186, v186\n"
  "v_mfma_f32_16x16x32_bf16 a[40:43], a[208:211], v[240:243], a[40:43]\n"
  "v_cvt_pk_bf16_f32 v251, v182, v183\n"
  "v_cvt_pk_bf16_f32 v252, v184, v185\n"
  "v_mfma_f32_16x16x32_bf16 a[40:43], a[212:215], v[244:247], a[40:43]\n"
  "v_exp_f32 v187, v187\n"
  "v_mfma_f32_16x16x32_bf16 a[56:59], a[216:219], v[240:243], a[56:59]\n"
  "v_exp_f32 v188, v188\n"
  "v_mfma_f32_16x16x32_bf16 a[56:59], a[220:223], v[244:247], a[56:59]\n"
  "v_exp_f32 v189, v189\n"
  "v_mfma_f32_16x16x32_bf16 a[72:75], a[224:227], v[240:243], a[72:75]\n"
  "ds_read_b128 a[148:151], v33 offset:53248\n"
  "v_cvt_pk_bf16_f32 v253, v186, v187\n"
  "v_mfma_f32_16x16x32_bf16 a[72:75], a[228:231], v[244:247], a[72:75]\n"
  "buffer_load_dwordx4 v27, s[52:55], s79 offen offset:1024 lds\n"
  "v_mfma_f32_16x16x32_bf16 a[88:91], a[232:235], v[240:243], a[88:91]\n"
  "v_exp_f32 v190, v190\n"
  "v_mfma_f32_16x16x32_bf16 a[88:91], a[236:239], v[244:247], a[88:91]\n"
  "v_exp_f32 v191, v191\n"
  "v_mfma_f32_16x16x32_bf16 a[104:107], a[240:243], v[240:243], a[104:107]\n"
  "v_cvt_pk_bf16_f32 v254, v188, v189\n"
  "v_cvt_pk_bf16_f32 v255, v190, v191\n"
  "v_mfma_f32_16x16x32_bf16 a[104:107], a[244:247], v[244:247], a[104:107]\n"
  "ds_read_b128 a[152:155], v34 offset:53248\n"
  "v_mov_b32 v2, v1\n"
  "v_mfma_f32_16x16x32_bf16 a[120:123], a[248:251], v[240:243], a[120:123]\n"
  "v_mov_b32 v3, v30\n"
  "v_mov_b32 v5, 0x7ffffff0\n"
  "v_mfma_f32_16x16x32_bf16 a[120:123], a[252:255], v[244:247], a[120:123]\n"
  "ds_read_b128 a[156:159], v35 offset:53248\n"
  "v_lshrrev_b32 v6, 4, v0\n"
  "v_mfma_f32_16x16x32_bf16 v[60:63], v[64:67], v[248:251], v[60:63]\n"
  "v_and_b32 v7, 15, v0\n"
  "v_mul_u32_u24 v7, 1152, v7\n"
  "v_mfma_f32_16x16x32_bf16 v[60:63], v[64:67], v[252:255], v[60:63]\n"
  "ds_read_b128 a[160:163], v32 offset:57344\n"
  "v_lshl_add_u32 v14, v6, 3, v7\n"
  "v_mfma_f32_16x16x32_bf16 a[12:15], a[192:195], v[248:251], a[12:15]\n"
  "v_and_b32 v13, 7, v0\n"
  "v_lshlrev_b32 v13, 2, v13\n"
  "v_mfma_f32_16x16x32_bf16 a[12:15], a[196:199], v[252:255], a[12:15]\n"
  "ds_read_b128 a[164:167], v33 offset:57344\n"
  "v_bfe_u32 v7, v0, 3, 1\n"
  "v_mfma_f32_16x16x32_bf16 a[28:31], a[200:203], v[248:251], a[28:31]\n"
  "v_lshl_add_u32 v13, v7, 8, v13\n"
  "v_mov_b32 v15, 1\n"
  "v_mfma_f32_16x16x32_bf16 a[28:31], a[204:207], v[252:255], a[28:31]\n"
  "ds_read_b128 a[168:171], v34 offset:57344\n"
  "buffer_load_dwordx4 v26, s[52:55], s79 offen offset:2048 lds\n"
  "v_mfma_f32_16x16x32_bf16 a[44:47], a[208:211], v[248:251], a[44:47]\n"
  "s_cmp_eq_u32 s82, s68\n s_cselect_b64 vcc, -1, 0\n"
  "v_cndmask_b32 v31, v5, v1, vcc\n"
  "v_mfma_f32_16x16x32_bf16 a[44:47], a[212:215], v[252:255], a[44:47]\n"
  "ds_read_b128 a[172:175], v35 offset:57344\n"
  "v_mfma_f32_16x16x32_bf16 a[60:63], a[216:219], v[248:251], a[60:63]\n"
  "v_add_u32 v36, 0xffff0000, v36\n"
  "v_add_u32 v37, 0xffff0000, v37\n"
  "v_mfma_f32_16x16x32_bf16 a[60:63], a[220:223], v[252:255], a[60:63]\n"
  "ds_read_b128 a[176:179], v32 offset:61440\n"
  "v_add_u32 v38, 0xffff0000, v38\n"
  "v_mfma_f32_16x16x32_bf16 a[76:79], a[224:227], v[248:251], a[76:79]\n"
  "v_add_u32 v39, 0xffff0000, v39\n"
  "v_add_u32 v40, 0xffff0000, v40\n"
  "v_mfma_f32_16x16x32_bf16 a[76:79], a[228:231], v[252:255], a[76:79]\n"
  "ds_read_b128 a[180:183], v33 offset:61440\n"
  "v_add_u32 v41, 0xffff0000, v41\n"
  "v_mfma_f32_16x16x32_bf16 a[92:95], a[232:235], v[248:251], a[92:95]\n"
  "v_add_u32 v42, 0xffff0000, v42\n"
  "v_add_u32 v43, 0xffff0000, v43\n"
  "v_mfma_f32_16x16x32_bf16 a[92:95], a[236:239], v[252:255], a[92:95]\n"
  "ds_read_b128 a[184:187], v34 offset:61440\n"
  "buffer_load_dwordx4 v27, s[52:55], s79 offen offset:3072 lds\n"
  "v_mfma_f32_16x16x32_bf16 a[108:111], a[240:243], v[248:251], a[108:111]\n"
  "s_add_u32 s83, s68, 1\n"
  "s_and_b32 s83, s83, 3\n"
  "s_mul_i32 s83, s83, 12288\n"
  "v_mfma_f32_16x16x32_bf16 a[108:111], a[244:247], v[252:255], a[108:111]\n"
  "s_add_u32 s83, s83, s58\n"
  "ds_read_b128 a[188:191], v35 offset:61440\n"
  "v_mfma_f32_16x16x32_bf16 a[124:127], a[248:251], v[248:251], a[124:127]\n"
  "v_mfma_f32_16x16x32_bf16 a[124:127], a[252:255], v[252:255], a[124:127]\n"
  "s_waitcnt lgkmcnt(2)\n"
  "buffer_store_dwordx4 v[76:79], v1, s[36:39], s83 offen offset:0 sc1\n"
  "v_mfma_f32_16x16x32_bf16 v[128:131], a[128:131], v[208:211], v[192:195]\n"
  "ds_read_b64_tr_b16 a[192:193], v36 offset:0\n"
  "v_mfma_f32_16x16x32_bf16 v[128:131], a[132:135], v[80:83], v[128:131]\n"
  "ds_read_b64_tr_b16 a[194:195], v36 offset:4096\n"
  "v_mfma_f32_16x16x32_bf16 v[128:131], a[136:139], v[96:99], v[128:131]\n"
  "ds_read_b64_tr_b16 a[196:197], v36 offset:8192\n"
  "v_add_u32 v32, 65536, v32\n"
  "v_mfma_f32_16x16x32_bf16 v[128:131], a[140:143], v[112:115], v[128:131]\n"
  "ds_read_b64_tr_b16 a[198:199], v36 offset:12288\n"
  "v_add_u32 v33, 65536, v33\n"
  "v_mfma_f32_16x16x32_bf16 v[132:135], a[144:147], v[208:211], v[192:195]\n"
  "ds_read_b64_tr_b16 a[200:201], v37 offset:0\n"
  "v_add_u32 v34, 65536, v34\n"
  "v_mfma_f32_16x16x32_bf16 v[132:135], a[148:151], v[80:83], v[132:135]\n"
  "ds_read_b64_tr_b16 a[202:203], v37 offset:4096\n"
  "v_add_u32 v35, 65536, v35\n"
  "v_mfma_f32_16x16x32_bf16 v[132:135], a[152:155], v[96:99], v[132:135]\n"
  "ds_read_b64_tr_b16 a[204:205], v37 offset:8192\n"
  "v_mfma_f32_16x16x32_bf16 v[132:135], a[156:159], v[112:115], v[132:135]\n"
  "v_exp_f32 v128, v128\n"
  "v_mfma_f32_16x16x32_bf16 v[136:139], a[160:163], v[208:211], v[192:195]\n"
  "ds_read_b64_tr_b16 a[206:207], v37 offset:12288\n"
  "v_mfma_f32_16x16x32_bf16 v[136:139], a[164:167], v[80:83], v[136:139]\n"
  "v_exp_f32 v129, v129\n"
  "v_mfma_f32_16x16x32_bf16 v[136:139], a[168:171], v[96:99], v[136:139]\n"
  "ds_read_b64_tr_b16 a[208:209], v38 offset:0\n"
  "v_cvt_pk_bf16_f32 v224, v128, v129\n"
  "v_mfma_f32_16x16x32_bf16 v[136:139], a[172:175], v[112:115], v[136:139]\n"
  "v_exp_f32 v130, v130\n"
  "v_mfma_f32_16x16x32_bf16 v[140:143], a[176:179], v[208:211], v[192:195]\n"
  "ds_read_b64_tr_b16 a[210:211], v38 offset:4096\n"
  "buffer_store_dwordx4 v[76:79], v1, s[36:39], s83 offen offset:1024 sc1\n"
  "v_mfma_f32_16x16x32_bf16 v[140:143], a[180:183], v[80:83], v[140:143]\n"
  "s_waitcnt lgkmcnt(3)\n"
  "v_exp_f32 v131, v131\n"
  "v_mfma_f32_16x16x32_bf16 v[140:143], a[184:187], v[96:99], v[140:143]\n"
  "ds_read_b64_tr_b16 a[212:213], v38 offset:8192\n"
  "v_cvt_pk_bf16_f32 v225, v130, v131\n"
  "v_mfma_f32_16x16x32_bf16 v[140:143], a[188:191], v[112:115], v[140:143]\n"
  "v_exp_f32 v132, v132\n"
  "v_mfma_f32_16x16x32_bf16 v[144:147], a[128:131], v[212:215], v[196:199]\n"
  "ds_read_b64_tr_b16 a[214:215], v38 offset:12288\n"
  "v_mfma_f32_16x16x32_bf16 v[144:147], a[132:135], v[84:87], v[144:147]\n"
  "v_exp_f32 v133, v133\n"
  "v_mfma_f32_16x16x32_bf16 v[144:147], a[136:139], v[100:103], v[144:147]\n"
  "ds_read_b64_tr_b16 a[216:217], v39 offset:0\n"
  "v_cvt_pk_bf16_f32 v226, v132, v133\n"
  "v_mfma_f32_16x16x32_bf16 v[144:147], a[140:143], v[116:119], v[144:147]\n"
  "v_exp_f32 v134, v134\n"
  "v_mfma_f32_16x16x32_bf16 v[148:151], a[144:147], v[212:215], v[196:199]\n"
  "ds_read_b64_tr_b16 a[218:219], v39 offset:4096\n"
  "v_mfma_f32_16x16x32_bf16 v[148:151], a[148:151], v[84:87], v[148:151]\n"
  "v_exp_f32 v135, v135\n"
  "v_mfma_f32_16x16x32_bf16 v[148:151], a[152:155], v[100:103], v[148:151]\n"
  "ds_read_b64_tr_b16 a[220:221], v39 offset:8192\n"
  "v_cvt_pk_bf16_f32 v227, v134, v135\n"
  "v_mfma_f32_16x16x32_bf16 v[148:151], a[156:159], v[116:119], v[148:151]\n"
  "v_exp_f32 v136, v136\n"
  "v_mfma_f32_16x16x32_bf16 v[152:155], a[160:163], v[212:215], v[196:199]\n"
  "ds_read_b64_tr_b16 a[222:223], v39 offset:12288\n"
  "buffer_store_dwordx4 v[76:79], v31, s[36:39], s83 offen offset:2048 sc1\n"
  "v_mfma_f32_16x16x32_bf16 v[152:155], a[164:167], v[84:87], v[152:155]\n"
  "v_exp_f32 v137, v137\n"
  "v_mfma_f32_16x16x32_bf16 v[152:155], a[168:171], v[100:103], v[152:155]\n"
  "s_add_u32 s83, s68, 2\n"
  "ds_read_b64_tr_b16 a[224:225], v40 offset:0\n"
  "v_cvt_pk_bf16_f32 v228, v136, v137\n"
  "v_mfma_f32_16x16x32_bf16 v[152:155], a[172:175], v[116:119], v[152:155]\n"
  "s_and_b32 s83, s83, 3\n"
  "v_exp_f32 v138, v138\n"
  "v_mfma_f32_16x16x32_bf16 v[156:159], a[176:179], v[212:215], v[196:199]\n"
  "s_mul_i32 s83, s83, 12288\n"
  "s_add_u32 s83, s83, s58\n"
  "ds_read_b64_tr_b16 a[226:227], v40 offset:4096\n"
  "v_mfma_f32_16x16x32_bf16 v[156:159], a[180:183], v[84:87], v[156:159]\n"
  "v_exp_f32 v139, v139\n"
  "v_mfma_f32_16x16x32_bf16 v[156:159], a[184:187], v[100:103], v[156:159]\n"
  "ds_read_b64_tr_b16 a[228:229], v40 offset:8192\n"
  "v_cvt_pk_bf16_f32 v229, v138, v139\n"
  "v_mfma_f32_16x16x32_bf16 v[156:159], a[188:191], v[116:119], v[156:159]\n"
  "v_exp_f32 v140, v140\n"
  "v_mfma_f32_16x16x32_bf16 v[160:163], a[128:131], v[216:219], v[200:203]\n"
  "ds_read_b64_tr_b16 a[230:231], v40 offset:12288\n"
  "v_mfma_f32_16x16x32_bf16 v[160:163], a[132:135], v[88:91], v[160:163]\n"
  "v_exp_f32 v141, v141\n"
  "v_mfma_f32_16x16x32_bf16 v[160:163], a[136:139], v[104:107], v[160:163]\n"
  "ds_read_b64_tr_b16 a[232:233], v41 offset:0\n"
  "v_cvt_pk_bf16_f32 v230, v140, v141\n"
  "v_mfma_f32_16x16x32_bf16 v[160:163], a[140:143], v[120:123], v[160:163]\n"
  "v_exp_f32 v142, v142\n"
  "v_mfma_f32_16x16x32_bf16 v[164:167], a[144:147], v[216:219], v[200:203]\n"
  "ds_read_b64_tr_b16 a[234:235], v41 offset:4096\n"
  "buffer_store_dwordx4 v[76:79], v1, s[36:39], s83 offen offset:0 sc1\n"
  "v_mfma_f32_16x16x32_bf16 v[164:167], a[148:151], v[88:91], v[164:167]\n"
  "v_exp_f32 v143, v143\n"
  "v_mfma_f32_16x16x32_bf16 v[164:167], a[152:155], v[104:107], v[164:167]\n"
  "ds_read_b64_tr_b16 a[236:237], v41 offset:8192\n"
  "v_cvt_pk_bf16_f32 v231, v142, v143\n"
  "v_mfma_f32_16x16x32_bf16 v[164:167], a[156:159], v[120:123], v[164:167]\n"
  "v_exp_f32 v144, v144\n"
  "v_mfma_f32_16x16x32_bf16 v[168:171], a[160:163], v[216:219], v[200:203]\n"
  "ds_read_b64_tr_b16 a[238:239], v41 offset:12288\n"
  "v_mfma_f32_16x16x32_bf16 v[168:171], a[164:167], v[88:91], v[168:171]\n"
  "v_exp_f32 v145, v145\n"
  "v_mfma_f32_16x16x32_bf16 v[168:171], a[168:171], v[104:107], v[168:171]\n"
  "ds_read_b64_tr_b16 a[240:241], v42 offset:0\n"
  "v_cvt_pk_bf16_f32 v232, v144, v145\n"
  "v_mfma_f32_16x16x32_bf16 v[168:171], a[172:175], v[120:123], v[168:171]\n"
  "v_exp_f32 v146, v146\n"
  "v_mfma_f32_16x16x32_bf16 v[172:175], a[176:179], v[216:219], v[200:203]\n"
  "ds_read_b64_tr_b16 a[242:243], v42 offset:4096\n"
  "v_mfma_f32_16x16x32_bf16 v[172:175], a[180:183], v[88:91], v[172:175]\n"
  "v_exp_f32 v147, v147\n"
  "v_mfma_f32_16x16x32_bf16 v[172:175], a[184:187], v[104:107], v[172:175]\n"
  "ds_read_b64_tr_b16 a[244:245], v42 offset:8192\n"
  "v_cvt_pk_bf16_f32 v233, v146, v147\n"
  "v_mfma_f32_16x16x32_bf16 v[172:175], a[188:191], v[120:123], v[172:175]\n"
  "v_exp_f32 v148, v148\n"
  "v_mfma_f32_16x16x32_bf16 v[176:179], a[128:131], v[220:223], v[204:207]\n"
  "ds_read_b64_tr_b16 a[246:247], v42 offset:12288\n"
  "buffer_store_dwordx4 v[76:79], v1, s[36:39], s83 offen offset:1024 sc1\n"
  "v_mfma_f32_16x16x32_bf16 v[176:179], a[132:135], v[92:95], v[176:179]\n"
  "v_exp_f32 v149, v149\n"
  "v_mfma_f32_16x16x32_bf16 v[176:179], a[136:139], v[108:111], v[176:179]\n"
  "ds_read_b64_tr_b16 a[248:249], v43 offset:0\n"
  "v_cvt_pk_bf16_f32 v234, v148, v149\n"
  "v_mfma_f32_16x16x32_bf16 v[176:179], a[140:143], v[124:127], v[176:179]\n"
  "v_exp_f32 v150, v150\n"
  "v_mfma_f32_16x16x32_bf16 v[180:183], a[144:147], v[220:223], v[204:207]\n"
  "ds_read_b64_tr_b16 a[250:251], v43 offset:4096\n"
  "v_mfma_f32_16x16x32_bf16 v[180:183], a[148:151], v[92:95], v[180:183]\n"
  "v_exp_f32 v151, v151\n"
  "v_mfma_f32_16x16x32_bf16 v[180:183], a[152:155], v[108:111], v[180:183]\n"
  "ds_read_b64_tr_b16 a[252:253], v43 offset:8192\n"
  "v_cvt_pk_bf16_f32 v235, v150, v151\n"
  "v_mfma_f32_16x16x32_bf16 v[180:183], a[156:159], v[124:127], v[180:183]\n"
  "v_exp_f32 v152, v152\n"
  "v_mfma_f32_16x16x32_bf16 v[184:187], a[160:163], v[220:223], v[204:207]\n"
  "ds_read_b64_tr_b16 a[254:255], v43 offset:12288\n"
  "v_mfma_f32_16x16x32_bf16 v[184:187], a[164:167], v[92:95], v[184:187]\n"
  "v_exp_f32 v153, v153\n"
  "v_mfma_f32_16x16x32_bf16 v[184:187], a[168:171], v[108:111], v[184:187]\n"
  "v_exp_f32 v154, v154\n"
  "v_mfma_f32_16x16x32_bf16 v[184:187], a[172:175], v[124:127], v[184:187]\n"
  "v_exp_f32 v155, v155\n"
  "v_mfma_f32_16x16x32_bf16 v[188:191], a[176:179], v[220:223], v[204:207]\n"
  "v_exp_f32 v156, v156\n"
  "v_mfma_f32_16x16x32_bf16 v[188:191], a[180:183], v[92:95], v[188:191]\n"
  "v_cvt_pk_bf16_f32 v236, v152, v153\n"
  "v_cvt_pk_bf16_f32 v237, v154, v155\n"
  "v_mfma_f32_16x16x32_bf16 v[188:191], a[184:187], v[108:111], v[188:191]\n"
  "v_exp_f32 v157, v157\n"
  "v_mfma_f32_16x16x32_bf16 v[188:191], a[188:191], v[124:127], v[188:191]\n"
  "s_waitcnt vmcnt(15) lgkmcnt(0)\n"
  "s_barrier\n"
  "v_exp_f32 v158, v158\n"
  "v_mfma_f32_16x16x32_bf16 v[48:51], v[64:67], v[224:227], v[48:51]\n"
  "v_exp_f32 v159, v159\n"
  "v_mfma_f32_16x16x32_bf16 v[48:51], v[64:67], v[228:231], v[48:51]\n"
  "ds_read_b128 a[128:131], v32 offset:16384\n"
  "v_cvt_pk_bf16_f32 v238, v156, v157\n"
  "v_mfma_f32_16x16x32_bf16 a[0:3], a[192:195], v[224:227], a[0:3]\n"
  "buffer_store_dwordx4 v[76:79], v31, s[36:39], s83 offen offset:2048 sc1\n"
  "v_mfma_f32_16x16x32_bf16 a[0:3], a[196:199], v[228:231], a[0:3]\n"
  "v_exp_f32 v160, v160\n"
  "v_mfma_f32_16x16x32_bf16 a[16:19], a[200:203], v[224:227], a[16:19]\n"
  "v_exp_f32 v161, v161\n"
  "v_mfma_f32_16x16x32_bf16 a[16:19], a[204:207], v[228:231], a[16:19]\n"
  "v_exp_f32 v162, v162\n"
  "v_mfma_f32_16x16x32_bf16 a[32:35], a[208:211], v[224:227], a[32:35]\n"
  "v_cvt_pk_bf16_f32 v239, v158, v159\n"
  "v_cvt_pk_bf16_f32 v240, v160, v161\n"
  "v_mfma_f32_16x16x32_bf16 a[32:35], a[212:215], v[228:231], a[32:35]\n"
  "v_exp_f32 v163, v163\n"
  "v_mfma_f32_16x16x32_bf16 a[48:51], a[216:219], v[224:227], a[48:51]\n"
  "s_add_u32 m0, s78, 49152\n"
  "v_exp_f32 v164, v164\n"
  "v_mfma_f32_16x16x32_bf16 a[48:51], a[220:223], v[228:231], a[48:51]\n"
  "s_add_u32 s79, s77, 114688\n"
  "v_exp_f32 v165, v165\n"
  "v_mfma_f32_16x16x32_bf16 a[64:67], a[224:227], v[224:227], a[64:67]\n"
  "ds_read_b128 a[132:135], v33 offset:16384\n"
  "v_cvt_pk_bf16_f32 v241, v162, v163\n"
  "v_mfma_f32_16x16x32_bf16 a[64:67], a[228:231], v[228:231], a[64:67]\n"
  "v_mfma_f32_16x16x32_bf16 a[80:83], a[232:235], v[224:227], a[80:83]\n"
  "v_exp_f32 v166, v166\n"
  "v_mfma_f32_16x16x32_bf16 a[80:83], a[236:239], v[228:231], a[80:83]\n"
  "v_exp_f32 v167, v167\n"
  "v_mfma_f32_16x16x32_bf16 a[96:99], a[240:243], v[224:227], a[96:99]\n"
  "v_exp_f32 v168, v168\n"
  "v_mfma_f32_16x16x32_bf16 a[96:99], a[244:247], v[228:231], a[96:99]\n"
  "v_cvt_pk_bf16_f32 v242, v164, v165\n"
  "v_cvt_pk_bf16_f32 v243, v166, v167\n"
  "v_mfma_f32_16x16x32_bf16 a[112:115], a[248:251], v[224:227], a[112:115]\n"
  "v_exp_f32 v169, v169\n"
  "v_mfma_f32_16x16x32_bf16 a[112:115], a[252:255], v[228:231], a[112:115]\n"
  "v_exp_f32 v170, v170\n"
  "v_mfma_f32_16x16x32_bf16 v[52:55], v[64:67], v[232:235], v[52:55]\n"
  "v_exp_f32 v171, v171\n"
  "v_mfma_f32_16x16x32_bf16 v[52:55], v[64:67], v[236:239], v[52:55]\n"
  "ds_read_b128 a[136:139], v34 offset:16384\n"
  "v_cvt_pk_bf16_f32 v244, v168, v169\n"
  "v_mfma_f32_16x16x32_bf16 a[4:7], a[192:195], v[232:235], a[4:7]\n"
  "buffer_load_dwordx4 v22, s[48:51], s79 offen offset:0 lds\n"
  "v_mfma_f32_16x16x32_bf16 a[4:7], a[196:199], v[236:239], a[4:7]\n"
  "v_exp_f32 v172, v172\n"
  "v_mfma_f32_16x16x32_bf16 a[20:23], a[200:203], v[232:235], a[20:23]\n"
  "v_exp_f32 v173, v173\n"
  "v_mfma_f32_16x16x32_bf16 a[20:23], a[204:207], v[236:239], a[20:23]\n"
  "v_exp_f32 v174, v174\n"
  "v_mfma_f32_16x16x32_bf16 a[36:39], a[208:211], v[232:235], a[36:39]\n"
  "v_cvt_pk_bf16_f32 v245, v170, v171\n"
  "v_cvt_pk_bf16_f32 v246, v172, v173\n"
  "v_mfma_f32_16x16x32_bf16 a[36:39], a[212:215], v[236:239], a[36:39]\n"
  "v_exp_f32 v175, v175\n"
  "v_mfma_f32_16x16x32_bf16 a[52:55], a[216:219], v[232:235], a[52:55]\n"
  "v_exp_f32 v176, v176\n"
  "v_mfma_f32_16x16x32_bf16 a[52:55], a[220:223], v[236:239], a[52:55]\n"
  "v_exp_f32 v177, v177\n"
  "v_mfma_f32_16x16x32_bf16 a[68:71], a[224:227], v[232:235], a[68:71]\n"
  "ds_read_b128 a[140:143], v35 offset:16384\n"
  "v_cvt_pk_bf16_f32 v247, v174, v175\n"
  "v_mfma_f32_16x16x32_bf16 a[68:71], a[228:231], v[236:239], a[68:71]\n"
  "buffer_load_dwordx4 v23, s[48:51], s79 offen offset:1024 lds\n"
  "v_mfma_f32_16x16x32_bf16 a[84:87], a[232:235], v[232:235], a[84:87]\n"
  "v_exp_f32 v178, v178\n"
  "v_mfma_f32_16x16x32_bf16 a[84:87], a[236:239], v[236:239], a[84:87]\n"
  "v_exp_f32 v179, v179\n"
  "v_mfma_f32_16x16x32_bf16 a[100:103], a[240:243], v[232:235], a[100:103]\n"
  "v_exp_f32 v180, v180\n"
  "v_mfma_f32_16x16x32_bf16 a[100:103], a[244:247], v[236:239], a[100:103]\n"
  "v_cvt_pk_bf16_f32 v248, v176, v177\n"
  "v_cvt_pk_bf16_f32 v249, v178, v179\n"
  "v_mfma_f32_16x16x32_bf16 a[116:119], a[248:251], v[232:235], a[116:119]\n"
  "v_exp_f32 v181, v181\n"
  "v_mfma_f32_16x16x32_bf16 a[116:119], a[252:255], v[236:239], a[116:119]\n"
  "v_exp_f32 v182, v182\n"
  "v_mfma_f32_16x16x32_bf16 v[56:59], v[64:67], v[240:243], v[56:59]\n"
  "v_exp_f32 v183, v183\n"
  "v_mfma_f32_16x16x32_bf16 v[56:59], v[64:67], v[244:247], v[56:59]\n"
  "ds_read_b128 a[144:147], v32 offset:20480\n"
  "v_cvt_pk_bf16_f32 v250, v180, v181\n"
  "v_mfma_f32_16x16x32_bf16 a[8:11], a[192:195], v[240:243], a[8:11]\n"
  "buffer_load_dwordx4 v24, s[48:51], s79 offen offset:2048 lds\n"
  "v_mfma_f32_16x16x32_bf16 a[8:11], a[196:199], v[244:247], a[8:11]\n"
  "v_exp_f32 v184, v184\n"
  "v_mfma_f32_16x16x32_bf16 a[24:27], a[200:203], v[240:243], a[24:27]\n"
  "v_exp_f32 v185, v185\n"
  "v_mfma_f32_16x16x32_bf16 a[24:27], a[204:207], v[244:247], a[24:27]\n"
  "v_exp_f32 v186, v186\n"
  "v_mfma_f32_16x16x32_bf16 a[40:43], a[208:211], v[240:243], a[40:43]\n"
  "v_cvt_pk_bf16_f32 v251, v182, v183\n"
  "v_cvt_pk_bf16_f32 v252, v184, v185\n"
  "v_mfma_f32_16x16x32_bf16 a[40:43], a[212:215], v[244:247], a[40:43]\n"
  "v_exp_f32 v187, v187\n"
  "v_mfma_f32_16x16x32_bf16 a[56:59], a[216:219], v[240:243], a[56:59]\n"
  "v_exp_f32 v188, v188\n"
  "v_mfma_f32_16x16x32_bf16 a[56:59], a[220:223], v[244:247], a[56:59]\n"
  "v_exp_f32 v189, v189\n"
  "v_mfma_f32_16x16x32_bf16 a[72:75], a[224:227], v[240:243], a[72:75]\n"
  "ds_read_b128 a[148:151], v33 offset:20480\n"
  "v_cvt_pk_bf16_f32 v253, v186, v187\n"
  "v_mfma_f32_16x16x32_bf16 a[72:75], a[228:231], v[244:247], a[72:75]\n"
  "buffer_load_dwordx4 v25, s[48:51], s79 offen offset:3072 lds\n"
  "v_mfma_f32_16x16x32_bf16 a[88:91], a[232:235], v[240:243], a[88:91]\n"
  "v_exp_f32 v190, v190\n"
  "v_mfma_f32_16x16x32_bf16 a[88:91], a[236:239], v[244:247], a[88:91]\n"
  "v_exp_f32 v191, v191\n"
  "v_mfma_f32_16x16x32_bf16 a[104:107], a[240:243], v[240:243], a[104:107]\n"
  "v_cvt_pk_bf16_f32 v254, v188, v189\n"
  "v_cvt_pk_bf16_f32 v255, v190, v191\n"
  "v_mfma_f32_16x16x32_bf16 a[104:107], a[244:247], v[244:247], a[104:107]\n"
  "ds_read_b128 a[152:155], v34 offset:20480\n"
  "v_mfma_f32_16x16x32_bf16 a[120:123], a[248:251], v[240:243], a[120:123]\n"
  "v_mfma_f32_16x16x32_bf16 a[120:123], a[252:255], v[244:247], a[120:123]\n"
  "ds_read_b128 a[156:159], v35 offset:20480\n"
  "v_mfma_f32_16x16x32_bf16 v[60:63], v[64:67], v[248:251], v[60:63]\n"
  "v_mfma_f32_16x16x32_bf16 v[60:63], v[64:67], v[252:255], v[60:63]\n"
  "ds_read_b128 a[160:163], v32 offset:24576\n"
  "v_mfma_f32_16x16x32_bf16 a[12:15], a[192:195], v[248:251], a[12:15]\n"
  "v_mfma_f32_16x16x32_bf16 a[12:15], a[196:199], v[252:255], a[12:15]\n"
  "ds_read_b128 a[164:167], v33 offset:24576\n"
  "v_mfma_f32_16x16x32_bf16 a[28:31], a[200:203], v[248:251], a[28:31]\n"
  "v_mfma_f32_16x16x32_bf16 a[28:31], a[204:207], v[252:255], a[28:31]\n"
  "ds_read_b128 a[168:171], v34 offset:24576\n"
  "buffer_load_dwordx4 v[72:75], v1, s[36:39], s69 offen offset:0 sc1\n"
  "v_mfma_f32_16x16x32_bf16 a[44:47], a[208:211], v[248:251], a[44:47]\n"
  "v_mfma_f32_16x16x32_bf16 a[44:47], a[212:215], v[252:255], a[44:47]\n"
  "ds_read_b128 a[172:175], v35 offset:24576\n"
  "v_mfma_f32_16x16x32_bf16 a[60:63], a[216:219], v[248:251], a[60:63]\n"
  "v_mfma_f32_16x16x32_bf16 a[60:63], a[220:223], v[252:255], a[60:63]\n"
  "ds_read_b128 a[176:179], v32 offset:28672\n"
  "buffer_load_dword v12, v13, s[36:39], s65 offen offset:0 sc1\n"
  "v_mfma_f32_16x16x32_bf16 a[76:79], a[224:227], v[248:251], a[76:79]\n"
  "v_mfma_f32_16x16x32_bf16 a[76:79], a[228:231], v[252:255], a[76:79]\n"
  "ds_read_b128 a[180:183], v33 offset:28672\n"
  "v_mfma_f32_16x16x32_bf16 a[92:95], a[232:235], v[248:251], a[92:95]\n"
  "v_mfma_f32_16x16x32_bf16 a[92:95], a[236:239], v[252:255], a[92:95]\n"
  "s_add_u32 s83, s68, 3\n"
  "s_and_b32 s83, s83, 3\n"
  "ds_read_b128 a[184:187], v34 offset:28672\n"
  "v_mfma_f32_16x16x32_bf16 a[108:111], a[240:243], v[248:251], a[108:111]\n"
  "s_mul_i32 s83, s83, 12288\n"
  "s_add_u32 s83, s83, s58\n"
  "v_mfma_f32_16x16x32_bf16 a[108:111], a[244:247], v[252:255], a[108:111]\n"
  "ds_read_b128 a[188:191], v35 offset:28672\n"
  "buffer_store_dwordx4 v[76:79], v1, s[36:39], s83 offen offset:0 sc1\n"
  "v_mfma_f32_16x16x32_bf16 a[124:127], a[248:251], v[248:251], a[124:127]\n"
  "v_mfma_f32_16x16x32_bf16 a[124:127], a[252:255], v[252:255], a[124:127]\n"
  "s_waitcnt lgkmcnt(2)\n"
  "v_mfma_f32_16x16x32_bf16 v[128:131], a[128:131], v[208:211], v[192:195]\n"
  "ds_read_b64_tr_b16 a[192:193], v36 offset:32768\n"
  "v_mfma_f32_16x16x32_bf16 v[128:131], a[132:135], v[80:83], v[128:131]\n"
  "ds_read_b64_tr_b16 a[194:195], v36 offset:36864\n"
  "v_mfma_f32_16x16x32_bf16 v[128:131], a[136:139], v[96:99], v[128:131]\n"
  "ds_read_b64_tr_b16 a[196:197], v36 offset:40960\n"
  "v_mfma_f32_16x16x32_bf16 v[128:131], a[140:143], v[112:115], v[128:131]\n"
  "ds_read_b64_tr_b16 a[198:199], v36 offset:45056\n"
  "v_mfma_f32_16x16x32_bf16 v[132:135], a[144:147], v[208:211], v[192:195]\n"
  "ds_read_b64_tr_b16 a[200:201], v37 offset:32768\n"
  "v_mfma_f32_16x16x32_bf16 v[132:135], a[148:151], v[80:83], v[132:135]\n"
  "ds_read_b64_tr_b16 a[202:203], v37 offset:36864\n"
  "v_mfma_f32_16x16x32_bf16 v[132:135], a[152:155], v[96:99], v[132:135]\n"
  "ds_read_b64_tr_b16 a[204:205], v37 offset:40960\n"
  "v_mfma_f32_16x16x32_bf16 v[132:135], a[156:159], v[112:115], v[132:135]\n"
  "v_exp_f32 v128, v128\n"
  "v_mfma_f32_16x16x32_bf16 v[136:139], a[160:163], v[208:211], v[192:195]\n"
  "ds_read_b64_tr_b16 a[206:207], v37 offset:45056\n"
  "v_mfma_f32_16x16x32_bf16 v[136:139], a[164:167], v[80:83], v[136:139]\n"
  "v_exp_f32 v129, v129\n"
  "v_mfma_f32_16x16x32_bf16 v[136:139], a[168:171], v[96:99], v[136:139]\n"
  "ds_read_b64_tr_b16 a[208:209], v38 offset:32768\n"
  "v_cvt_pk_bf16_f32 v224, v128, v129\n"
  "v_mfma_f32_16x16x32_bf16 v[136:139], a[172:175], v[112:115], v[136:139]\n"
  "v_exp_f32 v130, v130\n"
  "v_mfma_f32_16x16x32_bf16 v[140:143], a[176:179], v[208:211], v[192:195]\n"
  "ds_read_b64_tr_b16 a[210:211], v38 offset:36864\n"
  "buffer_store_dwordx4 v[76:79], v1, s[36:39], s83 offen offset:1024 sc1\n"
  "v_mfma_f32_16x16x32_bf16 v[140:143], a[180:183], v[80:83], v[140:143]\n"
  "s_waitcnt lgkmcnt(3)\n"
  "v_exp_f32 v131, v131\n"
  "v_mfma_f32_16x16x32_bf16 v[140:143], a[184:187], v[96:99], v[140:143]\n"
  "ds_read_b64_tr_b16 a[212:213], v38 offset:40960\n"
  "v_cvt_pk_bf16_f32 v225, v130, v131\n"
  "v_mfma_f32_16x16x32_bf16 v[140:143], a[188:191], v[112:115], v[140:143]\n"
  "v_exp_f32 v132, v132\n"
  "v_mfma_f32_16x16x32_bf16 v[144:147], a[128:131], v[212:215], v[196:199]\n"
  "ds_read_b64_tr_b16 a[214:215], v38 offset:45056\n"
  "v_mfma_f32_16x16x32_bf16 v[144:147], a[132:135], v[84:87], v[144:147]\n"
  "v_exp_f32 v133, v133\n"
  "v_mfma_f32_16x16x32_bf16 v[144:147], a[136:139], v[100:103], v[144:147]\n"
  "ds_read_b64_tr_b16 a[216:217], v39 offset:32768\n"
  "v_cvt_pk_bf16_f32 v226, v132, v133\n"
  "v_mfma_f32_16x16x32_bf16 v[144:147], a[140:143], v[116:119], v[144:147]\n"
  "v_exp_f32 v134, v134\n"
  "v_mfma_f32_16x16x32_bf16 v[148:151], a[144:147], v[212:215], v[196:199]\n"
  "ds_read_b64_tr_b16 a[218:219], v39 offset:36864\n"
  "v_mfma_f32_16x16x32_bf16 v[148:151], a[148:151], v[84:87], v[148:151]\n"
  "v_exp_f32 v135, v135\n"
  "v_mfma_f32_16x16x32_bf16 v[148:151], a[152:155], v[100:103], v[148:151]\n"
  "ds_read_b64_tr_b16 a[220:221], v39 offset:40960\n"
  "v_cvt_pk_bf16_f32 v227, v134, v135\n"
  "v_mfma_f32_16x16x32_bf16 v[148:151], a[156:159], v[116:119], v[148:151]\n"
  "v_exp_f32 v136, v136\n"
  "v_mfma_f32_16x16x32_bf16 v[152:155], a[160:163], v[212:215], v[196:199]\n"
  "ds_read_b64_tr_b16 a[222:223], v39 offset:45056\n"
  "buffer_store_dwordx4 v[76:79], v31, s[36:39], s83 offen offset:2048 sc1\n"
  "v_mfma_f32_16x16x32_bf16 v[152:155], a[164:167], v[84:87], v[152:155]\n"
  "v_exp_f32 v137, v137\n"
  "v_mfma_f32_16x16x32_bf16 v[152:155], a[168:171], v[100:103], v[152:155]\n"
  "ds_read_b64_tr_b16 a[224:225], v40 offset:32768\n"
  "v_cvt_pk_bf16_f32 v228, v136, v137\n"
  "v_mfma_f32_16x16x32_bf16 v[152:155], a[172:175], v[116:119], v[152:155]\n"
  "v_exp_f32 v138, v138\n"
  "v_mfma_f32_16x16x32_bf16 v[156:159], a[176:179], v[212:215], v[196:199]\n"
  "ds_read_b64_tr_b16 a[226:227], v40 offset:36864\n"
  "v_mfma_f32_16x16x32_bf16 v[156:159], a[180:183], v[84:87], v[156:159]\n"
  "v_exp_f32 v139, v139\n"
  "v_mfma_f32_16x16x32_bf16 v[156:159], a[184:187], v[100:103], v[156:159]\n"
  "ds_read_b64_tr_b16 a[228:229], v40 offset:40960\n"
  "v_cvt_pk_bf16_f32 v229, v138, v139\n"
  "v_mfma_f32_16x16x32_bf16 v[156:159], a[188:191], v[116:119], v[156:159]\n"
  "v_exp_f32 v140, v140\n"
  "v_mfma_f32_16x16x32_bf16 v[160:163], a[128:131], v[216:219], v[200:203]\n"
  "ds_read_b64_tr_b16 a[230:231], v40 offset:45056\n"
  "v_mfma_f32_16x16x32_bf16 v[160:163], a[132:135], v[88:91], v[160:163]\n"
  "v_exp_f32 v141, v141\n"
  "v_mfma_f32_16x16x32_bf16 v[160:163], a[136:139], v[104:107], v[160:163]\n"
  "ds_read_b64_tr_b16 a[232:233], v41 offset:32768\n"
  "v_cvt_pk_bf16_f32 v230, v140, v141\n"
  "v_mfma_f32_16x16x32_bf16 v[160:163], a[140:143], v[120:123], v[160:163]\n"
  "v_exp_f32 v142, v142\n"
  "v_mfma_f32_16x16x32_bf16 v[164:167], a[144:147], v[216:219], v[200:203]\n"
  "ds_read_b64_tr_b16 a[234:235], v41 offset:36864\n"
  "s_waitcnt vmcnt(4)\n"
  "v_xor_b32 v72, s24, v72\n"
  "v_xor_b32 v73, s24, v73\n"
  "v_mfma_f32_16x16x32_bf16 v[164:167], a[148:151], v[88:91], v[164:167]\n"
  "v_exp_f32 v143, v143\n"
  "v_xor_b32 v74, s24, v74\n"
  "v_mfma_f32_16x16x32_bf16 v[164:167], a[152:155], v[104:107], v[164:167]\n"
  "ds_read_b64_tr_b16 a[236:237], v41 offset:40960\n"
  "v_cvt_pk_bf16_f32 v231, v142, v143\n"
  "v_xor_b32 v75, s24, v75\n"
  "v_mfma_f32_16x16x32_bf16 v[164:167], a[156:159], v[120:123], v[164:167]\n"
  "v_exp_f32 v144, v144\n"
  "v_or3_b32 v72, v72, v73, v74\n"
  "v_mfma_f32_16x16x32_bf16 v[168:171], a[160:163], v[216:219], v[200:203]\n"
  "ds_read_b64_tr_b16 a[238:239], v41 offset:45056\n"
  "v_or_b32 v72, v72, v75\n"
  "v_cmp_ne_u32 vcc, 0, v72\n"
  "v_mfma_f32_16x16x32_bf16 v[168:171], a[164:167], v[88:91], v[168:171]\n"
  "v_exp_f32 v145, v145\n"
  "v_mfma_f32_16x16x32_bf16 v[168:171], a[168:171], v[104:107], v[168:171]\n"
  "ds_read_b64_tr_b16 a[240:241], v42 offset:32768\n"
  "v_cvt_pk_bf16_f32 v232, v144, v145\n"
  "v_mfma_f32_16x16x32_bf16 v[168:171], a[172:175], v[120:123], v[168:171]\n"
  "v_exp_f32 v146, v146\n"
  "v_mfma_f32_16x16x32_bf16 v[172:175], a[176:179], v[216:219], v[200:203]\n"
  "ds_read_b64_tr_b16 a[242:243], v42 offset:36864\n"
  "s_nop 1\n s_cbranch_vccnz Ls0_%=\n Lr0_%=:\n"
  "v_mfma_f32_16x16x32_bf16 v[172:175], a[180:183], v[88:91], v[172:175]\n"
  "v_exp_f32 v147, v147\n"
  "s_waitcnt vmcnt(3)\n"
  "v_cmp_ne_u32 vcc, s24, v12\n"
  "v_mfma_f32_16x16x32_bf16 v[172:175], a[184:187], v[104:107], v[172:175]\n"
  "ds_read_b64_tr_b16 a[244:245], v42 offset:40960\n"
  "v_cvt_pk_bf16_f32 v233, v146, v147\n"
  "v_mfma_f32_16x16x32_bf16 v[172:175], a[188:191], v[120:123], v[172:175]\n"
  "v_exp_f32 v148, v148\n"
  "v_mfma_f32_16x16x32_bf16 v[176:179], a[128:131], v[220:223], v[204:207]\n"
  "ds_read_b64_tr_b16 a[246:247], v42 offset:45056\n"
  "s_nop 1\n s_cbranch_vccnz Ls1_%=\n Lr1_%=:\n"
  "v_mfma_f32_16x16x32_bf16 v[176:179], a[132:135], v[92:95], v[176:179]\n"
  "s_add_u32 m0, s67, 0\n"
  "v_exp_f32 v149, v149\n"
  "v_mfma_f32_16x16x32_bf16 v[176:179], a[136:139], v[108:111], v[176:179]\n"
  "s_add_u32 s66, s70, 0\n"
  "ds_read_b64_tr_b16 a[248:249], v43 offset:32768\n"
  "v_cvt_pk_bf16_f32 v234, v148, v149\n"
  "v_mfma_f32_16x16x32_bf16 v[176:179], a[140:143], v[124:127], v[176:179]\n"
  "v_exp_f32 v150, v150\n"
  "v_mfma_f32_16x16x32_bf16 v[180:183], a[144:147], v[220:223], v[204:207]\n"
  "ds_read_b64_tr_b16 a[250:251], v43 offset:36864\n"
  "buffer_load_dwordx4 v1, s[36:39], s66 offen offset:0 sc1 lds\n"
  "v_mfma_f32_16x16x32_bf16 v[180:183], a[148:151], v[92:95], v[180:183]\n"
  "v_exp_f32 v151, v151\n"
  "v_mfma_f32_16x16x32_bf16 v[180:183], a[152:155], v[108:111], v[180:183]\n"
  "ds_read_b64_tr_b16 a[252:253], v43 offset:40960\n"
  "v_cvt_pk_bf16_f32 v235, v150, v151\n"
  "v_mfma_f32_16x16x32_bf16 v[180:183], a[156:159], v[124:127], v[180:183]\n"
  "v_exp_f32 v152, v152\n"
  "v_mfma_f32_16x16x32_bf16 v[184:187], a[160:163], v[220:223], v[204:207]\n"
  "ds_read_b64_tr_b16 a[254:255], v43 offset:45056\n"
  "buffer_load_dwordx4 v1, s[36:39], s66 offen offset:1024 sc1 lds\n"
  "v_mfma_f32_16x16x32_bf16 v[184:187], a[164:167], v[92:95], v[184:187]\n"
  "s_add_u32 m0, s67, 2048\n"
  "v_exp_f32 v153, v153\n"
  "v_mfma_f32_16x16x32_bf16 v[184:187], a[168:171], v[108:111], v[184:187]\n"
  "s_add_u32 s66, s71, 0\n"
  "v_exp_f32 v154, v154\n"
  "v_mfma_f32_16x16x32_bf16 v[184:187], a[172:175], v[124:127], v[184:187]\n"
  "v_exp_f32 v155, v155\n"
  "v_mfma_f32_16x16x32_bf16 v[188:191], a[176:179], v[220:223], v[204:207]\n"
  "v_exp_f32 v156, v156\n"
  "v_mfma_f32_16x16x32_bf16 v[188:191], a[180:183], v[92:95], v[188:191]\n"
  "v_cvt_pk_bf16_f32 v236, v152, v153\n"
  "v_cvt_pk_bf16_f32 v237, v154, v155\n"
  "v_mfma_f32_16x16x32_bf16 v[188:191], a[184:187], v[108:111], v[188:191]\n"
  "v_exp_f32 v157, v157\n"
  "v_mfma_f32_16x16x32_bf16 v[188:191], a[188:191], v[124:127], v[188:191]\n"
  "s_waitcnt lgkmcnt(0)\n"
  "s_barrier\n"
  "v_exp_f32 v158, v158\n"
  "v_mfma_f32_16x16x32_bf16 v[48:51], v[64:67], v[224:227], v[48:51]\n"
  "v_exp_f32 v159, v159\n"
  "v_mfma_f32_16x16x32_bf16 v[48:51], v[64:67], v[228:231], v[48:51]\n"
  "ds_read_b128 a[128:131], v32 offset:49152\n"
  "v_cvt_pk_bf16_f32 v238, v156, v157\n"
  "v_mfma_f32_16x16x32_bf16 a[0:3], a[192:195], v[224:227], a[0:3]\n"
  "buffer_load_dwordx4 v1, s[36:39], s66 offen offset:0 sc1 lds\n"
  "v_mfma_f32_16x16x32_bf16 a[0:3], a[196:199], v[228:231], a[0:3]\n"
  "v_exp_f32 v160, v160\n"
  "v_mfma_f32_16x16x32_bf16 a[16:19], a[200:203], v[224:227], a[16:19]\n"
  "v_exp_f32 v161, v161\n"
  "v_mfma_f32_16x16x32_bf16 a[16:19], a[204:207], v[228:231], a[16:19]\n"
  "v_exp_f32 v162, v162\n"
  "v_mfma_f32_16x16x32_bf16 a[32:35], a[208:211], v[224:227], a[32:35]\n"
  "v_cvt_pk_bf16_f32 v239, v158, v159\n"
  "v_cvt_pk_bf16_f32 v240, v160, v161\n"
  "v_mfma_f32_16x16x32_bf16 a[32:35], a[212:215], v[228:231], a[32:35]\n"
  "v_exp_f32 v163, v163\n"
  "v_mfma_f32_16x16x32_bf16 a[48:51], a[216:219], v[224:227], a[48:51]\n"
  "v_exp_f32 v164, v164\n"
  "v_mfma_f32_16x16x32_bf16 a[48:51], a[220:223], v[228:231], a[48:51]\n"
  "v_exp_f32 v165, v165\n"
  "v_mfma_f32_16x16x32_bf16 a[64:67], a[224:227], v[224:227], a[64:67]\n"
  "ds_read_b128 a[132:135], v33 offset:49152\n"
  "v_cvt_pk_bf16_f32 v241, v162, v163\n"
  "v_mfma_f32_16x16x32_bf16 a[64:67], a[228:231], v[228:231], a[64:67]\n"
  "buffer_load_dwordx4 v1, s[36:39], s66 offen offset:1024 sc1 lds\n"
  "v_mfma_f32_16x16x32_bf16 a[80:83], a[232:235], v[224:227], a[80:83]\n"
  "v_exp_f32 v166, v166\n"
  "v_mfma_f32_16x16x32_bf16 a[80:83], a[236:239], v[228:231], a[80:83]\n"
  "v_exp_f32 v167, v167\n"
  "v_mfma_f32_16x16x32_bf16 a[96:99], a[240:243], v[224:227], a[96:99]\n"
  "v_exp_f32 v168, v168\n"
  "v_mfma_f32_16x16x32_bf16 a[96:99], a[244:247], v[228:231], a[96:99]\n"
  "v_cvt_pk_bf16_f32 v242, v164, v165\n"
  "v_cvt_pk_bf16_f32 v243, v166, v167\n"
  "v_mfma_f32_16x16x32_bf16 a[112:115], a[248:251], v[224:227], a[112:115]\n"
  "v_exp_f32 v169, v169\n"
  "v_mfma_f32_16x16x32_bf16 a[112:115], a[252:255], v[228:231], a[112:115]\n"
  "v_exp_f32 v170, v170\n"
  "v_mfma_f32_16x16x32_bf16 v[52:55], v[64:67], v[232:235], v[52:55]\n"
  "v_exp_f32 v171, v171\n"
  "v_mfma_f32_16x16x32_bf16 v[52:55], v[64:67], v[236:239], v[52:55]\n"
  "ds_read_b128 a[136:139], v34 offset:49152\n"
  "v_cvt_pk_bf16_f32 v244, v168, v169\n"
  "v_mfma_f32_16x16x32_bf16 a[4:7], a[192:195], v[232:235], a[4:7]\n"
  "s_lshl_b32 s83, s68, 6\n"
  "s_add_u32 s81, s83, s95\n"
  "v_and_b32 v12, 0x7fff0000, v31\n"
  "v_mfma_f32_16x16x32_bf16 a[4:7], a[196:199], v[236:239], a[4:7]\n"
  "s_add_u32 s80, s81, 18432\n"
  "v_exp_f32 v172, v172\n"
  "v_mfma_f32_16x16x32_bf16 a[20:23], a[200:203], v[232:235], a[20:23]\n"
  "s_add_u32 s79, s95, 18432\n"
  "v_exp_f32 v173, v173\n"
  "v_mfma_f32_16x16x32_bf16 a[20:23], a[204:207], v[236:239], a[20:23]\n"
  "v_exp_f32 v174, v174\n"
  "v_mfma_f32_16x16x32_bf16 a[36:39], a[208:211], v[232:235], a[36:39]\n"
  "v_cvt_pk_bf16_f32 v245, v170, v171\n"
  "v_cvt_pk_bf16_f32 v246, v172, v173\n"
  "v_mfma_f32_16x16x32_bf16 a[36:39], a[212:215], v[236:239], a[36:39]\n"
  "v_exp_f32 v175, v175\n"
  "v_mfma_f32_16x16x32_bf16 a[52:55], a[216:219], v[232:235], a[52:55]\n"
  "v_exp_f32 v176, v176\n"
  "v_mfma_f32_16x16x32_bf16 a[52:55], a[220:223], v[236:239], a[52:55]\n"
  "v_exp_f32 v177, v177\n"
  "v_mfma_f32_16x16x32_bf16 a[68:71], a[224:227], v[232:235], a[68:71]\n"
  "ds_read_b128 a[140:143], v35 offset:49152\n"
  "v_cvt_pk_bf16_f32 v247, v174, v175\n"
  "v_mfma_f32_16x16x32_bf16 a[68:71], a[228:231], v[236:239], a[68:71]\n"
  "buffer_load_dwordx2 v[16:17], v14, s[44:47], s81 offen offset:0\n"
  "v_mfma_f32_16x16x32_bf16 a[84:87], a[232:235], v[232:235], a[84:87]\n"
  "s_add_u32 m0, s67, 4096\n"
  "v_exp_f32 v178, v178\n"
  "v_mfma_f32_16x16x32_bf16 a[84:87], a[236:239], v[236:239], a[84:87]\n"
  "s_add_u32 s66, s72, 0\n"
  "v_exp_f32 v179, v179\n"
  "v_mfma_f32_16x16x32_bf16 a[100:103], a[240:243], v[232:235], a[100:103]\n"
  "v_exp_f32 v180, v180\n"
  "v_mfma_f32_16x16x32_bf16 a[100:103], a[244:247], v[236:239], a[100:103]\n"
  "v_cvt_pk_bf16_f32 v248, v176, v177\n"
  "v_cvt_pk_bf16_f32 v249, v178, v179\n"
  "v_mfma_f32_16x16x32_bf16 a[116:119], a[248:251], v[232:235], a[116:119]\n"
  "v_exp_f32 v181, v181\n"
  "v_mfma_f32_16x16x32_bf16 a[116:119], a[252:255], v[236:239], a[116:119]\n"
  "v_exp_f32 v182, v182\n"
  "v_mfma_f32_16x16x32_bf16 v[56:59], v[64:67], v[240:243], v[56:59]\n"
  "v_exp_f32 v183, v183\n"
  "v_mfma_f32_16x16x32_bf16 v[56:59], v[64:67], v[244:247], v[56:59]\n"
  "ds_read_b128 a[144:147], v32 offset:53248\n"
  "v_cvt_pk_bf16_f32 v250, v180, v181\n"
  "v_mfma_f32_16x16x32_bf16 a[8:11], a[192:195], v[240:243], a[8:11]\n"
  "buffer_load_dwordx4 v1, s[36:39], s66 offen offset:0 sc1 lds\n"
  "v_mfma_f32_16x16x32_bf16 a[8:11], a[196:199], v[244:247], a[8:11]\n"
  "v_exp_f32 v184, v184\n"
  "v_mfma_f32_16x16x32_bf16 a[24:27], a[200:203], v[240:243], a[24:27]\n"
  "v_exp_f32 v185, v185\n"
  "v_mfma_f32_16x16x32_bf16 a[24:27], a[204:207], v[244:247], a[24:27]\n"
  "v_exp_f32 v186, v186\n"
  "v_mfma_f32_16x16x32_bf16 a[40:43], a[208:211], v[240:243], a[40:43]\n"
  "v_cvt_pk_bf16_f32 v251, v182, v183\n"
  "v_cvt_pk_bf16_f32 v252, v184, v185\n"
  "v_mfma_f32_16x16x32_bf16 a[40:43], a[212:215], v[244:247], a[40:43]\n"
  "v_exp_f32 v187, v187\n"
  "v_mfma_f32_16x16x32_bf16 a[56:59], a[216:219], v[240:243], a[56:59]\n"
  "v_exp_f32 v188, v188\n"
  "v_mfma_f32_16x16x32_bf16 a[56:59], a[220:223], v[244:247], a[56:59]\n"
  "v_exp_f32 v189, v189\n"
  "v_mfma_f32_16x16x32_bf16 a[72:75], a[224:227], v[240:243], a[72:75]\n"
  "ds_read_b128 a[148:151], v33 offset:53248\n"
  "v_cvt_pk_bf16_f32 v253, v186, v187\n"
  "v_mfma_f32_16x16x32_bf16 a[72:75], a[228:231], v[244:247], a[72:75]\n"
  "buffer_load_dwordx4 v1, s[36:39], s66 offen offset:1024 sc1 lds\n"
  "v_mfma_f32_16x16x32_bf16 a[88:91], a[232:235], v[240:243], a[88:91]\n"
  "s_add_u32 m0, s67, 6144\n"
  "v_exp_f32 v190, v190\n"
  "v_mfma_f32_16x16x32_bf16 a[88:91], a[236:239], v[244:247], a[88:91]\n"
  "s_add_u32 s66, s35, 0\n"
  "v_exp_f32 v191, v191\n"
  "v_mfma_f32_16x16x32_bf16 a[104:107], a[240:243], v[240:243], a[104:107]\n"
  "v_cvt_pk_bf16_f32 v254, v188, v189\n"
  "v_cvt_pk_bf16_f32 v255, v190, v191\n"
  "v_mfma_f32_16x16x32_bf16 a[104:107], a[244:247], v[244:247], a[104:107]\n"
  "ds_read_b128 a[152:155], v34 offset:53248\n"
  "buffer_load_dwordx4 v1, s[36:39], s66 offen offset:0 sc1 lds\n"
  "v_mfma_f32_16x16x32_bf16 a[120:123], a[248:251], v[240:243], a[120:123]\n"
  "v_mfma_f32_16x16x32_bf16 a[120:123], a[252:255], v[244:247], a[120:123]\n"
  "ds_read_b128 a[156:159], v35 offset:53248\n"
  "v_mfma_f32_16x16x32_bf16 v[60:63], v[64:67], v[248:251], v[60:63]\n"
  "v_mfma_f32_16x16x32_bf16 v[60:63], v[64:67], v[252:255], v[60:63]\n"
  "ds_read_b128 a[160:163], v32 offset:57344\n"
  "buffer_load_dwordx4 v1, s[36:39], s66 offen offset:1024 sc1 lds\n"
  "v_mfma_f32_16x16x32_bf16 a[12:15], a[192:195], v[248:251], a[12:15]\n"
  "v_mfma_f32_16x16x32_bf16 a[12:15], a[196:199], v[252:255], a[12:15]\n"
  "ds_read_b128 a[164:167], v33 offset:57344\n"
  "v_mfma_f32_16x16x32_bf16 a[28:31], a[200:203], v[248:251], a[28:31]\n"
  "v_mfma_f32_16x16x32_bf16 a[28:31], a[204:207], v[252:255], a[28:31]\n"
  "ds_read_b128 a[168:171], v34 offset:57344\n"
  "buffer_load_dwordx2 v[18:19], v14, s[44:47], s81 offen offset:32\n"
  "v_mfma_f32_16x16x32_bf16 a[44:47], a[208:211], v[248:251], a[44:47]\n"
  "s_add_u32 m0, s67, 8192\n"
  "s_add_u32 s66, s64, 8192\n"
  "v_mfma_f32_16x16x32_bf16 a[44:47], a[212:215], v[252:255], a[44:47]\n"
  "ds_read_b128 a[172:175], v35 offset:57344\n"
  "v_mfma_f32_16x16x32_bf16 a[60:63], a[216:219], v[248:251], a[60:63]\n"
  "v_add_u32 v36, 65536, v36\n"
  "v_add_u32 v37, 65536, v37\n"
  "v_mfma_f32_16x16x32_bf16 a[60:63], a[220:223], v[252:255], a[60:63]\n"
  "ds_read_b128 a[176:179], v32 offset:61440\n"
  "v_add_u32 v38, 65536, v38\n"
  "v_mfma_f32_16x16x32_bf16 a[76:79], a[224:227], v[248:251], a[76:79]\n"
  "v_add_u32 v39, 65536, v39\n"
  "v_add_u32 v40, 65536, v40\n"
  "v_mfma_f32_16x16x32_bf16 a[76:79], a[228:231], v[252:255], a[76:79]\n"
  "ds_read_b128 a[180:183], v33 offset:61440\n"
  "v_add_u32 v41, 65536, v41\n"
  "v_mfma_f32_16x16x32_bf16 a[92:95], a[232:235], v[248:251], a[92:95]\n"
  "v_add_u32 v42, 65536, v42\n"
  "v_add_u32 v43, 65536, v43\n"
  "v_mfma_f32_16x16x32_bf16 a[92:95], a[236:239], v[252:255], a[92:95]\n"
  "ds_read_b128 a[184:187], v34 offset:61440\n"
  "buffer_load_dwordx4 v1, s[36:39], s66 offen offset:0 sc1 lds\n"
  "v_mfma_f32_16x16x32_bf16 a[108:111], a[240:243], v[248:251], a[108:111]\n"
  "v_mfma_f32_16x16x32_bf16 a[108:111], a[244:247], v[252:255], a[108:111]\n"
  "ds_read_b128 a[188:191], v35 offset:61440\n"
  "v_mfma_f32_16x16x32_bf16 a[124:127], a[248:251], v[248:251], a[124:127]\n"
  "v_mfma_f32_16x16x32_bf16 a[124:127], a[252:255], v[252:255], a[124:127]\n"
  "s_waitcnt lgkmcnt(2)\n"
  "buffer_load_dwordx2 v[20:21], v14, s[44:47], s80 offen offset:0\n"
  "v_mfma_f32_16x16x32_bf16 v[128:131], a[128:131], v[208:211], v[192:195]\n"
  "s_add_u32 m0, s67, 73728\n"
  "s_add_u32 s66, s70, 73728\n"
  "ds_read_b64_tr_b16 a[192:193], v36 offset:0\n"
  "v_mfma_f32_16x16x32_bf16 v[128:131], a[132:135], v[80:83], v[128:131]\n"
  "ds_read_b64_tr_b16 a[194:195], v36 offset:4096\n"
  "v_mfma_f32_16x16x32_bf16 v[128:131], a[136:139], v[96:99], v[128:131]\n"
  "ds_read_b64_tr_b16 a[196:197], v36 offset:8192\n"
  "v_add_u32 v32, 0xffff0000, v32\n"
  "v_mfma_f32_16x16x32_bf16 v[128:131], a[140:143], v[112:115], v[128:131]\n"
  "ds_read_b64_tr_b16 a[198:199], v36 offset:12288\n"
  "v_add_u32 v33, 0xffff0000, v33\n"
  "v_mfma_f32_16x16x32_bf16 v[132:135], a[144:147], v[208:211], v[192:195]\n"
  "ds_read_b64_tr_b16 a[200:201], v37 offset:0\n"
  "v_add_u32 v34, 0xffff0000, v34\n"
  "v_mfma_f32_16x16x32_bf16 v[132:135], a[148:151], v[80:83], v[132:135]\n"
  "ds_read_b64_tr_b16 a[202:203], v37 offset:4096\n"
  "v_add_u32 v35, 0xffff0000, v35\n"
  "v_mfma_f32_16x16x32_bf16 v[132:135], a[152:155], v[96:99], v[132:135]\n"
  "ds_read_b64_tr_b16 a[204:205], v37 offset:8192\n"
  "buffer_load_dwordx4 v1, s[36:39], s66 offen offset:0 sc1 lds\n"
  "v_mfma_f32_16x16x32_bf16 v[132:135], a[156:159], v[112:115], v[132:135]\n"
  "v_exp_f32 v128, v128\n"
  "v_mfma_f32_16x16x32_bf16 v[136:139], a[160:163], v[208:211], v[192:195]\n"
  "ds_read_b64_tr_b16 a[206:207], v37 offset:12288\n"
  "v_mfma_f32_16x16x32_bf16 v[136:139], a[164:167], v[80:83], v[136:139]\n"
  "v_exp_f32 v129, v129\n"
  "v_mfma_f32_16x16x32_bf16 v[136:139], a[168:171], v[96:99], v[136:139]\n"
  "ds_read_b64_tr_b16 a[208:209], v38 offset:0\n"
  "v_cvt_pk_bf16_f32 v224, v128, v129\n"
  "v_mfma_f32_16x16x32_bf16 v[136:139], a[172:175], v[112:115], v[136:139]\n"
  "v_exp_f32 v130, v130\n"
  "v_mfma_f32_16x16x32_bf16 v[140:143], a[176:179], v[208:211], v[192:195]\n"
  "ds_read_b64_tr_b16 a[210:211], v38 offset:4096\n"
  "buffer_load_dwordx4 v1, s[36:39], s66 offen offset:1024 sc1 lds\n"
  "v_mfma_f32_16x16x32_bf16 v[140:143], a[180:183], v[80:83], v[140:143]\n"
  "s_add_u32 m0, s67, 75776\n"
  "v_exp_f32 v131, v131\n"
  "s_waitcnt lgkmcnt(2)\n"
  "v_mfma_f32_16x16x32_bf16 v[140:143], a[184:187], v[96:99], v[140:143]\n"
  "s_add_u32 s66, s71, 73728\n"
  "ds_read_b64_tr_b16 a[212:213], v38 offset:8192\n"
  "v_cvt_pk_bf16_f32 v225, v130, v131\n"
  "v_mfma_f32_16x16x32_bf16 v[140:143], a[188:191], v[112:115], v[140:143]\n"
  "v_exp_f32 v132, v132\n"
  "v_mfma_f32_16x16x32_bf16 v[144:147], a[128:131], v[212:215], v[196:199]\n"
  "ds_read_b64_tr_b16 a[214:215], v38 offset:12288\n"
  "buffer_load_dwordx4 v1, s[36:39], s66 offen offset:0 sc1 lds\n"
  "v_mfma_f32_16x16x32_bf16 v[144:147], a[132:135], v[84:87], v[144:147]\n"
  "v_exp_f32 v133, v133\n"
  "v_mfma_f32_16x16x32_bf16 v[144:147], a[136:139], v[100:103], v[144:147]\n"
  "ds_read_b64_tr_b16 a[216:217], v39 offset:0\n"
  "v_cvt_pk_bf16_f32 v226, v132, v133\n"
  "v_mfma_f32_16x16x32_bf16 v[144:147], a[140:143], v[116:119], v[144:147]\n"
  "v_exp_f32 v134, v134\n"
  "v_mfma_f32_16x16x32_bf16 v[148:151], a[144:147], v[212:215], v[196:199]\n"
  "ds_read_b64_tr_b16 a[218:219], v39 offset:4096\n"
  "buffer_load_dwordx4 v1, s[36:39], s66 offen offset:1024 sc1 lds\n"
  "v_mfma_f32_16x16x32_bf16 v[148:151], a[148:151], v[84:87], v[148:151]\n"
  "v_exp_f32 v135, v135\n"
  "v_mfma_f32_16x16x32_bf16 v[148:151], a[152:155], v[100:103], v[148:151]\n"
  "ds_read_b64_tr_b16 a[220:221], v39 offset:8192\n"
  "v_cvt_pk_bf16_f32 v227, v134, v135\n"
  "v_mfma_f32_16x16x32_bf16 v[148:151], a[156:159], v[116:119], v[148:151]\n"
  "v_exp_f32 v136, v136\n"
  "v_mfma_f32_16x16x32_bf16 v[152:155], a[160:163], v[212:215], v[196:199]\n"
  "ds_read_b64_tr_b16 a[222:223], v39 offset:12288\n"
  "buffer_load_dwordx2 v[44:45], v14, s[44:47], s80 offen offset:32\n"
  "v_mfma_f32_16x16x32_bf16 v[152:155], a[164:167], v[84:87], v[152:155]\n"
  "s_add_u32 m0, s67, 77824\n"
  "v_exp_f32 v137, v137\n"
  "v_mfma_f32_16x16x32_bf16 v[152:155], a[168:171], v[100:103], v[152:155]\n"
  "s_add_u32 s66, s72, 73728\n"
  "ds_read_b64_tr_b16 a[224:225], v40 offset:0\n"
  "v_cvt_pk_bf16_f32 v228, v136, v137\n"
  "v_mfma_f32_16x16x32_bf16 v[152:155], a[172:175], v[116:119], v[152:155]\n"
  "v_exp_f32 v138, v138\n"
  "v_mfma_f32_16x16x32_bf16 v[156:159], a[176:179], v[212:215], v[196:199]\n"
  "ds_read_b64_tr_b16 a[226:227], v40 offset:4096\n"
  "buffer_load_dwordx4 v1, s[36:39], s66 offen offset:0 sc1 lds\n"
  "v_mfma_f32_16x16x32_bf16 v[156:159], a[180:183], v[84:87], v[156:159]\n"
  "v_exp_f32 v139, v139\n"
  "v_mfma_f32_16x16x32_bf16 v[156:159], a[184:187], v[100:103], v[156:159]\n"
  "ds_read_b64_tr_b16 a[228:229], v40 offset:8192\n"
  "v_cvt_pk_bf16_f32 v229, v138, v139\n"
  "v_mfma_f32_16x16x32_bf16 v[156:159], a[188:191], v[116:119], v[156:159]\n"
  "v_exp_f32 v140, v140\n"
  "v_mfma_f32_16x16x32_bf16 v[160:163], a[128:131], v[216:219], v[200:203]\n"
  "ds_read_b64_tr_b16 a[230:231], v40 offset:12288\n"
  "buffer_load_dwordx4 v1, s[36:39], s66 offen offset:1024 sc1 lds\n"
  "v_mfma_f32_16x16x32_bf16 v[160:163], a[132:135], v[88:91], v[160:163]\n"
  "s_add_u32 m0, s67, 79872\n"
  "v_exp_f32 v141, v141\n"
  "v_mfma_f32_16x16x32_bf16 v[160:163], a[136:139], v[104:107], v[160:163]\n"
  "s_add_u32 s66, s35, 73728\n"
  "ds_read_b64_tr_b16 a[232:233], v41 offset:0\n"
  "v_cvt_pk_bf16_f32 v230, v140, v141\n"
  "v_mfma_f32_16x16x32_bf16 v[160:163], a[140:143], v[120:123], v[160:163]\n"
  "v_exp_f32 v142, v142\n"
  "v_mfma_f32_16x16x32_bf16 v[164:167], a[144:147], v[216:219], v[200:203]\n"
  "ds_read_b64_tr_b16 a[234:235], v41 offset:4096\n"
  "buffer_load_dwordx4 v1, s[36:39], s66 offen offset:0 sc1 lds\n"
  "v_mfma_f32_16x16x32_bf16 v[164:167], a[148:151], v[88:91], v[164:167]\n"
  "v_exp_f32 v143, v143\n"
  "v_mfma_f32_16x16x32_bf16 v[164:167], a[152:155], v[104:107], v[164:167]\n"
  "ds_read_b64_tr_b16 a[236:237], v41 offset:8192\n"
  "v_cvt_pk_bf16_f32 v231, v142, v143\n"
  "v_mfma_f32_16x16x32_bf16 v[164:167], a[156:159], v[120:123], v[164:167]\n"
  "v_exp_f32 v144, v144\n"
  "v_mfma_f32_16x16x32_bf16 v[168:171], a[160:163], v[216:219], v[200:203]\n"
  "ds_read_b64_tr_b16 a[238:239], v41 offset:12288\n"
  "buffer_load_dwordx4 v1, s[36:39], s66 offen offset:1024 sc1 lds\n"
  "v_mfma_f32_16x16x32_bf16 v[168:171], a[164:167], v[88:91], v[168:171]\n"
  "v_exp_f32 v145, v145\n"
  "v_mfma_f32_16x16x32_bf16 v[168:171], a[168:171], v[104:107], v[168:171]\n"
  "ds_read_b64_tr_b16 a[240:241], v42 offset:0\n"
  "v_cvt_pk_bf16_f32 v232, v144, v145\n"
  "v_mfma_f32_16x16x32_bf16 v[168:171], a[172:175], v[120:123], v[168:171]\n"
  "v_exp_f32 v146, v146\n"
  "v_mfma_f32_16x16x32_bf16 v[172:175], a[176:179], v[216:219], v[200:203]\n"
  "ds_read_b64_tr_b16 a[242:243], v42 offset:4096\n"
  "v_max_u32 v46, v14, v12\n"
  "v_mfma_f32_16x16x32_bf16 v[172:175], a[180:183], v[88:91], v[172:175]\n"
  "v_exp_f32 v147, v147\n"
  "v_mfma_f32_16x16x32_bf16 v[172:175], a[184:187], v[104:107], v[172:175]\n"
  "ds_read_b64_tr_b16 a[244:245], v42 offset:8192\n"
  "v_cvt_pk_bf16_f32 v233, v146, v147\n"
  "v_mfma_f32_16x16x32_bf16 v[172:175], a[188:191], v[120:123], v[172:175]\n"
  "v_exp_f32 v148, v148\n"
  "v_mfma_f32_16x16x32_bf16 v[176:179], a[128:131], v[220:223], v[204:207]\n"
  "ds_read_b64_tr_b16 a[246:247], v42 offset:12288\n"
  "buffer_load_dwordx2 v[46:47], v46, s[44:47], s95 offen offset:256\n"
  "v_mfma_f32_16x16x32_bf16 v[176:179], a[132:135], v[92:95], v[176:179]\n"
  "s_add_u32 m0, s67, 81920\n"
  "v_exp_f32 v149, v149\n"
  "v_mfma_f32_16x16x32_bf16 v[176:179], a[136:139], v[108:111], v[176:179]\n"
  "s_add_u32 s66, s64, 81920\n"
  "ds_read_b64_tr_b16 a[248:249], v43 offset:0\n"
  "v_cvt_pk_bf16_f32 v234, v148, v149\n"
  "v_mfma_f32_16x16x32_bf16 v[176:179], a[140:143], v[124:127], v[176:179]\n"
  "v_exp_f32 v150, v150\n"
  "v_mfma_f32_16x16x32_bf16 v[180:183], a[144:147], v[220:223], v[204:207]\n"
  "ds_read_b64_tr_b16 a[250:251], v43 offset:4096\n"
  "buffer_load_dwordx4 v1, s[36:39], s66 offen offset:0 sc1 lds\n"
  "v_mfma_f32_16x16x32_bf16 v[180:183], a[148:151], v[92:95], v[180:183]\n"
  "v_exp_f32 v151, v151\n"
  "v_mfma_f32_16x16x32_bf16 v[180:183], a[152:155], v[108:111], v[180:183]\n"
  "ds_read_b64_tr_b16 a[252:253], v43 offset:8192\n"
  "v_cvt_pk_bf16_f32 v235, v150, v151\n"
  "v_mfma_f32_16x16x32_bf16 v[180:183], a[156:159], v[124:127], v[180:183]\n"
  "v_exp_f32 v152, v152\n"
  "v_mfma_f32_16x16x32_bf16 v[184:187], a[160:163], v[220:223], v[204:207]\n"
  "ds_read_b64_tr_b16 a[254:255], v43 offset:12288\n"
  "v_max_u32 v6, v14, v12\n"
  "v_mfma_f32_16x16x32_bf16 v[184:187], a[164:167], v[92:95], v[184:187]\n"
  "v_exp_f32 v153, v153\n"
  "v_mfma_f32_16x16x32_bf16 v[184:187], a[168:171], v[108:111], v[184:187]\n"
  "v_exp_f32 v154, v154\n"
  "v_mfma_f32_16x16x32_bf16 v[184:187], a[172:175], v[124:127], v[184:187]\n"
  "v_exp_f32 v155, v155\n"
  "v_mfma_f32_16x16x32_bf16 v[188:191], a[176:179], v[220:223], v[204:207]\n"
  "v_exp_f32 v156, v156\n"
  "v_mfma_f32_16x16x32_bf16 v[188:191], a[180:183], v[92:95], v[188:191]\n"
  "v_cvt_pk_bf16_f32 v236, v152, v153\n"
  "v_cvt_pk_bf16_f32 v237, v154, v155\n"
  "v_mfma_f32_16x16x32_bf16 v[188:191], a[184:187], v[108:111], v[188:191]\n"
  "v_exp_f32 v157, v157\n"
  "v_mfma_f32_16x16x32_bf16 v[188:191], a[188:191], v[124:127], v[188:191]\n"
  "s_waitcnt lgkmcnt(0)\n"
  "s_barrier\n"
  "v_exp_f32 v158, v158\n"
  "v_mfma_f32_16x16x32_bf16 v[48:51], v[64:67], v[224:227], v[48:51]\n"
  "v_exp_f32 v159, v159\n"
  "v_mfma_f32_16x16x32_bf16 v[48:51], v[64:67], v[228:231], v[48:51]\n"
  "ds_read_b128 a[128:131], v32 offset:49152\n"
  "v_cvt_pk_bf16_f32 v238, v156, v157\n"
  "v_mfma_f32_16x16x32_bf16 a[0:3], a[192:195], v[224:227], a[0:3]\n"
  "buffer_load_dwordx2 v[6:7], v6, s[44:47], s79 offen offset:256\n"
  "v_mfma_f32_16x16x32_bf16 a[0:3], a[196:199], v[228:231], a[0:3]\n"
  "s_add_u32 m0, s67, 110592\n"
  "v_exp_f32 v160, v160\n"
  "v_mfma_f32_16x16x32_bf16 a[16:19], a[200:203], v[224:227], a[16:19]\n"
  "s_add_u32 s66, s70, 110592\n"
  "v_exp_f32 v161, v161\n"
  "v_mfma_f32_16x16x32_bf16 a[16:19], a[204:207], v[228:231], a[16:19]\n"
  "v_exp_f32 v162, v162\n"
  "v_mfma_f32_16x16x32_bf16 a[32:35], a[208:211], v[224:227], a[32:35]\n"
  "v_cvt_pk_bf16_f32 v239, v158, v159\n"
  "v_cvt_pk_bf16_f32 v240, v160, v161\n"
  "v_mfma_f32_16x16x32_bf16 a[32:35], a[212:215], v[228:231], a[32:35]\n"
  "v_exp_f32 v163, v163\n"
  "v_mfma_f32_16x16x32_bf16 a[48:51], a[216:219], v[224:227], a[48:51]\n"
  "v_exp_f32 v164, v164\n"
  "v_mfma_f32_16x16x32_bf16 a[48:51], a[220:223], v[228:231], a[48:51]\n"
  "v_exp_f32 v165, v165\n"
  "v_mfma_f32_16x16x32_bf16 a[64:67], a[224:227], v[224:227], a[64:67]\n"
  "ds_read_b128 a[132:135], v33 offset:49152\n"
  "v_cvt_pk_bf16_f32 v241, v162, v163\n"
  "v_mfma_f32_16x16x32_bf16 a[64:67], a[228:231], v[228:231], a[64:67]\n"
  "buffer_load_dwordx4 v1, s[36:39], s66 offen offset:0 sc1 lds\n"
  "v_mfma_f32_16x16x32_bf16 a[80:83], a[232:235], v[224:227], a[80:83]\n"
  "v_exp_f32 v166, v166\n"
  "v_mfma_f32_16x16x32_bf16 a[80:83], a[236:239], v[228:231], a[80:83]\n"
  "v_exp_f32 v167, v167\n"
  "v_mfma_f32_16x16x32_bf16 a[96:99], a[240:243], v[224:227], a[96:99]\n"
  "v_exp_f32 v168, v168\n"
  "v_mfma_f32_16x16x32_bf16 a[96:99], a[244:247], v[228:231], a[96:99]\n"
  "v_cvt_pk_bf16_f32 v242, v164, v165\n"
  "v_cvt_pk_bf16_f32 v243, v166, v167\n"
  "v_mfma_f32_16x16x32_bf16 a[112:115], a[248:251], v[224:227], a[112:115]\n"
  "v_exp_f32 v169, v169\n"
  "v_mfma_f32_16x16x32_bf16 a[112:115], a[252:255], v[228:231], a[112:115]\n"
  "v_exp_f32 v170, v170\n"
  "v_mfma_f32_16x16x32_bf16 v[52:55], v[64:67], v[232:235], v[52:55]\n"
  "v_exp_f32 v171, v171\n"
  "v_mfma_f32_16x16x32_bf16 v[52:55], v[64:67], v[236:239], v[52:55]\n"
  "ds_read_b128 a[136:139], v34 offset:49152\n"
  "v_cvt_pk_bf16_f32 v244, v168, v169\n"
  "v_mfma_f32_16x16x32_bf16 a[4:7], a[192:195], v[232:235], a[4:7]\n"
  "buffer_load_dwordx4 v1, s[36:39], s66 offen offset:1024 sc1 lds\n"
  "v_mfma_f32_16x16x32_bf16 a[4:7], a[196:199], v[236:239], a[4:7]\n"
  "s_add_u32 m0, s67, 112640\n"
  "v_exp_f32 v172, v172\n"
  "v_mfma_f32_16x16x32_bf16 a[20:23], a[200:203], v[232:235], a[20:23]\n"
  "s_add_u32 s66, s71, 110592\n"
  "v_exp_f32 v173, v173\n"
  "v_mfma_f32_16x16x32_bf16 a[20:23], a[204:207], v[236:239], a[20:23]\n"
  "v_exp_f32 v174, v174\n"
  "v_mfma_f32_16x16x32_bf16 a[36:39], a[208:211], v[232:235], a[36:39]\n"
  "v_cvt_pk_bf16_f32 v245, v170, v171\n"
  "v_cvt_pk_bf16_f32 v246, v172, v173\n"
  "v_mfma_f32_16x16x32_bf16 a[36:39], a[212:215], v[236:239], a[36:39]\n"
  "v_exp_f32 v175, v175\n"
  "v_mfma_f32_16x16x32_bf16 a[52:55], a[216:219], v[232:235], a[52:55]\n"
  "v_exp_f32 v176, v176\n"
  "v_mfma_f32_16x16x32_bf16 a[52:55], a[220:223], v[236:239], a[52:55]\n"
  "v_exp_f32 v177, v177\n"
  "v_mfma_f32_16x16x32_bf16 a[68:71], a[224:227], v[232:235], a[68:71]\n"
  "ds_read_b128 a[140:143], v35 offset:49152\n"
  "v_cvt_pk_bf16_f32 v247, v174, v175\n"
  "v_mfma_f32_16x16x32_bf16 a[68:71], a[228:231], v[236:239], a[68:71]\n"
  "buffer_load_dwordx4 v1, s[36:39], s66 offen offset:0 sc1 lds\n"
  "v_mfma_f32_16x16x32_bf16 a[84:87], a[232:235], v[232:235], a[84:87]\n"
  "v_exp_f32 v178, v178\n"
  "v_mfma_f32_16x16x32_bf16 a[84:87], a[236:239], v[236:239], a[84:87]\n"
  "v_exp_f32 v179, v179\n"
  "v_mfma_f32_16x16x32_bf16 a[100:103], a[240:243], v[232:235], a[100:103]\n"
  "v_exp_f32 v180, v180\n"
  "v_mfma_f32_16x16x32_bf16 a[100:103], a[244:247], v[236:239], a[100:103]\n"
  "v_cvt_pk_bf16_f32 v248, v176, v177\n"
  "v_cvt_pk_bf16_f32 v249, v178, v179\n"
  "v_mfma_f32_16x16x32_bf16 a[116:119], a[248:251], v[232:235], a[116:119]\n"
  "v_exp_f32 v181, v181\n"
  "v_mfma_f32_16x16x32_bf16 a[116:119], a[252:255], v[236:239], a[116:119]\n"
  "v_exp_f32 v182, v182\n"
  "v_mfma_f32_16x16x32_bf16 v[56:59], v[64:67], v[240:243], v[56:59]\n"
  "v_exp_f32 v183, v183\n"
  "v_mfma_f32_16x16x32_bf16 v[56:59], v[64:67], v[244:247], v[56:59]\n"
  "ds_read_b128 a[144:147], v32 offset:53248\n"
  "v_cvt_pk_bf16_f32 v250, v180, v181\n"
  "v_mfma_f32_16x16x32_bf16 a[8:11], a[192:195], v[240:243], a[8:11]\n"
  "buffer_load_dwordx4 v1, s[36:39], s66 offen offset:1024 sc1 lds\n"
  "v_mfma_f32_16x16x32_bf16 a[8:11], a[196:199], v[244:247], a[8:11]\n"
  "s_add_u32 m0, s67, 114688\n"
  "v_exp_f32 v184, v184\n"
  "v_mfma_f32_16x16x32_bf16 a[24:27], a[200:203], v[240:243], a[24:27]\n"
  "s_add_u32 s66, s72, 110592\n"
  "v_exp_f32 v185, v185\n"
  "v_mfma_f32_16x16x32_bf16 a[24:27], a[204:207], v[244:247], a[24:27]\n"
  "v_exp_f32 v186, v186\n"
  "v_mfma_f32_16x16x32_bf16 a[40:43], a[208:211], v[240:243], a[40:43]\n"
  "v_cvt_pk_bf16_f32 v251, v182, v183\n"
  "v_cvt_pk_bf16_f32 v252, v184, v185\n"
  "v_mfma_f32_16x16x32_bf16 a[40:43], a[212:215], v[244:247], a[40:43]\n"
  "v_exp_f32 v187, v187\n"
  "v_mfma_f32_16x16x32_bf16 a[56:59], a[216:219], v[240:243], a[56:59]\n"
  "v_exp_f32 v188, v188\n"
  "v_mfma_f32_16x16x32_bf16 a[56:59], a[220:223], v[244:247], a[56:59]\n"
  "v_exp_f32 v189, v189\n"
  "v_mfma_f32_16x16x32_bf16 a[72:75], a[224:227], v[240:243], a[72:75]\n"
  "ds_read_b128 a[148:151], v33 offset:53248\n"
  "v_cvt_pk_bf16_f32 v253, v186, v187\n"
  "v_mfma_f32_16x16x32_bf16 a[72:75], a[228:231], v[244:247], a[72:75]\n"
  "buffer_load_dwordx4 v1, s[36:39], s66 offen offset:0 sc1 lds\n"
  "v_mfma_f32_16x16x32_bf16 a[88:91], a[232:235], v[240:243], a[88:91]\n"
  "v_exp_f32 v190, v190\n"
  "v_mfma_f32_16x16x32_bf16 a[88:91], a[236:239], v[244:247], a[88:91]\n"
  "v_exp_f32 v191, v191\n"
  "v_mfma_f32_16x16x32_bf16 a[104:107], a[240:243], v[240:243], a[104:107]\n"
  "v_cvt_pk_bf16_f32 v254, v188, v189\n"
  "v_cvt_pk_bf16_f32 v255, v190, v191\n"
  "v_mfma_f32_16x16x32_bf16 a[104:107], a[244:247], v[244:247], a[104:107]\n"
  "ds_read_b128 a[152:155], v34 offset:53248\n"
  "buffer_load_dwordx4 v1, s[36:39], s66 offen offset:1024 sc1 lds\n"
  "v_mfma_f32_16x16x32_bf16 a[120:123], a[248:251], v[240:243], a[120:123]\n"
  "s_add_u32 m0, s67, 116736\n"
  "s_add_u32 s66, s35, 110592\n"
  "v_mfma_f32_16x16x32_bf16 a[120:123], a[252:255], v[244:247], a[120:123]\n"
  "ds_read_b128 a[156:159], v35 offset:53248\n"
  "v_mfma_f32_16x16x32_bf16 v[60:63], v[64:67], v[248:251], v[60:63]\n"
  "v_mfma_f32_16x16x32_bf16 v[60:63], v[64:67], v[252:255], v[60:63]\n"
  "ds_read_b128 a[160:163], v32 offset:57344\n"
  "buffer_load_dwordx4 v1, s[36:39], s66 offen offset:0 sc1 lds\n"
  "v_mfma_f32_16x16x32_bf16 a[12:15], a[192:195], v[248:251], a[12:15]\n"
  "v_mfma_f32_16x16x32_bf16 a[12:15], a[196:199], v[252:255], a[12:15]\n"
  "ds_read_b128 a[164:167], v33 offset:57344\n"
  "v_mfma_f32_16x16x32_bf16 a[28:31], a[200:203], v[248:251], a[28:31]\n"
  "v_mfma_f32_16x16x32_bf16 a[28:31], a[204:207], v[252:255], a[28:31]\n"
  "ds_read_b128 a[168:171], v34 offset:57344\n"
  "buffer_load_dwordx4 v1, s[36:39], s66 offen offset:1024 sc1 lds\n"
  "v_mfma_f32_16x16x32_bf16 a[44:47], a[208:211], v[248:251], a[44:47]\n"
  "s_add_u32 m0, s67, 118784\n"
  "s_add_u32 s66, s64, 118784\n"
  "v_mfma_f32_16x16x32_bf16 a[44:47], a[212:215], v[252:255], a[44:47]\n"
  "ds_read_b128 a[172:175], v35 offset:57344\n"
  "v_mfma_f32_16x16x32_bf16 a[60:63], a[216:219], v[248:251], a[60:63]\n"
  "v_mfma_f32_16x16x32_bf16 a[60:63], a[220:223], v[252:255], a[60:63]\n"
  "ds_read_b128 a[176:179], v32 offset:61440\n"
  "buffer_load_dwordx4 v1, s[36:39], s66 offen offset:0 sc1 lds\n"
  "v_mfma_f32_16x16x32_bf16 a[76:79], a[224:227], v[248:251], a[76:79]\n"
  "v_mfma_f32_16x16x32_bf16 a[76:79], a[228:231], v[252:255], a[76:79]\n"
  "ds_read_b128 a[180:183], v33 offset:61440\n"
  "v_mfma_f32_16x16x32_bf16 a[92:95], a[232:235], v[248:251], a[92:95]\n"
  "v_mfma_f32_16x16x32_bf16 a[92:95], a[236:239], v[252:255], a[92:95]\n"
  "ds_read_b128 a[184:187], v34 offset:61440\n"
  "v_mfma_f32_16x16x32_bf16 a[108:111], a[240:243], v[248:251], a[108:111]\n"
  "v_mfma_f32_16x16x32_bf16 a[108:111], a[244:247], v[252:255], a[108:111]\n"
  "ds_read_b128 a[188:191], v35 offset:61440\n"
  "v_mfma_f32_16x16x32_bf16 a[124:127], a[248:251], v[248:251], a[124:127]\n"
  "v_mfma_f32_16x16x32_bf16 a[124:127], a[252:255], v[252:255], a[124:127]\n"
  "s_waitcnt lgkmcnt(4)\n"
  "v_mfma_f32_16x16x32_bf16 v[128:131], a[128:131], v[208:211], v[192:195]\n"
  "ds_read_b64_tr_b16 a[192:193], v36 offset:16384\n"
  "v_mfma_f32_16x16x32_bf16 v[128:131], a[132:135], v[80:83], v[128:131]\n"
  "ds_read_b64_tr_b16 a[194:195], v36 offset:20480\n"
  "v_mfma_f32_16x16x32_bf16 v[128:131], a[136:139], v[96:99], v[128:131]\n"
  "ds_read_b64_tr_b16 a[196:197], v36 offset:24576\n"
  "v_mfma_f32_16x16x32_bf16 v[128:131], a[140:143], v[112:115], v[128:131]\n"
  "ds_read_b64_tr_b16 a[198:199], v36 offset:28672\n"
  "v_mfma_f32_16x16x32_bf16 v[132:135], a[144:147], v[208:211], v[192:195]\n"
  "ds_read_b64_tr_b16 a[200:201], v37 offset:16384\n"
  "v_mfma_f32_16x16x32_bf16 v[132:135], a[148:151], v[80:83], v[132:135]\n"
  "ds_read_b64_tr_b16 a[202:203], v37 offset:20480\n"
  "v_mfma_f32_16x16x32_bf16 v[132:135], a[152:155], v[96:99], v[132:135]\n"
  "ds_read_b64_tr_b16 a[204:205], v37 offset:24576\n"
  "v_mfma_f32_16x16x32_bf16 v[132:135], a[156:159], v[112:115], v[132:135]\n"
  "v_exp_f32 v128, v128\n"
  "v_mfma_f32_16x16x32_bf16 v[136:139], a[160:163], v[208:211], v[192:195]\n"
  "ds_read_b64_tr_b16 a[206:207], v37 offset:28672\n"
  "v_mfma_f32_16x16x32_bf16 v[136:139], a[164:167], v[80:83], v[136:139]\n"
  "v_exp_f32 v129, v129\n"
  "v_mfma_f32_16x16x32_bf16 v[136:139], a[168:171], v[96:99], v[136:139]\n"
  "ds_read_b64_tr_b16 a[208:209], v38 offset:16384\n"
  "v_cvt_pk_bf16_f32 v224, v128, v129\n"
  "v_mfma_f32_16x16x32_bf16 v[136:139], a[172:175], v[112:115], v[136:139]\n"
  "s_waitcnt lgkmcnt(3)\n"
  "v_exp_f32 v130, v130\n"
  "v_mfma_f32_16x16x32_bf16 v[140:143], a[176:179], v[208:211], v[192:195]\n"
  "ds_read_b64_tr_b16 a[210:211], v38 offset:20480\n"
  "v_mfma_f32_16x16x32_bf16 v[140:143], a[180:183], v[80:83], v[140:143]\n"
  "s_add_u32 m0, s67, 36864\n"
  "v_exp_f32 v131, v131\n"
  "v_mfma_f32_16x16x32_bf16 v[140:143], a[184:187], v[96:99], v[140:143]\n"
  "s_add_u32 s66, s70, 36864\n"
  "ds_read_b64_tr_b16 a[212:213], v38 offset:24576\n"
  "v_cvt_pk_bf16_f32 v225, v130, v131\n"
  "v_mfma_f32_16x16x32_bf16 v[140:143], a[188:191], v[112:115], v[140:143]\n"
  "v_exp_f32 v132, v132\n"
  "v_mfma_f32_16x16x32_bf16 v[144:147], a[128:131], v[212:215], v[196:199]\n"
  "ds_read_b64_tr_b16 a[214:215], v38 offset:28672\n"
  "v_mfma_f32_16x16x32_bf16 v[144:147], a[132:135], v[84:87], v[144:147]\n"
  "v_exp_f32 v133, v133\n"
  "v_mfma_f32_16x16x32_bf16 v[144:147], a[136:139], v[100:103], v[144:147]\n"
  "ds_read_b64_tr_b16 a[216:217], v39 offset:16384\n"
  "s_barrier\n"
  "v_cvt_pk_bf16_f32 v226, v132, v133\n"
  "v_mfma_f32_16x16x32_bf16 v[144:147], a[140:143], v[116:119], v[144:147]\n"
  "v_exp_f32 v134, v134\n"
  "v_mfma_f32_16x16x32_bf16 v[148:151], a[144:147], v[212:215], v[196:199]\n"
  "ds_read_b64_tr_b16 a[218:219], v39 offset:20480\n"
  "buffer_load_dwordx4 v1, s[36:39], s66 offen offset:0 sc1 lds\n"
  "v_mfma_f32_16x16x32_bf16 v[148:151], a[148:151], v[84:87], v[148:151]\n"
  "v_exp_f32 v135, v135\n"
  "v_mfma_f32_16x16x32_bf16 v[148:151], a[152:155], v[100:103], v[148:151]\n"
  "ds_read_b64_tr_b16 a[220:221], v39 offset:24576\n"
  "v_cvt_pk_bf16_f32 v227, v134, v135\n"
  "v_mfma_f32_16x16x32_bf16 v[148:151], a[156:159], v[116:119], v[148:151]\n"
  "v_exp_f32 v136, v136\n"
  "v_mfma_f32_16x16x32_bf16 v[152:155], a[160:163], v[212:215], v[196:199]\n"
  "ds_read_b64_tr_b16 a[222:223], v39 offset:28672\n"
  "buffer_load_dwordx4 v1, s[36:39], s66 offen offset:1024 sc1 lds\n"
  "v_mfma_f32_16x16x32_bf16 v[152:155], a[164:167], v[84:87], v[152:155]\n"
  "s_add_u32 m0, s67, 38912\n"
  "v_exp_f32 v137, v137\n"
  "v_mfma_f32_16x16x32_bf16 v[152:155], a[168:171], v[100:103], v[152:155]\n"
  "s_add_u32 s66, s71, 36864\n"
  "ds_read_b64_tr_b16 a[224:225], v40 offset:16384\n"
  "v_cvt_pk_bf16_f32 v228, v136, v137\n"
  "v_mfma_f32_16x16x32_bf16 v[152:155], a[172:175], v[116:119], v[152:155]\n"
  "v_exp_f32 v138, v138\n"
  "v_mfma_f32_16x16x32_bf16 v[156:159], a[176:179], v[212:215], v[196:199]\n"
  "ds_read_b64_tr_b16 a[226:227], v40 offset:20480\n"
  "buffer_load_dwordx4 v1, s[36:39], s66 offen offset:0 sc1 lds\n"
  "v_mfma_f32_16x16x32_bf16 v[156:159], a[180:183], v[84:87], v[156:159]\n"
  "v_exp_f32 v139, v139\n"
  "v_mfma_f32_16x16x32_bf16 v[156:159], a[184:187], v[100:103], v[156:159]\n"
  "ds_read_b64_tr_b16 a[228:229], v40 offset:24576\n"
  "v_cvt_pk_bf16_f32 v229, v138, v139\n"
  "v_mfma_f32_16x16x32_bf16 v[156:159], a[188:191], v[116:119], v[156:159]\n"
  "v_exp_f32 v140, v140\n"
  "v_mfma_f32_16x16x32_bf16 v[160:163], a[128:131], v[216:219], v[200:203]\n"
  "ds_read_b64_tr_b16 a[230:231], v40 offset:28672\n"
  "buffer_load_dwordx4 v1, s[36:39], s66 offen offset:1024 sc1 lds\n"
  "v_mfma_f32_16x16x32_bf16 v[160:163], a[132:135], v[88:91], v[160:163]\n"
  "s_add_u32 m0, s67, 40960\n"
  "v_exp_f32 v141, v141\n"
  "v_mfma_f32_16x16x32_bf16 v[160:163], a[136:139], v[104:107], v[160:163]\n"
  "s_add_u32 s66, s72, 36864\n"
  "ds_read_b64_tr_b16 a[232:233], v41 offset:16384\n"
  "v_cvt_pk_bf16_f32 v230, v140, v141\n"
  "v_mfma_f32_16x16x32_bf16 v[160:163], a[140:143], v[120:123], v[160:163]\n"
  "v_exp_f32 v142, v142\n"
  "v_mfma_f32_16x16x32_bf16 v[164:167], a[144:147], v[216:219], v[200:203]\n"
  "ds_read_b64_tr_b16 a[234:235], v41 offset:20480\n"
  "buffer_load_dwordx4 v1, s[36:39], s66 offen offset:0 sc1 lds\n"
  "v_mfma_f32_16x16x32_bf16 v[164:167], a[148:151], v[88:91], v[164:167]\n"
  "v_exp_f32 v143, v143\n"
  "v_mfma_f32_16x16x32_bf16 v[164:167], a[152:155], v[104:107], v[164:167]\n"
  "ds_read_b64_tr_b16 a[236:237], v41 offset:24576\n"
  "v_cvt_pk_bf16_f32 v231, v142, v143\n"
  "v_mfma_f32_16x16x32_bf16 v[164:167], a[156:159], v[120:123], v[164:167]\n"
  "v_exp_f32 v144, v144\n"
  "v_mfma_f32_16x16x32_bf16 v[168:171], a[160:163], v[216:219], v[200:203]\n"
  "ds_read_b64_tr_b16 a[238:239], v41 offset:28672\n"
  "buffer_load_dwordx4 v1, s[36:39], s66 offen offset:1024 sc1 lds\n"
  "v_mfma_f32_16x16x32_bf16 v[168:171], a[164:167], v[88:91], v[168:171]\n"
  "s_add_u32 m0, s67, 43008\n"
  "v_exp_f32 v145, v145\n"
  "v_mfma_f32_16x16x32_bf16 v[168:171], a[168:171], v[104:107], v[168:171]\n"
  "s_add_u32 s66, s35, 36864\n"
  "ds_read_b64_tr_b16 a[240:241], v42 offset:16384\n"
  "v_cvt_pk_bf16_f32 v232, v144, v145\n"
  "v_mfma_f32_16x16x32_bf16 v[168:171], a[172:175], v[120:123], v[168:171]\n"
  "v_exp_f32 v146, v146\n"
  "v_mfma_f32_16x16x32_bf16 v[172:175], a[176:179], v[216:219], v[200:203]\n"
  "ds_read_b64_tr_b16 a[242:243], v42 offset:20480\n"
  "buffer_load_dwordx4 v1, s[36:39], s66 offen offset:0 sc1 lds\n"
  "v_mfma_f32_16x16x32_bf16 v[172:175], a[180:183], v[88:91], v[172:175]\n"
  "v_exp_f32 v147, v147\n"
  "v_mfma_f32_16x16x32_bf16 v[172:175], a[184:187], v[104:107], v[172:175]\n"
  "ds_read_b64_tr_b16 a[244:245], v42 offset:24576\n"
  "v_cvt_pk_bf16_f32 v233, v146, v147\n"
  "v_mfma_f32_16x16x32_bf16 v[172:175], a[188:191], v[120:123], v[172:175]\n"
  "v_exp_f32 v148, v148\n"
  "v_mfma_f32_16x16x32_bf16 v[176:179], a[128:131], v[220:223], v[204:207]\n"
  "ds_read_b64_tr_b16 a[246:247], v42 offset:28672\n"
  "buffer_load_dwordx4 v1, s[36:39], s66 offen offset:1024 sc1 lds\n"
  "v_mfma_f32_16x16x32_bf16 v[176:179], a[132:135], v[92:95], v[176:179]\n"
  "s_add_u32 m0, s67, 45056\n"
  "v_exp_f32 v149, v149\n"
  "v_mfma_f32_16x16x32_bf16 v[176:179], a[136:139], v[108:111], v[176:179]\n"
  "s_add_u32 s66, s64, 45056\n"
  "ds_read_b64_tr_b16 a[248:249], v43 offset:16384\n"
  "v_cvt_pk_bf16_f32 v234, v148, v149\n"
  "v_mfma_f32_16x16x32_bf16 v[176:179], a[140:143], v[124:127], v[176:179]\n"
  "v_exp_f32 v150, v150\n"
  "v_mfma_f32_16x16x32_bf16 v[180:183], a[144:147], v[220:223], v[204:207]\n"
  "ds_read_b64_tr_b16 a[250:251], v43 offset:20480\n"
  "buffer_load_dwordx4 v1, s[36:39], s66 offen offset:0 sc1 lds\n"
  "v_mfma_f32_16x16x32_bf16 v[180:183], a[148:151], v[92:95], v[180:183]\n"
  "v_exp_f32 v151, v151\n"
  "v_mfma_f32_16x16x32_bf16 v[180:183], a[152:155], v[108:111], v[180:183]\n"
  "ds_read_b64_tr_b16 a[252:253], v43 offset:24576\n"
  "v_cvt_pk_bf16_f32 v235, v150, v151\n"
  "v_mfma_f32_16x16x32_bf16 v[180:183], a[156:159], v[124:127], v[180:183]\n"
  "v_exp_f32 v152, v152\n"
  "v_mfma_f32_16x16x32_bf16 v[184:187], a[160:163], v[220:223], v[204:207]\n"
  "ds_read_b64_tr_b16 a[254:255], v43 offset:28672\n"
  "v_mov_b32 v77, 98304\n"
  "v_mfma_f32_16x16x32_bf16 v[184:187], a[164:167], v[92:95], v[184:187]\n"
  "v_exp_f32 v153, v153\n"
  "v_mfma_f32_16x16x32_bf16 v[184:187], a[168:171], v[108:111], v[184:187]\n"
  "v_exp_f32 v154, v154\n"
  "v_mfma_f32_16x16x32_bf16 v[184:187], a[172:175], v[124:127], v[184:187]\n"
  "v_exp_f32 v155, v155\n"
  "v_mfma_f32_16x16x32_bf16 v[188:191], a[176:179], v[220:223], v[204:207]\n"
  "s_add_u32 s62, s60, 0\n"
  "v_mov_b32 v78, 0\n"
  "v_mfma_f32_16x16x32_bf16 v[188:191], a[180:183], v[92:95], v[188:191]\n"
  "v_exp_f32 v156, v156\n"
  "v_mfma_f32_16x16x32_bf16 v[188:191], a[184:187], v[108:111], v[188:191]\n"
  "v_exp_f32 v157, v157\n"
  "v_mfma_f32_16x16x32_bf16 v[188:191], a[188:191], v[124:127], v[188:191]\n"
  "s_waitcnt lgkmcnt(0)\n"
  "s_barrier\n"
  "v_cvt_pk_bf16_f32 v236, v152, v153\n"
  "v_cvt_pk_bf16_f32 v237, v154, v155\n"
  "v_mfma_f32_16x16x32_bf16 v[48:51], v[64:67], v[224:227], v[48:51]\n"
  "v_exp_f32 v158, v158\n"
  "v_mfma_f32_16x16x32_bf16 v[48:51], v[64:67], v[228:231], v[48:51]\n"
  "ds_write_b32 v77, v78 offset:65532\n"
  "v_exp_f32 v159, v159\n"
  "v_mfma_f32_16x16x32_bf16 a[0:3], a[192:195], v[224:227], a[0:3]\n"
  "buffer_load_dwordx4 a[128:131], v1, s[36:39], s62 offen offset:0 sc1\n"
  "v_mfma_f32_16x16x32_bf16 a[0:3], a[196:199], v[228:231], a[0:3]\n"
  "s_add_u32 s63, s61, 0\n"
  "v_exp_f32 v160, v160\n"
  "v_mfma_f32_16x16x32_bf16 a[16:19], a[200:203], v[224:227], a[16:19]\n"
  "v_exp_f32 v161, v161\n"
  "v_cmp_nge_f32 vcc, 0x5d800000, v48\n"
  "v_mfma_f32_16x16x32_bf16 a[16:19], a[204:207], v[228:231], a[16:19]\n"
  "s_nop 0\n"
  "s_mov_b64 s[98:99], vcc\n"
  "v_cvt_pk_bf16_f32 v238, v156, v157\n"
  "v_cvt_pk_bf16_f32 v239, v158, v159\n"
  "v_rcp_f32 v8, v48\n"
  "v_mfma_f32_16x16x32_bf16 a[32:35], a[208:211], v[224:227], a[32:35]\n"
  "v_exp_f32 v162, v162\n"
  "v_mfma_f32_16x16x32_bf16 a[32:35], a[212:215], v[228:231], a[32:35]\n"
  "v_exp_f32 v163, v163\n"
  "v_mfma_f32_16x16x32_bf16 a[48:51], a[216:219], v[224:227], a[48:51]\n"
  "buffer_load_dwordx4 a[132:135], v1, s[36:39], s63 offen offset:0 sc1\n"
  "v_mfma_f32_16x16x32_bf16 a[48:51], a[220:223], v[228:231], a[48:51]\n"
  "v_exp_f32 v164, v164\n"
  "v_mfma_f32_16x16x32_bf16 a[64:67], a[224:227], v[224:227], a[64:67]\n"
  "v_exp_f32 v165, v165\n"
  "v_mfma_f32_16x16x32_bf16 a[64:67], a[228:231], v[228:231], a[64:67]\n"
  "v_cvt_pk_bf16_f32 v240, v160, v161\n"
  "v_cvt_pk_bf16_f32 v241, v162, v163\n"
  "v_mfma_f32_16x16x32_bf16 a[80:83], a[232:235], v[224:227], a[80:83]\n"
  "v_exp_f32 v166, v166\n"
  "v_mfma_f32_16x16x32_bf16 a[80:83], a[236:239], v[228:231], a[80:83]\n"
  "v_exp_f32 v167, v167\n"
  "v_mfma_f32_16x16x32_bf16 a[96:99], a[240:243], v[224:227], a[96:99]\n"
  "buffer_load_dwordx4 a[136:139], v1, s[36:39], s62 offen offset:1024 sc1\n"
  "v_mfma_f32_16x16x32_bf16 a[96:99], a[244:247], v[228:231], a[96:99]\n"
  "v_exp_f32 v168, v168\n"
  "v_mfma_f32_16x16x32_bf16 a[112:115], a[248:251], v[224:227], a[112:115]\n"
  "s_waitcnt lgkmcnt(0)\n"
  "s_barrier\n"
  "v_exp_f32 v169, v169\n"
  "v_mfma_f32_16x16x32_bf16 a[112:115], a[252:255], v[228:231], a[112:115]\n"
  "v_cvt_pk_bf16_f32 v242, v164, v165\n"
  "v_cvt_pk_bf16_f32 v243, v166, v167\n"
  "v_mfma_f32_16x16x32_bf16 v[52:55], v[64:67], v[232:235], v[52:55]\n"
  "v_exp_f32 v170, v170\n"
  "v_mfma_f32_16x16x32_bf16 v[52:55], v[64:67], v[236:239], v[52:55]\n"
  "v_exp_f32 v171, v171\n"
  "v_mfma_f32_16x16x32_bf16 a[4:7], a[192:195], v[232:235], a[4:7]\n"
  "buffer_load_dwordx4 a[140:143], v1, s[36:39], s63 offen offset:1024 sc1\n"
  "v_mfma_f32_16x16x32_bf16 a[4:7], a[196:199], v[236:239], a[4:7]\n"
  "v_exp_f32 v172, v172\n"
  "v_mfma_f32_16x16x32_bf16 a[20:23], a[200:203], v[232:235], a[20:23]\n"
  "v_exp_f32 v173, v173\n"
  "s_nop 0\n"
  "v_cmp_nge_f32 vcc, 0x5d800000, v52\n"
  "v_mfma_f32_16x16x32_bf16 a[20:23], a[204:207], v[236:239], a[20:23]\n"
  "s_nop 0\n"
  "s_or_b64 s[98:99], s[98:99], vcc\n"
  "v_cvt_pk_bf16_f32 v244, v168, v169\n"
  "v_cvt_pk_bf16_f32 v245, v170, v171\n"
  "v_rcp_f32 v9, v52\n"
  "v_mfma_f32_16x16x32_bf16 a[36:39], a[208:211], v[232:235], a[36:39]\n"
  "v_exp_f32 v174, v174\n"
  "v_mfma_f32_16x16x32_bf16 a[36:39], a[212:215], v[236:239], a[36:39]\n"
  "v_exp_f32 v175, v175\n"
  "v_mfma_f32_16x16x32_bf16 a[52:55], a[216:219], v[232:235], a[52:55]\n"
  "buffer_load_dwordx4 a[144:147], v1, s[36:39], s62 offen offset:2048 sc1\n"
  "v_mfma_f32_16x16x32_bf16 a[52:55], a[220:223], v[236:239], a[52:55]\n"
  "v_exp_f32 v176, v176\n"
  "v_mfma_f32_16x16x32_bf16 a[68:71], a[224:227], v[232:235], a[68:71]\n"
  "v_exp_f32 v177, v177\n"
  "v_mfma_f32_16x16x32_bf16 a[68:71], a[228:231], v[236:239], a[68:71]\n"
  "v_cvt_pk_bf16_f32 v246, v172, v173\n"
  "v_cvt_pk_bf16_f32 v247, v174, v175\n"
  "v_mfma_f32_16x16x32_bf16 a[84:87], a[232:235], v[232:235], a[84:87]\n"
  "v_exp_f32 v178, v178\n"
  "v_mfma_f32_16x16x32_bf16 a[84:87], a[236:239], v[236:239], a[84:87]\n"
  "v_exp_f32 v179, v179\n"
  "v_mfma_f32_16x16x32_bf16 a[100:103], a[240:243], v[232:235], a[100:103]\n"
  "buffer_load_dwordx4 a[148:151], v1, s[36:39], s63 offen offset:2048 sc1\n"
  "v_mfma_f32_16x16x32_bf16 a[100:103], a[244:247], v[236:239], a[100:103]\n"
  "v_exp_f32 v180, v180\n"
  "v_mfma_f32_16x16x32_bf16 a[116:119], a[248:251], v[232:235], a[116:119]\n"
  "v_exp_f32 v181, v181\n"
  "v_mfma_f32_16x16x32_bf16 a[116:119], a[252:255], v[236:239], a[116:119]\n"
  "v_cvt_pk_bf16_f32 v248, v176, v177\n"
  "v_cvt_pk_bf16_f32 v249, v178, v179\n"
  "v_mfma_f32_16x16x32_bf16 v[56:59], v[64:67], v[240:243], v[56:59]\n"
  "v_exp_f32 v182, v182\n"
  "v_mfma_f32_16x16x32_bf16 v[56:59], v[64:67], v[244:247], v[56:59]\n"
  "v_exp_f32 v183, v183\n"
  "v_mfma_f32_16x16x32_bf16 a[8:11], a[192:195], v[240:243], a[8:11]\n"
  "buffer_load_dwordx4 a[152:155], v1, s[36:39], s62 offen offset:3072 sc1\n"
  "v_mfma_f32_16x16x32_bf16 a[8:11], a[196:199], v[244:247], a[8:11]\n"
  "v_exp_f32 v184, v184\n"
  "v_mfma_f32_16x16x32_bf16 a[24:27], a[200:203], v[240:243], a[24:27]\n"
  "v_exp_f32 v185, v185\n"
  "s_nop 0\n"
  "v_cmp_nge_f32 vcc, 0x5d800000, v56\n"
  "v_mfma_f32_16x16x32_bf16 a[24:27], a[204:207], v[244:247], a[24:27]\n"
  "s_nop 0\n"
  "s_or_b64 s[98:99], s[98:99], vcc\n"
  "v_cvt_pk_bf16_f32 v250, v180, v181\n"
  "v_cvt_pk_bf16_f32 v251, v182, v183\n"
  "v_rcp_f32 v10, v56\n"
  "v_mfma_f32_16x16x32_bf16 a[40:43], a[208:211], v[240:243], a[40:43]\n"
  "v_exp_f32 v186, v186\n"
  "v_mfma_f32_16x16x32_bf16 a[40:43], a[212:215], v[244:247], a[40:43]\n"
  "v_exp_f32 v187, v187\n"
  "v_mfma_f32_16x16x32_bf16 a[56:59], a[216:219], v[240:243], a[56:59]\n"
  "buffer_load_dwordx4 a[156:159], v1, s[36:39], s63 offen offset:3072 sc1\n"
  "v_mfma_f32_16x16x32_bf16 a[56:59], a[220:223], v[244:247], a[56:59]\n"
  "s_add_u32 s62, s60, 4096\n"
  "v_exp_f32 v188, v188\n"
  "v_mfma_f32_16x16x32_bf16 a[72:75], a[224:227], v[240:243], a[72:75]\n"
  "v_exp_f32 v189, v189\n"
  "v_mfma_f32_16x16x32_bf16 a[72:75], a[228:231], v[244:247], a[72:75]\n"
  "v_cvt_pk_bf16_f32 v252, v184, v185\n"
  "v_cvt_pk_bf16_f32 v253, v186, v187\n"
  "v_mfma_f32_16x16x32_bf16 a[88:91], a[232:235], v[240:243], a[88:91]\n"
  "v_exp_f32 v190, v190\n"
  "v_mfma_f32_16x16x32_bf16 a[88:91], a[236:239], v[244:247], a[88:91]\n"
  "v_exp_f32 v191, v191\n"
  "v_mfma_f32_16x16x32_bf16 a[104:107], a[240:243], v[240:243], a[104:107]\n"
  "buffer_load_dwordx4 a[160:163], v1, s[36:39], s62 offen offset:0 sc1\n"
  "v_mfma_f32_16x16x32_bf16 a[104:107], a[244:247], v[244:247], a[104:107]\n"
  "s_add_u32 s63, s61, 4096\n"
  "v_cvt_pk_bf16_f32 v254, v188, v189\n"
  "v_cvt_pk_bf16_f32 v255, v190, v191\n"
  "v_mfma_f32_16x16x32_bf16 a[120:123], a[248:251], v[240:243], a[120:123]\n"
  "v_mfma_f32_16x16x32_bf16 a[120:123], a[252:255], v[244:247], a[120:123]\n"
  "buffer_load_dwordx4 a[164:167], v1, s[36:39], s63 offen offset:0 sc1\n"
  "v_mfma_f32_16x16x32_bf16 v[60:63], v[64:67], v[248:251], v[60:63]\n"
  "v_mfma_f32_16x16x32_bf16 v[60:63], v[64:67], v[252:255], v[60:63]\n"
  "v_mfma_f32_16x16x32_bf16 a[12:15], a[192:195], v[248:251], a[12:15]\n"
  "buffer_load_dwordx4 a[168:171], v1, s[36:39], s62 offen offset:1024 sc1\n"
  "v_mfma_f32_16x16x32_bf16 a[12:15], a[196:199], v[252:255], a[12:15]\n"
  "v_mfma_f32_16x16x32_bf16 a[28:31], a[200:203], v[248:251], a[28:31]\n"
  "s_nop 3\n"
  "v_cmp_nge_f32 vcc, 0x5d800000, v60\n"
  "s_nop 1\n"
  "s_or_b64 s[98:99], s[98:99], vcc\n"
  "v_mfma_f32_16x16x32_bf16 a[28:31], a[204:207], v[252:255], a[28:31]\n"
  "buffer_load_dwordx4 a[172:175], v1, s[36:39], s63 offen offset:1024 sc1\n"
  "v_mfma_f32_16x16x32_bf16 a[44:47], a[208:211], v[248:251], a[44:47]\n"
  "v_rcp_f32 v11, v60\n"
  "v_mfma_f32_16x16x32_bf16 a[44:47], a[212:215], v[252:255], a[44:47]\n"
  "s_nop 0\n"
  "s_mov_b64 exec, s[98:99]\n ds_write_b32 v77, v15 offset:65532\n s_mov_b64 exec, -1\n"
  "s_waitcnt lgkmcnt(0)\n"
  "s_barrier\n"
  "ds_read_b32 v76, v77 offset:65532\n"
  "v_mfma_f32_16x16x32_bf16 a[60:63], a[216:219], v[248:251], a[60:63]\n"
  "buffer_load_dwordx4 a[176:179], v1, s[36:39], s62 offen offset:2048 sc1\n"
  "v_mfma_f32_16x16x32_bf16 a[60:63], a[220:223], v[252:255], a[60:63]\n"
  "v_mfma_f32_16x16x32_bf16 a[76:79], a[224:227], v[248:251], a[76:79]\n"
  "v_mfma_f32_16x16x32_bf16 a[76:79], a[228:231], v[252:255], a[76:79]\n"
  "buffer_load_dwordx4 a[180:183], v1, s[36:39], s63 offen offset:2048 sc1\n"
  "v_mfma_f32_16x16x32_bf16 a[92:95], a[232:235], v[248:251], a[92:95]\n"
  "v_mfma_f32_16x16x32_bf16 a[92:95], a[236:239], v[252:255], a[92:95]\n"
  "v_mfma_f32_16x16x32_bf16 a[108:111], a[240:243], v[248:251], a[108:111]\n"
  "buffer_load_dwordx4 a[184:187], v1, s[36:39], s62 offen offset:3072 sc1\n"
  "v_mfma_f32_16x16x32_bf16 a[108:111], a[244:247], v[252:255], a[108:111]\n"
  "v_mfma_f32_16x16x32_bf16 a[124:127], a[248:251], v[248:251], a[124:127]\n"
  "v_mfma_f32_16x16x32_bf16 a[124:127], a[252:255], v[252:255], a[124:127]\n"
  "buffer_load_dwordx4 a[188:191], v1, s[36:39], s63 offen offset:3072 sc1\n"
  "s_waitcnt lgkmcnt(0)\n"
  "v_readfirstlane_b32 s83, v76\n"
  "s_nop 1\n"
  "s_andn2_b32 s83, s83, s34\n"
  "s_cbranch_scc1 Lfall_%=\n"
  "s_waitcnt vmcnt(30)\n"
  "s_barrier\n"
  "ds_read_b128 v[224:227], v2 offset:0\n"
  "ds_read_b128 v[228:231], v2 offset:9216\n"
  "ds_read_b128 v[232:235], v2 offset:18432\n"
  "ds_read_b128 v[236:239], v2 offset:27648\n"
  "ds_read_b128 v[240:243], v2 offset:1024\n"
  "ds_read_b128 v[244:247], v2 offset:10240\n"
  "s_waitcnt vmcnt(15) lgkmcnt(5)\n"
  "v_mfma_f32_16x16x32_bf16 v[80:83], v[224:227], a[128:131], 0\n"
  "s_add_u32 s62, s60, 8192\n"
  "ds_read_b128 v[248:251], v2 offset:19456\n"
  "v_mov_b32 v22, s24\n"
  "v_accvgpr_read_b32 v48, a0\n"
  "s_waitcnt vmcnt(13) lgkmcnt(5)\n"
  "v_mfma_f32_16x16x32_bf16 v[80:83], v[228:231], a[136:139], v[80:83]\n"
  "s_mov_b32 s56, s91\n"
  "buffer_load_dwordx4 a[192:195], v1, s[36:39], s62 offen offset:0 sc1\n"
  "v_accvgpr_read_b32 v49, a1\n"
  "s_waitcnt vmcnt(12) lgkmcnt(3)\n"
  "v_mfma_f32_16x16x32_bf16 v[80:83], v[232:235], a[144:147], v[80:83]\n"
  "s_and_b32 s57, s92, 0xffff\n"
  "ds_read_b128 v[252:255], v2 offset:28672\n"
  "v_accvgpr_read_b32 v50, a2\n"
  "v_accvgpr_read_b32 v51, a3\n"
  "s_waitcnt vmcnt(10)\n"
  "v_mfma_f32_16x16x32_bf16 v[80:83], v[236:239], a[152:155], v[80:83]\n"
  "s_mov_b32 s58, 2359296\n"
  "v_accvgpr_read_b32 v52, a16\n"
  "v_accvgpr_read_b32 v53, a17\n"
  "v_accvgpr_read_b32 v54, a18\n"
  "v_mfma_f32_16x16x32_bf16 v[84:87], v[224:227], a[132:135], 0\n"
  "s_mov_b32 s59, 0x20000\n"
  "ds_read_b128 v[224:227], v2 offset:2048\n"
  "v_accvgpr_read_b32 v55, a19\n"
  "v_mul_f32 v48, v8, v48\n"
  "v_mfma_f32_16x16x32_bf16 v[84:87], v[228:231], a[140:143], v[84:87]\n"
  "s_add_u32 s63, s61, 8192\n"
  "v_mul_f32 v49, v8, v49\n"
  "v_mul_f32 v50, v8, v50\n"
  "v_mul_f32 v51, v8, v51\n"
  "v_mfma_f32_16x16x32_bf16 v[84:87], v[232:235], a[148:151], v[84:87]\n"
  "s_and_b32 s79, s93, 1\n"
  "ds_read_b128 v[228:231], v2 offset:11264\n"
  "v_mul_f32 v52, v8, v52\n"
  "v_mul_f32 v53, v8, v53\n"
  "s_waitcnt vmcnt(9)\n"
  "v_mfma_f32_16x16x32_bf16 v[84:87], v[236:239], a[156:159], v[84:87]\n"
  "s_lshl_b32 s79, s79, 3\n"
  "v_mul_f32 v54, v8, v54\n"
  "v_mul_f32 v55, v8, v55\n"
  "v_cvt_pk_bf16_f32 v64, v48, v49\n"
  "s_waitcnt lgkmcnt(1)\n"
  "v_mfma_f32_16x16x32_bf16 v[88:91], v[240:243], a[128:131], 0\n"
  "s_lshr_b32 s81, s93, 5\n"
  "ds_read_b128 v[232:235], v2 offset:20480\n"
  "v_cvt_pk_bf16_f32 v65, v50, v51\n"
  "v_cvt_pk_bf16_f32 v66, v52, v53\n"
  "v_mfma_f32_16x16x32_bf16 v[88:91], v[244:247], a[136:139], v[88:91]\n"
  "s_add_u32 s79, s79, s81\n"
  "buffer_load_dwordx4 a[196:199], v1, s[36:39], s63 offen offset:0 sc1\n"
  "v_cvt_pk_bf16_f32 v67, v54, v55\n"
  "v_mfma_f32_16x16x32_bf16 v[88:91], v[248:251], a[144:147], v[88:91]\n"
  "s_bfe_u32 s80, s93, 131075\n"
  "ds_read_b128 v[236:239], v2 offset:29696\n"
  "buffer_store_dwordx4 v[64:67], v1, s[36:39], s73 offen offset:0 sc1\n"
  "v_mfma_f32_16x16x32_bf16 v[88:91], v[252:255], a[152:155], v[88:91]\n"
  "s_mul_i32 s84, s68, 36864\n"
  "v_accvgpr_read_b32 v56, a32\n"
  "v_accvgpr_read_b32 v57, a33\n"
  "v_accvgpr_read_b32 v58, a34\n"
  "v_mfma_f32_16x16x32_bf16 v[92:95], v[240:243], a[132:135], 0\n"
  "s_add_u32 s84, s84, 1179648\n"
  "ds_read_b128 v[240:243], v2 offset:3072\n"
  "v_accvgpr_read_b32 v59, a35\n"
  "v_accvgpr_read_b32 v60, a48\n"
  "v_mfma_f32_16x16x32_bf16 v[92:95], v[244:247], a[140:143], v[92:95]\n"
  "s_sub_u32 s84, 0, s84\n"
  "v_accvgpr_read_b32 v61, a49\n"
  "v_accvgpr_read_b32 v62, a50\n"
  "v_accvgpr_read_b32 v63, a51\n"
  "v_mfma_f32_16x16x32_bf16 v[92:95], v[248:251], a[148:151], v[92:95]\n"
  "s_lshl_b32 s81, s79, 2\n"
  "ds_read_b128 v[244:247], v2 offset:12288\n"
  "v_mul_f32 v56, v8, v56\n"
  "v_mul_f32 v57, v8, v57\n"
  "v_mfma_f32_16x16x32_bf16 v[92:95], v[252:255], a[156:159], v[92:95]\n"
  "s_add_u32 s81, s81, s82\n"
  "v_mul_f32 v58, v8, v58\n"
  "v_mul_f32 v59, v8, v59\n"
  "v_mul_f32 v60, v8, v60\n"
  "v_mfma_f32_16x16x32_bf16 v[96:99], v[224:227], a[128:131], 0\n"
  "s_lshl_b32 s81, s81, 1\n"
  "ds_read_b128 v[248:251], v2 offset:21504\n"
  "v_mul_f32 v61, v8, v61\n"
  "v_mul_f32 v62, v8, v62\n"
  "s_waitcnt lgkmcnt(2)\n"
  "v_mfma_f32_16x16x32_bf16 v[96:99], v[228:231], a[136:139], v[96:99]\n"
  "s_lshl_b32 s85, s81, 17\n"
  "v_mul_f32 v63, v8, v63\n"
  "v_cvt_pk_bf16_f32 v68, v56, v57\n"
  "v_cvt_pk_bf16_f32 v69, v58, v59\n"
  "v_mfma_f32_16x16x32_bf16 v[96:99], v[232:235], a[144:147], v[96:99]\n"
  "s_lshl_b32 s83, s68, 15\n"
  "ds_read_b128 v[252:255], v2 offset:30720\n"
  "v_cvt_pk_bf16_f32 v70, v60, v61\n"
  "v_cvt_pk_bf16_f32 v71, v62, v63\n"
  "v_mfma_f32_16x16x32_bf16 v[96:99], v[236:239], a[152:155], v[96:99]\n"
  "s_add_u32 s85, s85, s83\n"
  "buffer_load_dwordx4 a[200:203], v1, s[36:39], s62 offen offset:1024 sc1\n"
  "v_accvgpr_read_b32 v48, a64\n"
  "v_mfma_f32_16x16x32_bf16 v[100:103], v[224:227], a[132:135], 0\n"
  "s_add_u32 s85, s85, 21233664\n"
  "ds_read_b128 v[224:227], v2 offset:4096\n"
  "v_accvgpr_read_b32 v49, a65\n"
  "v_accvgpr_read_b32 v50, a66\n"
  "v_mfma_f32_16x16x32_bf16 v[100:103], v[228:231], a[140:143], v[100:103]\n"
  "s_add_u32 s86, s85, 131072\n"
  "v_accvgpr_read_b32 v51, a67\n"
  "v_accvgpr_read_b32 v52, a80\n"
  "v_accvgpr_read_b32 v53, a81\n"
  "v_mfma_f32_16x16x32_bf16 v[100:103], v[232:235], a[148:151], v[100:103]\n"
  "s_lshl_b32 s81, s79, 2\n"
  "ds_read_b128 v[228:231], v2 offset:13312\n"
  "v_accvgpr_read_b32 v54, a82\n"
  "v_accvgpr_read_b32 v55, a83\n"
  "v_mfma_f32_16x16x32_bf16 v[100:103], v[236:239], a[156:159], v[100:103]\n"
  "s_add_u32 s81, s81, s80\n"
  "v_mul_f32 v48, v8, v48\n"
  "v_mul_f32 v49, v8, v49\n"
  "v_mul_f32 v50, v8, v50\n"
  "v_mfma_f32_16x16x32_bf16 v[104:107], v[240:243], a[128:131], 0\n"
  "s_lshl_b32 s78, s81, 2\n"
  "ds_read_b128 v[232:235], v2 offset:22528\n"
  "v_mul_f32 v51, v8, v51\n"
  "v_mul_f32 v52, v8, v52\n"
  "s_waitcnt lgkmcnt(2)\n"
  "v_mfma_f32_16x16x32_bf16 v[104:107], v[244:247], a[136:139], v[104:107]\n"
  "s_add_u32 s78, s78, s82\n"
  "buffer_store_dwordx4 v[68:71], v1, s[36:39], s73 offen offset:1024 sc1\n"
  "v_mul_f32 v53, v8, v53\n"
  "v_mfma_f32_16x16x32_bf16 v[104:107], v[248:251], a[144:147], v[104:107]\n"
  "s_lshl_b32 s78, s78, 6\n"
  "ds_read_b128 v[236:239], v2 offset:31744\n"
  "v_mul_f32 v54, v8, v54\n"
  "v_mul_f32 v55, v8, v55\n"
  "v_mfma_f32_16x16x32_bf16 v[104:107], v[252:255], a[152:155], v[104:107]\n"
  "s_add_u32 s78, s78, 73220096\n"
  "buffer_load_dwordx4 a[204:207], v1, s[36:39], s63 offen offset:1024 sc1\n"
  "v_cvt_pk_bf16_f32 v72, v48, v49\n"
  "v_mfma_f32_16x16x32_bf16 v[108:111], v[240:243], a[132:135], 0\n"
  "s_lshl_b32 s81, s81, 6\n"
  "ds_read_b128 v[240:243], v2 offset:5120\n"
  "v_cvt_pk_bf16_f32 v73, v50, v51\n"
  "v_cvt_pk_bf16_f32 v74, v52, v53\n"
  "v_mfma_f32_16x16x32_bf16 v[108:111], v[244:247], a[140:143], v[108:111]\n"
  "s_add_u32 s81, s81, s82\n"
  "v_cvt_pk_bf16_f32 v75, v54, v55\n"
  "v_accvgpr_read_b32 v56, a96\n"
  "v_accvgpr_read_b32 v57, a97\n"
  "v_mfma_f32_16x16x32_bf16 v[108:111], v[248:251], a[148:151], v[108:111]\n"
  "s_lshl_b32 s83, s68, 2\n"
  "ds_read_b128 v[244:247], v2 offset:14336\n"
  "v_accvgpr_read_b32 v58, a98\n"
  "v_accvgpr_read_b32 v59, a99\n"
  "v_mfma_f32_16x16x32_bf16 v[108:111], v[252:255], a[156:159], v[108:111]\n"
  "s_add_u32 s83, s83, s81\n"
  "v_accvgpr_read_b32 v60, a112\n"
  "v_accvgpr_read_b32 v61, a113\n"
  "v_accvgpr_read_b32 v62, a114\n"
  "v_mfma_f32_16x16x32_bf16 v[112:115], v[224:227], a[128:131], 0\n"
  "s_mul_i32 s87, s83, 3072\n"
  "ds_read_b128 v[248:251], v2 offset:23552\n"
  "v_accvgpr_read_b32 v63, a115\n"
  "v_mul_f32 v56, v8, v56\n"
  "s_waitcnt lgkmcnt(2)\n"
  "v_mfma_f32_16x16x32_bf16 v[112:115], v[228:231], a[136:139], v[112:115]\n"
  "s_add_u32 s87, s87, 48037888\n"
  "v_mul_f32 v57, v8, v57\n"
  "v_mul_f32 v58, v8, v58\n"
  "v_mul_f32 v59, v8, v59\n"
  "v_mfma_f32_16x16x32_bf16 v[112:115], v[232:235], a[144:147], v[112:115]\n"
  "s_lshl_b32 s83, s68, 4\n"
  "ds_read_b128 v[252:255], v2 offset:32768\n"
  "v_mul_f32 v60, v8, v60\n"
  "v_mul_f32 v61, v8, v61\n"
  "v_mfma_f32_16x16x32_bf16 v[112:115], v[236:239], a[152:155], v[112:115]\n"
  "s_add_u32 s83, s83, s81\n"
  "v_mul_f32 v62, v8, v62\n"
  "v_mul_f32 v63, v8, v63\n"
  "v_cvt_pk_bf16_f32 v76, v56, v57\n"
  "v_mfma_f32_16x16x32_bf16 v[116:119], v[224:227], a[132:135], 0\n"
  "s_mul_i32 s88, s83, 3072\n"
  "ds_read_b128 v[224:227], v2 offset:6144\n"
  "v_cvt_pk_bf16_f32 v77, v58, v59\n"
  "v_cvt_pk_bf16_f32 v78, v60, v61\n"
  "v_mfma_f32_16x16x32_bf16 v[116:119], v[228:231], a[140:143], v[116:119]\n"
  "s_add_u32 s88, s88, 48037888\n"
  "buffer_load_dwordx4 a[208:211], v1, s[36:39], s62 offen offset:2048 sc1\n"
  "v_cvt_pk_bf16_f32 v79, v62, v63\n"
  "v_mfma_f32_16x16x32_bf16 v[116:119], v[232:235], a[148:151], v[116:119]\n"
  "ds_read_b128 v[228:231], v2 offset:15360\n"
  "v_mfma_f32_16x16x32_bf16 v[116:119], v[236:239], a[156:159], v[116:119]\n"
  "v_mfma_f32_16x16x32_bf16 v[120:123], v[240:243], a[128:131], 0\n"
  "s_waitcnt lgkmcnt(2)\n"
  "ds_read_b128 v[232:235], v2 offset:24576\n"
  "buffer_store_dwordx4 v[72:75], v1, s[36:39], s73 offen offset:2048 sc1\n"
  "v_mfma_f32_16x16x32_bf16 v[120:123], v[244:247], a[136:139], v[120:123]\n"
  "v_mfma_f32_16x16x32_bf16 v[120:123], v[248:251], a[144:147], v[120:123]\n"
  "ds_read_b128 v[236:239], v2 offset:33792\n"
  "v_accvgpr_read_b32 v48, a4\n"
  "v_accvgpr_read_b32 v49, a5\n"
  "v_mfma_f32_16x16x32_bf16 v[120:123], v[252:255], a[152:155], v[120:123]\n"
  "v_accvgpr_read_b32 v50, a6\n"
  "v_accvgpr_read_b32 v51, a7\n"
  "v_accvgpr_read_b32 v52, a20\n"
  "v_mfma_f32_16x16x32_bf16 v[124:127], v[240:243], a[132:135], 0\n"
  "ds_read_b128 v[240:243], v2 offset:7168\n"
  "v_accvgpr_read_b32 v53, a21\n"
  "v_accvgpr_read_b32 v54, a22\n"
  "v_mfma_f32_16x16x32_bf16 v[124:127], v[244:247], a[140:143], v[124:127]\n"
  "buffer_load_dwordx4 a[212:215], v1, s[36:39], s63 offen offset:2048 sc1\n"
  "v_accvgpr_read_b32 v55, a23\n"
  "v_mfma_f32_16x16x32_bf16 v[124:127], v[248:251], a[148:151], v[124:127]\n"
  "ds_read_b128 v[244:247], v2 offset:16384\n"
  "v_mul_f32 v48, v9, v48\n"
  "v_mul_f32 v49, v9, v49\n"
  "v_mfma_f32_16x16x32_bf16 v[124:127], v[252:255], a[156:159], v[124:127]\n"
  "s_waitcnt lgkmcnt(1)\n"
  "v_mul_f32 v50, v9, v50\n"
  "v_mul_f32 v51, v9, v51\n"
  "v_mul_f32 v52, v9, v52\n"
  "v_mfma_f32_16x16x32_bf16 v[128:131], v[224:227], a[128:131], 0\n"
  "ds_read_b128 v[248:251], v2 offset:25600\n"
  "v_mul_f32 v53, v9, v53\n"
  "v_mul_f32 v54, v9, v54\n"
  "v_mfma_f32_16x16x32_bf16 v[128:131], v[228:231], a[136:139], v[128:131]\n"
  "v_mul_f32 v55, v9, v55\n"
  "v_cvt_pk_bf16_f32 v64, v48, v49\n"
  "v_cvt_pk_bf16_f32 v65, v50, v51\n"
  "v_mfma_f32_16x16x32_bf16 v[128:131], v[232:235], a[144:147], v[128:131]\n"
  "ds_read_b128 v[252:255], v2 offset:34816\n"
  "v_cvt_pk_bf16_f32 v66, v52, v53\n"
  "v_cvt_pk_bf16_f32 v67, v54, v55\n"
  "v_mfma_f32_16x16x32_bf16 v[128:131], v[236:239], a[152:155], v[128:131]\n"
  "v_mfma_f32_16x16x32_bf16 v[132:135], v[224:227], a[132:135], 0\n"
  "ds_read_b128 v[224:227], v2 offset:8192\n"
  "v_mfma_f32_16x16x32_bf16 v[132:135], v[228:231], a[140:143], v[132:135]\n"
  "v_mfma_f32_16x16x32_bf16 v[132:135], v[232:235], a[148:151], v[132:135]\n"
  "ds_read_b128 v[228:231], v2 offset:17408\n"
  "v_mfma_f32_16x16x32_bf16 v[132:135], v[236:239], a[156:159], v[132:135]\n"
  "buffer_load_dwordx4 a[216:219], v1, s[36:39], s62 offen offset:3072 sc1\n"
  "v_mfma_f32_16x16x32_bf16 v[136:139], v[240:243], a[128:131], 0\n"
  "s_waitcnt lgkmcnt(2)\n"
  "ds_read_b128 v[232:235], v2 offset:26624\n"
  "buffer_store_dwordx4 v[76:79], v1, s[36:39], s73 offen offset:3072 sc1\n"
  "v_mfma_f32_16x16x32_bf16 v[136:139], v[244:247], a[136:139], v[136:139]\n"
  "v_accvgpr_read_b32 v56, a36\n"
  "v_accvgpr_read_b32 v57, a37\n"
  "v_accvgpr_read_b32 v58, a38\n"
  "v_mfma_f32_16x16x32_bf16 v[136:139], v[248:251], a[144:147], v[136:139]\n"
  "ds_read_b128 v[236:239], v2 offset:35840\n"
  "v_accvgpr_read_b32 v59, a39\n"
  "v_accvgpr_read_b32 v60, a52\n"
  "v_mfma_f32_16x16x32_bf16 v[136:139], v[252:255], a[152:155], v[136:139]\n"
  "v_accvgpr_read_b32 v61, a53\n"
  "v_accvgpr_read_b32 v62, a54\n"
  "v_accvgpr_read_b32 v63, a55\n"
  "v_mfma_f32_16x16x32_bf16 v[140:143], v[240:243], a[132:135], 0\n"
  "s_barrier\n"
  "ds_read_b128 v[240:243], v2 offset:36864\n"
  "v_mul_f32 v56, v9, v56\n"
  "v_mul_f32 v57, v9, v57\n"
  "v_mfma_f32_16x16x32_bf16 v[140:143], v[244:247], a[140:143], v[140:143]\n"
  "v_mul_f32 v58, v9, v58\n"
  "v_mul_f32 v59, v9, v59\n"
  "v_mul_f32 v60, v9, v60\n"
  "v_mfma_f32_16x16x32_bf16 v[140:143], v[248:251], a[148:151], v[140:143]\n"
  "ds_read_b128 v[244:247], v2 offset:46080\n"
  "v_mul_f32 v61, v9, v61\n"
  "v_mul_f32 v62, v9, v62\n"
  "v_mfma_f32_16x16x32_bf16 v[140:143], v[252:255], a[156:159], v[140:143]\n"
  "s_waitcnt lgkmcnt(1)\n"
  "buffer_load_dwordx4 a[220:223], v1, s[36:39], s63 offen offset:3072 sc1\n"
  "v_mul_f32 v63, v9, v63\n"
  "v_mfma_f32_16x16x32_bf16 v[144:147], v[224:227], a[128:131], 0\n"
  "ds_read_b128 v[248:251], v2 offset:55296\n"
  "v_cvt_pk_bf16_f32 v68, v56, v57\n"
  "v_cvt_pk_bf16_f32 v69, v58, v59\n"
  "v_mfma_f32_16x16x32_bf16 v[144:147], v[228:231], a[136:139], v[144:147]\n"
  "v_cvt_pk_bf16_f32 v70, v60, v61\n"
  "v_cvt_pk_bf16_f32 v71, v62, v63\n"
  "v_mfma_f32_16x16x32_bf16 v[144:147], v[232:235], a[144:147], v[144:147]\n"
  "ds_read_b128 v[252:255], v2 offset:64512\n"
  "v_mfma_f32_16x16x32_bf16 v[144:147], v[236:239], a[152:155], v[144:147]\n"
  "v_mfma_f32_16x16x32_bf16 v[148:151], v[224:227], a[132:135], 0\n"
  "s_add_u32 s62, s60, 12288\n"
  "ds_read_b128 v[224:227], v2 offset:37888\n"
  "v_mfma_f32_16x16x32_bf16 v[148:151], v[228:231], a[140:143], v[148:151]\n"
  "v_mfma_f32_16x16x32_bf16 v[148:151], v[232:235], a[148:151], v[148:151]\n"
  "ds_read_b128 v[228:231], v2 offset:47104\n"
  "v_mfma_f32_16x16x32_bf16 v[148:151], v[236:239], a[156:159], v[148:151]\n"
  "s_waitcnt vmcnt(19)\n"
  "buffer_store_dwordx4 v[64:67], v1, s[36:39], s74 offen offset:0 sc1\n"
  "v_mfma_f32_16x16x32_bf16 v[80:83], v[240:243], a[160:163], v[80:83]\n"
  "s_waitcnt vmcnt(18) lgkmcnt(2)\n"
  "ds_read_b128 v[232:235], v2 offset:56320\n"
  "v_add_u32 v2, 36864, v2\n"
  "v_accvgpr_read_b32 v48, a68\n"
  "v_mfma_f32_16x16x32_bf16 v[80:83], v[244:247], a[168:171], v[80:83]\n"
  "s_waitcnt vmcnt(16)\n"
  "buffer_load_dwordx4 a[224:227], v1, s[36:39], s62 offen offset:0 sc1\n"
  "v_accvgpr_read_b32 v49, a69\n"
  "v_mfma_f32_16x16x32_bf16 v[80:83], v[248:251], a[176:179], v[80:83]\n"
  "s_waitcnt vmcnt(15)\n"
  "ds_read_b128 v[236:239], v2 offset:28672\n"
  "v_accvgpr_read_b32 v50, a70\n"
  "v_accvgpr_read_b32 v51, a71\n"
  "v_mfma_f32_16x16x32_bf16 v[80:83], v[252:255], a[184:187], v[80:83]\n"
  "v_accvgpr_read_b32 v52, a84\n"
  "v_accvgpr_read_b32 v53, a85\n"
  "v_accvgpr_read_b32 v54, a86\n"
  "v_mfma_f32_16x16x32_bf16 v[84:87], v[240:243], a[164:167], v[84:87]\n"
  "ds_read_b128 v[240:243], v2 offset:2048\n"
  "v_accvgpr_read_b32 v55, a87\n"
  "v_mul_f32 v48, v9, v48\n"
  "v_mfma_f32_16x16x32_bf16 v[84:87], v[244:247], a[172:175], v[84:87]\n"
  "s_add_u32 s63, s61, 12288\n"
  "v_mul_f32 v49, v9, v49\n"
  "v_mul_f32 v50, v9, v50\n"
  "v_mul_f32 v51, v9, v51\n"
  "v_mfma_f32_16x16x32_bf16 v[84:87], v[248:251], a[180:183], v[84:87]\n"
  "s_waitcnt vmcnt(14)\n"
  "ds_read_b128 v[244:247], v2 offset:11264\n"
  "v_mul_f32 v52, v9, v52\n"
  "v_mul_f32 v53, v9, v53\n"
  "v_mfma_f32_16x16x32_bf16 v[84:87], v[252:255], a[188:191], v[84:87]\n"
  "s_waitcnt lgkmcnt(1)\n"
  "v_mul_f32 v54, v9, v54\n"
  "v_mul_f32 v55, v9, v55\n"
  "v_cvt_pk_bf16_f32 v72, v48, v49\n"
  "v_mfma_f32_16x16x32_bf16 v[88:91], v[224:227], a[160:163], v[88:91]\n"
  "ds_read_b128 v[248:251], v2 offset:20480\n"
  "v_cvt_pk_bf16_f32 v73, v50, v51\n"
  "v_cvt_pk_bf16_f32 v74, v52, v53\n"
  "v_mfma_f32_16x16x32_bf16 v[88:91], v[228:231], a[168:171], v[88:91]\n"
  "buffer_load_dwordx4 a[228:231], v1, s[36:39], s63 offen offset:0 sc1\n"
  "v_cvt_pk_bf16_f32 v75, v54, v55\n"
  "v_mfma_f32_16x16x32_bf16 v[88:91], v[232:235], a[176:179], v[88:91]\n"
  "ds_read_b128 v[252:255], v2 offset:29696\n"
  "v_mfma_f32_16x16x32_bf16 v[88:91], v[236:239], a[184:187], v[88:91]\n"
  "v_mfma_f32_16x16x32_bf16 v[92:95], v[224:227], a[164:167], v[92:95]\n"
  "ds_read_b128 v[224:227], v2 offset:3072\n"
  "v_mfma_f32_16x16x32_bf16 v[92:95], v[228:231], a[172:175], v[92:95]\n"
  "v_mfma_f32_16x16x32_bf16 v[92:95], v[232:235], a[180:183], v[92:95]\n"
  "ds_read_b128 v[228:231], v2 offset:12288\n"
  "buffer_store_dwordx4 v[68:71], v1, s[36:39], s74 offen offset:1024 sc1\n"
  "v_mfma_f32_16x16x32_bf16 v[92:95], v[236:239], a[188:191], v[92:95]\n"
  "v_accvgpr_read_b32 v56, a100\n"
  "v_accvgpr_read_b32 v57, a101\n"
  "v_accvgpr_read_b32 v58, a102\n"
  "v_mfma_f32_16x16x32_bf16 v[96:99], v[240:243], a[160:163], v[96:99]\n"
  "s_waitcnt lgkmcnt(2)\n"
  "ds_read_b128 v[232:235], v2 offset:21504\n"
  "v_accvgpr_read_b32 v59, a103\n"
  "v_accvgpr_read_b32 v60, a116\n"
  "v_mfma_f32_16x16x32_bf16 v[96:99], v[244:247], a[168:171], v[96:99]\n"
  "v_accvgpr_read_b32 v61, a117\n"
  "v_accvgpr_read_b32 v62, a118\n"
  "v_accvgpr_read_b32 v63, a119\n"
  "v_mfma_f32_16x16x32_bf16 v[96:99], v[248:251], a[176:179], v[96:99]\n"
  "ds_read_b128 v[236:239], v2 offset:30720\n"
  "v_mul_f32 v56, v9, v56\n"
  "v_mul_f32 v57, v9, v57\n"
  "v_mfma_f32_16x16x32_bf16 v[96:99], v[252:255], a[184:187], v[96:99]\n"
  "buffer_load_dwordx4 a[232:235], v1, s[36:39], s62 offen offset:1024 sc1\n"
  "v_mul_f32 v58, v9, v58\n"
  "v_mfma_f32_16x16x32_bf16 v[100:103], v[240:243], a[164:167], v[100:103]\n"
  "ds_read_b128 v[240:243], v2 offset:4096\n"
  "v_mul_f32 v59, v9, v59\n"
  "v_mul_f32 v60, v9, v60\n"
  "v_mfma_f32_16x16x32_bf16 v[100:103], v[244:247], a[172:175], v[100:103]\n"
  "v_mul_f32 v61, v9, v61\n"
  "v_mul_f32 v62, v9, v62\n"
  "v_mul_f32 v63, v9, v63\n"
  "v_mfma_f32_16x16x32_bf16 v[100:103], v[248:251], a[180:183], v[100:103]\n"
  "ds_read_b128 v[244:247], v2 offset:13312\n"
  "v_cvt_pk_bf16_f32 v76, v56, v57\n"
  "v_cvt_pk_bf16_f32 v77, v58, v59\n"
  "v_mfma_f32_16x16x32_bf16 v[100:103], v[252:255], a[188:191], v[100:103]\n"
  "s_waitcnt lgkmcnt(1)\n"
  "v_cvt_pk_bf16_f32 v78, v60, v61\n"
  "v_cvt_pk_bf16_f32 v79, v62, v63\n"
  "v_mfma_f32_16x16x32_bf16 v[104:107], v[224:227], a[160:163], v[104:107]\n"
  "ds_read_b128 v[248:251], v2 offset:22528\n"
  "v_mfma_f32_16x16x32_bf16 v[104:107], v[228:231], a[168:171], v[104:107]\n"
  "v_mfma_f32_16x16x32_bf16 v[104:107], v[232:235], a[176:179], v[104:107]\n"
  "ds_read_b128 v[252:255], v2 offset:31744\n"
  "v_mfma_f32_16x16x32_bf16 v[104:107], v[236:239], a[184:187], v[104:107]\n"
  "buffer_load_dwordx4 a[236:239], v1, s[36:39], s63 offen offset:1024 sc1\n"
  "v_mfma_f32_16x16x32_bf16 v[108:111], v[224:227], a[164:167], v[108:111]\n"
  "ds_read_b128 v[224:227], v2 offset:5120\n"
  "v_mfma_f32_16x16x32_bf16 v[108:111], v[228:231], a[172:175], v[108:111]\n"
  "buffer_store_dwordx4 v[72:75], v1, s[36:39], s74 offen offset:2048 sc1\n"
  "v_mfma_f32_16x16x32_bf16 v[108:111], v[232:235], a[180:183], v[108:111]\n"
  "ds_read_b128 v[228:231], v2 offset:14336\n"
  "v_accvgpr_read_b32 v48, a8\n"
  "v_accvgpr_read_b32 v49, a9\n"
  "v_mfma_f32_16x16x32_bf16 v[108:111], v[236:239], a[188:191], v[108:111]\n"
  "v_accvgpr_read_b32 v50, a10\n"
  "v_accvgpr_read_b32 v51, a11\n"
  "v_accvgpr_read_b32 v52, a24\n"
  "v_mfma_f32_16x16x32_bf16 v[112:115], v[240:243], a[160:163], v[112:115]\n"
  "s_waitcnt lgkmcnt(1)\n"
  "ds_read_b128 v[232:235], v2 offset:23552\n"
  "v_accvgpr_read_b32 v53, a25\n"
  "v_accvgpr_read_b32 v54, a26\n"
  "v_mfma_f32_16x16x32_bf16 v[112:115], v[244:247], a[168:171], v[112:115]\n"
  "v_accvgpr_read_b32 v55, a27\n"
  "v_mul_f32 v48, v10, v48\n"
  "v_mul_f32 v49, v10, v49\n"
  "v_mfma_f32_16x16x32_bf16 v[112:115], v[248:251], a[176:179], v[112:115]\n"
  "ds_read_b128 v[236:239], v2 offset:32768\n"
  "v_mul_f32 v50, v10, v50\n"
  "v_mul_f32 v51, v10, v51\n"
  "v_mfma_f32_16x16x32_bf16 v[112:115], v[252:255], a[184:187], v[112:115]\n"
  "v_mul_f32 v52, v10, v52\n"
  "v_mul_f32 v53, v10, v53\n"
  "v_mul_f32 v54, v10, v54\n"
  "v_mfma_f32_16x16x32_bf16 v[116:119], v[240:243], a[164:167], v[116:119]\n"
  "ds_read_b128 v[240:243], v2 offset:6144\n"
  "v_mul_f32 v55, v10, v55\n"
  "v_cvt_pk_bf16_f32 v64, v48, v49\n"
  "v_mfma_f32_16x16x32_bf16 v[116:119], v[244:247], a[172:175], v[116:119]\n"
  "buffer_load_dwordx4 a[240:243], v1, s[36:39], s62 offen offset:2048 sc1\n"
  "v_cvt_pk_bf16_f32 v65, v50, v51\n"
  "v_mfma_f32_16x16x32_bf16 v[116:119], v[248:251], a[180:183], v[116:119]\n"
  "ds_read_b128 v[244:247], v2 offset:15360\n"
  "v_cvt_pk_bf16_f32 v66, v52, v53\n"
  "v_cvt_pk_bf16_f32 v67, v54, v55\n"
  "v_mfma_f32_16x16x32_bf16 v[116:119], v[252:255], a[188:191], v[116:119]\n"
  "v_mfma_f32_16x16x32_bf16 v[120:123], v[224:227], a[160:163], v[120:123]\n"
  "s_waitcnt lgkmcnt(2)\n"
  "ds_read_b128 v[248:251], v2 offset:24576\n"
  "v_mfma_f32_16x16x32_bf16 v[120:123], v[228:231], a[168:171], v[120:123]\n"
  "v_mfma_f32_16x16x32_bf16 v[120:123], v[232:235], a[176:179], v[120:123]\n"
  "ds_read_b128 v[252:255], v2 offset:33792\n"
  "v_mfma_f32_16x16x32_bf16 v[120:123], v[236:239], a[184:187], v[120:123]\n"
  "v_mfma_f32_16x16x32_bf16 v[124:127], v[224:227], a[164:167], v[124:127]\n"
  "ds_read_b128 v[224:227], v2 offset:7168\n"
  "buffer_store_dwordx4 v[76:79], v1, s[36:39], s74 offen offset:3072 sc1\n"
  "v_mfma_f32_16x16x32_bf16 v[124:127], v[228:231], a[172:175], v[124:127]\n"
  "buffer_load_dwordx4 a[244:247], v1, s[36:39], s63 offen offset:2048 sc1\n"
  "v_accvgpr_read_b32 v56, a40\n"
  "v_mfma_f32_16x16x32_bf16 v[124:127], v[232:235], a[180:183], v[124:127]\n"
  "ds_read_b128 v[228:231], v2 offset:16384\n"
  "v_accvgpr_read_b32 v57, a41\n"
  "v_accvgpr_read_b32 v58, a42\n"
  "v_mfma_f32_16x16x32_bf16 v[124:127], v[236:239], a[188:191], v[124:127]\n"
  "s_waitcnt lgkmcnt(2)\n"
  "v_accvgpr_read_b32 v59, a43\n"
  "v_accvgpr_read_b32 v60, a56\n"
  "v_accvgpr_read_b32 v61, a57\n"
  "v_mfma_f32_16x16x32_bf16 v[128:131], v[240:243], a[160:163], v[128:131]\n"
  "ds_read_b128 v[232:235], v2 offset:25600\n"
  "v_accvgpr_read_b32 v62, a58\n"
  "v_accvgpr_read_b32 v63, a59\n"
  "v_mfma_f32_16x16x32_bf16 v[128:131], v[244:247], a[168:171], v[128:131]\n"
  "v_mul_f32 v56, v10, v56\n"
  "v_mul_f32 v57, v10, v57\n"
  "v_mul_f32 v58, v10, v58\n"
  "v_mfma_f32_16x16x32_bf16 v[128:131], v[248:251], a[176:179], v[128:131]\n"
  "ds_read_b128 v[236:239], v2 offset:34816\n"
  "v_mul_f32 v59, v10, v59\n"
  "v_mul_f32 v60, v10, v60\n"
  "v_mfma_f32_16x16x32_bf16 v[128:131], v[252:255], a[184:187], v[128:131]\n"
  "v_mul_f32 v61, v10, v61\n"
  "v_mul_f32 v62, v10, v62\n"
  "v_mul_f32 v63, v10, v63\n"
  "v_mfma_f32_16x16x32_bf16 v[132:135], v[240:243], a[164:167], v[132:135]\n"
  "ds_read_b128 v[240:243], v2 offset:8192\n"
  "v_cvt_pk_bf16_f32 v68, v56, v57\n"
  "v_cvt_pk_bf16_f32 v69, v58, v59\n"
  "v_mfma_f32_16x16x32_bf16 v[132:135], v[244:247], a[172:175], v[132:135]\n"
  "v_cvt_pk_bf16_f32 v70, v60, v61\n"
  "v_cvt_pk_bf16_f32 v71, v62, v63\n"
  "v_mfma_f32_16x16x32_bf16 v[132:135], v[248:251], a[180:183], v[132:135]\n"
  "ds_read_b128 v[244:247], v2 offset:17408\n"
  "v_mfma_f32_16x16x32_bf16 v[132:135], v[252:255], a[188:191], v[132:135]\n"
  "s_waitcnt lgkmcnt(2)\n"
  "buffer_load_dwordx4 a[248:251], v1, s[36:39], s62 offen offset:3072 sc1\n"
  "v_mfma_f32_16x16x32_bf16 v[136:139], v[224:227], a[160:163], v[136:139]\n"
  "ds_read_b128 v[248:251], v2 offset:26624\n"
  "v_mfma_f32_16x16x32_bf16 v[136:139], v[228:231], a[168:171], v[136:139]\n"
  "v_mfma_f32_16x16x32_bf16 v[136:139], v[232:235], a[176:179], v[136:139]\n"
  "ds_read_b128 v[252:255], v2 offset:35840\n"
  "v_mfma_f32_16x16x32_bf16 v[136:139], v[236:239], a[184:187], v[136:139]\n"
  "buffer_store_dwordx4 v[64:67], v1, s[36:39], s75 offen offset:0 sc1\n"
  "v_mfma_f32_16x16x32_bf16 v[140:143], v[224:227], a[164:167], v[140:143]\n"
  "s_barrier\n"
  "ds_read_b128 v[224:227], v2 offset:36864\n"
  "v_accvgpr_read_b32 v48, a72\n"
  "v_accvgpr_read_b32 v49, a73\n"
  "v_mfma_f32_16x16x32_bf16 v[140:143], v[228:231], a[172:175], v[140:143]\n"
  "v_accvgpr_read_b32 v50, a74\n"
  "v_accvgpr_read_b32 v51, a75\n"
  "v_accvgpr_read_b32 v52, a88\n"
  "v_mfma_f32_16x16x32_bf16 v[140:143], v[232:235], a[180:183], v[140:143]\n"
  "ds_read_b128 v[228:231], v2 offset:46080\n"
  "v_accvgpr_read_b32 v53, a89\n"
  "v_accvgpr_read_b32 v54, a90\n"
  "v_mfma_f32_16x16x32_bf16 v[140:143], v[236:239], a[188:191], v[140:143]\n"
  "s_waitcnt lgkmcnt(1)\n"
  "buffer_load_dwordx4 a[252:255], v1, s[36:39], s63 offen offset:3072 sc1\n"
  "v_accvgpr_read_b32 v55, a91\n"
  "v_mfma_f32_16x16x32_bf16 v[144:147], v[240:243], a[160:163], v[144:147]\n"
  "ds_read_b128 v[232:235], v2 offset:55296\n"
  "v_mul_f32 v48, v10, v48\n"
  "v_mul_f32 v49, v10, v49\n"
  "v_mfma_f32_16x16x32_bf16 v[144:147], v[244:247], a[168:171], v[144:147]\n"
  "v_mul_f32 v50, v10, v50\n"
  "v_mul_f32 v51, v10, v51\n"
  "v_mul_f32 v52, v10, v52\n"
  "v_mfma_f32_16x16x32_bf16 v[144:147], v[248:251], a[176:179], v[144:147]\n"
  "s_add_u32 m0, s67, 0\n"
  "ds_read_b128 v[236:239], v2 offset:64512\n"
  "v_mul_f32 v53, v10, v53\n"
  "v_mul_f32 v54, v10, v54\n"
  "v_mfma_f32_16x16x32_bf16 v[144:147], v[252:255], a[184:187], v[144:147]\n"
  "s_add_u32 s66, s70, 147456\n"
  "v_mul_f32 v55, v10, v55\n"
  "v_cvt_pk_bf16_f32 v72, v48, v49\n"
  "v_cvt_pk_bf16_f32 v73, v50, v51\n"
  "v_mfma_f32_16x16x32_bf16 v[148:151], v[240:243], a[164:167], v[148:151]\n"
  "ds_read_b128 v[240:243], v2 offset:37888\n"
  "v_cvt_pk_bf16_f32 v74, v52, v53\n"
  "v_cvt_pk_bf16_f32 v75, v54, v55\n"
  "v_mfma_f32_16x16x32_bf16 v[148:151], v[244:247], a[172:175], v[148:151]\n"
  "v_mfma_f32_16x16x32_bf16 v[148:151], v[248:251], a[180:183], v[148:151]\n"
  "ds_read_b128 v[244:247], v2 offset:47104\n"
  "v_mfma_f32_16x16x32_bf16 v[148:151], v[252:255], a[188:191], v[148:151]\n"
  "s_waitcnt vmcnt(20)\n"
  "v_mfma_f32_16x16x32_bf16 v[80:83], v[224:227], a[192:195], v[80:83]\n"
  "s_waitcnt lgkmcnt(2)\n"
  "ds_read_b128 v[248:251], v2 offset:56320\n"
  "v_add_u32 v2, 36864, v2\n"
  "v_mfma_f32_16x16x32_bf16 v[80:83], v[228:231], a[200:203], v[80:83]\n"
  "s_waitcnt vmcnt(18)\n"
  "buffer_load_dwordx4 v1, s[36:39], s66 offen offset:0 sc1 lds\n"
  "v_mfma_f32_16x16x32_bf16 v[80:83], v[232:235], a[208:211], v[80:83]\n"
  "s_add_u32 s62, s60, 16384\n"
  "ds_read_b128 v[252:255], v2 offset:28672\n"
  "buffer_store_dwordx4 v[68:71], v1, s[36:39], s75 offen offset:1024 sc1\n"
  "s_waitcnt vmcnt(17)\n"
  "v_mfma_f32_16x16x32_bf16 v[80:83], v[236:239], a[216:219], v[80:83]\n"
  "v_mfma_f32_16x16x32_bf16 v[84:87], v[224:227], a[196:199], v[84:87]\n"
  "ds_read_b128 v[224:227], v2 offset:2048\n"
  "v_accvgpr_read_b32 v56, a104\n"
  "v_accvgpr_read_b32 v57, a105\n"
  "v_mfma_f32_16x16x32_bf16 v[84:87], v[228:231], a[204:207], v[84:87]\n"
  "buffer_load_dwordx4 a[128:131], v1, s[36:39], s62 offen offset:0 sc1\n"
  "v_accvgpr_read_b32 v58, a106\n"
  "v_mfma_f32_16x16x32_bf16 v[84:87], v[232:235], a[212:215], v[84:87]\n"
  "s_waitcnt vmcnt(16)\n"
  "ds_read_b128 v[228:231], v2 offset:11264\n"
  "v_accvgpr_read_b32 v59, a107\n"
  "v_accvgpr_read_b32 v60, a120\n"
  "v_mfma_f32_16x16x32_bf16 v[84:87], v[236:239], a[220:223], v[84:87]\n"
  "s_waitcnt lgkmcnt(1)\n"
  "v_accvgpr_read_b32 v61, a121\n"
  "v_accvgpr_read_b32 v62, a122\n"
  "v_accvgpr_read_b32 v63, a123\n"
  "v_mfma_f32_16x16x32_bf16 v[88:91], v[240:243], a[192:195], v[88:91]\n"
  "ds_read_b128 v[232:235], v2 offset:20480\n"
  "v_mul_f32 v56, v10, v56\n"
  "v_mul_f32 v57, v10, v57\n"
  "v_mfma_f32_16x16x32_bf16 v[88:91], v[244:247], a[200:203], v[88:91]\n"
  "buffer_load_dwordx4 v1, s[36:39], s66 offen offset:1024 sc1 lds\n"
  "v_mul_f32 v58, v10, v58\n"
  "v_mfma_f32_16x16x32_bf16 v[88:91], v[248:251], a[208:211], v[88:91]\n"
  "s_add_u32 s63, s61, 16384\n"
  "ds_read_b128 v[236:239], v2 offset:29696\n"
  "v_mul_f32 v59, v10, v59\n"
  "v_mul_f32 v60, v10, v60\n"
  "v_mfma_f32_16x16x32_bf16 v[88:91], v[252:255], a[216:219], v[88:91]\n"
  "v_mul_f32 v61, v10, v61\n"
  "v_mul_f32 v62, v10, v62\n"
  "v_mul_f32 v63, v10, v63\n"
  "v_mfma_f32_16x16x32_bf16 v[92:95], v[240:243], a[196:199], v[92:95]\n"
  "ds_read_b128 v[240:243], v2 offset:3072\n"
  "v_cvt_pk_bf16_f32 v76, v56, v57\n"
  "v_cvt_pk_bf16_f32 v77, v58, v59\n"
  "v_mfma_f32_16x16x32_bf16 v[92:95], v[244:247], a[204:207], v[92:95]\n"
  "buffer_load_dwordx4 a[132:135], v1, s[36:39], s63 offen offset:0 sc1\n"
  "v_cvt_pk_bf16_f32 v78, v60, v61\n"
  "v_mfma_f32_16x16x32_bf16 v[92:95], v[248:251], a[212:215], v[92:95]\n"
  "s_add_u32 m0, s67, 2048\n"
  "ds_read_b128 v[244:247], v2 offset:12288\n"
  "v_cvt_pk_bf16_f32 v79, v62, v63\n"
  "v_mfma_f32_16x16x32_bf16 v[92:95], v[252:255], a[220:223], v[92:95]\n"
  "s_add_u32 s66, s71, 147456\n"
  "v_mfma_f32_16x16x32_bf16 v[96:99], v[224:227], a[192:195], v[96:99]\n"
  "s_waitcnt lgkmcnt(1)\n"
  "ds_read_b128 v[248:251], v2 offset:21504\n"
  "v_mfma_f32_16x16x32_bf16 v[96:99], v[228:231], a[200:203], v[96:99]\n"
  "buffer_load_dwordx4 v1, s[36:39], s66 offen offset:0 sc1 lds\n"
  "v_mfma_f32_16x16x32_bf16 v[96:99], v[232:235], a[208:211], v[96:99]\n"
  "ds_read_b128 v[252:255], v2 offset:30720\n"
  "buffer_store_dwordx4 v[72:75], v1, s[36:39], s75 offen offset:2048 sc1\n"
  "v_mfma_f32_16x16x32_bf16 v[96:99], v[236:239], a[216:219], v[96:99]\n"
  "v_mfma_f32_16x16x32_bf16 v[100:103], v[224:227], a[196:199], v[100:103]\n"
  "ds_read_b128 v[224:227], v2 offset:4096\n"
  "v_accvgpr_read_b32 v48, a12\n"
  "v_accvgpr_read_b32 v49, a13\n"
  "v_mfma_f32_16x16x32_bf16 v[100:103], v[228:231], a[204:207], v[100:103]\n"
  "buffer_load_dwordx4 a[136:139], v1, s[36:39], s62 offen offset:1024 sc1\n"
  "v_accvgpr_read_b32 v50, a14\n"
  "v_mfma_f32_16x16x32_bf16 v[100:103], v[232:235], a[212:215], v[100:103]\n"
  "ds_read_b128 v[228:231], v2 offset:13312\n"
  "v_accvgpr_read_b32 v51, a15\n"
  "v_accvgpr_read_b32 v52, a28\n"
  "v_mfma_f32_16x16x32_bf16 v[100:103], v[236:239], a[220:223], v[100:103]\n"
  "v_accvgpr_read_b32 v53, a29\n"
  "v_accvgpr_read_b32 v54, a30\n"
  "v_accvgpr_read_b32 v55, a31\n"
  "v_mfma_f32_16x16x32_bf16 v[104:107], v[240:243], a[192:195], v[104:107]\n"
  "s_waitcnt lgkmcnt(1)\n"
  "ds_read_b128 v[232:235], v2 offset:22528\n"
  "v_mul_f32 v48, v11, v48\n"
  "v_mul_f32 v49, v11, v49\n"
  "v_mfma_f32_16x16x32_bf16 v[104:107], v[244:247], a[200:203], v[104:107]\n"
  "buffer_load_dwordx4 v1, s[36:39], s66 offen offset:1024 sc1 lds\n"
  "v_mul_f32 v50, v11, v50\n"
  "v_mfma_f32_16x16x32_bf16 v[104:107], v[248:251], a[208:211], v[104:107]\n"
  "ds_read_b128 v[236:239], v2 offset:31744\n"
  "v_mul_f32 v51, v11, v51\n"
  "v_mul_f32 v52, v11, v52\n"
  "v_mfma_f32_16x16x32_bf16 v[104:107], v[252:255], a[216:219], v[104:107]\n"
  "v_mul_f32 v53, v11, v53\n"
  "v_mul_f32 v54, v11, v54\n"
  "v_mul_f32 v55, v11, v55\n"
  "v_mfma_f32_16x16x32_bf16 v[108:111], v[240:243], a[196:199], v[108:111]\n"
  "ds_read_b128 v[240:243], v2 offset:5120\n"
  "v_cvt_pk_bf16_f32 v64, v48, v49\n"
  "v_cvt_pk_bf16_f32 v65, v50, v51\n"
  "v_mfma_f32_16x16x32_bf16 v[108:111], v[244:247], a[204:207], v[108:111]\n"
  "buffer_load_dwordx4 a[140:143], v1, s[36:39], s63 offen offset:1024 sc1\n"
  "v_cvt_pk_bf16_f32 v66, v52, v53\n"
  "v_mfma_f32_16x16x32_bf16 v[108:111], v[248:251], a[212:215], v[108:111]\n"
  "s_add_u32 m0, s67, 4096\n"
  "ds_read_b128 v[244:247], v2 offset:14336\n"
  "v_cvt_pk_bf16_f32 v67, v54, v55\n"
  "v_mfma_f32_16x16x32_bf16 v[108:111], v[252:255], a[220:223], v[108:111]\n"
  "s_add_u32 s66, s72, 147456\n"
  "v_mfma_f32_16x16x32_bf16 v[112:115], v[224:227], a[192:195], v[112:115]\n"
  "s_waitcnt lgkmcnt(1)\n"
  "ds_read_b128 v[248:251], v2 offset:23552\n"
  "v_mfma_f32_16x16x32_bf16 v[112:115], v[228:231], a[200:203], v[112:115]\n"
  "buffer_load_dwordx4 v1, s[36:39], s66 offen offset:0 sc1 lds\n"
  "v_mfma_f32_16x16x32_bf16 v[112:115], v[232:235], a[208:211], v[112:115]\n"
  "ds_read_b128 v[252:255], v2 offset:32768\n"
  "buffer_store_dwordx4 v[76:79], v1, s[36:39], s75 offen offset:3072 sc1\n"
  "v_mfma_f32_16x16x32_bf16 v[112:115], v[236:239], a[216:219], v[112:115]\n"
  "v_accvgpr_read_b32 v56, a44\n"
  "v_accvgpr_read_b32 v57, a45\n"
  "v_accvgpr_read_b32 v58, a46\n"
  "v_mfma_f32_16x16x32_bf16 v[116:119], v[224:227], a[196:199], v[116:119]\n"
  "ds_read_b128 v[224:227], v2 offset:6144\n"
  "v_accvgpr_read_b32 v59, a47\n"
  "v_accvgpr_read_b32 v60, a60\n"
  "v_mfma_f32_16x16x32_bf16 v[116:119], v[228:231], a[204:207], v[116:119]\n"
  "v_accvgpr_read_b32 v61, a61\n"
  "v_accvgpr_read_b32 v62, a62\n"
  "v_accvgpr_read_b32 v63, a63\n"
  "v_mfma_f32_16x16x32_bf16 v[116:119], v[232:235], a[212:215], v[116:119]\n"
  "ds_read_b128 v[228:231], v2 offset:15360\n"
  "v_mul_f32 v56, v11, v56\n"
  "v_mul_f32 v57, v11, v57\n"
  "v_mfma_f32_16x16x32_bf16 v[116:119], v[236:239], a[220:223], v[116:119]\n"
  "buffer_load_dwordx4 a[144:147], v1, s[36:39], s62 offen offset:2048 sc1\n"
  "v_mul_f32 v58, v11, v58\n"
  "v_mfma_f32_16x16x32_bf16 v[120:123], v[240:243], a[192:195], v[120:123]\n"
  "s_waitcnt lgkmcnt(1)\n"
  "ds_read_b128 v[232:235], v2 offset:24576\n"
  "v_mul_f32 v59, v11, v59\n"
  "v_mul_f32 v60, v11, v60\n"
  "v_mfma_f32_16x16x32_bf16 v[120:123], v[244:247], a[200:203], v[120:123]\n"
  "v_mul_f32 v61, v11, v61\n"
  "v_mul_f32 v62, v11, v62\n"
  "v_mul_f32 v63, v11, v63\n"
  "v_mfma_f32_16x16x32_bf16 v[120:123], v[248:251], a[208:211], v[120:123]\n"
  "ds_read_b128 v[236:239], v2 offset:33792\n"
  "v_cvt_pk_bf16_f32 v68, v56, v57\n"
  "v_cvt_pk_bf16_f32 v69, v58, v59\n"
  "v_mfma_f32_16x16x32_bf16 v[120:123], v[252:255], a[216:219], v[120:123]\n"
  "buffer_load_dwordx4 v1, s[36:39], s66 offen offset:1024 sc1 lds\n"
  "v_cvt_pk_bf16_f32 v70, v60, v61\n"
  "v_mfma_f32_16x16x32_bf16 v[124:127], v[240:243], a[196:199], v[124:127]\n"
  "ds_read_b128 v[240:243], v2 offset:7168\n"
  "v_cvt_pk_bf16_f32 v71, v62, v63\n"
  "v_mfma_f32_16x16x32_bf16 v[124:127], v[244:247], a[204:207], v[124:127]\n"
  "v_mfma_f32_16x16x32_bf16 v[124:127], v[248:251], a[212:215], v[124:127]\n"
  "ds_read_b128 v[244:247], v2 offset:16384\n"
  "v_mfma_f32_16x16x32_bf16 v[124:127], v[252:255], a[220:223], v[124:127]\n"
  "buffer_load_dwordx4 a[148:151], v1, s[36:39], s63 offen offset:2048 sc1\n"
  "v_mfma_f32_16x16x32_bf16 v[128:131], v[224:227], a[192:195], v[128:131]\n"
  "s_add_u32 m0, s67, 6144\n"
  "ds_read_b128 v[248:251], v2 offset:25600\n"
  "s_waitcnt lgkmcnt(3)\n"
  "v_mfma_f32_16x16x32_bf16 v[128:131], v[228:231], a[200:203], v[128:131]\n"
  "s_add_u32 s66, s35, 147456\n"
  "buffer_store_dwordx4 v[64:67], v1, s[36:39], s76 offen offset:0 sc1\n"
  "v_mfma_f32_16x16x32_bf16 v[128:131], v[232:235], a[208:211], v[128:131]\n"
  "ds_read_b128 v[252:255], v2 offset:34816\n"
  "v_accvgpr_read_b32 v48, a76\n"
  "v_accvgpr_read_b32 v49, a77\n"
  "v_mfma_f32_16x16x32_bf16 v[128:131], v[236:239], a[216:219], v[128:131]\n"
  "buffer_load_dwordx4 v1, s[36:39], s66 offen offset:0 sc1 lds\n"
  "v_accvgpr_read_b32 v50, a78\n"
  "v_mfma_f32_16x16x32_bf16 v[132:135], v[224:227], a[196:199], v[132:135]\n"
  "ds_read_b128 v[224:227], v2 offset:8192\n"
  "v_accvgpr_read_b32 v51, a79\n"
  "v_accvgpr_read_b32 v52, a92\n"
  "v_mfma_f32_16x16x32_bf16 v[132:135], v[228:231], a[204:207], v[132:135]\n"
  "v_accvgpr_read_b32 v53, a93\n"
  "v_accvgpr_read_b32 v54, a94\n"
  "v_accvgpr_read_b32 v55, a95\n"
  "v_mfma_f32_16x16x32_bf16 v[132:135], v[232:235], a[212:215], v[132:135]\n"
  "ds_read_b128 v[228:231], v2 offset:17408\n"
  "v_mul_f32 v48, v11, v48\n"
  "v_mul_f32 v49, v11, v49\n"
  "v_mfma_f32_16x16x32_bf16 v[132:135], v[236:239], a[220:223], v[132:135]\n"
  "s_waitcnt lgkmcnt(1)\n"
  "buffer_load_dwordx4 a[152:155], v1, s[36:39], s62 offen offset:3072 sc1\n"
  "v_mul_f32 v50, v11, v50\n"
  "v_mfma_f32_16x16x32_bf16 v[136:139], v[240:243], a[192:195], v[136:139]\n"
  "ds_read_b128 v[232:235], v2 offset:26624\n"
  "v_mul_f32 v51, v11, v51\n"
  "v_mul_f32 v52, v11, v52\n"
  "v_mfma_f32_16x16x32_bf16 v[136:139], v[244:247], a[200:203], v[136:139]\n"
  "v_mul_f32 v53, v11, v53\n"
  "v_mul_f32 v54, v11, v54\n"
  "v_mul_f32 v55, v11, v55\n"
  "v_mfma_f32_16x16x32_bf16 v[136:139], v[248:251], a[208:211], v[136:139]\n"
  "ds_read_b128 v[236:239], v2 offset:35840\n"
  "v_cvt_pk_bf16_f32 v72, v48, v49\n"
  "v_cvt_pk_bf16_f32 v73, v50, v51\n"
  "v_mfma_f32_16x16x32_bf16 v[136:139], v[252:255], a[216:219], v[136:139]\n"
  "buffer_load_dwordx4 v1, s[36:39], s66 offen offset:1024 sc1 lds\n"
  "v_cvt_pk_bf16_f32 v74, v52, v53\n"
  "v_mfma_f32_16x16x32_bf16 v[140:143], v[240:243], a[196:199], v[140:143]\n"
  "s_barrier\n"
  "ds_read_b128 v[240:243], v2 offset:36864\n"
  "v_cvt_pk_bf16_f32 v75, v54, v55\n"
  "v_mfma_f32_16x16x32_bf16 v[140:143], v[244:247], a[204:207], v[140:143]\n"
  "v_mfma_f32_16x16x32_bf16 v[140:143], v[248:251], a[212:215], v[140:143]\n"
  "ds_read_b128 v[244:247], v2 offset:46080\n"
  "v_mfma_f32_16x16x32_bf16 v[140:143], v[252:255], a[220:223], v[140:143]\n"
  "buffer_load_dwordx4 a[156:159], v1, s[36:39], s63 offen offset:3072 sc1\n"
  "v_mfma_f32_16x16x32_bf16 v[144:147], v[224:227], a[192:195], v[144:147]\n"
  "s_add_u32 m0, s67, 8192\n"
  "ds_read_b128 v[248:251], v2 offset:55296\n"
  "buffer_store_dwordx4 v[68:71], v1, s[36:39], s76 offen offset:1024 sc1\n"
  "s_waitcnt lgkmcnt(3)\n"
  "v_mfma_f32_16x16x32_bf16 v[144:147], v[228:231], a[200:203], v[144:147]\n"
  "s_add_u32 s66, s64, 155648\n"
  "v_accvgpr_read_b32 v56, a108\n"
  "v_accvgpr_read_b32 v57, a109\n"
  "v_accvgpr_read_b32 v58, a110\n"
  "v_mfma_f32_16x16x32_bf16 v[144:147], v[232:235], a[208:211], v[144:147]\n"
  "ds_read_b128 v[252:255], v2 offset:64512\n"
  "v_accvgpr_read_b32 v59, a111\n"
  "v_accvgpr_read_b32 v60, a124\n"
  "v_mfma_f32_16x16x32_bf16 v[144:147], v[236:239], a[216:219], v[144:147]\n"
  "buffer_load_dwordx4 v1, s[36:39], s66 offen offset:0 sc1 lds\n"
  "v_accvgpr_read_b32 v61, a125\n"
  "v_mfma_f32_16x16x32_bf16 v[148:151], v[224:227], a[196:199], v[148:151]\n"
  "s_add_u32 m0, s67, 36864\n"
  "ds_read_b128 v[224:227], v2 offset:37888\n"
  "v_accvgpr_read_b32 v62, a126\n"
  "v_accvgpr_read_b32 v63, a127\n"
  "v_mfma_f32_16x16x32_bf16 v[148:151], v[228:231], a[204:207], v[148:151]\n"
  "s_add_u32 s66, s70, 184320\n"
  "v_mul_f32 v56, v11, v56\n"
  "v_mul_f32 v57, v11, v57\n"
  "v_mul_f32 v58, v11, v58\n"
  "v_mfma_f32_16x16x32_bf16 v[148:151], v[232:235], a[212:215], v[148:151]\n"
  "ds_read_b128 v[228:231], v2 offset:47104\n"
  "v_mul_f32 v59, v11, v59\n"
  "v_mul_f32 v60, v11, v60\n"
  "v_mfma_f32_16x16x32_bf16 v[148:151], v[236:239], a[220:223], v[148:151]\n"
  "s_waitcnt vmcnt(30) lgkmcnt(1)\n"
  "v_mul_f32 v61, v11, v61\n"
  "v_mul_f32 v62, v11, v62\n"
  "v_mul_f32 v63, v11, v63\n"
  "v_mfma_f32_16x16x32_bf16 v[80:83], v[240:243], a[224:227], v[80:83]\n"
  "ds_read_b128 v[232:235], v2 offset:56320\n"
  "v_add_u32 v2, 36864, v2\n"
  "v_cvt_pk_bf16_f32 v76, v56, v57\n"
  "v_mfma_f32_16x16x32_bf16 v[80:83], v[244:247], a[232:235], v[80:83]\n"
  "s_waitcnt vmcnt(27)\n"
  "buffer_load_dwordx4 v1, s[36:39], s66 offen offset:0 sc1 lds\n"
  "v_cvt_pk_bf16_f32 v77, v58, v59\n"
  "v_mfma_f32_16x16x32_bf16 v[80:83], v[248:251], a[240:243], v[80:83]\n"
  "s_add_u32 s62, s60, 20480\n"
  "ds_read_b128 v[236:239], v2 offset:28672\n"
  "v_cvt_pk_bf16_f32 v78, v60, v61\n"
  "v_cvt_pk_bf16_f32 v79, v62, v63\n"
  "s_waitcnt vmcnt(25)\n"
  "v_mfma_f32_16x16x32_bf16 v[80:83], v[252:255], a[248:251], v[80:83]\n"
  "v_mfma_f32_16x16x32_bf16 v[84:87], v[240:243], a[228:231], v[84:87]\n"
  "ds_read_b128 v[240:243], v2 offset:2048\n"
  "v_mfma_f32_16x16x32_bf16 v[84:87], v[244:247], a[236:239], v[84:87]\n"
  "buffer_load_dwordx4 a[160:163], v1, s[36:39], s62 offen offset:0 sc1\n"
  "v_mfma_f32_16x16x32_bf16 v[84:87], v[248:251], a[244:247], v[84:87]\n"
  "s_waitcnt vmcnt(24)\n"
  "ds_read_b128 v[244:247], v2 offset:11264\n"
  "v_mfma_f32_16x16x32_bf16 v[84:87], v[252:255], a[252:255], v[84:87]\n"
  "buffer_store_dwordx4 v[72:75], v1, s[36:39], s76 offen offset:2048 sc1\n"
  "v_mfma_f32_16x16x32_bf16 v[88:91], v[224:227], a[224:227], v[88:91]\n"
  "s_waitcnt lgkmcnt(2)\n"
  "ds_read_b128 v[248:251], v2 offset:20480\n"
  "v_mfma_f32_16x16x32_bf16 v[88:91], v[228:231], a[232:235], v[88:91]\n"
  "buffer_load_dwordx4 v1, s[36:39], s66 offen offset:1024 sc1 lds\n"
  "v_mfma_f32_16x16x32_bf16 v[88:91], v[232:235], a[240:243], v[88:91]\n"
  "s_add_u32 s63, s61, 20480\n"
  "ds_read_b128 v[252:255], v2 offset:29696\n"
  "v_mfma_f32_16x16x32_bf16 v[88:91], v[236:239], a[248:251], v[88:91]\n"
  "v_mfma_f32_16x16x32_bf16 v[92:95], v[224:227], a[228:231], v[92:95]\n"
  "ds_read_b128 v[224:227], v2 offset:3072\n"
  "v_mfma_f32_16x16x32_bf16 v[92:95], v[228:231], a[236:239], v[92:95]\n"
  "buffer_load_dwordx4 a[164:167], v1, s[36:39], s63 offen offset:0 sc1\n"
  "v_mfma_f32_16x16x32_bf16 v[92:95], v[232:235], a[244:247], v[92:95]\n"
  "s_add_u32 m0, s67, 38912\n"
  "ds_read_b128 v[228:231], v2 offset:12288\n"
  "v_mfma_f32_16x16x32_bf16 v[92:95], v[236:239], a[252:255], v[92:95]\n"
  "s_add_u32 s66, s71, 184320\n"
  "s_waitcnt lgkmcnt(3)\n"
  "v_mfma_f32_16x16x32_bf16 v[96:99], v[240:243], a[224:227], v[96:99]\n"
  "ds_read_b128 v[232:235], v2 offset:21504\n"
  "v_mfma_f32_16x16x32_bf16 v[96:99], v[244:247], a[232:235], v[96:99]\n"
  "buffer_load_dwordx4 v1, s[36:39], s66 offen offset:0 sc1 lds\n"
  "v_mfma_f32_16x16x32_bf16 v[96:99], v[248:251], a[240:243], v[96:99]\n"
  "s_waitcnt lgkmcnt(2)\n"
  "ds_read_b128 v[236:239], v2 offset:30720\n"
  "v_mfma_f32_16x16x32_bf16 v[96:99], v[252:255], a[248:251], v[96:99]\n"
  "v_mfma_f32_16x16x32_bf16 v[100:103], v[240:243], a[228:231], v[100:103]\n"
  "ds_read_b128 v[240:243], v2 offset:4096\n"
  "v_mfma_f32_16x16x32_bf16 v[100:103], v[244:247], a[236:239], v[100:103]\n"
  "buffer_load_dwordx4 a[168:171], v1, s[36:39], s62 offen offset:1024 sc1\n"
  "v_mfma_f32_16x16x32_bf16 v[100:103], v[248:251], a[244:247], v[100:103]\n"
  "ds_read_b128 v[244:247], v2 offset:13312\n"
  "buffer_store_dwordx4 v[76:79], v1, s[36:39], s76 offen offset:3072 sc1\n"
  "v_mfma_f32_16x16x32_bf16 v[100:103], v[252:255], a[252:255], v[100:103]\n"
  "v_mfma_f32_16x16x32_bf16 v[104:107], v[224:227], a[224:227], v[104:107]\n"
  "s_waitcnt lgkmcnt(3)\n"
  "ds_read_b128 v[248:251], v2 offset:22528\n"
  "v_mfma_f32_16x16x32_bf16 v[104:107], v[228:231], a[232:235], v[104:107]\n"
  "buffer_load_dwordx4 v1, s[36:39], s66 offen offset:1024 sc1 lds\n"
  "v_mfma_f32_16x16x32_bf16 v[104:107], v[232:235], a[240:243], v[104:107]\n"
  "s_waitcnt lgkmcnt(2)\n"
  "ds_read_b128 v[252:255], v2 offset:31744\n"
  "v_mfma_f32_16x16x32_bf16 v[104:107], v[236:239], a[248:251], v[104:107]\n"
  "v_mfma_f32_16x16x32_bf16 v[108:111], v[224:227], a[228:231], v[108:111]\n"
  "ds_read_b128 v[224:227], v2 offset:5120\n"
  "v_mfma_f32_16x16x32_bf16 v[108:111], v[228:231], a[236:239], v[108:111]\n"
  "buffer_load_dwordx4 a[172:175], v1, s[36:39], s63 offen offset:1024 sc1\n"
  "v_mfma_f32_16x16x32_bf16 v[108:111], v[232:235], a[244:247], v[108:111]\n"
  "s_add_u32 m0, s67, 40960\n"
  "ds_read_b128 v[228:231], v2 offset:14336\n"
  "v_mfma_f32_16x16x32_bf16 v[108:111], v[236:239], a[252:255], v[108:111]\n"
  "s_add_u32 s66, s72, 184320\n"
  "v_mfma_f32_16x16x32_bf16 v[112:115], v[240:243], a[224:227], v[112:115]\n"
  "s_waitcnt lgkmcnt(2)\n"
  "ds_read_b128 v[232:235], v2 offset:23552\n"
  "v_mfma_f32_16x16x32_bf16 v[112:115], v[244:247], a[232:235], v[112:115]\n"
  "buffer_load_dwordx4 v1, s[36:39], s66 offen offset:0 sc1 lds\n"
  "v_mfma_f32_16x16x32_bf16 v[112:115], v[248:251], a[240:243], v[112:115]\n"
  "ds_read_b128 v[236:239], v2 offset:32768\n"
  "v_mfma_f32_16x16x32_bf16 v[112:115], v[252:255], a[248:251], v[112:115]\n"
  "v_mfma_f32_16x16x32_bf16 v[116:119], v[240:243], a[228:231], v[116:119]\n"
  "ds_read_b128 v[240:243], v2 offset:6144\n"
  "v_mfma_f32_16x16x32_bf16 v[116:119], v[244:247], a[236:239], v[116:119]\n"
  "v_mfma_f32_16x16x32_bf16 v[116:119], v[248:251], a[244:247], v[116:119]\n"
  "ds_read_b128 v[244:247], v2 offset:15360\n"
  "v_mfma_f32_16x16x32_bf16 v[116:119], v[252:255], a[252:255], v[116:119]\n"
  "s_waitcnt lgkmcnt(3)\n"
  "buffer_load_dwordx4 a[176:179], v1, s[36:39], s62 offen offset:2048 sc1\n"
  "v_mfma_f32_16x16x32_bf16 v[120:123], v[224:227], a[224:227], v[120:123]\n"
  "ds_read_b128 v[248:251], v2 offset:24576\n"
  "v_mfma_f32_16x16x32_bf16 v[120:123], v[228:231], a[232:235], v[120:123]\n"
  "v_mfma_f32_16x16x32_bf16 v[120:123], v[232:235], a[240:243], v[120:123]\n"
  "s_waitcnt lgkmcnt(3)\n"
  "ds_read_b128 v[252:255], v2 offset:33792\n"
  "v_mfma_f32_16x16x32_bf16 v[120:123], v[236:239], a[248:251], v[120:123]\n"
  "buffer_load_dwordx4 v1, s[36:39], s66 offen offset:1024 sc1 lds\n"
  "v_mfma_f32_16x16x32_bf16 v[124:127], v[224:227], a[228:231], v[124:127]\n"
  "ds_read_b128 v[224:227], v2 offset:7168\n"
  "v_mfma_f32_16x16x32_bf16 v[124:127], v[228:231], a[236:239], v[124:127]\n"
  "v_mfma_f32_16x16x32_bf16 v[124:127], v[232:235], a[244:247], v[124:127]\n"
  "ds_read_b128 v[228:231], v2 offset:16384\n"
  "v_mfma_f32_16x16x32_bf16 v[124:127], v[236:239], a[252:255], v[124:127]\n"
  "s_waitcnt lgkmcnt(3)\n"
  "buffer_load_dwordx4 a[180:183], v1, s[36:39], s63 offen offset:2048 sc1\n"
  "v_mfma_f32_16x16x32_bf16 v[128:131], v[240:243], a[224:227], v[128:131]\n"
  "s_add_u32 m0, s67, 43008\n"
  "ds_read_b128 v[232:235], v2 offset:25600\n"
  "v_mfma_f32_16x16x32_bf16 v[128:131], v[244:247], a[232:235], v[128:131]\n"
  "s_add_u32 s66, s35, 184320\n"
  "v_mfma_f32_16x16x32_bf16 v[128:131], v[248:251], a[240:243], v[128:131]\n"
  "s_waitcnt lgkmcnt(2)\n"
  "ds_read_b128 v[236:239], v2 offset:34816\n"
  "v_mfma_f32_16x16x32_bf16 v[128:131], v[252:255], a[248:251], v[128:131]\n"
  "buffer_load_dwordx4 v1, s[36:39], s66 offen offset:0 sc1 lds\n"
  "v_mfma_f32_16x16x32_bf16 v[132:135], v[240:243], a[228:231], v[132:135]\n"
  "ds_read_b128 v[240:243], v2 offset:8192\n"
  "v_mfma_f32_16x16x32_bf16 v[132:135], v[244:247], a[236:239], v[132:135]\n"
  "v_mfma_f32_16x16x32_bf16 v[132:135], v[248:251], a[244:247], v[132:135]\n"
  "ds_read_b128 v[244:247], v2 offset:17408\n"
  "v_mfma_f32_16x16x32_bf16 v[132:135], v[252:255], a[252:255], v[132:135]\n"
  "buffer_load_dwordx4 a[184:187], v1, s[36:39], s62 offen offset:3072 sc1\n"
  "v_mfma_f32_16x16x32_bf16 v[136:139], v[224:227], a[224:227], v[136:139]\n"
  "s_waitcnt lgkmcnt(3)\n"
  "ds_read_b128 v[248:251], v2 offset:26624\n"
  "v_mfma_f32_16x16x32_bf16 v[136:139], v[228:231], a[232:235], v[136:139]\n"
  "v_mfma_f32_16x16x32_bf16 v[136:139], v[232:235], a[240:243], v[136:139]\n"
  "s_waitcnt lgkmcnt(3)\n"
  "ds_read_b128 v[252:255], v2 offset:35840\n"
  "v_add_u32 v2, 0xfffe5000, v2\n"
  "v_mfma_f32_16x16x32_bf16 v[136:139], v[236:239], a[248:251], v[136:139]\n"
  "buffer_load_dwordx4 v1, s[36:39], s66 offen offset:1024 sc1 lds\n"
  "v_mfma_f32_16x16x32_bf16 v[140:143], v[224:227], a[228:231], v[140:143]\n"
  "s_waitcnt vmcnt(17)\n"
  "s_barrier\n"
  "ds_read_b128 v[224:227], v2 offset:0\n"
  "v_mfma_f32_16x16x32_bf16 v[140:143], v[228:231], a[236:239], v[140:143]\n"
  "v_mfma_f32_16x16x32_bf16 v[140:143], v[232:235], a[244:247], v[140:143]\n"
  "ds_read_b128 v[228:231], v2 offset:9216\n"
  "v_mfma_f32_16x16x32_bf16 v[140:143], v[236:239], a[252:255], v[140:143]\n"
  "s_waitcnt lgkmcnt(2)\n"
  "buffer_load_dwordx4 a[188:191], v1, s[36:39], s63 offen offset:3072 sc1\n"
  "v_mfma_f32_16x16x32_bf16 v[144:147], v[240:243], a[224:227], v[144:147]\n"
  "s_add_u32 m0, s67, 45056\n"
  "ds_read_b128 v[232:235], v2 offset:18432\n"
  "v_mfma_f32_16x16x32_bf16 v[144:147], v[244:247], a[232:235], v[144:147]\n"
  "s_add_u32 s66, s64, 192512\n"
  "v_mfma_f32_16x16x32_bf16 v[144:147], v[248:251], a[240:243], v[144:147]\n"
  "ds_read_b128 v[236:239], v2 offset:27648\n"
  "v_mfma_f32_16x16x32_bf16 v[144:147], v[252:255], a[248:251], v[144:147]\n"
  "buffer_load_dwordx4 v1, s[36:39], s66 offen offset:0 sc1 lds\n"
  "v_mfma_f32_16x16x32_bf16 v[148:151], v[240:243], a[228:231], v[148:151]\n"
  "s_add_u32 m0, s67, 73728\n"
  "ds_read_b128 v[240:243], v2 offset:1024\n"
  "v_mfma_f32_16x16x32_bf16 v[148:151], v[244:247], a[236:239], v[148:151]\n"
  "s_add_u32 s66, s70, 221184\n"
  "v_mfma_f32_16x16x32_bf16 v[148:151], v[248:251], a[244:247], v[148:151]\n"
  "ds_read_b128 v[244:247], v2 offset:10240\n"
  "v_mfma_f32_16x16x32_bf16 v[148:151], v[252:255], a[252:255], v[148:151]\n"
  "s_waitcnt lgkmcnt(3)\n"
  "v_mfma_f32_16x16x32_bf16 v[80:83], v[224:227], a[128:131], v[80:83]\n"
  "ds_read_b128 v[248:251], v2 offset:19456\n"
  "v_mfma_f32_16x16x32_bf16 v[80:83], v[228:231], a[136:139], v[80:83]\n"
  "buffer_load_dwordx4 v1, s[36:39], s66 offen offset:0 sc1 lds\n"
  "v_mfma_f32_16x16x32_bf16 v[80:83], v[232:235], a[144:147], v[80:83]\n"
  "s_add_u32 s62, s60, 24576\n"
  "ds_read_b128 v[252:255], v2 offset:28672\n"
  "s_waitcnt lgkmcnt(3)\n"
  "v_mfma_f32_16x16x32_bf16 v[80:83], v[236:239], a[152:155], v[80:83]\n"
  "v_mfma_f32_16x16x32_bf16 v[84:87], v[224:227], a[132:135], v[84:87]\n"
  "ds_read_b128 v[224:227], v2 offset:2048\n"
  "v_mfma_f32_16x16x32_bf16 v[84:87], v[228:231], a[140:143], v[84:87]\n"
  "s_waitcnt vmcnt(12)\n"
  "buffer_load_dwordx4 a[192:195], v1, s[36:39], s62 offen offset:0 sc1\n"
  "v_mfma_f32_16x16x32_bf16 v[84:87], v[232:235], a[148:151], v[84:87]\n"
  "ds_read_b128 v[228:231], v2 offset:11264\n"
  "v_mfma_f32_16x16x32_bf16 v[84:87], v[236:239], a[156:159], v[84:87]\n"
  "buffer_store_dword v22, v3, s[36:39], s94 offen offset:32 sc1\n"
  "v_mfma_f32_16x16x32_bf16 v[88:91], v[240:243], a[128:131], v[88:91]\n"
  "s_waitcnt lgkmcnt(2)\n"
  "ds_read_b128 v[232:235], v2 offset:20480\n"
  "v_mfma_f32_16x16x32_bf16 v[88:91], v[244:247], a[136:139], v[88:91]\n"
  "buffer_load_dwordx4 v1, s[36:39], s66 offen offset:1024 sc1 lds\n"
  "v_mfma_f32_16x16x32_bf16 v[88:91], v[248:251], a[144:147], v[88:91]\n"
  "s_add_u32 s63, s61, 24576\n"
  "ds_read_b128 v[236:239], v2 offset:29696\n"
  "v_mfma_f32_16x16x32_bf16 v[88:91], v[252:255], a[152:155], v[88:91]\n"
  "v_mfma_f32_16x16x32_bf16 v[92:95], v[240:243], a[132:135], v[92:95]\n"
  "ds_read_b128 v[240:243], v2 offset:3072\n"
  "v_mfma_f32_16x16x32_bf16 v[92:95], v[244:247], a[140:143], v[92:95]\n"
  "buffer_load_dwordx4 a[196:199], v1, s[36:39], s63 offen offset:0 sc1\n"
  "v_mfma_f32_16x16x32_bf16 v[92:95], v[248:251], a[148:151], v[92:95]\n"
  "s_add_u32 m0, s67, 75776\n"
  "ds_read_b128 v[244:247], v2 offset:12288\n"
  "v_mfma_f32_16x16x32_bf16 v[92:95], v[252:255], a[156:159], v[92:95]\n"
  "s_add_u32 s66, s71, 221184\n"
  "s_waitcnt lgkmcnt(3)\n"
  "v_mfma_f32_16x16x32_bf16 v[96:99], v[224:227], a[128:131], v[96:99]\n"
  "ds_read_b128 v[248:251], v2 offset:21504\n"
  "v_mfma_f32_16x16x32_bf16 v[96:99], v[228:231], a[136:139], v[96:99]\n"
  "buffer_load_dwordx4 v1, s[36:39], s66 offen offset:0 sc1 lds\n"
  "v_mfma_f32_16x16x32_bf16 v[96:99], v[232:235], a[144:147], v[96:99]\n"
  "s_waitcnt lgkmcnt(2)\n"
  "ds_read_b128 v[252:255], v2 offset:30720\n"
  "v_mfma_f32_16x16x32_bf16 v[96:99], v[236:239], a[152:155], v[96:99]\n"
  "v_mfma_f32_16x16x32_bf16 v[100:103], v[224:227], a[132:135], v[100:103]\n"
  "ds_read_b128 v[224:227], v2 offset:4096\n"
  "v_mfma_f32_16x16x32_bf16 v[100:103], v[228:231], a[140:143], v[100:103]\n"
  "buffer_load_dwordx4 a[200:203], v1, s[36:39], s62 offen offset:1024 sc1\n"
  "v_mfma_f32_16x16x32_bf16 v[100:103], v[232:235], a[148:151], v[100:103]\n"
  "ds_read_b128 v[228:231], v2 offset:13312\n"
  "v_mfma_f32_16x16x32_bf16 v[100:103], v[236:239], a[156:159], v[100:103]\n"
  "v_mfma_f32_16x16x32_bf16 v[104:107], v[240:243], a[128:131], v[104:107]\n"
  "s_waitcnt lgkmcnt(3)\n"
  "ds_read_b128 v[232:235], v2 offset:22528\n"
  "v_mfma_f32_16x16x32_bf16 v[104:107], v[244:247], a[136:139], v[104:107]\n"
  "buffer_load_dwordx4 v1, s[36:39], s66 offen offset:1024 sc1 lds\n"
  "v_mfma_f32_16x16x32_bf16 v[104:107], v[248:251], a[144:147], v[104:107]\n"
  "s_waitcnt lgkmcnt(3)\n"
  "ds_read_b128 v[236:239], v2 offset:31744\n"
  "v_mfma_f32_16x16x32_bf16 v[104:107], v[252:255], a[152:155], v[104:107]\n"
  "v_mfma_f32_16x16x32_bf16 v[108:111], v[240:243], a[132:135], v[108:111]\n"
  "ds_read_b128 v[240:243], v2 offset:5120\n"
  "v_mfma_f32_16x16x32_bf16 v[108:111], v[244:247], a[140:143], v[108:111]\n"
  "buffer_load_dwordx4 a[204:207], v1, s[36:39], s63 offen offset:1024 sc1\n"
  "v_mfma_f32_16x16x32_bf16 v[108:111], v[248:251], a[148:151], v[108:111]\n"
  "s_add_u32 m0, s67, 77824\n"
  "ds_read_b128 v[244:247], v2 offset:14336\n"
  "v_mfma_f32_16x16x32_bf16 v[108:111], v[252:255], a[156:159], v[108:111]\n"
  "s_add_u32 s66, s72, 221184\n"
  "s_waitcnt lgkmcnt(3)\n"
  "v_mfma_f32_16x16x32_bf16 v[112:115], v[224:227], a[128:131], v[112:115]\n"
  "ds_read_b128 v[248:251], v2 offset:23552\n"
  "v_mfma_f32_16x16x32_bf16 v[112:115], v[228:231], a[136:139], v[112:115]\n"
  "buffer_load_dwordx4 v1, s[36:39], s66 offen offset:0 sc1 lds\n"
  "v_mfma_f32_16x16x32_bf16 v[112:115], v[232:235], a[144:147], v[112:115]\n"
  "s_waitcnt lgkmcnt(2)\n"
  "ds_read_b128 v[252:255], v2 offset:32768\n"
  "v_mfma_f32_16x16x32_bf16 v[112:115], v[236:239], a[152:155], v[112:115]\n"
  "v_mfma_f32_16x16x32_bf16 v[116:119], v[224:227], a[132:135], v[116:119]\n"
  "ds_read_b128 v[224:227], v2 offset:6144\n"
  "v_mfma_f32_16x16x32_bf16 v[116:119], v[228:231], a[140:143], v[116:119]\n"
  "v_mfma_f32_16x16x32_bf16 v[116:119], v[232:235], a[148:151], v[116:119]\n"
  "ds_read_b128 v[228:231], v2 offset:15360\n"
  "v_mfma_f32_16x16x32_bf16 v[116:119], v[236:239], a[156:159], v[116:119]\n"
  "buffer_load_dwordx4 a[208:211], v1, s[36:39], s62 offen offset:2048 sc1\n"
  "v_mfma_f32_16x16x32_bf16 v[120:123], v[240:243], a[128:131], v[120:123]\n"
  "s_waitcnt lgkmcnt(3)\n"
  "ds_read_b128 v[232:235], v2 offset:24576\n"
  "v_mfma_f32_16x16x32_bf16 v[120:123], v[244:247], a[136:139], v[120:123]\n"
  "v_mfma_f32_16x16x32_bf16 v[120:123], v[248:251], a[144:147], v[120:123]\n"
  "s_waitcnt lgkmcnt(3)\n"
  "ds_read_b128 v[236:239], v2 offset:33792\n"
  "v_mfma_f32_16x16x32_bf16 v[120:123], v[252:255], a[152:155], v[120:123]\n"
  "buffer_load_dwordx4 v1, s[36:39], s66 offen offset:1024 sc1 lds\n"
  "v_mfma_f32_16x16x32_bf16 v[124:127], v[240:243], a[132:135], v[124:127]\n"
  "ds_read_b128 v[240:243], v2 offset:7168\n"
  "v_mfma_f32_16x16x32_bf16 v[124:127], v[244:247], a[140:143], v[124:127]\n"
  "v_mfma_f32_16x16x32_bf16 v[124:127], v[248:251], a[148:151], v[124:127]\n"
  "ds_read_b128 v[244:247], v2 offset:16384\n"
  "v_mfma_f32_16x16x32_bf16 v[124:127], v[252:255], a[156:159], v[124:127]\n"
  "s_waitcnt lgkmcnt(3)\n"
  "buffer_load_dwordx4 a[212:215], v1, s[36:39], s63 offen offset:2048 sc1\n"
  "v_mfma_f32_16x16x32_bf16 v[128:131], v[224:227], a[128:131], v[128:131]\n"
  "s_add_u32 m0, s67, 79872\n"
  "ds_read_b128 v[248:251], v2 offset:25600\n"
  "v_mfma_f32_16x16x32_bf16 v[128:131], v[228:231], a[136:139], v[128:131]\n"
  "s_add_u32 s66, s35, 221184\n"
  "v_mfma_f32_16x16x32_bf16 v[128:131], v[232:235], a[144:147], v[128:131]\n"
  "s_waitcnt lgkmcnt(2)\n"
  "ds_read_b128 v[252:255], v2 offset:34816\n"
  "v_mfma_f32_16x16x32_bf16 v[128:131], v[236:239], a[152:155], v[128:131]\n"
  "buffer_load_dwordx4 v1, s[36:39], s66 offen offset:0 sc1 lds\n"
  "v_mfma_f32_16x16x32_bf16 v[132:135], v[224:227], a[132:135], v[132:135]\n"
  "ds_read_b128 v[224:227], v2 offset:8192\n"
  "v_mfma_f32_16x16x32_bf16 v[132:135], v[228:231], a[140:143], v[132:135]\n"
  "v_mfma_f32_16x16x32_bf16 v[132:135], v[232:235], a[148:151], v[132:135]\n"
  "ds_read_b128 v[228:231], v2 offset:17408\n"
  "v_mfma_f32_16x16x32_bf16 v[132:135], v[236:239], a[156:159], v[132:135]\n"
  "buffer_load_dwordx4 a[216:219], v1, s[36:39], s62 offen offset:3072 sc1\n"
  "v_mfma_f32_16x16x32_bf16 v[136:139], v[240:243], a[128:131], v[136:139]\n"
  "s_waitcnt lgkmcnt(3)\n"
  "ds_read_b128 v[232:235], v2 offset:26624\n"
  "v_mfma_f32_16x16x32_bf16 v[136:139], v[244:247], a[136:139], v[136:139]\n"
  "v_mfma_f32_16x16x32_bf16 v[136:139], v[248:251], a[144:147], v[136:139]\n"
  "s_waitcnt lgkmcnt(3)\n"
  "ds_read_b128 v[236:239], v2 offset:35840\n"
  "v_mfma_f32_16x16x32_bf16 v[136:139], v[252:255], a[152:155], v[136:139]\n"
  "buffer_load_dwordx4 v1, s[36:39], s66 offen offset:1024 sc1 lds\n"
  "v_mfma_f32_16x16x32_bf16 v[140:143], v[240:243], a[132:135], v[140:143]\n"
  "s_waitcnt vmcnt(16)\n"
  "s_barrier\n"
  "ds_read_b128 v[240:243], v2 offset:36864\n"
  "v_mfma_f32_16x16x32_bf16 v[140:143], v[244:247], a[140:143], v[140:143]\n"
  "v_mfma_f32_16x16x32_bf16 v[140:143], v[248:251], a[148:151], v[140:143]\n"
  "ds_read_b128 v[244:247], v2 offset:46080\n"
  "v_mfma_f32_16x16x32_bf16 v[140:143], v[252:255], a[156:159], v[140:143]\n"
  "s_waitcnt lgkmcnt(3)\n"
  "buffer_load_dwordx4 a[220:223], v1, s[36:39], s63 offen offset:3072 sc1\n"
  "v_mfma_f32_16x16x32_bf16 v[144:147], v[224:227], a[128:131], v[144:147]\n"
  "s_add_u32 m0, s67, 81920\n"
  "ds_read_b128 v[248:251], v2 offset:55296\n"
  "v_mfma_f32_16x16x32_bf16 v[144:147], v[228:231], a[136:139], v[144:147]\n"
  "s_add_u32 s66, s64, 229376\n"
  "v_mfma_f32_16x16x32_bf16 v[144:147], v[232:235], a[144:147], v[144:147]\n"
  "s_waitcnt lgkmcnt(2)\n"
  "ds_read_b128 v[252:255], v2 offset:64512\n"
  "v_mfma_f32_16x16x32_bf16 v[144:147], v[236:239], a[152:155], v[144:147]\n"
  "buffer_load_dwordx4 v1, s[36:39], s66 offen offset:0 sc1 lds\n"
  "v_mfma_f32_16x16x32_bf16 v[148:151], v[224:227], a[132:135], v[148:151]\n"
  "s_add_u32 m0, s67, 110592\n"
  "ds_read_b128 v[224:227], v2 offset:37888\n"
  "v_mfma_f32_16x16x32_bf16 v[148:151], v[228:231], a[140:143], v[148:151]\n"
  "s_add_u32 s66, s70, 258048\n"
  "v_mfma_f32_16x16x32_bf16 v[148:151], v[232:235], a[148:151], v[148:151]\n"
  "ds_read_b128 v[228:231], v2 offset:47104\n"
  "v_mfma_f32_16x16x32_bf16 v[148:151], v[236:239], a[156:159], v[148:151]\n"
  "v_mfma_f32_16x16x32_bf16 v[80:83], v[240:243], a[160:163], v[80:83]\n"
  "s_waitcnt lgkmcnt(2)\n"
  "ds_read_b128 v[232:235], v2 offset:56320\n"
  "v_add_u32 v2, 36864, v2\n"
  "v_mfma_f32_16x16x32_bf16 v[80:83], v[244:247], a[168:171], v[80:83]\n"
  "buffer_load_dwordx4 v1, s[36:39], s66 offen offset:0 sc1 lds\n"
  "v_mfma_f32_16x16x32_bf16 v[80:83], v[248:251], a[176:179], v[80:83]\n"
  "s_add_u32 s62, s60, 28672\n"
  "ds_read_b128 v[236:239], v2 offset:28672\n"
  "v_mfma_f32_16x16x32_bf16 v[80:83], v[252:255], a[184:187], v[80:83]\n"
  "v_mfma_f32_16x16x32_bf16 v[84:87], v[240:243], a[164:167], v[84:87]\n"
  "ds_read_b128 v[240:243], v2 offset:2048\n"
  "v_mfma_f32_16x16x32_bf16 v[84:87], v[244:247], a[172:175], v[84:87]\n"
  "buffer_load_dwordx4 a[224:227], v1, s[36:39], s62 offen offset:0 sc1\n"
  "v_mfma_f32_16x16x32_bf16 v[84:87], v[248:251], a[180:183], v[84:87]\n"
  "ds_read_b128 v[244:247], v2 offset:11264\n"
  "v_mfma_f32_16x16x32_bf16 v[84:87], v[252:255], a[188:191], v[84:87]\n"
  "s_waitcnt lgkmcnt(3)\n"
  "v_mfma_f32_16x16x32_bf16 v[88:91], v[224:227], a[160:163], v[88:91]\n"
  "ds_read_b128 v[248:251], v2 offset:20480\n"
  "v_mfma_f32_16x16x32_bf16 v[88:91], v[228:231], a[168:171], v[88:91]\n"
  "buffer_load_dwordx4 v1, s[36:39], s66 offen offset:1024 sc1 lds\n"
  "v_mfma_f32_16x16x32_bf16 v[88:91], v[232:235], a[176:179], v[88:91]\n"
  "s_add_u32 s63, s61, 28672\n"
  "ds_read_b128 v[252:255], v2 offset:29696\n"
  "v_bfe_u32 v24, v0, 2, 2\n"
  "s_waitcnt lgkmcnt(3)\n"
  "v_mfma_f32_16x16x32_bf16 v[88:91], v[236:239], a[184:187], v[88:91]\n"
  "v_lshrrev_b32 v25, 4, v0\n"
  "v_cmp_eq_u32 vcc, v24, v25\n"
  "v_mfma_f32_16x16x32_bf16 v[92:95], v[224:227], a[164:167], v[92:95]\n"
  "ds_read_b128 v[224:227], v2 offset:3072\n"
  "v_and_b32 v24, 1, v0\n"
  "v_mfma_f32_16x16x32_bf16 v[92:95], v[228:231], a[172:175], v[92:95]\n"
  "buffer_load_dwordx4 a[228:231], v1, s[36:39], s63 offen offset:0 sc1\n"
  "v_mfma_f32_16x16x32_bf16 v[92:95], v[232:235], a[180:183], v[92:95]\n"
  "s_add_u32 m0, s67, 112640\n"
  "ds_read_b128 v[228:231], v2 offset:12288\n"
  "v_lshlrev_b32 v24, 4, v24\n"
  "v_mfma_f32_16x16x32_bf16 v[92:95], v[236:239], a[188:191], v[92:95]\n"
  "s_add_u32 s66, s71, 258048\n"
  "v_mov_b32 v25, 0x3f80\n"
  "v_lshlrev_b32 v24, v24, v25\n"
  "v_mfma_f32_16x16x32_bf16 v[96:99], v[240:243], a[160:163], v[96:99]\n"
  "s_waitcnt lgkmcnt(1)\n"
  "ds_read_b128 v[232:235], v2 offset:21504\n"
  "v_cndmask_b32 v24, 0, v24, vcc\n"
  "v_mfma_f32_16x16x32_bf16 v[96:99], v[244:247], a[168:171], v[96:99]\n"
  "buffer_load_dwordx4 v1, s[36:39], s66 offen offset:0 sc1 lds\n"
  "v_mfma_f32_16x16x32_bf16 v[96:99], v[248:251], a[176:179], v[96:99]\n"
  "ds_read_b128 v[236:239], v2 offset:30720\n"
  "v_and_b32 v25, 2, v0\n"
  "v_mfma_f32_16x16x32_bf16 v[96:99], v[252:255], a[184:187], v[96:99]\n"
  "v_cmp_eq_u32 vcc, 0, v25\n"
  "s_nop 1\n"
  "v_cndmask_b32 v26, 0, v24, vcc\n"
  "v_mfma_f32_16x16x32_bf16 v[100:103], v[240:243], a[164:167], v[100:103]\n"
  "ds_read_b128 v[240:243], v2 offset:4096\n"
  "v_cndmask_b32 v27, v24, 0, vcc\n"
  "v_mfma_f32_16x16x32_bf16 v[100:103], v[244:247], a[172:175], v[100:103]\n"
  "buffer_load_dwordx4 a[232:235], v1, s[36:39], s62 offen offset:1024 sc1\n"
  "v_mfma_f32_16x16x32_bf16 v[100:103], v[248:251], a[180:183], v[100:103]\n"
  "ds_read_b128 v[244:247], v2 offset:13312\n"
  "v_mov_b32 v24, 0\n"
  "v_mfma_f32_16x16x32_bf16 v[100:103], v[252:255], a[188:191], v[100:103]\n"
  "v_mov_b32 v25, 0\n"
  "v_mov_b32 v28, 0\n"
  "v_mfma_f32_16x16x32_bf16 v[104:107], v[224:227], a[160:163], v[104:107]\n"
  "s_waitcnt lgkmcnt(2)\n"
  "ds_read_b128 v[248:251], v2 offset:22528\n"
  "v_mov_b32 v29, 0\n"
  "v_mfma_f32_16x16x32_bf16 v[104:107], v[228:231], a[168:171], v[104:107]\n"
  "buffer_load_dwordx4 v1, s[36:39], s66 offen offset:1024 sc1 lds\n"
  "v_mfma_f32_16x16x32_bf16 v[104:107], v[232:235], a[176:179], v[104:107]\n"
  "ds_read_b128 v[252:255], v2 offset:31744\n"
  "v_mfma_f32_16x16x32_bf16 v[104:107], v[236:239], a[184:187], v[104:107]\n"
  "v_mfma_f32_16x16x32_bf16 v[108:111], v[224:227], a[164:167], v[108:111]\n"
  "ds_read_b128 v[224:227], v2 offset:5120\n"
  "v_mfma_f32_16x16x32_bf16 v[108:111], v[228:231], a[172:175], v[108:111]\n"
  "buffer_load_dwordx4 a[236:239], v1, s[36:39], s63 offen offset:1024 sc1\n"
  "v_mfma_f32_16x16x32_bf16 v[108:111], v[232:235], a[180:183], v[108:111]\n"
  "s_add_u32 m0, s67, 114688\n"
  "ds_read_b128 v[228:231], v2 offset:14336\n"
  "v_mfma_f32_16x16x32_bf16 v[108:111], v[236:239], a[188:191], v[108:111]\n"
  "s_add_u32 s66, s72, 258048\n"
  "s_waitcnt lgkmcnt(3)\n"
  "v_mfma_f32_16x16x32_bf16 v[112:115], v[240:243], a[160:163], v[112:115]\n"
  "ds_read_b128 v[232:235], v2 offset:23552\n"
  "v_mfma_f32_16x16x32_bf16 v[112:115], v[244:247], a[168:171], v[112:115]\n"
  "buffer_load_dwordx4 v1, s[36:39], s66 offen offset:0 sc1 lds\n"
  "v_mfma_f32_16x16x32_bf16 v[112:115], v[248:251], a[176:179], v[112:115]\n"
  "s_waitcnt lgkmcnt(2)\n"
  "ds_read_b128 v[236:239], v2 offset:32768\n"
  "v_mfma_f32_16x16x32_bf16 v[112:115], v[252:255], a[184:187], v[112:115]\n"
  "v_mfma_f32_16x16x32_bf16 v[116:119], v[240:243], a[164:167], v[116:119]\n"
  "ds_read_b128 v[240:243], v2 offset:6144\n"
  "v_mfma_f32_16x16x32_bf16 v[116:119], v[244:247], a[172:175], v[116:119]\n"
  "v_mfma_f32_16x16x32_bf16 v[116:119], v[248:251], a[180:183], v[116:119]\n"
  "ds_read_b128 v[244:247], v2 offset:15360\n"
  "v_mfma_f32_16x16x32_bf16 v[116:119], v[252:255], a[188:191], v[116:119]\n"
  "buffer_load_dwordx4 a[240:243], v1, s[36:39], s62 offen offset:2048 sc1\n"
  "v_mfma_f32_16x16x32_bf16 v[120:123], v[224:227], a[160:163], v[120:123]\n"
  "s_waitcnt lgkmcnt(3)\n"
  "ds_read_b128 v[248:251], v2 offset:24576\n"
  "v_mfma_f32_16x16x32_bf16 v[120:123], v[228:231], a[168:171], v[120:123]\n"
  "v_mfma_f32_16x16x32_bf16 v[120:123], v[232:235], a[176:179], v[120:123]\n"
  "s_waitcnt lgkmcnt(3)\n"
  "ds_read_b128 v[252:255], v2 offset:33792\n"
  "v_mfma_f32_16x16x32_bf16 v[120:123], v[236:239], a[184:187], v[120:123]\n"
  "buffer_load_dwordx4 v1, s[36:39], s66 offen offset:1024 sc1 lds\n"
  "v_mfma_f32_16x16x32_bf16 v[124:127], v[224:227], a[164:167], v[124:127]\n"
  "ds_read_b128 v[224:227], v2 offset:7168\n"
  "v_mfma_f32_16x16x32_bf16 v[124:127], v[228:231], a[172:175], v[124:127]\n"
  "buffer_load_dword v12, v13, s[36:39], s65 offen offset:32 sc1\n"
  "v_mfma_f32_16x16x32_bf16 v[124:127], v[232:235], a[180:183], v[124:127]\n"
  "ds_read_b128 v[228:231], v2 offset:16384\n"
  "v_mfma_f32_16x16x32_bf16 v[124:127], v[236:239], a[188:191], v[124:127]\n"
  "s_waitcnt lgkmcnt(3)\n"
  "buffer_load_dwordx4 a[244:247], v1, s[36:39], s63 offen offset:2048 sc1\n"
  "v_mfma_f32_16x16x32_bf16 v[128:131], v[240:243], a[160:163], v[128:131]\n"
  "s_add_u32 m0, s67, 116736\n"
  "ds_read_b128 v[232:235], v2 offset:25600\n"
  "v_mfma_f32_16x16x32_bf16 v[128:131], v[244:247], a[168:171], v[128:131]\n"
  "s_add_u32 s66, s35, 258048\n"
  "v_mfma_f32_16x16x32_bf16 v[128:131], v[248:251], a[176:179], v[128:131]\n"
  "s_waitcnt lgkmcnt(2)\n"
  "ds_read_b128 v[236:239], v2 offset:34816\n"
  "v_mfma_f32_16x16x32_bf16 v[128:131], v[252:255], a[184:187], v[128:131]\n"
  "buffer_load_dwordx4 v1, s[36:39], s66 offen offset:0 sc1 lds\n"
  "v_mfma_f32_16x16x32_bf16 v[132:135], v[240:243], a[164:167], v[132:135]\n"
  "ds_read_b128 v[240:243], v2 offset:8192\n"
  "v_mfma_f32_16x16x32_bf16 v[132:135], v[244:247], a[172:175], v[132:135]\n"
  "v_mfma_f32_16x16x32_bf16 v[132:135], v[248:251], a[180:183], v[132:135]\n"
  "ds_read_b128 v[244:247], v2 offset:17408\n"
  "v_mfma_f32_16x16x32_bf16 v[132:135], v[252:255], a[188:191], v[132:135]\n"
  "buffer_load_dwordx4 a[248:251], v1, s[36:39], s62 offen offset:3072 sc1\n"
  "v_mfma_f32_16x16x32_bf16 v[136:139], v[224:227], a[160:163], v[136:139]\n"
  "s_waitcnt lgkmcnt(3)\n"
  "ds_read_b128 v[248:251], v2 offset:26624\n"
  "v_mfma_f32_16x16x32_bf16 v[136:139], v[228:231], a[168:171], v[136:139]\n"
  "v_mfma_f32_16x16x32_bf16 v[136:139], v[232:235], a[176:179], v[136:139]\n"
  "s_waitcnt lgkmcnt(3)\n"
  "ds_read_b128 v[252:255], v2 offset:35840\n"
  "v_mfma_f32_16x16x32_bf16 v[136:139], v[236:239], a[184:187], v[136:139]\n"
  "buffer_load_dwordx4 v1, s[36:39], s66 offen offset:1024 sc1 lds\n"
  "v_mfma_f32_16x16x32_bf16 v[140:143], v[224:227], a[164:167], v[140:143]\n"
  "s_waitcnt vmcnt(16)\n"
  "s_barrier\n"
  "ds_read_b128 v[224:227], v2 offset:36864\n"
  "v_mfma_f32_16x16x32_bf16 v[140:143], v[228:231], a[172:175], v[140:143]\n"
  "v_mfma_f32_16x16x32_bf16 v[140:143], v[232:235], a[180:183], v[140:143]\n"
  "ds_read_b128 v[228:231], v2 offset:46080\n"
  "v_mfma_f32_16x16x32_bf16 v[140:143], v[236:239], a[188:191], v[140:143]\n"
  "s_waitcnt lgkmcnt(3)\n"
  "buffer_load_dwordx4 a[252:255], v1, s[36:39], s63 offen offset:3072 sc1\n"
  "v_mfma_f32_16x16x32_bf16 v[144:147], v[240:243], a[160:163], v[144:147]\n"
  "s_add_u32 m0, s67, 118784\n"
  "ds_read_b128 v[232:235], v2 offset:55296\n"
  "v_mfma_f32_16x16x32_bf16 v[144:147], v[244:247], a[168:171], v[144:147]\n"
  "s_add_u32 s66, s64, 266240\n"
  "v_mfma_f32_16x16x32_bf16 v[144:147], v[248:251], a[176:179], v[144:147]\n"
  "s_waitcnt lgkmcnt(2)\n"
  "ds_read_b128 v[236:239], v2 offset:64512\n"
  "v_mfma_f32_16x16x32_bf16 v[144:147], v[252:255], a[184:187], v[144:147]\n"
  "buffer_load_dwordx4 v1, s[36:39], s66 offen offset:0 sc1 lds\n"
  "v_mfma_f32_16x16x32_bf16 v[148:151], v[240:243], a[164:167], v[148:151]\n"
  "s_add_u32 m0, s67, 0\n"
  "ds_read_b128 v[240:243], v2 offset:37888\n"
  "v_mfma_f32_16x16x32_bf16 v[148:151], v[244:247], a[172:175], v[148:151]\n"
  "s_add_u32 s66, s70, 294912\n"
  "v_mfma_f32_16x16x32_bf16 v[148:151], v[248:251], a[180:183], v[148:151]\n"
  "ds_read_b128 v[244:247], v2 offset:47104\n"
  "v_mfma_f32_16x16x32_bf16 v[148:151], v[252:255], a[188:191], v[148:151]\n"
  "v_mfma_f32_16x16x32_bf16 v[80:83], v[224:227], a[192:195], v[80:83]\n"
  "s_waitcnt lgkmcnt(2)\n"
  "ds_read_b128 v[248:251], v2 offset:56320\n"
  "v_add_u32 v2, 36864, v2\n"
  "v_mfma_f32_16x16x32_bf16 v[80:83], v[228:231], a[200:203], v[80:83]\n"
  "buffer_load_dwordx4 v1, s[36:39], s66 offen offset:0 sc1 lds\n"
  "v_mfma_f32_16x16x32_bf16 v[80:83], v[232:235], a[208:211], v[80:83]\n"
  "s_add_u32 s62, s60, 32768\n"
  "ds_read_b128 v[252:255], v2 offset:28672\n"
  "v_mfma_f32_16x16x32_bf16 v[80:83], v[236:239], a[216:219], v[80:83]\n"
  "v_mfma_f32_16x16x32_bf16 v[84:87], v[224:227], a[196:199], v[84:87]\n"
  "ds_read_b128 v[224:227], v2 offset:2048\n"
  "v_mfma_f32_16x16x32_bf16 v[84:87], v[228:231], a[204:207], v[84:87]\n"
  "buffer_load_dwordx4 a[128:131], v1, s[36:39], s62 offen offset:0 sc1\n"
  "v_mfma_f32_16x16x32_bf16 v[84:87], v[232:235], a[212:215], v[84:87]\n"
  "ds_read_b128 v[228:231], v2 offset:11264\n"
  "v_mfma_f32_16x16x32_bf16 v[84:87], v[236:239], a[220:223], v[84:87]\n"
  "s_waitcnt lgkmcnt(3)\n"
  "v_mfma_f32_16x16x32_bf16 v[88:91], v[240:243], a[192:195], v[88:91]\n"
  "ds_read_b128 v[232:235], v2 offset:20480\n"
  "v_mfma_f32_16x16x32_bf16 v[88:91], v[244:247], a[200:203], v[88:91]\n"
  "buffer_load_dwordx4 v1, s[36:39], s66 offen offset:1024 sc1 lds\n"
  "v_mfma_f32_16x16x32_bf16 v[88:91], v[248:251], a[208:211], v[88:91]\n"
  "s_add_u32 s63, s61, 32768\n"
  "ds_read_b128 v[236:239], v2 offset:29696\n"
  "s_waitcnt lgkmcnt(3)\n"
  "v_mfma_f32_16x16x32_bf16 v[88:91], v[252:255], a[216:219], v[88:91]\n"
  "v_mfma_f32_16x16x32_bf16 v[92:95], v[240:243], a[196:199], v[92:95]\n"
  "ds_read_b128 v[240:243], v2 offset:3072\n"
  "v_mfma_f32_16x16x32_bf16 v[92:95], v[244:247], a[204:207], v[92:95]\n"
  "buffer_load_dwordx4 a[132:135], v1, s[36:39], s63 offen offset:0 sc1\n"
  "v_mfma_f32_16x16x32_bf16 v[92:95], v[248:251], a[212:215], v[92:95]\n"
  "s_add_u32 m0, s67, 2048\n"
  "ds_read_b128 v[244:247], v2 offset:12288\n"
  "v_mfma_f32_16x16x32_bf16 v[92:95], v[252:255], a[220:223], v[92:95]\n"
  "s_add_u32 s66, s71, 294912\n"
  "v_mfma_f32_16x16x32_bf16 v[96:99], v[224:227], a[192:195], v[96:99]\n"
  "s_waitcnt lgkmcnt(2)\n"
  "ds_read_b128 v[248:251], v2 offset:21504\n"
  "v_mfma_f32_16x16x32_bf16 v[96:99], v[228:231], a[200:203], v[96:99]\n"
  "buffer_load_dwordx4 v1, s[36:39], s66 offen offset:0 sc1 lds\n"
  "v_mfma_f32_16x16x32_bf16 v[96:99], v[232:235], a[208:211], v[96:99]\n"
  "ds_read_b128 v[252:255], v2 offset:30720\n"
  "v_mfma_f32_16x16x32_bf16 v[96:99], v[236:239], a[216:219], v[96:99]\n"
  "v_mfma_f32_16x16x32_bf16 v[100:103], v[224:227], a[196:199], v[100:103]\n"
  "ds_read_b128 v[224:227], v2 offset:4096\n"
  "v_mfma_f32_16x16x32_bf16 v[100:103], v[228:231], a[204:207], v[100:103]\n"
  "buffer_load_dwordx4 a[136:139], v1, s[36:39], s62 offen offset:1024 sc1\n"
  "v_mfma_f32_16x16x32_bf16 v[100:103], v[232:235], a[212:215], v[100:103]\n"
  "ds_read_b128 v[228:231], v2 offset:13312\n"
  "v_mfma_f32_16x16x32_bf16 v[100:103], v[236:239], a[220:223], v[100:103]\n"
  "s_waitcnt lgkmcnt(3)\n"
  "v_mfma_f32_16x16x32_bf16 v[104:107], v[240:243], a[192:195], v[104:107]\n"
  "ds_read_b128 v[232:235], v2 offset:22528\n"
  "v_mfma_f32_16x16x32_bf16 v[104:107], v[244:247], a[200:203], v[104:107]\n"
  "buffer_load_dwordx4 v1, s[36:39], s66 offen offset:1024 sc1 lds\n"
  "v_mfma_f32_16x16x32_bf16 v[104:107], v[248:251], a[208:211], v[104:107]\n"
  "s_waitcnt lgkmcnt(3)\n"
  "ds_read_b128 v[236:239], v2 offset:31744\n"
  "v_mfma_f32_16x16x32_bf16 v[104:107], v[252:255], a[216:219], v[104:107]\n"
  "v_mfma_f32_16x16x32_bf16 v[108:111], v[240:243], a[196:199], v[108:111]\n"
  "ds_read_b128 v[240:243], v2 offset:5120\n"
  "v_mfma_f32_16x16x32_bf16 v[108:111], v[244:247], a[204:207], v[108:111]\n"
  "buffer_load_dwordx4 a[140:143], v1, s[36:39], s63 offen offset:1024 sc1\n"
  "v_mfma_f32_16x16x32_bf16 v[108:111], v[248:251], a[212:215], v[108:111]\n"
  "s_add_u32 m0, s67, 4096\n"
  "ds_read_b128 v[244:247], v2 offset:14336\n"
  "v_mfma_f32_16x16x32_bf16 v[108:111], v[252:255], a[220:223], v[108:111]\n"
  "s_add_u32 s66, s72, 294912\n"
  "s_waitcnt lgkmcnt(3)\n"
  "v_mfma_f32_16x16x32_bf16 v[112:115], v[224:227], a[192:195], v[112:115]\n"
  "ds_read_b128 v[248:251], v2 offset:23552\n"
  "v_mfma_f32_16x16x32_bf16 v[112:115], v[228:231], a[200:203], v[112:115]\n"
  "buffer_load_dwordx4 v1, s[36:39], s66 offen offset:0 sc1 lds\n"
  "v_mfma_f32_16x16x32_bf16 v[112:115], v[232:235], a[208:211], v[112:115]\n"
  "s_waitcnt lgkmcnt(2)\n"
  "ds_read_b128 v[252:255], v2 offset:32768\n"
  "v_mfma_f32_16x16x32_bf16 v[112:115], v[236:239], a[216:219], v[112:115]\n"
  "v_mfma_f32_16x16x32_bf16 v[116:119], v[224:227], a[196:199], v[116:119]\n"
  "ds_read_b128 v[224:227], v2 offset:6144\n"
  "v_mfma_f32_16x16x32_bf16 v[116:119], v[228:231], a[204:207], v[116:119]\n"
  "v_mfma_f32_16x16x32_bf16 v[116:119], v[232:235], a[212:215], v[116:119]\n"
  "ds_read_b128 v[228:231], v2 offset:15360\n"
  "v_mfma_f32_16x16x32_bf16 v[116:119], v[236:239], a[220:223], v[116:119]\n"
  "buffer_load_dwordx4 a[144:147], v1, s[36:39], s62 offen offset:2048 sc1\n"
  "v_mfma_f32_16x16x32_bf16 v[120:123], v[240:243], a[192:195], v[120:123]\n"
  "s_waitcnt lgkmcnt(3)\n"
  "ds_read_b128 v[232:235], v2 offset:24576\n"
  "v_mfma_f32_16x16x32_bf16 v[120:123], v[244:247], a[200:203], v[120:123]\n"
  "v_mfma_f32_16x16x32_bf16 v[120:123], v[248:251], a[208:211], v[120:123]\n"
  "s_waitcnt lgkmcnt(3)\n"
  "ds_read_b128 v[236:239], v2 offset:33792\n"
  "v_mfma_f32_16x16x32_bf16 v[120:123], v[252:255], a[216:219], v[120:123]\n"
  "buffer_load_dwordx4 v1, s[36:39], s66 offen offset:1024 sc1 lds\n"
  "v_mfma_f32_16x16x32_bf16 v[124:127], v[240:243], a[196:199], v[124:127]\n"
  "ds_read_b128 v[240:243], v2 offset:7168\n"
  "v_mfma_f32_16x16x32_bf16 v[124:127], v[244:247], a[204:207], v[124:127]\n"
  "v_mfma_f32_16x16x32_bf16 v[124:127], v[248:251], a[212:215], v[124:127]\n"
  "ds_read_b128 v[244:247], v2 offset:16384\n"
  "v_mfma_f32_16x16x32_bf16 v[124:127], v[252:255], a[220:223], v[124:127]\n"
  "s_waitcnt lgkmcnt(3)\n"
  "buffer_load_dwordx4 a[148:151], v1, s[36:39], s63 offen offset:2048 sc1\n"
  "v_mfma_f32_16x16x32_bf16 v[128:131], v[224:227], a[192:195], v[128:131]\n"
  "s_add_u32 m0, s67, 6144\n"
  "ds_read_b128 v[248:251], v2 offset:25600\n"
  "v_mfma_f32_16x16x32_bf16 v[128:131], v[228:231], a[200:203], v[128:131]\n"
  "s_add_u32 s66, s35, 294912\n"
  "v_mfma_f32_16x16x32_bf16 v[128:131], v[232:235], a[208:211], v[128:131]\n"
  "s_waitcnt lgkmcnt(2)\n"
  "ds_read_b128 v[252:255], v2 offset:34816\n"
  "v_mfma_f32_16x16x32_bf16 v[128:131], v[236:239], a[216:219], v[128:131]\n"
  "buffer_load_dwordx4 v1, s[36:39], s66 offen offset:0 sc1 lds\n"
  "v_mfma_f32_16x16x32_bf16 v[132:135], v[224:227], a[196:199], v[132:135]\n"
  "ds_read_b128 v[224:227], v2 offset:8192\n"
  "v_mfma_f32_16x16x32_bf16 v[132:135], v[228:231], a[204:207], v[132:135]\n"
  "v_mfma_f32_16x16x32_bf16 v[132:135], v[232:235], a[212:215], v[132:135]\n"
  "ds_read_b128 v[228:231], v2 offset:17408\n"
  "v_mfma_f32_16x16x32_bf16 v[132:135], v[236:239], a[220:223], v[132:135]\n"
  "buffer_load_dwordx4 a[152:155], v1, s[36:39], s62 offen offset:3072 sc1\n"
  "v_mfma_f32_16x16x32_bf16 v[136:139], v[240:243], a[192:195], v[136:139]\n"
  "s_waitcnt lgkmcnt(3)\n"
  "ds_read_b128 v[232:235], v2 offset:26624\n"
  "v_mfma_f32_16x16x32_bf16 v[136:139], v[244:247], a[200:203], v[136:139]\n"
  "v_mfma_f32_16x16x32_bf16 v[136:139], v[248:251], a[208:211], v[136:139]\n"
  "s_waitcnt lgkmcnt(3)\n"
  "ds_read_b128 v[236:239], v2 offset:35840\n"
  "v_mfma_f32_16x16x32_bf16 v[136:139], v[252:255], a[216:219], v[136:139]\n"
  "buffer_load_dwordx4 v1, s[36:39], s66 offen offset:1024 sc1 lds\n"
  "v_mfma_f32_16x16x32_bf16 v[140:143], v[240:243], a[196:199], v[140:143]\n"
  "s_waitcnt vmcnt(15)\n"
  "s_barrier\n"
  "ds_read_b128 v[240:243], v2 offset:36864\n"
  "v_mfma_f32_16x16x32_bf16 v[140:143], v[244:247], a[204:207], v[140:143]\n"
  "v_mfma_f32_16x16x32_bf16 v[140:143], v[248:251], a[212:215], v[140:143]\n"
  "ds_read_b128 v[244:247], v2 offset:46080\n"
  "v_mfma_f32_16x16x32_bf16 v[140:143], v[252:255], a[220:223], v[140:143]\n"
  "s_waitcnt lgkmcnt(3)\n"
  "buffer_load_dwordx4 a[156:159], v1, s[36:39], s63 offen offset:3072 sc1\n"
  "v_mfma_f32_16x16x32_bf16 v[144:147], v[224:227], a[192:195], v[144:147]\n"
  "s_add_u32 m0, s67, 8192\n"
  "ds_read_b128 v[248:251], v2 offset:55296\n"
  "v_mfma_f32_16x16x32_bf16 v[144:147], v[228:231], a[200:203], v[144:147]\n"
  "s_add_u32 s66, s64, 303104\n"
  "v_mfma_f32_16x16x32_bf16 v[144:147], v[232:235], a[208:211], v[144:147]\n"
  "s_waitcnt lgkmcnt(2)\n"
  "ds_read_b128 v[252:255], v2 offset:64512\n"
  "v_mfma_f32_16x16x32_bf16 v[144:147], v[236:239], a[216:219], v[144:147]\n"
  "buffer_load_dwordx4 v1, s[36:39], s66 offen offset:0 sc1 lds\n"
  "v_mfma_f32_16x16x32_bf16 v[148:151], v[224:227], a[196:199], v[148:151]\n"
  "ds_read_b128 v[224:227], v2 offset:37888\n"
  "v_cmp_ne_u32 vcc, s24, v12\n"
  "v_mfma_f32_16x16x32_bf16 v[148:151], v[228:231], a[204:207], v[148:151]\n"
  "v_mfma_f32_16x16x32_bf16 v[148:151], v[232:235], a[212:215], v[148:151]\n"
  "ds_read_b128 v[228:231], v2 offset:47104\n"
  "v_mfma_f32_16x16x32_bf16 v[148:151], v[236:239], a[220:223], v[148:151]\n"
  "v_mfma_f32_16x16x32_bf16 v[80:83], v[240:243], a[224:227], v[80:83]\n"
  "s_waitcnt lgkmcnt(2)\n"
  "ds_read_b128 v[232:235], v2 offset:56320\n"
  "v_add_u32 v2, 36864, v2\n"
  "v_mfma_f32_16x16x32_bf16 v[80:83], v[244:247], a[232:235], v[80:83]\n"
  "s_nop 1\n s_cbranch_vccnz Ls2_%=\n Lr2_%=:\n"
  "v_mfma_f32_16x16x32_bf16 v[80:83], v[248:251], a[240:243], v[80:83]\n"
  "s_add_u32 s70, s70, s84\n"
  "ds_read_b128 v[236:239], v2 offset:28672\n"
  "v_mfma_f32_16x16x32_bf16 v[80:83], v[252:255], a[248:251], v[80:83]\n"
  "s_add_u32 s71, s71, s84\n"
  "v_mfma_f32_16x16x32_bf16 v[84:87], v[240:243], a[228:231], v[84:87]\n"
  "s_add_u32 s72, s72, s84\n"
  "ds_read_b128 v[240:243], v2 offset:2048\n"
  "v_mfma_f32_16x16x32_bf16 v[84:87], v[244:247], a[236:239], v[84:87]\n"
  "s_add_u32 s35, s35, s84\n"
  "v_mfma_f32_16x16x32_bf16 v[84:87], v[248:251], a[244:247], v[84:87]\n"
  "s_add_u32 s64, s64, s84\n"
  "ds_read_b128 v[244:247], v2 offset:11264\n"
  "v_mfma_f32_16x16x32_bf16 v[84:87], v[252:255], a[252:255], v[84:87]\n"
  "s_add_u32 m0, s67, 36864\n"
  "s_waitcnt lgkmcnt(3)\n"
  "v_mfma_f32_16x16x32_bf16 v[88:91], v[224:227], a[224:227], v[88:91]\n"
  "s_add_u32 s66, s70, 0\n"
  "ds_read_b128 v[248:251], v2 offset:20480\n"
  "v_mfma_f32_16x16x32_bf16 v[88:91], v[228:231], a[232:235], v[88:91]\n"
  "buffer_load_dwordx4 v1, s[36:39], s66 offen offset:0 sc1 lds\n"
  "v_mfma_f32_16x16x32_bf16 v[88:91], v[232:235], a[240:243], v[88:91]\n"
  "s_add_u32 s62, s85, 0\n"
  "ds_read_b128 v[252:255], v2 offset:29696\n"
  "s_waitcnt lgkmcnt(3)\n"
  "v_mfma_f32_16x16x32_bf16 v[88:91], v[236:239], a[248:251], v[88:91]\n"
  "buffer_load_dwordx4 a[160:163], v1, s[36:39], s62 offen offset:0 sc1\n"
  "v_mfma_f32_16x16x32_bf16 v[92:95], v[224:227], a[228:231], v[92:95]\n"
  "ds_read_b128 v[224:227], v2 offset:3072\n"
  "v_mfma_f32_16x16x32_bf16 v[92:95], v[228:231], a[236:239], v[92:95]\n"
  "buffer_load_dwordx4 v1, s[36:39], s66 offen offset:1024 sc1 lds\n"
  "v_mfma_f32_16x16x32_bf16 v[92:95], v[232:235], a[244:247], v[92:95]\n"
  "s_add_u32 s63, s86, 0\n"
  "ds_read_b128 v[228:231], v2 offset:12288\n"
  "v_mfma_f32_16x16x32_bf16 v[92:95], v[236:239], a[252:255], v[92:95]\n"
  "v_mfma_f32_16x16x32_bf16 v[96:99], v[240:243], a[224:227], v[96:99]\n"
  "s_waitcnt lgkmcnt(2)\n"
  "ds_read_b128 v[232:235], v2 offset:21504\n"
  "v_mfma_f32_16x16x32_bf16 v[96:99], v[244:247], a[232:235], v[96:99]\n"
  "buffer_load_dwordx4 a[164:167], v1, s[36:39], s63 offen offset:0 sc1\n"
  "v_mfma_f32_16x16x32_bf16 v[96:99], v[248:251], a[240:243], v[96:99]\n"
  "s_add_u32 m0, s67, 38912\n"
  "ds_read_b128 v[236:239], v2 offset:30720\n"
  "v_mfma_f32_16x16x32_bf16 v[96:99], v[252:255], a[248:251], v[96:99]\n"
  "s_add_u32 s66, s71, 0\n"
  "v_mfma_f32_16x16x32_bf16 v[100:103], v[240:243], a[228:231], v[100:103]\n"
  "ds_read_b128 v[240:243], v2 offset:4096\n"
  "v_mfma_f32_16x16x32_bf16 v[100:103], v[244:247], a[236:239], v[100:103]\n"
  "buffer_load_dwordx4 v1, s[36:39], s66 offen offset:0 sc1 lds\n"
  "v_mfma_f32_16x16x32_bf16 v[100:103], v[248:251], a[244:247], v[100:103]\n"
  "ds_read_b128 v[244:247], v2 offset:13312\n"
  "v_mfma_f32_16x16x32_bf16 v[100:103], v[252:255], a[252:255], v[100:103]\n"
  "s_waitcnt lgkmcnt(3)\n"
  "v_mfma_f32_16x16x32_bf16 v[104:107], v[224:227], a[224:227], v[104:107]\n"
  "ds_read_b128 v[248:251], v2 offset:22528\n"
  "v_mfma_f32_16x16x32_bf16 v[104:107], v[228:231], a[232:235], v[104:107]\n"
  "buffer_load_dwordx4 a[168:171], v1, s[36:39], s62 offen offset:1024 sc1\n"
  "v_mfma_f32_16x16x32_bf16 v[104:107], v[232:235], a[240:243], v[104:107]\n"
  "s_waitcnt lgkmcnt(3)\n"
  "ds_read_b128 v[252:255], v2 offset:31744\n"
  "v_mfma_f32_16x16x32_bf16 v[104:107], v[236:239], a[248:251], v[104:107]\n"
  "v_mfma_f32_16x16x32_bf16 v[108:111], v[224:227], a[228:231], v[108:111]\n"
  "ds_read_b128 v[224:227], v2 offset:5120\n"
  "v_mfma_f32_16x16x32_bf16 v[108:111], v[228:231], a[236:239], v[108:111]\n"
  "buffer_load_dwordx4 v1, s[36:39], s66 offen offset:1024 sc1 lds\n"
  "v_mfma_f32_16x16x32_bf16 v[108:111], v[232:235], a[244:247], v[108:111]\n"
  "ds_read_b128 v[228:231], v2 offset:14336\n"
  "v_mfma_f32_16x16x32_bf16 v[108:111], v[236:239], a[252:255], v[108:111]\n"
  "s_waitcnt lgkmcnt(3)\n"
  "v_mfma_f32_16x16x32_bf16 v[112:115], v[240:243], a[224:227], v[112:115]\n"
  "ds_read_b128 v[232:235], v2 offset:23552\n"
  "v_mfma_f32_16x16x32_bf16 v[112:115], v[244:247], a[232:235], v[112:115]\n"
  "buffer_load_dwordx4 a[172:175], v1, s[36:39], s63 offen offset:1024 sc1\n"
  "v_mfma_f32_16x16x32_bf16 v[112:115], v[248:251], a[240:243], v[112:115]\n"
  "s_add_u32 m0, s67, 40960\n"
  "ds_read_b128 v[236:239], v2 offset:32768\n"
  "s_waitcnt lgkmcnt(3)\n"
  "v_mfma_f32_16x16x32_bf16 v[112:115], v[252:255], a[248:251], v[112:115]\n"
  "s_add_u32 s66, s72, 0\n"
  "v_mfma_f32_16x16x32_bf16 v[116:119], v[240:243], a[228:231], v[116:119]\n"
  "ds_read_b128 v[240:243], v2 offset:6144\n"
  "v_mfma_f32_16x16x32_bf16 v[116:119], v[244:247], a[236:239], v[116:119]\n"
  "buffer_load_dwordx4 v1, s[36:39], s66 offen offset:0 sc1 lds\n"
  "v_mfma_f32_16x16x32_bf16 v[116:119], v[248:251], a[244:247], v[116:119]\n"
  "ds_read_b128 v[244:247], v2 offset:15360\n"
  "v_mfma_f32_16x16x32_bf16 v[116:119], v[252:255], a[252:255], v[116:119]\n"
  "v_mfma_f32_16x16x32_bf16 v[120:123], v[224:227], a[224:227], v[120:123]\n"
  "s_waitcnt lgkmcnt(3)\n"
  "ds_read_b128 v[248:251], v2 offset:24576\n"
  "v_mfma_f32_16x16x32_bf16 v[120:123], v[228:231], a[232:235], v[120:123]\n"
  "buffer_load_dwordx4 a[176:179], v1, s[36:39], s62 offen offset:2048 sc1\n"
  "v_mfma_f32_16x16x32_bf16 v[120:123], v[232:235], a[240:243], v[120:123]\n"
  "s_waitcnt lgkmcnt(3)\n"
  "ds_read_b128 v[252:255], v2 offset:33792\n"
  "v_mfma_f32_16x16x32_bf16 v[120:123], v[236:239], a[248:251], v[120:123]\n"
  "v_mfma_f32_16x16x32_bf16 v[124:127], v[224:227], a[228:231], v[124:127]\n"
  "ds_read_b128 v[224:227], v2 offset:7168\n"
  "v_mfma_f32_16x16x32_bf16 v[124:127], v[228:231], a[236:239], v[124:127]\n"
  "buffer_load_dwordx4 v1, s[36:39], s66 offen offset:1024 sc1 lds\n"
  "v_mfma_f32_16x16x32_bf16 v[124:127], v[232:235], a[244:247], v[124:127]\n"
  "ds_read_b128 v[228:231], v2 offset:16384\n"
  "v_mfma_f32_16x16x32_bf16 v[124:127], v[236:239], a[252:255], v[124:127]\n"
  "s_waitcnt lgkmcnt(3)\n"
  "v_mfma_f32_16x16x32_bf16 v[128:131], v[240:243], a[224:227], v[128:131]\n"
  "ds_read_b128 v[232:235], v2 offset:25600\n"
  "v_mfma_f32_16x16x32_bf16 v[128:131], v[244:247], a[232:235], v[128:131]\n"
  "buffer_load_dwordx4 a[180:183], v1, s[36:39], s63 offen offset:2048 sc1\n"
  "v_mfma_f32_16x16x32_bf16 v[128:131], v[248:251], a[240:243], v[128:131]\n"
  "s_add_u32 m0, s67, 43008\n"
  "ds_read_b128 v[236:239], v2 offset:34816\n"
  "s_waitcnt lgkmcnt(3)\n"
  "v_mfma_f32_16x16x32_bf16 v[128:131], v[252:255], a[248:251], v[128:131]\n"
  "s_add_u32 s66, s35, 0\n"
  "v_mfma_f32_16x16x32_bf16 v[132:135], v[240:243], a[228:231], v[132:135]\n"
  "ds_read_b128 v[240:243], v2 offset:8192\n"
  "v_mfma_f32_16x16x32_bf16 v[132:135], v[244:247], a[236:239], v[132:135]\n"
  "buffer_load_dwordx4 v1, s[36:39], s66 offen offset:0 sc1 lds\n"
  "v_mfma_f32_16x16x32_bf16 v[132:135], v[248:251], a[244:247], v[132:135]\n"
  "ds_read_b128 v[244:247], v2 offset:17408\n"
  "v_mfma_f32_16x16x32_bf16 v[132:135], v[252:255], a[252:255], v[132:135]\n"
  "v_mfma_f32_16x16x32_bf16 v[136:139], v[224:227], a[224:227], v[136:139]\n"
  "s_waitcnt lgkmcnt(3)\n"
  "ds_read_b128 v[248:251], v2 offset:26624\n"
  "v_mfma_f32_16x16x32_bf16 v[136:139], v[228:231], a[232:235], v[136:139]\n"
  "buffer_load_dwordx4 a[184:187], v1, s[36:39], s62 offen offset:3072 sc1\n"
  "v_mfma_f32_16x16x32_bf16 v[136:139], v[232:235], a[240:243], v[136:139]\n"
  "s_waitcnt lgkmcnt(2)\n"
  "ds_read_b128 v[252:255], v2 offset:35840\n"
  "v_add_u32 v2, 0xfffe5000, v2\n"
  "v_mfma_f32_16x16x32_bf16 v[136:139], v[236:239], a[248:251], v[136:139]\n"
  "v_mfma_f32_16x16x32_bf16 v[140:143], v[224:227], a[228:231], v[140:143]\n"
  "s_waitcnt vmcnt(14)\n"
  "s_barrier\n"
  "ds_read_b128 v[224:227], v2 offset:2048\n"
  "v_mfma_f32_16x16x32_bf16 v[140:143], v[228:231], a[236:239], v[140:143]\n"
  "buffer_load_dwordx4 v1, s[36:39], s66 offen offset:1024 sc1 lds\n"
  "v_mfma_f32_16x16x32_bf16 v[140:143], v[232:235], a[244:247], v[140:143]\n"
  "ds_read_b128 v[228:231], v2 offset:11264\n"
  "v_mfma_f32_16x16x32_bf16 v[140:143], v[236:239], a[252:255], v[140:143]\n"
  "v_mfma_f32_16x16x32_bf16 v[144:147], v[240:243], a[224:227], v[144:147]\n"
  "s_waitcnt lgkmcnt(2)\n"
  "ds_read_b128 v[232:235], v2 offset:20480\n"
  "v_mfma_f32_16x16x32_bf16 v[144:147], v[244:247], a[232:235], v[144:147]\n"
  "buffer_load_dwordx4 a[188:191], v1, s[36:39], s63 offen offset:3072 sc1\n"
  "v_mfma_f32_16x16x32_bf16 v[144:147], v[248:251], a[240:243], v[144:147]\n"
  "s_add_u32 m0, s67, 45056\n"
  "ds_read_b128 v[236:239], v2 offset:29696\n"
  "v_mfma_f32_16x16x32_bf16 v[144:147], v[252:255], a[248:251], v[144:147]\n"
  "s_add_u32 s66, s64, 8192\n"
  "v_mfma_f32_16x16x32_bf16 v[148:151], v[240:243], a[228:231], v[148:151]\n"
  "ds_read_b128 v[240:243], v2 offset:3072\n"
  "v_mfma_f32_16x16x32_bf16 v[148:151], v[244:247], a[236:239], v[148:151]\n"
  "buffer_load_dwordx4 v1, s[36:39], s66 offen offset:0 sc1 lds\n"
  "v_mfma_f32_16x16x32_bf16 v[148:151], v[248:251], a[244:247], v[148:151]\n"
  "s_add_u32 m0, s67, 73728\n"
  "ds_read_b128 v[244:247], v2 offset:12288\n"
  "v_mfma_f32_16x16x32_bf16 v[148:151], v[252:255], a[252:255], v[148:151]\n"
  "s_add_u32 s66, s70, 36864\n"
  "s_waitcnt lgkmcnt(3)\n"
  "v_mfma_f32_16x16x32_bf16 v[96:99], v[224:227], a[128:131], v[96:99]\n"
  "ds_read_b128 v[248:251], v2 offset:21504\n"
  "v_mfma_f32_16x16x32_bf16 v[96:99], v[228:231], a[136:139], v[96:99]\n"
  "buffer_load_dwordx4 v1, s[36:39], s66 offen offset:0 sc1 lds\n"
  "v_mfma_f32_16x16x32_bf16 v[96:99], v[232:235], a[144:147], v[96:99]\n"
  "s_add_u32 s62, s85, 4096\n"
  "ds_read_b128 v[252:255], v2 offset:30720\n"
  "s_waitcnt lgkmcnt(3)\n"
  "v_mfma_f32_16x16x32_bf16 v[96:99], v[236:239], a[152:155], v[96:99]\n"
  "v_mfma_f32_16x16x32_bf16 v[100:103], v[224:227], a[132:135], v[100:103]\n"
  "ds_read_b128 v[224:227], v2 offset:4096\n"
  "v_mfma_f32_16x16x32_bf16 v[100:103], v[228:231], a[140:143], v[100:103]\n"
  "buffer_load_dwordx4 a[192:195], v1, s[36:39], s62 offen offset:0 sc1\n"
  "v_mfma_f32_16x16x32_bf16 v[100:103], v[232:235], a[148:151], v[100:103]\n"
  "ds_read_b128 v[228:231], v2 offset:13312\n"
  "v_mfma_f32_16x16x32_bf16 v[100:103], v[236:239], a[156:159], v[100:103]\n"
  "v_mfma_f32_16x16x32_bf16 v[104:107], v[240:243], a[128:131], v[104:107]\n"
  "s_waitcnt lgkmcnt(3)\n"
  "ds_read_b128 v[232:235], v2 offset:22528\n"
  "v_mfma_f32_16x16x32_bf16 v[104:107], v[244:247], a[136:139], v[104:107]\n"
  "buffer_load_dwordx4 v1, s[36:39], s66 offen offset:1024 sc1 lds\n"
  "v_mfma_f32_16x16x32_bf16 v[104:107], v[248:251], a[144:147], v[104:107]\n"
  "s_add_u32 s63, s86, 4096\n"
  "ds_read_b128 v[236:239], v2 offset:31744\n"
  "s_waitcnt lgkmcnt(3)\n"
  "v_mfma_f32_16x16x32_bf16 v[104:107], v[252:255], a[152:155], v[104:107]\n"
  "v_mfma_f32_16x16x32_bf16 v[108:111], v[240:243], a[132:135], v[108:111]\n"
  "ds_read_b128 v[240:243], v2 offset:5120\n"
  "v_mfma_f32_16x16x32_bf16 v[108:111], v[244:247], a[140:143], v[108:111]\n"
  "buffer_load_dwordx4 a[196:199], v1, s[36:39], s63 offen offset:0 sc1\n"
  "v_mfma_f32_16x16x32_bf16 v[108:111], v[248:251], a[148:151], v[108:111]\n"
  "s_add_u32 m0, s67, 75776\n"
  "ds_read_b128 v[244:247], v2 offset:14336\n"
  "v_cvt_pk_bf16_f32 v96, v96, v97\n"
  "v_mfma_f32_16x16x32_bf16 v[108:111], v[252:255], a[156:159], v[108:111]\n"
  "s_add_u32 s66, s71, 36864\n"
  "v_cvt_pk_bf16_f32 v97, v98, v99\n"
  "v_cvt_pk_bf16_f32 v98, v104, v105\n"
  "v_mfma_f32_16x16x32_bf16 v[112:115], v[224:227], a[128:131], v[112:115]\n"
  "s_add_u32 s83, s68, 1\n"
  "ds_read_b128 v[248:251], v2 offset:23552\n"
  "v_cvt_pk_bf16_f32 v99, v106, v107\n"
  "s_waitcnt lgkmcnt(2)\n"
  "v_mfma_f32_16x16x32_bf16 v[112:115], v[228:231], a[136:139], v[112:115]\n"
  "s_and_b32 s83, s83, 3\n"
  "buffer_load_dwordx4 v1, s[36:39], s66 offen offset:0 sc1 lds\n"
  "v_mfma_f32_16x16x32_bf16 v[112:115], v[232:235], a[144:147], v[112:115]\n"
  "s_mul_i32 s83, s83, 49152\n"
  "ds_read_b128 v[252:255], v2 offset:32768\n"
  "v_mfma_f32_16x16x32_bf16 v[112:115], v[236:239], a[152:155], v[112:115]\n"
  "s_add_u32 s83, s83, s87\n"
  "buffer_store_dwordx4 v[96:99], v1, s[36:39], s83 offen offset:0 sc1\n"
  "v_mfma_f32_16x16x32_bf16 v[116:119], v[224:227], a[132:135], v[116:119]\n"
  "ds_read_b128 v[224:227], v2 offset:6144\n"
  "v_cvt_pk_bf16_f32 v100, v100, v101\n"
  "v_mfma_f32_16x16x32_bf16 v[116:119], v[228:231], a[140:143], v[116:119]\n"
  "buffer_load_dwordx4 a[200:203], v1, s[36:39], s62 offen offset:1024 sc1\n"
  "v_mfma_f32_16x16x32_bf16 v[116:119], v[232:235], a[148:151], v[116:119]\n"
  "ds_read_b128 v[228:231], v2 offset:15360\n"
  "v_cvt_pk_bf16_f32 v101, v102, v103\n"
  "v_mfma_f32_16x16x32_bf16 v[116:119], v[236:239], a[156:159], v[116:119]\n"
  "v_cvt_pk_bf16_f32 v102, v108, v109\n"
  "v_cvt_pk_bf16_f32 v103, v110, v111\n"
  "v_mfma_f32_16x16x32_bf16 v[120:123], v[240:243], a[128:131], v[120:123]\n"
  "s_waitcnt lgkmcnt(2)\n"
  "ds_read_b128 v[232:235], v2 offset:24576\n"
  "v_mfma_f32_16x16x32_bf16 v[120:123], v[244:247], a[136:139], v[120:123]\n"
  "buffer_load_dwordx4 v1, s[36:39], s66 offen offset:1024 sc1 lds\n"
  "v_mfma_f32_16x16x32_bf16 v[120:123], v[248:251], a[144:147], v[120:123]\n"
  "ds_read_b128 v[236:239], v2 offset:33792\n"
  "v_mfma_f32_16x16x32_bf16 v[120:123], v[252:255], a[152:155], v[120:123]\n"
  "buffer_store_dwordx4 v[100:103], v1, s[36:39], s83 offen offset:1024 sc1\n"
  "v_mfma_f32_16x16x32_bf16 v[124:127], v[240:243], a[132:135], v[124:127]\n"
  "ds_read_b128 v[240:243], v2 offset:7168\n"
  "v_mfma_f32_16x16x32_bf16 v[124:127], v[244:247], a[140:143], v[124:127]\n"
  "buffer_load_dwordx4 a[204:207], v1, s[36:39], s63 offen offset:1024 sc1\n"
  "v_mfma_f32_16x16x32_bf16 v[124:127], v[248:251], a[148:151], v[124:127]\n"
  "s_add_u32 m0, s67, 77824\n"
  "ds_read_b128 v[244:247], v2 offset:16384\n"
  "v_cvt_pk_bf16_f32 v112, v112, v113\n"
  "v_mfma_f32_16x16x32_bf16 v[124:127], v[252:255], a[156:159], v[124:127]\n"
  "s_add_u32 s66, s72, 36864\n"
  "v_cvt_pk_bf16_f32 v113, v114, v115\n"
  "v_cvt_pk_bf16_f32 v114, v120, v121\n"
  "s_waitcnt lgkmcnt(2)\n"
  "v_mfma_f32_16x16x32_bf16 v[128:131], v[224:227], a[128:131], v[128:131]\n"
  "s_add_u32 s83, s68, 2\n"
  "ds_read_b128 v[248:251], v2 offset:25600\n"
  "v_cvt_pk_bf16_f32 v115, v122, v123\n"
  "v_mfma_f32_16x16x32_bf16 v[128:131], v[228:231], a[136:139], v[128:131]\n"
  "s_and_b32 s83, s83, 3\n"
  "buffer_load_dwordx4 v1, s[36:39], s66 offen offset:0 sc1 lds\n"
  "v_mfma_f32_16x16x32_bf16 v[128:131], v[232:235], a[144:147], v[128:131]\n"
  "s_mul_i32 s83, s83, 49152\n"
  "ds_read_b128 v[252:255], v2 offset:34816\n"
  "v_mfma_f32_16x16x32_bf16 v[128:131], v[236:239], a[152:155], v[128:131]\n"
  "s_add_u32 s83, s83, s87\n"
  "buffer_store_dwordx4 v[112:115], v1, s[36:39], s83 offen offset:0 sc1\n"
  "v_mfma_f32_16x16x32_bf16 v[132:135], v[224:227], a[132:135], v[132:135]\n"
  "ds_read_b128 v[224:227], v2 offset:8192\n"
  "v_cvt_pk_bf16_f32 v116, v116, v117\n"
  "v_mfma_f32_16x16x32_bf16 v[132:135], v[228:231], a[140:143], v[132:135]\n"
  "v_cvt_pk_bf16_f32 v117, v118, v119\n"
  "v_cvt_pk_bf16_f32 v118, v124, v125\n"
  "v_mfma_f32_16x16x32_bf16 v[132:135], v[232:235], a[148:151], v[132:135]\n"
  "ds_read_b128 v[228:231], v2 offset:17408\n"
  "v_cvt_pk_bf16_f32 v119, v126, v127\n"
  "v_mfma_f32_16x16x32_bf16 v[132:135], v[236:239], a[156:159], v[132:135]\n"
  "s_waitcnt lgkmcnt(2)\n"
  "buffer_load_dwordx4 a[208:211], v1, s[36:39], s62 offen offset:2048 sc1\n"
  "v_mfma_f32_16x16x32_bf16 v[136:139], v[240:243], a[128:131], v[136:139]\n"
  "ds_read_b128 v[232:235], v2 offset:26624\n"
  "v_mfma_f32_16x16x32_bf16 v[136:139], v[244:247], a[136:139], v[136:139]\n"
  "buffer_store_dwordx4 v[116:119], v1, s[36:39], s83 offen offset:1024 sc1\n"
  "v_mfma_f32_16x16x32_bf16 v[136:139], v[248:251], a[144:147], v[136:139]\n"
  "ds_read_b128 v[236:239], v2 offset:35840\n"
  "v_mfma_f32_16x16x32_bf16 v[136:139], v[252:255], a[152:155], v[136:139]\n"
  "buffer_load_dwordx4 v1, s[36:39], s66 offen offset:1024 sc1 lds\n"
  "v_mfma_f32_16x16x32_bf16 v[140:143], v[240:243], a[132:135], v[140:143]\n"
  "ds_read_b128 v[240:243], v2 offset:0\n"
  "v_mfma_f32_16x16x32_bf16 v[140:143], v[244:247], a[140:143], v[140:143]\n"
  "v_mfma_f32_16x16x32_bf16 v[140:143], v[248:251], a[148:151], v[140:143]\n"
  "ds_read_b128 v[244:247], v2 offset:9216\n"
  "v_cvt_pk_bf16_f32 v128, v128, v129\n"
  "v_mfma_f32_16x16x32_bf16 v[140:143], v[252:255], a[156:159], v[140:143]\n"
  "s_waitcnt lgkmcnt(3)\n"
  "buffer_load_dwordx4 a[212:215], v1, s[36:39], s63 offen offset:2048 sc1\n"
  "v_mfma_f32_16x16x32_bf16 v[144:147], v[224:227], a[128:131], v[144:147]\n"
  "s_add_u32 m0, s67, 79872\n"
  "ds_read_b128 v[248:251], v2 offset:18432\n"
  "v_cvt_pk_bf16_f32 v129, v130, v131\n"
  "v_mfma_f32_16x16x32_bf16 v[144:147], v[228:231], a[136:139], v[144:147]\n"
  "s_add_u32 s66, s35, 36864\n"
  "v_cvt_pk_bf16_f32 v130, v136, v137\n"
  "v_cvt_pk_bf16_f32 v131, v138, v139\n"
  "v_mfma_f32_16x16x32_bf16 v[144:147], v[232:235], a[144:147], v[144:147]\n"
  "s_add_u32 s83, s68, 3\n"
  "ds_read_b128 v[252:255], v2 offset:27648\n"
  "s_waitcnt lgkmcnt(2)\n"
  "v_mfma_f32_16x16x32_bf16 v[144:147], v[236:239], a[152:155], v[144:147]\n"
  "s_and_b32 s83, s83, 3\n"
  "buffer_load_dwordx4 v1, s[36:39], s66 offen offset:0 sc1 lds\n"
  "v_mfma_f32_16x16x32_bf16 v[148:151], v[224:227], a[132:135], v[148:151]\n"
  "s_mul_i32 s83, s83, 49152\n"
  "ds_read_b128 v[224:227], v2 offset:1024\n"
  "v_mfma_f32_16x16x32_bf16 v[148:151], v[228:231], a[140:143], v[148:151]\n"
  "s_add_u32 s83, s83, s87\n"
  "buffer_store_dwordx4 v[128:131], v1, s[36:39], s83 offen offset:0 sc1\n"
  "v_mfma_f32_16x16x32_bf16 v[148:151], v[232:235], a[148:151], v[148:151]\n"
  "ds_read_b128 v[228:231], v2 offset:10240\n"
  "v_cvt_pk_bf16_f32 v132, v132, v133\n"
  "v_mfma_f32_16x16x32_bf16 v[148:151], v[236:239], a[156:159], v[148:151]\n"
  "buffer_load_dwordx4 a[216:219], v1, s[36:39], s62 offen offset:3072 sc1\n"
  "v_mfma_f32_16x16x32_bf16 v[80:83], v[240:243], a[128:131], v[80:83]\n"
  "ds_read_b128 v[232:235], v2 offset:19456\n"
  "v_cvt_pk_bf16_f32 v133, v134, v135\n"
  "v_mfma_f32_16x16x32_bf16 v[80:83], v[244:247], a[136:139], v[80:83]\n"
  "s_waitcnt lgkmcnt(2)\n"
  "v_cvt_pk_bf16_f32 v134, v140, v141\n"
  "v_cvt_pk_bf16_f32 v135, v142, v143\n"
  "v_mfma_f32_16x16x32_bf16 v[80:83], v[248:251], a[144:147], v[80:83]\n"
  "ds_read_b128 v[236:239], v2 offset:28672\n"
  "v_mfma_f32_16x16x32_bf16 v[80:83], v[252:255], a[152:155], v[80:83]\n"
  "buffer_load_dwordx4 v1, s[36:39], s66 offen offset:1024 sc1 lds\n"
  "v_mfma_f32_16x16x32_bf16 v[84:87], v[240:243], a[132:135], v[84:87]\n"
  "s_waitcnt vmcnt(20)\n"
  "s_barrier\n"
  "ds_read_b128 v[240:243], v2 offset:36864\n"
  "v_mfma_f32_16x16x32_bf16 v[84:87], v[244:247], a[140:143], v[84:87]\n"
  "buffer_store_dwordx4 v[132:135], v1, s[36:39], s83 offen offset:1024 sc1\n"
  "v_mfma_f32_16x16x32_bf16 v[84:87], v[248:251], a[148:151], v[84:87]\n"
  "ds_read_b128 v[244:247], v2 offset:46080\n"
  "v_cvt_pk_bf16_f32 v136, v144, v145\n"
  "v_mfma_f32_16x16x32_bf16 v[84:87], v[252:255], a[156:159], v[84:87]\n"
  "buffer_load_dwordx4 a[220:223], v1, s[36:39], s63 offen offset:3072 sc1\n"
  "v_mfma_f32_16x16x32_bf16 v[88:91], v[224:227], a[128:131], v[88:91]\n"
  "s_add_u32 m0, s67, 81920\n"
  "ds_read_b128 v[248:251], v2 offset:55296\n"
  "v_cvt_pk_bf16_f32 v137, v146, v147\n"
  "s_waitcnt lgkmcnt(3)\n"
  "v_mfma_f32_16x16x32_bf16 v[88:91], v[228:231], a[136:139], v[88:91]\n"
  "s_add_u32 s66, s64, 45056\n"
  "v_cvt_pk_bf16_f32 v138, v148, v149\n"
  "v_cvt_pk_bf16_f32 v139, v150, v151\n"
  "v_mfma_f32_16x16x32_bf16 v[88:91], v[232:235], a[144:147], v[88:91]\n"
  "s_mul_i32 s83, s82, 49152\n"
  "ds_read_b128 v[252:255], v2 offset:64512\n"
  "v_mfma_f32_16x16x32_bf16 v[88:91], v[236:239], a[152:155], v[88:91]\n"
  "s_add_u32 s83, s83, s87\n"
  "buffer_load_dwordx4 v1, s[36:39], s66 offen offset:0 sc1 lds\n"
  "v_mfma_f32_16x16x32_bf16 v[92:95], v[224:227], a[132:135], v[92:95]\n"
  "s_add_u32 m0, s67, 110592\n"
  "ds_read_b128 v[224:227], v2 offset:37888\n"
  "v_mfma_f32_16x16x32_bf16 v[92:95], v[228:231], a[140:143], v[92:95]\n"
  "s_add_u32 s66, s70, 73728\n"
  "buffer_store_dwordx4 v[136:139], v1, s[36:39], s83 offen offset:2048 sc1\n"
  "v_mfma_f32_16x16x32_bf16 v[92:95], v[232:235], a[148:151], v[92:95]\n"
  "ds_read_b128 v[228:231], v2 offset:47104\n"
  "v_mfma_f32_16x16x32_bf16 v[92:95], v[236:239], a[156:159], v[92:95]\n"
  "s_waitcnt lgkmcnt(3)\n"
  "v_mfma_f32_16x16x32_bf16 v[152:155], v[240:243], a[160:163], 0\n"
  "ds_read_b128 v[232:235], v2 offset:56320\n"
  "v_add_u32 v2, 36864, v2\n"
  "v_mfma_f32_16x16x32_bf16 v[152:155], v[244:247], a[168:171], v[152:155]\n"
  "buffer_load_dwordx4 v1, s[36:39], s66 offen offset:0 sc1 lds\n"
  "v_mfma_f32_16x16x32_bf16 v[152:155], v[248:251], a[176:179], v[152:155]\n"
  "s_add_u32 s62, s85, 8192\n"
  "ds_read_b128 v[236:239], v2 offset:28672\n"
  "s_waitcnt lgkmcnt(3)\n"
  "v_mfma_f32_16x16x32_bf16 v[152:155], v[252:255], a[184:187], v[152:155]\n"
  "v_mfma_f32_16x16x32_bf16 v[156:159], v[240:243], a[164:167], 0\n"
  "ds_read_b128 v[240:243], v2 offset:2048\n"
  "v_mfma_f32_16x16x32_bf16 v[156:159], v[244:247], a[172:175], v[156:159]\n"
  "buffer_load_dwordx4 a[224:227], v1, s[36:39], s62 offen offset:0 sc1\n"
  "v_mfma_f32_16x16x32_bf16 v[156:159], v[248:251], a[180:183], v[156:159]\n"
  "ds_read_b128 v[244:247], v2 offset:11264\n"
  "v_mfma_f32_16x16x32_bf16 v[156:159], v[252:255], a[188:191], v[156:159]\n"
  "v_mfma_f32_16x16x32_bf16 v[160:163], v[224:227], a[160:163], 0\n"
  "s_waitcnt lgkmcnt(3)\n"
  "ds_read_b128 v[248:251], v2 offset:20480\n"
  "v_mfma_f32_16x16x32_bf16 v[160:163], v[228:231], a[168:171], v[160:163]\n"
  "buffer_load_dwordx4 v1, s[36:39], s66 offen offset:1024 sc1 lds\n"
  "v_mfma_f32_16x16x32_bf16 v[160:163], v[232:235], a[176:179], v[160:163]\n"
  "s_add_u32 s63, s86, 8192\n"
  "ds_read_b128 v[252:255], v2 offset:29696\n"
  "s_waitcnt lgkmcnt(3)\n"
  "v_mfma_f32_16x16x32_bf16 v[160:163], v[236:239], a[184:187], v[160:163]\n"
  "v_mfma_f32_16x16x32_bf16 v[164:167], v[224:227], a[164:167], 0\n"
  "ds_read_b128 v[224:227], v2 offset:3072\n"
  "v_mfma_f32_16x16x32_bf16 v[164:167], v[228:231], a[172:175], v[164:167]\n"
  "buffer_load_dwordx4 a[228:231], v1, s[36:39], s63 offen offset:0 sc1\n"
  "v_mfma_f32_16x16x32_bf16 v[164:167], v[232:235], a[180:183], v[164:167]\n"
  "s_add_u32 m0, s67, 112640\n"
  "ds_read_b128 v[228:231], v2 offset:12288\n"
  "v_mfma_f32_16x16x32_bf16 v[164:167], v[236:239], a[188:191], v[164:167]\n"
  "s_add_u32 s66, s71, 73728\n"
  "v_mfma_f32_16x16x32_bf16 v[168:171], v[240:243], a[160:163], 0\n"
  "s_waitcnt lgkmcnt(2)\n"
  "ds_read_b128 v[232:235], v2 offset:21504\n"
  "v_mfma_f32_16x16x32_bf16 v[168:171], v[244:247], a[168:171], v[168:171]\n"
  "buffer_load_dwordx4 v1, s[36:39], s66 offen offset:0 sc1 lds\n"
  "v_mfma_f32_16x16x32_bf16 v[168:171], v[248:251], a[176:179], v[168:171]\n"
  "ds_read_b128 v[236:239], v2 offset:30720\n"
  "v_mfma_f32_16x16x32_bf16 v[168:171], v[252:255], a[184:187], v[168:171]\n"
  "v_mfma_f32_16x16x32_bf16 v[172:175], v[240:243], a[164:167], 0\n"
  "ds_read_b128 v[240:243], v2 offset:4096\n"
  "v_mfma_f32_16x16x32_bf16 v[172:175], v[244:247], a[172:175], v[172:175]\n"
  "buffer_load_dwordx4 a[232:235], v1, s[36:39], s62 offen offset:1024 sc1\n"
  "v_mfma_f32_16x16x32_bf16 v[172:175], v[248:251], a[180:183], v[172:175]\n"
  "ds_read_b128 v[244:247], v2 offset:13312\n"
  "v_mfma_f32_16x16x32_bf16 v[172:175], v[252:255], a[188:191], v[172:175]\n"
  "s_waitcnt lgkmcnt(3)\n"
  "v_mfma_f32_16x16x32_bf16 v[176:179], v[224:227], a[160:163], 0\n"
  "ds_read_b128 v[248:251], v2 offset:22528\n"
  "v_mfma_f32_16x16x32_bf16 v[176:179], v[228:231], a[168:171], v[176:179]\n"
  "buffer_load_dwordx4 v1, s[36:39], s66 offen offset:1024 sc1 lds\n"
  "v_mfma_f32_16x16x32_bf16 v[176:179], v[232:235], a[176:179], v[176:179]\n"
  "s_waitcnt lgkmcnt(3)\n"
  "ds_read_b128 v[252:255], v2 offset:31744\n"
  "v_mfma_f32_16x16x32_bf16 v[176:179], v[236:239], a[184:187], v[176:179]\n"
  "v_mfma_f32_16x16x32_bf16 v[180:183], v[224:227], a[164:167], 0\n"
  "ds_read_b128 v[224:227], v2 offset:5120\n"
  "v_mfma_f32_16x16x32_bf16 v[180:183], v[228:231], a[172:175], v[180:183]\n"
  "buffer_load_dwordx4 a[236:239], v1, s[36:39], s63 offen offset:1024 sc1\n"
  "v_mfma_f32_16x16x32_bf16 v[180:183], v[232:235], a[180:183], v[180:183]\n"
  "s_add_u32 m0, s67, 114688\n"
  "ds_read_b128 v[228:231], v2 offset:14336\n"
  "v_mfma_f32_16x16x32_bf16 v[180:183], v[236:239], a[188:191], v[180:183]\n"
  "s_add_u32 s66, s72, 73728\n"
  "s_waitcnt lgkmcnt(3)\n"
  "v_mfma_f32_16x16x32_bf16 v[184:187], v[240:243], a[160:163], 0\n"
  "ds_read_b128 v[232:235], v2 offset:23552\n"
  "v_mfma_f32_16x16x32_bf16 v[184:187], v[244:247], a[168:171], v[184:187]\n"
  "buffer_load_dwordx4 v1, s[36:39], s66 offen offset:0 sc1 lds\n"
  "v_mfma_f32_16x16x32_bf16 v[184:187], v[248:251], a[176:179], v[184:187]\n"
  "s_waitcnt lgkmcnt(2)\n"
  "ds_read_b128 v[236:239], v2 offset:32768\n"
  "v_mfma_f32_16x16x32_bf16 v[184:187], v[252:255], a[184:187], v[184:187]\n"
  "v_mfma_f32_16x16x32_bf16 v[188:191], v[240:243], a[164:167], 0\n"
  "ds_read_b128 v[240:243], v2 offset:6144\n"
  "v_mfma_f32_16x16x32_bf16 v[188:191], v[244:247], a[172:175], v[188:191]\n"
  "v_mfma_f32_16x16x32_bf16 v[188:191], v[248:251], a[180:183], v[188:191]\n"
  "ds_read_b128 v[244:247], v2 offset:15360\n"
  "v_mfma_f32_16x16x32_bf16 v[188:191], v[252:255], a[188:191], v[188:191]\n"
  "buffer_load_dwordx4 a[240:243], v1, s[36:39], s62 offen offset:2048 sc1\n"
  "v_mfma_f32_16x16x32_bf16 v[192:195], v[224:227], a[160:163], 0\n"
  "s_waitcnt lgkmcnt(3)\n"
  "ds_read_b128 v[248:251], v2 offset:24576\n"
  "v_mfma_f32_16x16x32_bf16 v[192:195], v[228:231], a[168:171], v[192:195]\n"
  "v_mfma_f32_16x16x32_bf16 v[192:195], v[232:235], a[176:179], v[192:195]\n"
  "s_waitcnt lgkmcnt(3)\n"
  "ds_read_b128 v[252:255], v2 offset:33792\n"
  "v_mfma_f32_16x16x32_bf16 v[192:195], v[236:239], a[184:187], v[192:195]\n"
  "buffer_load_dwordx4 v1, s[36:39], s66 offen offset:1024 sc1 lds\n"
  "v_mfma_f32_16x16x32_bf16 v[196:199], v[224:227], a[164:167], 0\n"
  "ds_read_b128 v[224:227], v2 offset:7168\n"
  "v_mfma_f32_16x16x32_bf16 v[196:199], v[228:231], a[172:175], v[196:199]\n"
  "v_mfma_f32_16x16x32_bf16 v[196:199], v[232:235], a[180:183], v[196:199]\n"
  "ds_read_b128 v[228:231], v2 offset:16384\n"
  "v_mfma_f32_16x16x32_bf16 v[196:199], v[236:239], a[188:191], v[196:199]\n"
  "s_waitcnt lgkmcnt(3)\n"
  "buffer_load_dwordx4 a[244:247], v1, s[36:39], s63 offen offset:2048 sc1\n"
  "v_mfma_f32_16x16x32_bf16 v[200:203], v[240:243], a[160:163], 0\n"
  "s_add_u32 m0, s67, 116736\n"
  "ds_read_b128 v[232:235], v2 offset:25600\n"
  "v_mfma_f32_16x16x32_bf16 v[200:203], v[244:247], a[168:171], v[200:203]\n"
  "s_add_u32 s66, s35, 73728\n"
  "v_mfma_f32_16x16x32_bf16 v[200:203], v[248:251], a[176:179], v[200:203]\n"
  "s_waitcnt lgkmcnt(2)\n"
  "ds_read_b128 v[236:239], v2 offset:34816\n"
  "v_mfma_f32_16x16x32_bf16 v[200:203], v[252:255], a[184:187], v[200:203]\n"
  "buffer_load_dwordx4 v1, s[36:39], s66 offen offset:0 sc1 lds\n"
  "v_mfma_f32_16x16x32_bf16 v[204:207], v[240:243], a[164:167], 0\n"
  "ds_read_b128 v[240:243], v2 offset:8192\n"
  "v_mfma_f32_16x16x32_bf16 v[204:207], v[244:247], a[172:175], v[204:207]\n"
  "v_mfma_f32_16x16x32_bf16 v[204:207], v[248:251], a[180:183], v[204:207]\n"
  "ds_read_b128 v[244:247], v2 offset:17408\n"
  "v_mfma_f32_16x16x32_bf16 v[204:207], v[252:255], a[188:191], v[204:207]\n"
  "buffer_load_dwordx4 a[248:251], v1, s[36:39], s62 offen offset:3072 sc1\n"
  "v_mfma_f32_16x16x32_bf16 v[208:211], v[224:227], a[160:163], 0\n"
  "s_waitcnt lgkmcnt(3)\n"
  "ds_read_b128 v[248:251], v2 offset:26624\n"
  "v_mfma_f32_16x16x32_bf16 v[208:211], v[228:231], a[168:171], v[208:211]\n"
  "v_mfma_f32_16x16x32_bf16 v[208:211], v[232:235], a[176:179], v[208:211]\n"
  "s_waitcnt lgkmcnt(3)\n"
  "ds_read_b128 v[252:255], v2 offset:35840\n"
  "v_mfma_f32_16x16x32_bf16 v[208:211], v[236:239], a[184:187], v[208:211]\n"
  "buffer_load_dwordx4 v1, s[36:39], s66 offen offset:1024 sc1 lds\n"
  "v_mfma_f32_16x16x32_bf16 v[212:215], v[224:227], a[164:167], 0\n"
  "s_waitcnt vmcnt(16)\n"
  "s_barrier\n"
  "ds_read_b128 v[224:227], v2 offset:36864\n"
  "v_mfma_f32_16x16x32_bf16 v[212:215], v[228:231], a[172:175], v[212:215]\n"
  "v_mfma_f32_16x16x32_bf16 v[212:215], v[232:235], a[180:183], v[212:215]\n"
  "ds_read_b128 v[228:231], v2 offset:46080\n"
  "v_mfma_f32_16x16x32_bf16 v[212:215], v[236:239], a[188:191], v[212:215]\n"
  "s_waitcnt lgkmcnt(3)\n"
  "buffer_load_dwordx4 a[252:255], v1, s[36:39], s63 offen offset:3072 sc1\n"
  "v_mfma_f32_16x16x32_bf16 v[216:219], v[240:243], a[160:163], 0\n"
  "s_add_u32 m0, s67, 118784\n"
  "ds_read_b128 v[232:235], v2 offset:55296\n"
  "v_mfma_f32_16x16x32_bf16 v[216:219], v[244:247], a[168:171], v[216:219]\n"
  "s_add_u32 s66, s64, 81920\n"
  "v_mfma_f32_16x16x32_bf16 v[216:219], v[248:251], a[176:179], v[216:219]\n"
  "s_waitcnt lgkmcnt(2)\n"
  "ds_read_b128 v[236:239], v2 offset:64512\n"
  "v_mfma_f32_16x16x32_bf16 v[216:219], v[252:255], a[184:187], v[216:219]\n"
  "buffer_load_dwordx4 v1, s[36:39], s66 offen offset:0 sc1 lds\n"
  "v_mfma_f32_16x16x32_bf16 v[220:223], v[240:243], a[164:167], 0\n"
  "s_add_u32 m0, s67, 0\n"
  "ds_read_b128 v[240:243], v2 offset:37888\n"
  "v_mfma_f32_16x16x32_bf16 v[220:223], v[244:247], a[172:175], v[220:223]\n"
  "s_add_u32 s66, s70, 110592\n"
  "v_mfma_f32_16x16x32_bf16 v[220:223], v[248:251], a[180:183], v[220:223]\n"
  "ds_read_b128 v[244:247], v2 offset:47104\n"
  "v_mfma_f32_16x16x32_bf16 v[220:223], v[252:255], a[188:191], v[220:223]\n"
  "v_mfma_f32_16x16x32_bf16 v[152:155], v[224:227], a[192:195], v[152:155]\n"
  "s_lshl_b32 s83, s68, 2\n"
  "ds_read_b128 v[248:251], v2 offset:56320\n"
  "s_waitcnt vmcnt(17)\n"
  "v_add_u32 v2, 36864, v2\n"
  "s_waitcnt lgkmcnt(3)\n"
  "v_mfma_f32_16x16x32_bf16 v[152:155], v[228:231], a[200:203], v[152:155]\n"
  "s_add_u32 s83, s83, s78\n"
  "buffer_load_dwordx4 v1, s[36:39], s66 offen offset:0 sc1 lds\n"
  "v_mfma_f32_16x16x32_bf16 v[152:155], v[232:235], a[208:211], v[152:155]\n"
  "s_add_u32 s62, s85, 12288\n"
  "ds_read_b128 v[252:255], v2 offset:28672\n"
  "v_mfma_f32_16x16x32_bf16 v[152:155], v[236:239], a[216:219], v[152:155]\n"
  "buffer_store_dword v22, v3, s[36:39], s83 offen offset:0 sc1\n"
  "v_mfma_f32_16x16x32_bf16 v[156:159], v[224:227], a[196:199], v[156:159]\n"
  "ds_read_b128 v[224:227], v2 offset:2048\n"
  "v_and_b32 v13, 3, v0\n"
  "v_mfma_f32_16x16x32_bf16 v[156:159], v[228:231], a[204:207], v[156:159]\n"
  "buffer_load_dwordx4 a[128:131], v1, s[36:39], s62 offen offset:0 sc1\n"
  "v_mfma_f32_16x16x32_bf16 v[156:159], v[232:235], a[212:215], v[156:159]\n"
  "s_add_u32 s87, s87, 12582912\n"
  "ds_read_b128 v[228:231], v2 offset:11264\n"
  "v_lshlrev_b32 v13, 2, v13\n"
  "v_mfma_f32_16x16x32_bf16 v[156:159], v[236:239], a[220:223], v[156:159]\n"
  "s_waitcnt lgkmcnt(3)\n"
  "v_mfma_f32_16x16x32_bf16 v[160:163], v[240:243], a[192:195], v[160:163]\n"
  "ds_read_b128 v[232:235], v2 offset:20480\n"
  "v_mfma_f32_16x16x32_bf16 v[160:163], v[244:247], a[200:203], v[160:163]\n"
  "buffer_load_dwordx4 v1, s[36:39], s66 offen offset:1024 sc1 lds\n"
  "v_mfma_f32_16x16x32_bf16 v[160:163], v[248:251], a[208:211], v[160:163]\n"
  "s_add_u32 s63, s86, 12288\n"
  "ds_read_b128 v[236:239], v2 offset:29696\n"
  "s_waitcnt lgkmcnt(3)\n"
  "v_mfma_f32_16x16x32_bf16 v[160:163], v[252:255], a[216:219], v[160:163]\n"
  "v_mfma_f32_16x16x32_bf16 v[164:167], v[240:243], a[196:199], v[164:167]\n"
  "ds_read_b128 v[240:243], v2 offset:3072\n"
  "v_mfma_f32_16x16x32_bf16 v[164:167], v[244:247], a[204:207], v[164:167]\n"
  "buffer_load_dwordx4 a[132:135], v1, s[36:39], s63 offen offset:0 sc1\n"
  "v_mfma_f32_16x16x32_bf16 v[164:167], v[248:251], a[212:215], v[164:167]\n"
  "s_add_u32 m0, s67, 2048\n"
  "ds_read_b128 v[244:247], v2 offset:12288\n"
  "v_mfma_f32_16x16x32_bf16 v[164:167], v[252:255], a[220:223], v[164:167]\n"
  "s_add_u32 s66, s71, 110592\n"
  "v_mfma_f32_16x16x32_bf16 v[168:171], v[224:227], a[192:195], v[168:171]\n"
  "s_waitcnt lgkmcnt(2)\n"
  "ds_read_b128 v[248:251], v2 offset:21504\n"
  "v_mfma_f32_16x16x32_bf16 v[168:171], v[228:231], a[200:203], v[168:171]\n"
  "buffer_load_dwordx4 v1, s[36:39], s66 offen offset:0 sc1 lds\n"
  "v_mfma_f32_16x16x32_bf16 v[168:171], v[232:235], a[208:211], v[168:171]\n"
  "ds_read_b128 v[252:255], v2 offset:30720\n"
  "v_mfma_f32_16x16x32_bf16 v[168:171], v[236:239], a[216:219], v[168:171]\n"
  "v_mfma_f32_16x16x32_bf16 v[172:175], v[224:227], a[196:199], v[172:175]\n"
  "ds_read_b128 v[224:227], v2 offset:4096\n"
  "v_mfma_f32_16x16x32_bf16 v[172:175], v[228:231], a[204:207], v[172:175]\n"
  "buffer_load_dwordx4 a[136:139], v1, s[36:39], s62 offen offset:1024 sc1\n"
  "v_mfma_f32_16x16x32_bf16 v[172:175], v[232:235], a[212:215], v[172:175]\n"
  "ds_read_b128 v[228:231], v2 offset:13312\n"
  "v_mfma_f32_16x16x32_bf16 v[172:175], v[236:239], a[220:223], v[172:175]\n"
  "s_waitcnt lgkmcnt(3)\n"
  "v_mfma_f32_16x16x32_bf16 v[176:179], v[240:243], a[192:195], v[176:179]\n"
  "ds_read_b128 v[232:235], v2 offset:22528\n"
  "v_mfma_f32_16x16x32_bf16 v[176:179], v[244:247], a[200:203], v[176:179]\n"
  "buffer_load_dwordx4 v1, s[36:39], s66 offen offset:1024 sc1 lds\n"
  "v_mfma_f32_16x16x32_bf16 v[176:179], v[248:251], a[208:211], v[176:179]\n"
  "s_waitcnt lgkmcnt(3)\n"
  "ds_read_b128 v[236:239], v2 offset:31744\n"
  "v_mfma_f32_16x16x32_bf16 v[176:179], v[252:255], a[216:219], v[176:179]\n"
  "v_mfma_f32_16x16x32_bf16 v[180:183], v[240:243], a[196:199], v[180:183]\n"
  "ds_read_b128 v[240:243], v2 offset:5120\n"
  "v_mfma_f32_16x16x32_bf16 v[180:183], v[244:247], a[204:207], v[180:183]\n"
  "buffer_load_dwordx4 a[140:143], v1, s[36:39], s63 offen offset:1024 sc1\n"
  "v_mfma_f32_16x16x32_bf16 v[180:183], v[248:251], a[212:215], v[180:183]\n"
  "s_add_u32 m0, s67, 4096\n"
  "ds_read_b128 v[244:247], v2 offset:14336\n"
  "v_mfma_f32_16x16x32_bf16 v[180:183], v[252:255], a[220:223], v[180:183]\n"
  "s_add_u32 s66, s72, 110592\n"
  "s_waitcnt lgkmcnt(3)\n"
  "v_mfma_f32_16x16x32_bf16 v[184:187], v[224:227], a[192:195], v[184:187]\n"
  "ds_read_b128 v[248:251], v2 offset:23552\n"
  "v_mfma_f32_16x16x32_bf16 v[184:187], v[228:231], a[200:203], v[184:187]\n"
  "buffer_load_dwordx4 v1, s[36:39], s66 offen offset:0 sc1 lds\n"
  "v_mfma_f32_16x16x32_bf16 v[184:187], v[232:235], a[208:211], v[184:187]\n"
  "s_waitcnt lgkmcnt(2)\n"
  "ds_read_b128 v[252:255], v2 offset:32768\n"
  "v_mfma_f32_16x16x32_bf16 v[184:187], v[236:239], a[216:219], v[184:187]\n"
  "v_mfma_f32_16x16x32_bf16 v[188:191], v[224:227], a[196:199], v[188:191]\n"
  "ds_read_b128 v[224:227], v2 offset:6144\n"
  "v_mfma_f32_16x16x32_bf16 v[188:191], v[228:231], a[204:207], v[188:191]\n"
  "v_mfma_f32_16x16x32_bf16 v[188:191], v[232:235], a[212:215], v[188:191]\n"
  "ds_read_b128 v[228:231], v2 offset:15360\n"
  "v_mfma_f32_16x16x32_bf16 v[188:191], v[236:239], a[220:223], v[188:191]\n"
  "buffer_load_dwordx4 a[144:147], v1, s[36:39], s62 offen offset:2048 sc1\n"
  "v_mfma_f32_16x16x32_bf16 v[192:195], v[240:243], a[192:195], v[192:195]\n"
  "s_waitcnt lgkmcnt(3)\n"
  "ds_read_b128 v[232:235], v2 offset:24576\n"
  "v_mfma_f32_16x16x32_bf16 v[192:195], v[244:247], a[200:203], v[192:195]\n"
  "v_mfma_f32_16x16x32_bf16 v[192:195], v[248:251], a[208:211], v[192:195]\n"
  "s_waitcnt lgkmcnt(3)\n"
  "ds_read_b128 v[236:239], v2 offset:33792\n"
  "v_mfma_f32_16x16x32_bf16 v[192:195], v[252:255], a[216:219], v[192:195]\n"
  "buffer_load_dwordx4 v1, s[36:39], s66 offen offset:1024 sc1 lds\n"
  "v_mfma_f32_16x16x32_bf16 v[196:199], v[240:243], a[196:199], v[196:199]\n"
  "ds_read_b128 v[240:243], v2 offset:7168\n"
  "v_mfma_f32_16x16x32_bf16 v[196:199], v[244:247], a[204:207], v[196:199]\n"
  "v_mfma_f32_16x16x32_bf16 v[196:199], v[248:251], a[212:215], v[196:199]\n"
  "ds_read_b128 v[244:247], v2 offset:16384\n"
  "v_mfma_f32_16x16x32_bf16 v[196:199], v[252:255], a[220:223], v[196:199]\n"
  "s_waitcnt lgkmcnt(3)\n"
  "buffer_load_dwordx4 a[148:151], v1, s[36:39], s63 offen offset:2048 sc1\n"
  "v_mfma_f32_16x16x32_bf16 v[200:203], v[224:227], a[192:195], v[200:203]\n"
  "s_add_u32 m0, s67, 6144\n"
  "ds_read_b128 v[248:251], v2 offset:25600\n"
  "v_mfma_f32_16x16x32_bf16 v[200:203], v[228:231], a[200:203], v[200:203]\n"
  "s_add_u32 s66, s35, 110592\n"
  "v_mfma_f32_16x16x32_bf16 v[200:203], v[232:235], a[208:211], v[200:203]\n"
  "s_waitcnt lgkmcnt(2)\n"
  "ds_read_b128 v[252:255], v2 offset:34816\n"
  "v_mfma_f32_16x16x32_bf16 v[200:203], v[236:239], a[216:219], v[200:203]\n"
  "buffer_load_dwordx4 v1, s[36:39], s66 offen offset:0 sc1 lds\n"
  "v_mfma_f32_16x16x32_bf16 v[204:207], v[224:227], a[196:199], v[204:207]\n"
  "ds_read_b128 v[224:227], v2 offset:8192\n"
  "v_mfma_f32_16x16x32_bf16 v[204:207], v[228:231], a[204:207], v[204:207]\n"
  "v_mfma_f32_16x16x32_bf16 v[204:207], v[232:235], a[212:215], v[204:207]\n"
  "ds_read_b128 v[228:231], v2 offset:17408\n"
  "v_mfma_f32_16x16x32_bf16 v[204:207], v[236:239], a[220:223], v[204:207]\n"
  "buffer_load_dwordx4 a[152:155], v1, s[36:39], s62 offen offset:3072 sc1\n"
  "v_mfma_f32_16x16x32_bf16 v[208:211], v[240:243], a[192:195], v[208:211]\n"
  "s_waitcnt lgkmcnt(3)\n"
  "ds_read_b128 v[232:235], v2 offset:26624\n"
  "v_mfma_f32_16x16x32_bf16 v[208:211], v[244:247], a[200:203], v[208:211]\n"
  "v_mfma_f32_16x16x32_bf16 v[208:211], v[248:251], a[208:211], v[208:211]\n"
  "s_waitcnt lgkmcnt(3)\n"
  "ds_read_b128 v[236:239], v2 offset:35840\n"
  "v_mfma_f32_16x16x32_bf16 v[208:211], v[252:255], a[216:219], v[208:211]\n"
  "buffer_load_dwordx4 v1, s[36:39], s66 offen offset:1024 sc1 lds\n"
  "v_mfma_f32_16x16x32_bf16 v[212:215], v[240:243], a[196:199], v[212:215]\n"
  "s_waitcnt vmcnt(16)\n"
  "s_barrier\n"
  "ds_read_b128 v[240:243], v2 offset:36864\n"
  "v_mfma_f32_16x16x32_bf16 v[212:215], v[244:247], a[204:207], v[212:215]\n"
  "v_mfma_f32_16x16x32_bf16 v[212:215], v[248:251], a[212:215], v[212:215]\n"
  "ds_read_b128 v[244:247], v2 offset:46080\n"
  "v_mfma_f32_16x16x32_bf16 v[212:215], v[252:255], a[220:223], v[212:215]\n"
  "s_waitcnt lgkmcnt(3)\n"
  "buffer_load_dwordx4 a[156:159], v1, s[36:39], s63 offen offset:3072 sc1\n"
  "v_mfma_f32_16x16x32_bf16 v[216:219], v[224:227], a[192:195], v[216:219]\n"
  "s_add_u32 m0, s67, 8192\n"
  "ds_read_b128 v[248:251], v2 offset:55296\n"
  "v_mfma_f32_16x16x32_bf16 v[216:219], v[228:231], a[200:203], v[216:219]\n"
  "s_add_u32 s66, s64, 118784\n"
  "v_mfma_f32_16x16x32_bf16 v[216:219], v[232:235], a[208:211], v[216:219]\n"
  "s_waitcnt lgkmcnt(2)\n"
  "ds_read_b128 v[252:255], v2 offset:64512\n"
  "v_mfma_f32_16x16x32_bf16 v[216:219], v[236:239], a[216:219], v[216:219]\n"
  "buffer_load_dwordx4 v1, s[36:39], s66 offen offset:0 sc1 lds\n"
  "v_mfma_f32_16x16x32_bf16 v[220:223], v[224:227], a[196:199], v[220:223]\n"
  "s_add_u32 m0, s67, 36864\n"
  "ds_read_b128 v[224:227], v2 offset:37888\n"
  "v_mfma_f32_16x16x32_bf16 v[220:223], v[228:231], a[204:207], v[220:223]\n"
  "s_add_u32 s66, s70, 147456\n"
  "v_mfma_f32_16x16x32_bf16 v[220:223], v[232:235], a[212:215], v[220:223]\n"
  "ds_read_b128 v[228:231], v2 offset:47104\n"
  "v_mfma_f32_16x16x32_bf16 v[220:223], v[236:239], a[220:223], v[220:223]\n"
  "v_mfma_f32_16x16x32_bf16 v[152:155], v[240:243], a[224:227], v[152:155]\n"
  "s_waitcnt lgkmcnt(2)\n"
  "ds_read_b128 v[232:235], v2 offset:56320\n"
  "v_add_u32 v2, 36864, v2\n"
  "v_mfma_f32_16x16x32_bf16 v[152:155], v[244:247], a[232:235], v[152:155]\n"
  "buffer_load_dwordx4 v1, s[36:39], s66 offen offset:0 sc1 lds\n"
  "v_mfma_f32_16x16x32_bf16 v[152:155], v[248:251], a[240:243], v[152:155]\n"
  "s_add_u32 s62, s85, 16384\n"
  "ds_read_b128 v[236:239], v2 offset:28672\n"
  "v_lshlrev_b32 v96, 16, v16\n"
  "v_mfma_f32_16x16x32_bf16 v[152:155], v[252:255], a[248:251], v[152:155]\n"
  "v_and_b32 v97, 0xffff0000, v16\n"
  "v_lshlrev_b32 v98, 16, v17\n"
  "v_mfma_f32_16x16x32_bf16 v[156:159], v[240:243], a[228:231], v[156:159]\n"
  "ds_read_b128 v[240:243], v2 offset:2048\n"
  "v_and_b32 v99, 0xffff0000, v17\n"
  "v_mfma_f32_16x16x32_bf16 v[156:159], v[244:247], a[236:239], v[156:159]\n"
  "buffer_load_dwordx4 a[160:163], v1, s[36:39], s62 offen offset:0 sc1\n"
  "v_mfma_f32_16x16x32_bf16 v[156:159], v[248:251], a[244:247], v[156:159]\n"
  "ds_read_b128 v[244:247], v2 offset:11264\n"
  "v_lshlrev_b32 v100, 16, v18\n"
  "v_mfma_f32_16x16x32_bf16 v[156:159], v[252:255], a[252:255], v[156:159]\n"
  "s_waitcnt lgkmcnt(2)\n"
  "v_and_b32 v101, 0xffff0000, v18\n"
  "v_lshlrev_b32 v102, 16, v19\n"
  "v_mfma_f32_16x16x32_bf16 v[160:163], v[224:227], a[224:227], v[160:163]\n"
  "ds_read_b128 v[248:251], v2 offset:20480\n"
  "v_and_b32 v103, 0xffff0000, v19\n"
  "v_mfma_f32_16x16x32_bf16 v[160:163], v[228:231], a[232:235], v[160:163]\n"
  "buffer_load_dwordx4 v1, s[36:39], s66 offen offset:1024 sc1 lds\n"
  "v_mfma_f32_16x16x32_bf16 v[160:163], v[232:235], a[240:243], v[160:163]\n"
  "s_add_u32 s63, s86, 16384\n"
  "ds_read_b128 v[252:255], v2 offset:29696\n"
  "v_lshlrev_b32 v104, 16, v20\n"
  "v_mfma_f32_16x16x32_bf16 v[160:163], v[236:239], a[248:251], v[160:163]\n"
  "v_and_b32 v105, 0xffff0000, v20\n"
  "v_lshlrev_b32 v106, 16, v21\n"
  "v_mfma_f32_16x16x32_bf16 v[164:167], v[224:227], a[228:231], v[164:167]\n"
  "ds_read_b128 v[224:227], v2 offset:3072\n"
  "v_and_b32 v107, 0xffff0000, v21\n"
  "v_mfma_f32_16x16x32_bf16 v[164:167], v[228:231], a[236:239], v[164:167]\n"
  "buffer_load_dwordx4 a[164:167], v1, s[36:39], s63 offen offset:0 sc1\n"
  "v_mfma_f32_16x16x32_bf16 v[164:167], v[232:235], a[244:247], v[164:167]\n"
  "s_add_u32 m0, s67, 38912\n"
  "ds_read_b128 v[228:231], v2 offset:12288\n"
  "v_lshlrev_b32 v108, 16, v44\n"
  "v_mfma_f32_16x16x32_bf16 v[164:167], v[236:239], a[252:255], v[164:167]\n"
  "s_add_u32 s66, s71, 147456\n"
  "v_and_b32 v109, 0xffff0000, v44\n"
  "v_lshlrev_b32 v110, 16, v45\n"
  "s_waitcnt lgkmcnt(2)\n"
  "v_mfma_f32_16x16x32_bf16 v[168:171], v[240:243], a[224:227], v[168:171]\n"
  "ds_read_b128 v[232:235], v2 offset:21504\n"
  "v_and_b32 v111, 0xffff0000, v45\n"
  "v_mfma_f32_16x16x32_bf16 v[168:171], v[244:247], a[232:235], v[168:171]\n"
  "buffer_load_dwordx4 v1, s[36:39], s66 offen offset:0 sc1 lds\n"
  "v_mfma_f32_16x16x32_bf16 v[168:171], v[248:251], a[240:243], v[168:171]\n"
  "ds_read_b128 v[236:239], v2 offset:30720\n"
  "v_lshlrev_b32 v112, 16, v46\n"
  "v_mfma_f32_16x16x32_bf16 v[168:171], v[252:255], a[248:251], v[168:171]\n"
  "v_and_b32 v113, 0xffff0000, v46\n"
  "v_lshlrev_b32 v114, 16, v47\n"
  "v_mfma_f32_16x16x32_bf16 v[172:175], v[240:243], a[228:231], v[172:175]\n"
  "ds_read_b128 v[240:243], v2 offset:4096\n"
  "v_and_b32 v115, 0xffff0000, v47\n"
  "v_mfma_f32_16x16x32_bf16 v[172:175], v[244:247], a[236:239], v[172:175]\n"
  "buffer_load_dwordx4 a[168:171], v1, s[36:39], s62 offen offset:1024 sc1\n"
  "v_mfma_f32_16x16x32_bf16 v[172:175], v[248:251], a[244:247], v[172:175]\n"
  "ds_read_b128 v[244:247], v2 offset:13312\n"
  "v_lshlrev_b32 v116, 16, v6\n"
  "v_mfma_f32_16x16x32_bf16 v[172:175], v[252:255], a[252:255], v[172:175]\n"
  "s_waitcnt lgkmcnt(2)\n"
  "v_and_b32 v117, 0xffff0000, v6\n"
  "v_lshlrev_b32 v118, 16, v7\n"
  "v_mfma_f32_16x16x32_bf16 v[176:179], v[224:227], a[224:227], v[176:179]\n"
  "ds_read_b128 v[248:251], v2 offset:22528\n"
  "v_and_b32 v119, 0xffff0000, v7\n"
  "v_mfma_f32_16x16x32_bf16 v[176:179], v[228:231], a[232:235], v[176:179]\n"
  "buffer_load_dwordx4 v1, s[36:39], s66 offen offset:1024 sc1 lds\n"
  "v_mfma_f32_16x16x32_bf16 v[176:179], v[232:235], a[240:243], v[176:179]\n"
  "ds_read_b128 v[252:255], v2 offset:31744\n"
  "v_mfma_f32_16x16x32_bf16 v[176:179], v[236:239], a[248:251], v[176:179]\n"
  "v_mfma_f32_16x16x32_bf16 v[180:183], v[224:227], a[228:231], v[180:183]\n"
  "ds_read_b128 v[224:227], v2 offset:5120\n"
  "v_mfma_f32_16x16x32_bf16 v[180:183], v[228:231], a[236:239], v[180:183]\n"
  "buffer_load_dwordx4 a[172:175], v1, s[36:39], s63 offen offset:1024 sc1\n"
  "v_mfma_f32_16x16x32_bf16 v[180:183], v[232:235], a[244:247], v[180:183]\n"
  "s_add_u32 m0, s67, 40960\n"
  "ds_read_b128 v[228:231], v2 offset:14336\n"
  "v_mfma_f32_16x16x32_bf16 v[180:183], v[236:239], a[252:255], v[180:183]\n"
  "s_add_u32 s66, s72, 147456\n"
  "buffer_load_dword v12, v13, s[36:39], s78 offen offset:0 sc1\n"
  "s_waitcnt lgkmcnt(3)\n"
  "v_mfma_f32_16x16x32_bf16 v[184:187], v[240:243], a[224:227], v[184:187]\n"
  "ds_read_b128 v[232:235], v2 offset:23552\n"
  "v_mfma_f32_16x16x32_bf16 v[184:187], v[244:247], a[232:235], v[184:187]\n"
  "buffer_load_dwordx4 v1, s[36:39], s66 offen offset:0 sc1 lds\n"
  "v_mfma_f32_16x16x32_bf16 v[184:187], v[248:251], a[240:243], v[184:187]\n"
  "s_waitcnt lgkmcnt(2)\n"
  "ds_read_b128 v[236:239], v2 offset:32768\n"
  "v_mfma_f32_16x16x32_bf16 v[184:187], v[252:255], a[248:251], v[184:187]\n"
  "v_mfma_f32_16x16x32_bf16 v[188:191], v[240:243], a[228:231], v[188:191]\n"
  "ds_read_b128 v[240:243], v2 offset:6144\n"
  "v_mfma_f32_16x16x32_bf16 v[188:191], v[244:247], a[236:239], v[188:191]\n"
  "v_mfma_f32_16x16x32_bf16 v[188:191], v[248:251], a[244:247], v[188:191]\n"
  "ds_read_b128 v[244:247], v2 offset:15360\n"
  "v_mfma_f32_16x16x32_bf16 v[188:191], v[252:255], a[252:255], v[188:191]\n"
  "buffer_load_dwordx4 a[176:179], v1, s[36:39], s62 offen offset:2048 sc1\n"
  "v_mfma_f32_16x16x32_bf16 v[192:195], v[224:227], a[224:227], v[192:195]\n"
  "s_waitcnt lgkmcnt(3)\n"
  "ds_read_b128 v[248:251], v2 offset:24576\n"
  "v_mfma_f32_16x16x32_bf16 v[192:195], v[228:231], a[232:235], v[192:195]\n"
  "v_mfma_f32_16x16x32_bf16 v[192:195], v[232:235], a[240:243], v[192:195]\n"
  "s_waitcnt lgkmcnt(3)\n"
  "ds_read_b128 v[252:255], v2 offset:33792\n"
  "v_mfma_f32_16x16x32_bf16 v[192:195], v[236:239], a[248:251], v[192:195]\n"
  "buffer_load_dwordx4 v1, s[36:39], s66 offen offset:1024 sc1 lds\n"
  "v_mfma_f32_16x16x32_bf16 v[196:199], v[224:227], a[228:231], v[196:199]\n"
  "ds_read_b128 v[224:227], v2 offset:7168\n"
  "v_mfma_f32_16x16x32_bf16 v[196:199], v[228:231], a[236:239], v[196:199]\n"
  "v_mfma_f32_16x16x32_bf16 v[196:199], v[232:235], a[244:247], v[196:199]\n"
  "ds_read_b128 v[228:231], v2 offset:16384\n"
  "v_mfma_f32_16x16x32_bf16 v[196:199], v[236:239], a[252:255], v[196:199]\n"
  "s_waitcnt lgkmcnt(3)\n"
  "buffer_load_dwordx4 a[180:183], v1, s[36:39], s63 offen offset:2048 sc1\n"
  "v_mfma_f32_16x16x32_bf16 v[200:203], v[240:243], a[224:227], v[200:203]\n"
  "s_add_u32 m0, s67, 43008\n"
  "ds_read_b128 v[232:235], v2 offset:25600\n"
  "v_mfma_f32_16x16x32_bf16 v[200:203], v[244:247], a[232:235], v[200:203]\n"
  "s_add_u32 s66, s35, 147456\n"
  "v_mfma_f32_16x16x32_bf16 v[200:203], v[248:251], a[240:243], v[200:203]\n"
  "s_waitcnt lgkmcnt(2)\n"
  "ds_read_b128 v[236:239], v2 offset:34816\n"
  "v_mfma_f32_16x16x32_bf16 v[200:203], v[252:255], a[248:251], v[200:203]\n"
  "buffer_load_dwordx4 v1, s[36:39], s66 offen offset:0 sc1 lds\n"
  "v_mfma_f32_16x16x32_bf16 v[204:207], v[240:243], a[228:231], v[204:207]\n"
  "ds_read_b128 v[240:243], v2 offset:8192\n"
  "v_mfma_f32_16x16x32_bf16 v[204:207], v[244:247], a[236:239], v[204:207]\n"
  "v_mfma_f32_16x16x32_bf16 v[204:207], v[248:251], a[244:247], v[204:207]\n"
  "ds_read_b128 v[244:247], v2 offset:17408\n"
  "v_mfma_f32_16x16x32_bf16 v[204:207], v[252:255], a[252:255], v[204:207]\n"
  "buffer_load_dwordx4 a[184:187], v1, s[36:39], s62 offen offset:3072 sc1\n"
  "v_mfma_f32_16x16x32_bf16 v[208:211], v[224:227], a[224:227], v[208:211]\n"
  "s_waitcnt lgkmcnt(3)\n"
  "ds_read_b128 v[248:251], v2 offset:26624\n"
  "v_mfma_f32_16x16x32_bf16 v[208:211], v[228:231], a[232:235], v[208:211]\n"
  "v_mfma_f32_16x16x32_bf16 v[208:211], v[232:235], a[240:243], v[208:211]\n"
  "s_waitcnt lgkmcnt(3)\n"
  "ds_read_b128 v[252:255], v2 offset:35840\n"
  "v_add_u32 v2, 0xfffe5000, v2\n"
  "v_mfma_f32_16x16x32_bf16 v[208:211], v[236:239], a[248:251], v[208:211]\n"
  "buffer_load_dwordx4 v1, s[36:39], s66 offen offset:1024 sc1 lds\n"
  "v_mfma_f32_16x16x32_bf16 v[212:215], v[224:227], a[228:231], v[212:215]\n"
  "s_waitcnt vmcnt(16)\n"
  "s_barrier\n"
  "ds_read_b128 v[224:227], v2 offset:0\n"
  "v_mfma_f32_16x16x32_bf16 v[212:215], v[228:231], a[236:239], v[212:215]\n"
  "v_mfma_f32_16x16x32_bf16 v[212:215], v[232:235], a[244:247], v[212:215]\n"
  "ds_read_b128 v[228:231], v2 offset:9216\n"
  "v_mfma_f32_16x16x32_bf16 v[212:215], v[236:239], a[252:255], v[212:215]\n"
  "s_waitcnt lgkmcnt(2)\n"
  "buffer_load_dwordx4 a[188:191], v1, s[36:39], s63 offen offset:3072 sc1\n"
  "v_mfma_f32_16x16x32_bf16 v[216:219], v[240:243], a[224:227], v[216:219]\n"
  "s_add_u32 m0, s67, 45056\n"
  "ds_read_b128 v[232:235], v2 offset:18432\n"
  "v_mfma_f32_16x16x32_bf16 v[216:219], v[244:247], a[232:235], v[216:219]\n"
  "s_add_u32 s66, s64, 155648\n"
  "v_mfma_f32_16x16x32_bf16 v[216:219], v[248:251], a[240:243], v[216:219]\n"
  "ds_read_b128 v[236:239], v2 offset:27648\n"
  "v_mfma_f32_16x16x32_bf16 v[216:219], v[252:255], a[248:251], v[216:219]\n"
  "buffer_load_dwordx4 v1, s[36:39], s66 offen offset:0 sc1 lds\n"
  "v_mfma_f32_16x16x32_bf16 v[220:223], v[240:243], a[228:231], v[220:223]\n"
  "s_add_u32 m0, s67, 73728\n"
  "ds_read_b128 v[240:243], v2 offset:1024\n"
  "v_mfma_f32_16x16x32_bf16 v[220:223], v[244:247], a[236:239], v[220:223]\n"
  "s_add_u32 s66, s70, 184320\n"
  "v_mfma_f32_16x16x32_bf16 v[220:223], v[248:251], a[244:247], v[220:223]\n"
  "ds_read_b128 v[244:247], v2 offset:10240\n"
  "v_mfma_f32_16x16x32_bf16 v[220:223], v[252:255], a[252:255], v[220:223]\n"
  "s_waitcnt lgkmcnt(3)\n"
  "v_mfma_f32_16x16x32_bf16 v[152:155], v[224:227], a[128:131], v[152:155]\n"
  "ds_read_b128 v[248:251], v2 offset:19456\n"
  "v_mfma_f32_16x16x32_bf16 v[152:155], v[228:231], a[136:139], v[152:155]\n"
  "buffer_load_dwordx4 v1, s[36:39], s66 offen offset:0 sc1 lds\n"
  "v_mfma_f32_16x16x32_bf16 v[152:155], v[232:235], a[144:147], v[152:155]\n"
  "s_add_u32 s62, s85, 20480\n"
  "ds_read_b128 v[252:255], v2 offset:28672\n"
  "s_waitcnt lgkmcnt(3)\n"
  "v_mfma_f32_16x16x32_bf16 v[152:155], v[236:239], a[152:155], v[152:155]\n"
  "s_add_u32 s83, s68, 1\n"
  "buffer_load_dwordx4 a[192:195], v1, s[36:39], s62 offen offset:0 sc1\n"
  "v_mfma_f32_16x16x32_bf16 v[156:159], v[224:227], a[132:135], v[156:159]\n"
  "s_and_b32 s83, s83, 3\n"
  "ds_read_b128 v[224:227], v2 offset:2048\n"
  "s_waitcnt vmcnt(11)\n"
  "v_cmp_ne_u32 vcc, s24, v12\n"
  "v_mfma_f32_16x16x32_bf16 v[156:159], v[228:231], a[140:143], v[156:159]\n"
  "s_nop 1\n s_cbranch_vccnz Ls3_%=\n Lr3_%=:\n"
  "v_mfma_f32_16x16x32_bf16 v[156:159], v[232:235], a[148:151], v[156:159]\n"
  "s_mul_i32 s83, s83, 12288\n"
  "ds_read_b128 v[228:231], v2 offset:11264\n"
  "v_mfma_f32_16x16x32_bf16 v[156:159], v[236:239], a[156:159], v[156:159]\n"
  "s_add_u32 s83, s83, s88\n"
  "buffer_load_dwordx4 v1, s[36:39], s66 offen offset:1024 sc1 lds\n"
  "v_mfma_f32_16x16x32_bf16 v[160:163], v[240:243], a[128:131], v[160:163]\n"
  "s_add_u32 s63, s86, 20480\n"
  "ds_read_b128 v[232:235], v2 offset:20480\n"
  "s_waitcnt lgkmcnt(2)\n"
  "v_mfma_f32_16x16x32_bf16 v[160:163], v[244:247], a[136:139], v[160:163]\n"
  "buffer_load_dwordx4 a[196:199], v1, s[36:39], s63 offen offset:0 sc1\n"
  "v_mfma_f32_16x16x32_bf16 v[160:163], v[248:251], a[144:147], v[160:163]\n"
  "s_add_u32 m0, s67, 75776\n"
  "ds_read_b128 v[236:239], v2 offset:29696\n"
  "v_mfma_f32_16x16x32_bf16 v[160:163], v[252:255], a[152:155], v[160:163]\n"
  "s_add_u32 s66, s71, 184320\n"
  "buffer_load_dwordx4 v[48:51], v1, s[36:39], s83 offen offset:0 sc1\n"
  "v_mfma_f32_16x16x32_bf16 v[164:167], v[240:243], a[132:135], v[164:167]\n"
  "ds_read_b128 v[240:243], v2 offset:3072\n"
  "v_mfma_f32_16x16x32_bf16 v[164:167], v[244:247], a[140:143], v[164:167]\n"
  "buffer_load_dwordx4 v1, s[36:39], s66 offen offset:0 sc1 lds\n"
  "v_mfma_f32_16x16x32_bf16 v[164:167], v[248:251], a[148:151], v[164:167]\n"
  "ds_read_b128 v[244:247], v2 offset:12288\n"
  "v_mfma_f32_16x16x32_bf16 v[164:167], v[252:255], a[156:159], v[164:167]\n"
  "buffer_load_dwordx4 v[52:55], v1, s[36:39], s83 offen offset:1024 sc1\n"
  "v_mfma_f32_16x16x32_bf16 v[168:171], v[224:227], a[128:131], v[168:171]\n"
  "s_waitcnt lgkmcnt(2)\n"
  "ds_read_b128 v[248:251], v2 offset:21504\n"
  "v_mfma_f32_16x16x32_bf16 v[168:171], v[228:231], a[136:139], v[168:171]\n"
  "buffer_load_dwordx4 a[200:203], v1, s[36:39], s62 offen offset:1024 sc1\n"
  "v_mfma_f32_16x16x32_bf16 v[168:171], v[232:235], a[144:147], v[168:171]\n"
  "ds_read_b128 v[252:255], v2 offset:30720\n"
  "v_mfma_f32_16x16x32_bf16 v[168:171], v[236:239], a[152:155], v[168:171]\n"
  "buffer_load_dwordx4 v1, s[36:39], s66 offen offset:1024 sc1 lds\n"
  "v_mfma_f32_16x16x32_bf16 v[172:175], v[224:227], a[132:135], v[172:175]\n"
  "ds_read_b128 v[224:227], v2 offset:4096\n"
  "v_mfma_f32_16x16x32_bf16 v[172:175], v[228:231], a[140:143], v[172:175]\n"
  "buffer_load_dwordx4 v[32:35], v31, s[36:39], s83 offen offset:2048 sc1\n"
  "v_mfma_f32_16x16x32_bf16 v[172:175], v[232:235], a[148:151], v[172:175]\n"
  "s_add_u32 s83, s68, 2\n"
  "ds_read_b128 v[228:231], v2 offset:13312\n"
  "v_mfma_f32_16x16x32_bf16 v[172:175], v[236:239], a[156:159], v[172:175]\n"
  "s_and_b32 s83, s83, 3\n"
  "buffer_load_dwordx4 a[204:207], v1, s[36:39], s63 offen offset:1024 sc1\n"
  "s_waitcnt lgkmcnt(3)\n"
  "v_mfma_f32_16x16x32_bf16 v[176:179], v[240:243], a[128:131], v[176:179]\n"
  "s_add_u32 m0, s67, 77824\n"
  "ds_read_b128 v[232:235], v2 offset:22528\n"
  "v_mfma_f32_16x16x32_bf16 v[176:179], v[244:247], a[136:139], v[176:179]\n"
  "s_add_u32 s66, s72, 184320\n"
  "v_mfma_f32_16x16x32_bf16 v[176:179], v[248:251], a[144:147], v[176:179]\n"
  "s_mul_i32 s83, s83, 12288\n"
  "ds_read_b128 v[236:239], v2 offset:31744\n"
  "s_waitcnt lgkmcnt(3)\n"
  "v_mfma_f32_16x16x32_bf16 v[176:179], v[252:255], a[152:155], v[176:179]\n"
  "s_add_u32 s83, s83, s88\n"
  "buffer_load_dwordx4 v1, s[36:39], s66 offen offset:0 sc1 lds\n"
  "v_mfma_f32_16x16x32_bf16 v[180:183], v[240:243], a[132:135], v[180:183]\n"
  "ds_read_b128 v[240:243], v2 offset:5120\n"
  "v_mfma_f32_16x16x32_bf16 v[180:183], v[244:247], a[140:143], v[180:183]\n"
  "buffer_load_dwordx4 a[208:211], v1, s[36:39], s62 offen offset:2048 sc1\n"
  "v_mfma_f32_16x16x32_bf16 v[180:183], v[248:251], a[148:151], v[180:183]\n"
  "ds_read_b128 v[244:247], v2 offset:14336\n"
  "v_mfma_f32_16x16x32_bf16 v[180:183], v[252:255], a[156:159], v[180:183]\n"
  "buffer_load_dwordx4 v[56:59], v1, s[36:39], s83 offen offset:0 sc1\n"
  "v_mfma_f32_16x16x32_bf16 v[184:187], v[224:227], a[128:131], v[184:187]\n"
  "s_waitcnt lgkmcnt(2)\n"
  "ds_read_b128 v[248:251], v2 offset:23552\n"
  "v_mfma_f32_16x16x32_bf16 v[184:187], v[228:231], a[136:139], v[184:187]\n"
  "buffer_load_dwordx4 v1, s[36:39], s66 offen offset:1024 sc1 lds\n"
  "v_mfma_f32_16x16x32_bf16 v[184:187], v[232:235], a[144:147], v[184:187]\n"
  "ds_read_b128 v[252:255], v2 offset:32768\n"
  "v_mfma_f32_16x16x32_bf16 v[184:187], v[236:239], a[152:155], v[184:187]\n"
  "buffer_load_dwordx4 a[212:215], v1, s[36:39], s63 offen offset:2048 sc1\n"
  "v_mfma_f32_16x16x32_bf16 v[188:191], v[224:227], a[132:135], v[188:191]\n"
  "s_add_u32 m0, s67, 79872\n"
  "ds_read_b128 v[224:227], v2 offset:6144\n"
  "v_mfma_f32_16x16x32_bf16 v[188:191], v[228:231], a[140:143], v[188:191]\n"
  "s_add_u32 s66, s35, 184320\n"
  "buffer_load_dwordx4 v[60:63], v1, s[36:39], s83 offen offset:1024 sc1\n"
  "v_mfma_f32_16x16x32_bf16 v[188:191], v[232:235], a[148:151], v[188:191]\n"
  "ds_read_b128 v[228:231], v2 offset:15360\n"
  "v_mfma_f32_16x16x32_bf16 v[188:191], v[236:239], a[156:159], v[188:191]\n"
  "s_waitcnt lgkmcnt(3)\n"
  "buffer_load_dwordx4 v1, s[36:39], s66 offen offset:0 sc1 lds\n"
  "v_mfma_f32_16x16x32_bf16 v[192:195], v[240:243], a[128:131], v[192:195]\n"
  "ds_read_b128 v[232:235], v2 offset:24576\n"
  "v_mfma_f32_16x16x32_bf16 v[192:195], v[244:247], a[136:139], v[192:195]\n"
  "buffer_load_dwordx4 v[36:39], v31, s[36:39], s83 offen offset:2048 sc1\n"
  "v_mfma_f32_16x16x32_bf16 v[192:195], v[248:251], a[144:147], v[192:195]\n"
  "s_add_u32 s83, s68, 3\n"
  "ds_read_b128 v[236:239], v2 offset:33792\n"
  "s_waitcnt lgkmcnt(3)\n"
  "v_mfma_f32_16x16x32_bf16 v[192:195], v[252:255], a[152:155], v[192:195]\n"
  "s_and_b32 s83, s83, 3\n"
  "buffer_load_dwordx4 a[216:219], v1, s[36:39], s62 offen offset:3072 sc1\n"
  "v_mfma_f32_16x16x32_bf16 v[196:199], v[240:243], a[132:135], v[196:199]\n"
  "s_mul_i32 s83, s83, 12288\n"
  "ds_read_b128 v[240:243], v2 offset:7168\n"
  "v_mfma_f32_16x16x32_bf16 v[196:199], v[244:247], a[140:143], v[196:199]\n"
  "s_add_u32 s83, s83, s88\n"
  "buffer_load_dwordx4 v1, s[36:39], s66 offen offset:1024 sc1 lds\n"
  "v_mfma_f32_16x16x32_bf16 v[196:199], v[248:251], a[148:151], v[196:199]\n"
  "ds_read_b128 v[244:247], v2 offset:16384\n"
  "v_mfma_f32_16x16x32_bf16 v[196:199], v[252:255], a[156:159], v[196:199]\n"
  "buffer_load_dwordx4 v[64:67], v1, s[36:39], s83 offen offset:0 sc1\n"
  "v_mfma_f32_16x16x32_bf16 v[200:203], v[224:227], a[128:131], v[200:203]\n"
  "s_waitcnt lgkmcnt(2)\n"
  "ds_read_b128 v[248:251], v2 offset:25600\n"
  "v_mfma_f32_16x16x32_bf16 v[200:203], v[228:231], a[136:139], v[200:203]\n"
  "buffer_load_dwordx4 a[220:223], v1, s[36:39], s63 offen offset:3072 sc1\n"
  "v_mfma_f32_16x16x32_bf16 v[200:203], v[232:235], a[144:147], v[200:203]\n"
  "s_add_u32 m0, s67, 81920\n"
  "ds_read_b128 v[252:255], v2 offset:34816\n"
  "v_mfma_f32_16x16x32_bf16 v[200:203], v[236:239], a[152:155], v[200:203]\n"
  "s_add_u32 s66, s64, 192512\n"
  "buffer_load_dwordx4 v[68:71], v1, s[36:39], s83 offen offset:1024 sc1\n"
  "v_mfma_f32_16x16x32_bf16 v[204:207], v[224:227], a[132:135], v[204:207]\n"
  "ds_read_b128 v[224:227], v2 offset:8192\n"
  "v_mfma_f32_16x16x32_bf16 v[204:207], v[228:231], a[140:143], v[204:207]\n"
  "buffer_load_dwordx4 v1, s[36:39], s66 offen offset:0 sc1 lds\n"
  "v_mfma_f32_16x16x32_bf16 v[204:207], v[232:235], a[148:151], v[204:207]\n"
  "s_add_u32 m0, s67, 110592\n"
  "ds_read_b128 v[228:231], v2 offset:17408\n"
  "v_mfma_f32_16x16x32_bf16 v[204:207], v[236:239], a[156:159], v[204:207]\n"
  "s_add_u32 s66, s70, 221184\n"
  "buffer_load_dwordx4 v[40:43], v31, s[36:39], s83 offen offset:2048 sc1\n"
  "s_waitcnt lgkmcnt(2)\n"
  "v_mfma_f32_16x16x32_bf16 v[208:211], v[240:243], a[128:131], v[208:211]\n"
  "ds_read_b128 v[232:235], v2 offset:26624\n"
  "v_mfma_f32_16x16x32_bf16 v[208:211], v[244:247], a[136:139], v[208:211]\n"
  "buffer_load_dwordx4 v1, s[36:39], s66 offen offset:0 sc1 lds\n"
  "v_mfma_f32_16x16x32_bf16 v[208:211], v[248:251], a[144:147], v[208:211]\n"
  "s_add_u32 s62, s85, 24576\n"
  "ds_read_b128 v[236:239], v2 offset:35840\n"
  "v_mfma_f32_16x16x32_bf16 v[208:211], v[252:255], a[152:155], v[208:211]\n"
  "buffer_load_dwordx4 a[224:227], v1, s[36:39], s62 offen offset:0 sc1\n"
  "v_mfma_f32_16x16x32_bf16 v[212:215], v[240:243], a[132:135], v[212:215]\n"
  "s_waitcnt vmcnt(28)\n"
  "s_barrier\n"
  "ds_read_b128 v[240:243], v2 offset:38912\n"
  "v_mfma_f32_16x16x32_bf16 v[212:215], v[244:247], a[140:143], v[212:215]\n"
  "v_mfma_f32_16x16x32_bf16 v[212:215], v[248:251], a[148:151], v[212:215]\n"
  "ds_read_b128 v[244:247], v2 offset:48128\n"
  "v_mfma_f32_16x16x32_bf16 v[212:215], v[252:255], a[156:159], v[212:215]\n"
  "s_waitcnt lgkmcnt(3)\n"
  "buffer_load_dwordx4 v1, s[36:39], s66 offen offset:1024 sc1 lds\n"
  "v_mfma_f32_16x16x32_bf16 v[216:219], v[224:227], a[128:131], v[216:219]\n"
  "s_add_u32 s63, s86, 24576\n"
  "ds_read_b128 v[248:251], v2 offset:57344\n"
  "v_add_u32 v2, 36864, v2\n"
  "v_mfma_f32_16x16x32_bf16 v[216:219], v[228:231], a[136:139], v[216:219]\n"
  "buffer_load_dwordx4 a[228:231], v1, s[36:39], s63 offen offset:0 sc1\n"
  "v_mfma_f32_16x16x32_bf16 v[216:219], v[232:235], a[144:147], v[216:219]\n"
  "s_add_u32 m0, s67, 112640\n"
  "ds_read_b128 v[252:255], v2 offset:29696\n"
  "s_waitcnt lgkmcnt(3)\n"
  "v_mfma_f32_16x16x32_bf16 v[216:219], v[236:239], a[152:155], v[216:219]\n"
  "s_add_u32 s66, s71, 221184\n"
  "v_mfma_f32_16x16x32_bf16 v[220:223], v[224:227], a[132:135], v[220:223]\n"
  "buffer_load_dwordx4 v1, s[36:39], s66 offen offset:0 sc1 lds\n"
  "v_mfma_f32_16x16x32_bf16 v[220:223], v[228:231], a[140:143], v[220:223]\n"
  "v_mfma_f32_16x16x32_bf16 v[220:223], v[232:235], a[148:151], v[220:223]\n"
  "v_mfma_f32_16x16x32_bf16 v[220:223], v[236:239], a[156:159], v[220:223]\n"
  "buffer_load_dwordx4 a[232:235], v1, s[36:39], s62 offen offset:1024 sc1\n"
  "s_barrier\n"
  "v_mfma_f32_16x16x32_bf16 v[168:171], v[240:243], a[160:163], v[168:171]\n"
  "s_waitcnt lgkmcnt(1)\n"
  "v_mfma_f32_16x16x32_bf16 v[168:171], v[244:247], a[168:171], v[168:171]\n"
  "v_mfma_f32_16x16x32_bf16 v[168:171], v[248:251], a[176:179], v[168:171]\n"
  "s_waitcnt lgkmcnt(0)\n"
  "v_mfma_f32_16x16x32_bf16 v[168:171], v[252:255], a[184:187], v[168:171]\n"
  "buffer_load_dwordx4 v1, s[36:39], s66 offen offset:1024 sc1 lds\n"
  "v_mfma_f32_16x16x32_bf16 v[172:175], v[240:243], a[164:167], v[172:175]\n"
  "v_mfma_f32_16x16x32_bf16 v[172:175], v[244:247], a[172:175], v[172:175]\n"
  "v_mfma_f32_16x16x32_bf16 v[172:175], v[248:251], a[180:183], v[172:175]\n"
  "buffer_load_dwordx4 a[236:239], v1, s[36:39], s63 offen offset:1024 sc1\n"
  "v_mfma_f32_16x16x32_bf16 v[172:175], v[252:255], a[188:191], v[172:175]\n"
  "s_add_u32 m0, s67, 114688\n"
  "s_waitcnt vmcnt(29)\n"
  "v_mfma_f32_16x16x32_bf16 v[80:83], v[26:29], v[48:51], v[80:83]\n"
  "s_add_u32 s66, s72, 221184\n"
  "s_waitcnt vmcnt(20)\n"
  "v_mfma_f32_16x16x32_bf16 v[80:83], v[26:29], v[56:59], v[80:83]\n"
  "s_waitcnt vmcnt(12)\n"
  "buffer_load_dwordx4 v1, s[36:39], s66 offen offset:0 sc1 lds\n"
  "v_mfma_f32_16x16x32_bf16 v[80:83], v[26:29], v[64:67], v[80:83]\n"
  "v_mfma_f32_16x16x32_bf16 v[84:87], v[26:29], v[52:55], v[84:87]\n"
  "v_mfma_f32_16x16x32_bf16 v[84:87], v[26:29], v[60:63], v[84:87]\n"
  "s_waitcnt vmcnt(11)\n"
  "buffer_load_dwordx4 a[240:243], v1, s[36:39], s62 offen offset:2048 sc1\n"
  "v_mfma_f32_16x16x32_bf16 v[84:87], v[26:29], v[68:71], v[84:87]\n"
  "v_mfma_f32_16x16x32_bf16 v[88:91], v[24:27], v[48:51], v[88:91]\n"
  "ds_read_b128 v[224:227], v2 offset:3072\n"
  "v_mfma_f32_16x16x32_bf16 v[88:91], v[24:27], v[56:59], v[88:91]\n"
  "v_mfma_f32_16x16x32_bf16 v[88:91], v[24:27], v[64:67], v[88:91]\n"
  "ds_read_b128 v[228:231], v2 offset:12288\n"
  "v_mfma_f32_16x16x32_bf16 v[92:95], v[24:27], v[52:55], v[92:95]\n"
  "buffer_load_dwordx4 v1, s[36:39], s66 offen offset:1024 sc1 lds\n"
  "v_mfma_f32_16x16x32_bf16 v[92:95], v[24:27], v[60:63], v[92:95]\n"
  "ds_read_b128 v[232:235], v2 offset:21504\n"
  "v_mfma_f32_16x16x32_bf16 v[92:95], v[24:27], v[68:71], v[92:95]\n"
  "buffer_load_dwordx4 a[244:247], v1, s[36:39], s63 offen offset:2048 sc1\n"
  "v_mfma_f32_16x16x32_bf16 v[144:147], v[26:29], v[32:35], v[144:147]\n"
  "s_add_u32 m0, s67, 116736\n"
  "ds_read_b128 v[236:239], v2 offset:30720\n"
  "v_mfma_f32_16x16x32_bf16 v[144:147], v[26:29], v[36:39], v[144:147]\n"
  "s_add_u32 s66, s35, 221184\n"
  "s_waitcnt vmcnt(12)\n"
  "v_mfma_f32_16x16x32_bf16 v[144:147], v[26:29], v[40:43], v[144:147]\n"
  "ds_read_b128 v[240:243], v2 offset:4096\n"
  "v_mfma_f32_16x16x32_bf16 v[148:151], v[24:27], v[32:35], v[148:151]\n"
  "buffer_load_dwordx4 v1, s[36:39], s66 offen offset:0 sc1 lds\n"
  "v_mfma_f32_16x16x32_bf16 v[148:151], v[24:27], v[36:39], v[148:151]\n"
  "ds_read_b128 v[244:247], v2 offset:13312\n"
  "v_mfma_f32_16x16x32_bf16 v[148:151], v[24:27], v[40:43], v[148:151]\n"
  "s_waitcnt lgkmcnt(3)\n"
  "buffer_load_dwordx4 a[248:251], v1, s[36:39], s62 offen offset:3072 sc1\n"
  "v_mfma_f32_16x16x32_bf16 v[176:179], v[224:227], a[160:163], v[176:179]\n"
  "ds_read_b128 v[248:251], v2 offset:22528\n"
  "v_mfma_f32_16x16x32_bf16 v[176:179], v[228:231], a[168:171], v[176:179]\n"
  "v_mfma_f32_16x16x32_bf16 v[176:179], v[232:235], a[176:179], v[176:179]\n"
  "s_waitcnt lgkmcnt(3)\n"
  "ds_read_b128 v[252:255], v2 offset:31744\n"
  "v_mfma_f32_16x16x32_bf16 v[176:179], v[236:239], a[184:187], v[176:179]\n"
  "s_add_u32 s88, s88, 12582912\n"
  "buffer_load_dwordx4 v1, s[36:39], s66 offen offset:1024 sc1 lds\n"
  "v_mfma_f32_16x16x32_bf16 v[180:183], v[224:227], a[164:167], v[180:183]\n"
  "ds_read_b128 v[224:227], v2 offset:5120\n"
  "v_mul_f32 v72, 0xbfb8aa3b, v80\n"
  "v_mfma_f32_16x16x32_bf16 v[180:183], v[228:231], a[172:175], v[180:183]\n"
  "v_mul_f32 v73, 0xbfb8aa3b, v81\n"
  "v_mul_f32 v74, 0xbfb8aa3b, v82\n"
  "v_mfma_f32_16x16x32_bf16 v[180:183], v[232:235], a[180:183], v[180:183]\n"
  "ds_read_b128 v[228:231], v2 offset:14336\n"
  "v_mul_f32 v75, 0xbfb8aa3b, v83\n"
  "v_mfma_f32_16x16x32_bf16 v[180:183], v[236:239], a[188:191], v[180:183]\n"
  "s_waitcnt lgkmcnt(2)\n"
  "buffer_load_dwordx4 a[252:255], v1, s[36:39], s63 offen offset:3072 sc1\n"
  "v_mfma_f32_16x16x32_bf16 v[184:187], v[240:243], a[160:163], v[184:187]\n"
  "s_add_u32 m0, s67, 118784\n"
  "ds_read_b128 v[232:235], v2 offset:23552\n"
  "v_mfma_f32_16x16x32_bf16 v[184:187], v[244:247], a[168:171], v[184:187]\n"
  "s_add_u32 s66, s64, 229376\n"
  "v_exp_f32 v72, v72\n"
  "v_mfma_f32_16x16x32_bf16 v[184:187], v[248:251], a[176:179], v[184:187]\n"
  "ds_read_b128 v[236:239], v2 offset:32768\n"
  "v_mfma_f32_16x16x32_bf16 v[184:187], v[252:255], a[184:187], v[184:187]\n"
  "buffer_load_dwordx4 v1, s[36:39], s66 offen offset:0 sc1 lds\n"
  "v_mfma_f32_16x16x32_bf16 v[188:191], v[240:243], a[164:167], v[188:191]\n"
  "s_add_u32 m0, s67, 0\n"
  "ds_read_b128 v[240:243], v2 offset:6144\n"
  "v_mfma_f32_16x16x32_bf16 v[188:191], v[244:247], a[172:175], v[188:191]\n"
  "s_add_u32 s66, s70, 258048\n"
  "v_exp_f32 v73, v73\n"
  "v_mfma_f32_16x16x32_bf16 v[188:191], v[248:251], a[180:183], v[188:191]\n"
  "ds_read_b128 v[244:247], v2 offset:15360\n"
  "v_mfma_f32_16x16x32_bf16 v[188:191], v[252:255], a[188:191], v[188:191]\n"
  "s_waitcnt lgkmcnt(3)\n"
  "buffer_load_dwordx4 v1, s[36:39], s66 offen offset:0 sc1 lds\n"
  "v_mfma_f32_16x16x32_bf16 v[192:195], v[224:227], a[160:163], v[192:195]\n"
  "s_add_u32 s62, s85, 28672\n"
  "ds_read_b128 v[248:251], v2 offset:24576\n"
  "v_mfma_f32_16x16x32_bf16 v[192:195], v[228:231], a[168:171], v[192:195]\n"
  "buffer_load_dwordx4 a[128:131], v1, s[36:39], s62 offen offset:0 sc1\n"
  "v_mfma_f32_16x16x32_bf16 v[192:195], v[232:235], a[176:179], v[192:195]\n"
  "s_waitcnt lgkmcnt(2)\n"
  "ds_read_b128 v[252:255], v2 offset:33792\n"
  "v_mfma_f32_16x16x32_bf16 v[192:195], v[236:239], a[184:187], v[192:195]\n"
  "buffer_load_dwordx4 v1, s[36:39], s66 offen offset:1024 sc1 lds\n"
  "v_mfma_f32_16x16x32_bf16 v[196:199], v[224:227], a[164:167], v[196:199]\n"
  "s_add_u32 s63, s86, 28672\n"
  "ds_read_b128 v[224:227], v2 offset:7168\n"
  "v_mfma_f32_16x16x32_bf16 v[196:199], v[228:231], a[172:175], v[196:199]\n"
  "v_exp_f32 v74, v74\n"
  "v_mfma_f32_16x16x32_bf16 v[196:199], v[232:235], a[180:183], v[196:199]\n"
  "ds_read_b128 v[228:231], v2 offset:16384\n"
  "v_mfma_f32_16x16x32_bf16 v[196:199], v[236:239], a[188:191], v[196:199]\n"
  "buffer_load_dwordx4 a[132:135], v1, s[36:39], s63 offen offset:0 sc1\n"
  "v_mfma_f32_16x16x32_bf16 v[200:203], v[240:243], a[160:163], v[200:203]\n"
  "s_add_u32 m0, s67, 2048\n"
  "ds_read_b128 v[232:235], v2 offset:25600\n"
  "s_waitcnt lgkmcnt(3)\n"
  "v_mfma_f32_16x16x32_bf16 v[200:203], v[244:247], a[168:171], v[200:203]\n"
  "s_add_u32 s66, s71, 258048\n"
  "v_exp_f32 v75, v75\n"
  "v_mfma_f32_16x16x32_bf16 v[200:203], v[248:251], a[176:179], v[200:203]\n"
  "ds_read_b128 v[236:239], v2 offset:34816\n"
  "v_add_f32 v72, 1.0, v72\n"
  "v_mfma_f32_16x16x32_bf16 v[200:203], v[252:255], a[184:187], v[200:203]\n"
  "buffer_load_dwordx4 v1, s[36:39], s66 offen offset:0 sc1 lds\n"
  "v_mfma_f32_16x16x32_bf16 v[204:207], v[240:243], a[164:167], v[204:207]\n"
  "ds_read_b128 v[240:243], v2 offset:8192\n"
  "v_add_f32 v73, 1.0, v73\n"
  "v_mfma_f32_16x16x32_bf16 v[204:207], v[244:247], a[172:175], v[204:207]\n"
  "v_add_f32 v74, 1.0, v74\n"
  "v_add_f32 v75, 1.0, v75\n"
  "v_mfma_f32_16x16x32_bf16 v[204:207], v[248:251], a[180:183], v[204:207]\n"
  "ds_read_b128 v[244:247], v2 offset:17408\n"
  "v_mfma_f32_16x16x32_bf16 v[204:207], v[252:255], a[188:191], v[204:207]\n"
  "s_waitcnt lgkmcnt(2)\n"
  "buffer_load_dwordx4 a[136:139], v1, s[36:39], s62 offen offset:1024 sc1\n"
  "v_mfma_f32_16x16x32_bf16 v[208:211], v[224:227], a[160:163], v[208:211]\n"
  "ds_read_b128 v[248:251], v2 offset:26624\n"
  "v_mfma_f32_16x16x32_bf16 v[208:211], v[228:231], a[168:171], v[208:211]\n"
  "v_rcp_f32 v72, v72\n"
  "v_mfma_f32_16x16x32_bf16 v[208:211], v[232:235], a[176:179], v[208:211]\n"
  "ds_read_b128 v[252:255], v2 offset:35840\n"
  "v_mfma_f32_16x16x32_bf16 v[208:211], v[236:239], a[184:187], v[208:211]\n"
  "buffer_load_dwordx4 v1, s[36:39], s66 offen offset:1024 sc1 lds\n"
  "v_mfma_f32_16x16x32_bf16 v[212:215], v[224:227], a[164:167], v[212:215]\n"
  "s_barrier\n"
  "ds_read_b128 v[224:227], v2 offset:38912\n"
  "v_mfma_f32_16x16x32_bf16 v[212:215], v[228:231], a[172:175], v[212:215]\n"
  "v_rcp_f32 v73, v73\n"
  "v_mfma_f32_16x16x32_bf16 v[212:215], v[232:235], a[180:183], v[212:215]\n"
  "ds_read_b128 v[228:231], v2 offset:48128\n"
  "v_mfma_f32_16x16x32_bf16 v[212:215], v[236:239], a[188:191], v[212:215]\n"
  "s_waitcnt lgkmcnt(3)\n"
  "buffer_load_dwordx4 a[140:143], v1, s[36:39], s63 offen offset:1024 sc1\n"
  "v_mfma_f32_16x16x32_bf16 v[216:219], v[240:243], a[160:163], v[216:219]\n"
  "s_add_u32 m0, s67, 4096\n"
  "ds_read_b128 v[232:235], v2 offset:57344\n"
  "v_add_u32 v2, 36864, v2\n"
  "v_mfma_f32_16x16x32_bf16 v[216:219], v[244:247], a[168:171], v[216:219]\n"
  "s_add_u32 s66, s72, 258048\n"
  "v_rcp_f32 v74, v74\n"
  "v_mfma_f32_16x16x32_bf16 v[216:219], v[248:251], a[176:179], v[216:219]\n"
  "s_waitcnt lgkmcnt(2)\n"
  "ds_read_b128 v[236:239], v2 offset:29696\n"
  "v_mfma_f32_16x16x32_bf16 v[216:219], v[252:255], a[184:187], v[216:219]\n"
  "buffer_load_dwordx4 v1, s[36:39], s66 offen offset:0 sc1 lds\n"
  "v_mfma_f32_16x16x32_bf16 v[220:223], v[240:243], a[164:167], v[220:223]\n"
  "ds_read_b128 v[240:243], v2 offset:3072\n"
  "v_mfma_f32_16x16x32_bf16 v[220:223], v[244:247], a[172:175], v[220:223]\n"
  "v_rcp_f32 v75, v75\n"
  "v_mfma_f32_16x16x32_bf16 v[220:223], v[248:251], a[180:183], v[220:223]\n"
  "ds_read_b128 v[244:247], v2 offset:12288\n"
  "v_mul_f32 v80, v80, v72\n"
  "v_mfma_f32_16x16x32_bf16 v[220:223], v[252:255], a[188:191], v[220:223]\n"
  "buffer_load_dwordx4 a[144:147], v1, s[36:39], s62 offen offset:2048 sc1\n"
  "v_mfma_f32_16x16x32_bf16 v[168:171], v[224:227], a[192:195], v[168:171]\n"
  "s_waitcnt lgkmcnt(2)\n"
  "ds_read_b128 v[248:251], v2 offset:21504\n"
  "v_mul_f32 v81, v81, v73\n"
  "v_mfma_f32_16x16x32_bf16 v[168:171], v[228:231], a[200:203], v[168:171]\n"
  "v_mul_f32 v82, v82, v74\n"
  "v_mul_f32 v83, v83, v75\n"
  "v_mfma_f32_16x16x32_bf16 v[168:171], v[232:235], a[208:211], v[168:171]\n"
  "ds_read_b128 v[252:255], v2 offset:30720\n"
  "v_mul_f32 v76, 0xbfb8aa3b, v88\n"
  "v_mfma_f32_16x16x32_bf16 v[168:171], v[236:239], a[216:219], v[168:171]\n"
  "buffer_load_dwordx4 v1, s[36:39], s66 offen offset:1024 sc1 lds\n"
  "v_mfma_f32_16x16x32_bf16 v[172:175], v[224:227], a[196:199], v[172:175]\n"
  "ds_read_b128 v[224:227], v2 offset:4096\n"
  "v_mul_f32 v77, 0xbfb8aa3b, v89\n"
  "v_mfma_f32_16x16x32_bf16 v[172:175], v[228:231], a[204:207], v[172:175]\n"
  "v_mul_f32 v78, 0xbfb8aa3b, v90\n"
  "v_mul_f32 v79, 0xbfb8aa3b, v91\n"
  "v_mfma_f32_16x16x32_bf16 v[172:175], v[232:235], a[212:215], v[172:175]\n"
  "ds_read_b128 v[228:231], v2 offset:13312\n"
  "v_mfma_f32_16x16x32_bf16 v[172:175], v[236:239], a[220:223], v[172:175]\n"
  "s_waitcnt lgkmcnt(2)\n"
  "buffer_load_dwordx4 a[148:151], v1, s[36:39], s63 offen offset:2048 sc1\n"
  "v_mfma_f32_16x16x32_bf16 v[176:179], v[240:243], a[192:195], v[176:179]\n"
  "s_add_u32 m0, s67, 6144\n"
  "ds_read_b128 v[232:235], v2 offset:22528\n"
  "v_mfma_f32_16x16x32_bf16 v[176:179], v[244:247], a[200:203], v[176:179]\n"
  "s_add_u32 s66, s35, 258048\n"
  "v_exp_f32 v76, v76\n"
  "v_mfma_f32_16x16x32_bf16 v[176:179], v[248:251], a[208:211], v[176:179]\n"
  "ds_read_b128 v[236:239], v2 offset:31744\n"
  "v_mfma_f32_16x16x32_bf16 v[176:179], v[252:255], a[216:219], v[176:179]\n"
  "buffer_load_dwordx4 v1, s[36:39], s66 offen offset:0 sc1 lds\n"
  "v_mfma_f32_16x16x32_bf16 v[180:183], v[240:243], a[196:199], v[180:183]\n"
  "s_waitcnt vmcnt(13)\n"
  "s_barrier\n"
  "ds_read_b128 v[240:243], v2 offset:38912\n"
  "v_mfma_f32_16x16x32_bf16 v[180:183], v[244:247], a[204:207], v[180:183]\n"
  "buffer_load_dwordx4 a[152:155], v1, s[36:39], s62 offen offset:3072 sc1\n"
  "v_mfma_f32_16x16x32_bf16 v[180:183], v[248:251], a[212:215], v[180:183]\n"
  "ds_read_b128 v[244:247], v2 offset:48128\n"
  "v_mfma_f32_16x16x32_bf16 v[180:183], v[252:255], a[220:223], v[180:183]\n"
  "s_waitcnt lgkmcnt(3)\n"
  "v_exp_f32 v77, v77\n"
  "v_mfma_f32_16x16x32_bf16 v[184:187], v[224:227], a[192:195], v[184:187]\n"
  "ds_read_b128 v[248:251], v2 offset:57344\n"
  "v_add_u32 v2, 36864, v2\n"
  "v_mfma_f32_16x16x32_bf16 v[184:187], v[228:231], a[200:203], v[184:187]\n"
  "buffer_load_dwordx4 v1, s[36:39], s66 offen offset:1024 sc1 lds\n"
  "v_mfma_f32_16x16x32_bf16 v[184:187], v[232:235], a[208:211], v[184:187]\n"
  "s_waitcnt lgkmcnt(2)\n"
  "ds_read_b128 v[252:255], v2 offset:29696\n"
  "v_mfma_f32_16x16x32_bf16 v[184:187], v[236:239], a[216:219], v[184:187]\n"
  "v_exp_f32 v78, v78\n"
  "v_mfma_f32_16x16x32_bf16 v[188:191], v[224:227], a[196:199], v[188:191]\n"
  "ds_read_b128 v[224:227], v2 offset:3072\n"
  "v_mfma_f32_16x16x32_bf16 v[188:191], v[228:231], a[204:207], v[188:191]\n"
  "buffer_load_dwordx4 a[156:159], v1, s[36:39], s63 offen offset:3072 sc1\n"
  "v_mfma_f32_16x16x32_bf16 v[188:191], v[232:235], a[212:215], v[188:191]\n"
  "s_add_u32 m0, s67, 8192\n"
  "ds_read_b128 v[228:231], v2 offset:12288\n"
  "v_mfma_f32_16x16x32_bf16 v[188:191], v[236:239], a[220:223], v[188:191]\n"
  "s_add_u32 s66, s64, 266240\n"
  "v_exp_f32 v79, v79\n"
  "v_mfma_f32_16x16x32_bf16 v[168:171], v[240:243], a[224:227], v[168:171]\n"
  "s_waitcnt lgkmcnt(2)\n"
  "ds_read_b128 v[232:235], v2 offset:21504\n"
  "v_add_f32 v76, 1.0, v76\n"
  "v_mfma_f32_16x16x32_bf16 v[168:171], v[244:247], a[232:235], v[168:171]\n"
  "buffer_load_dwordx4 v1, s[36:39], s66 offen offset:0 sc1 lds\n"
  "v_mfma_f32_16x16x32_bf16 v[168:171], v[248:251], a[240:243], v[168:171]\n"
  "ds_read_b128 v[236:239], v2 offset:30720\n"
  "v_add_u32 v2, 0xffff7000, v2\n"
  "v_mfma_f32_16x16x32_bf16 v[168:171], v[252:255], a[248:251], v[168:171]\n"
  "v_add_f32 v77, 1.0, v77\n"
  "v_add_f32 v78, 1.0, v78\n"
  "v_mfma_f32_16x16x32_bf16 v[172:175], v[240:243], a[228:231], v[172:175]\n"
  "ds_read_b128 v[240:243], v2 offset:5120\n"
  "v_add_f32 v79, 1.0, v79\n"
  "v_mfma_f32_16x16x32_bf16 v[172:175], v[244:247], a[236:239], v[172:175]\n"
  "v_rcp_f32 v76, v76\n"
  "v_mfma_f32_16x16x32_bf16 v[172:175], v[248:251], a[244:247], v[172:175]\n"
  "ds_read_b128 v[244:247], v2 offset:14336\n"
  "v_mfma_f32_16x16x32_bf16 v[172:175], v[252:255], a[252:255], v[172:175]\n"
  "s_waitcnt lgkmcnt(2)\n"
  "v_rcp_f32 v77, v77\n"
  "v_mfma_f32_16x16x32_bf16 v[176:179], v[224:227], a[224:227], v[176:179]\n"
  "ds_read_b128 v[248:251], v2 offset:23552\n"
  "v_mfma_f32_16x16x32_bf16 v[176:179], v[228:231], a[232:235], v[176:179]\n"
  "v_rcp_f32 v78, v78\n"
  "v_mfma_f32_16x16x32_bf16 v[176:179], v[232:235], a[240:243], v[176:179]\n"
  "ds_read_b128 v[252:255], v2 offset:32768\n"
  "v_mfma_f32_16x16x32_bf16 v[176:179], v[236:239], a[248:251], v[176:179]\n"
  "v_rcp_f32 v79, v79\n"
  "v_mfma_f32_16x16x32_bf16 v[180:183], v[224:227], a[228:231], v[180:183]\n"
  "ds_read_b128 v[224:227], v2 offset:40960\n"
  "v_mul_f32 v88, v88, v76\n"
  "v_mfma_f32_16x16x32_bf16 v[180:183], v[228:231], a[236:239], v[180:183]\n"
  "v_mul_f32 v89, v89, v77\n"
  "v_mul_f32 v90, v90, v78\n"
  "v_mfma_f32_16x16x32_bf16 v[180:183], v[232:235], a[244:247], v[180:183]\n"
  "ds_read_b128 v[228:231], v2 offset:50176\n"
  "v_mul_f32 v91, v91, v79\n"
  "v_mfma_f32_16x16x32_bf16 v[180:183], v[236:239], a[252:255], v[180:183]\n"
  "s_waitcnt lgkmcnt(2)\n"
  "v_mul_f32 v72, 0xbfb8aa3b, v84\n"
  "v_mul_f32 v73, 0xbfb8aa3b, v85\n"
  "v_mfma_f32_16x16x32_bf16 v[192:195], v[240:243], a[192:195], v[192:195]\n"
  "ds_read_b128 v[232:235], v2 offset:59392\n"
  "v_add_u32 v2, 36864, v2\n"
  "v_mfma_f32_16x16x32_bf16 v[192:195], v[244:247], a[200:203], v[192:195]\n"
  "v_mul_f32 v74, 0xbfb8aa3b, v86\n"
  "v_mul_f32 v75, 0xbfb8aa3b, v87\n"
  "v_mfma_f32_16x16x32_bf16 v[192:195], v[248:251], a[208:211], v[192:195]\n"
  "ds_read_b128 v[236:239], v2 offset:31744\n"
  "v_mfma_f32_16x16x32_bf16 v[192:195], v[252:255], a[216:219], v[192:195]\n"
  "v_exp_f32 v72, v72\n"
  "v_mfma_f32_16x16x32_bf16 v[196:199], v[240:243], a[196:199], v[196:199]\n"
  "ds_read_b128 v[240:243], v2 offset:5120\n"
  "v_mfma_f32_16x16x32_bf16 v[196:199], v[244:247], a[204:207], v[196:199]\n"
  "v_exp_f32 v73, v73\n"
  "v_mfma_f32_16x16x32_bf16 v[196:199], v[248:251], a[212:215], v[196:199]\n"
  "ds_read_b128 v[244:247], v2 offset:14336\n"
  "v_mfma_f32_16x16x32_bf16 v[196:199], v[252:255], a[220:223], v[196:199]\n"
  "s_waitcnt lgkmcnt(3)\n"
  "v_exp_f32 v74, v74\n"
  "v_mfma_f32_16x16x32_bf16 v[184:187], v[224:227], a[224:227], v[184:187]\n"
  "ds_read_b128 v[248:251], v2 offset:23552\n"
  "v_mfma_f32_16x16x32_bf16 v[184:187], v[228:231], a[232:235], v[184:187]\n"
  "v_exp_f32 v75, v75\n"
  "v_mfma_f32_16x16x32_bf16 v[184:187], v[232:235], a[240:243], v[184:187]\n"
  "s_waitcnt lgkmcnt(2)\n"
  "ds_read_b128 v[252:255], v2 offset:32768\n"
  "v_add_u32 v2, 0xfffe5000, v2\n"
  "v_mfma_f32_16x16x32_bf16 v[184:187], v[236:239], a[248:251], v[184:187]\n"
  "v_add_f32 v72, 1.0, v72\n"
  "v_add_f32 v73, 1.0, v73\n"
  "v_mfma_f32_16x16x32_bf16 v[188:191], v[224:227], a[228:231], v[188:191]\n"
  "s_waitcnt vmcnt(0)\n"
  "s_barrier\n"
  "ds_read_b128 v[224:227], v2 offset:2048\n"
  "v_add_f32 v74, 1.0, v74\n"
  "v_mfma_f32_16x16x32_bf16 v[188:191], v[228:231], a[236:239], v[188:191]\n"
  "v_add_f32 v75, 1.0, v75\n"
  "v_mfma_f32_16x16x32_bf16 v[188:191], v[232:235], a[244:247], v[188:191]\n"
  "ds_read_b128 v[228:231], v2 offset:11264\n"
  "v_mfma_f32_16x16x32_bf16 v[188:191], v[236:239], a[252:255], v[188:191]\n"
  "v_rcp_f32 v72, v72\n"
  "v_mfma_f32_16x16x32_bf16 v[192:195], v[240:243], a[224:227], v[192:195]\n"
  "s_waitcnt lgkmcnt(2)\n"
  "ds_read_b128 v[232:235], v2 offset:20480\n"
  "v_mfma_f32_16x16x32_bf16 v[192:195], v[244:247], a[232:235], v[192:195]\n"
  "v_rcp_f32 v73, v73\n"
  "v_mfma_f32_16x16x32_bf16 v[192:195], v[248:251], a[240:243], v[192:195]\n"
  "ds_read_b128 v[236:239], v2 offset:29696\n"
  "v_mfma_f32_16x16x32_bf16 v[192:195], v[252:255], a[248:251], v[192:195]\n"
  "v_rcp_f32 v74, v74\n"
  "v_mfma_f32_16x16x32_bf16 v[196:199], v[240:243], a[228:231], v[196:199]\n"
  "ds_read_b128 v[240:243], v2 offset:3072\n"
  "v_mfma_f32_16x16x32_bf16 v[196:199], v[244:247], a[236:239], v[196:199]\n"
  "v_rcp_f32 v75, v75\n"
  "v_mfma_f32_16x16x32_bf16 v[196:199], v[248:251], a[244:247], v[196:199]\n"
  "ds_read_b128 v[244:247], v2 offset:12288\n"
  "v_mul_f32 v84, v84, v72\n"
  "v_mfma_f32_16x16x32_bf16 v[196:199], v[252:255], a[252:255], v[196:199]\n"
  "s_waitcnt lgkmcnt(3)\n"
  "v_mul_f32 v85, v85, v73\n"
  "v_mul_f32 v86, v86, v74\n"
  "v_mfma_f32_16x16x32_bf16 v[168:171], v[224:227], a[128:131], v[168:171]\n"
  "ds_read_b128 v[248:251], v2 offset:21504\n"
  "v_mul_f32 v87, v87, v75\n"
  "v_mfma_f32_16x16x32_bf16 v[168:171], v[228:231], a[136:139], v[168:171]\n"
  "v_mul_f32 v76, 0xbfb8aa3b, v92\n"
  "v_mul_f32 v77, 0xbfb8aa3b, v93\n"
  "v_mfma_f32_16x16x32_bf16 v[168:171], v[232:235], a[144:147], v[168:171]\n"
  "s_waitcnt lgkmcnt(1)\n"
  "ds_read_b128 v[252:255], v2 offset:30720\n"
  "v_mul_f32 v78, 0xbfb8aa3b, v94\n"
  "v_mfma_f32_16x16x32_bf16 v[168:171], v[236:239], a[152:155], v[168:171]\n"
  "v_mul_f32 v79, 0xbfb8aa3b, v95\n"
  "v_mfma_f32_16x16x32_bf16 v[172:175], v[224:227], a[132:135], v[172:175]\n"
  "ds_read_b128 v[224:227], v2 offset:4096\n"
  "v_mfma_f32_16x16x32_bf16 v[172:175], v[228:231], a[140:143], v[172:175]\n"
  "v_exp_f32 v76, v76\n"
  "v_mfma_f32_16x16x32_bf16 v[172:175], v[232:235], a[148:151], v[172:175]\n"
  "ds_read_b128 v[228:231], v2 offset:13312\n"
  "v_mfma_f32_16x16x32_bf16 v[172:175], v[236:239], a[156:159], v[172:175]\n"
  "v_exp_f32 v77, v77\n"
  "v_mfma_f32_16x16x32_bf16 v[176:179], v[240:243], a[128:131], v[176:179]\n"
  "ds_read_b128 v[232:235], v2 offset:22528\n"
  "v_mfma_f32_16x16x32_bf16 v[176:179], v[244:247], a[136:139], v[176:179]\n"
  "s_waitcnt lgkmcnt(3)\n"
  "v_exp_f32 v78, v78\n"
  "v_mfma_f32_16x16x32_bf16 v[176:179], v[248:251], a[144:147], v[176:179]\n"
  "ds_read_b128 v[236:239], v2 offset:31744\n"
  "v_mfma_f32_16x16x32_bf16 v[176:179], v[252:255], a[152:155], v[176:179]\n"
  "v_exp_f32 v79, v79\n"
  "v_mfma_f32_16x16x32_bf16 v[180:183], v[240:243], a[132:135], v[180:183]\n"
  "ds_read_b128 v[240:243], v2 offset:5120\n"
  "v_add_f32 v76, 1.0, v76\n"
  "v_mfma_f32_16x16x32_bf16 v[180:183], v[244:247], a[140:143], v[180:183]\n"
  "v_add_f32 v77, 1.0, v77\n"
  "v_add_f32 v78, 1.0, v78\n"
  "v_mfma_f32_16x16x32_bf16 v[180:183], v[248:251], a[148:151], v[180:183]\n"
  "ds_read_b128 v[244:247], v2 offset:14336\n"
  "v_cvt_pk_bf16_f32 v168, v168, v169\n"
  "v_mfma_f32_16x16x32_bf16 v[180:183], v[252:255], a[156:159], v[180:183]\n"
  "s_waitcnt lgkmcnt(2)\n"
  "v_cvt_pk_bf16_f32 v169, v170, v171\n"
  "v_cvt_pk_bf16_f32 v170, v176, v177\n"
  "v_mfma_f32_16x16x32_bf16 v[184:187], v[224:227], a[128:131], v[184:187]\n"
  "s_add_u32 s83, s68, 1\n"
  "ds_read_b128 v[248:251], v2 offset:23552\n"
  "v_cvt_pk_bf16_f32 v171, v178, v179\n"
  "v_mfma_f32_16x16x32_bf16 v[184:187], v[228:231], a[136:139], v[184:187]\n"
  "s_and_b32 s83, s83, 3\n"
  "v_add_f32 v79, 1.0, v79\n"
  "v_mfma_f32_16x16x32_bf16 v[184:187], v[232:235], a[144:147], v[184:187]\n"
  "s_mul_i32 s83, s83, 49152\n"
  "ds_read_b128 v[252:255], v2 offset:32768\n"
  "v_add_u32 v2, 73728, v2\n"
  "v_mfma_f32_16x16x32_bf16 v[184:187], v[236:239], a[152:155], v[184:187]\n"
  "s_add_u32 s83, s83, s87\n"
  "buffer_store_dwordx4 v[168:171], v1, s[36:39], s83 offen offset:0 sc1\n"
  "v_mfma_f32_16x16x32_bf16 v[188:191], v[224:227], a[132:135], v[188:191]\n"
  "ds_read_b128 v[224:227], v2 offset:6144\n"
  "v_cvt_pk_bf16_f32 v172, v172, v173\n"
  "v_mfma_f32_16x16x32_bf16 v[188:191], v[228:231], a[140:143], v[188:191]\n"
  "v_cvt_pk_bf16_f32 v173, v174, v175\n"
  "v_cvt_pk_bf16_f32 v174, v180, v181\n"
  "v_mfma_f32_16x16x32_bf16 v[188:191], v[232:235], a[148:151], v[188:191]\n"
  "ds_read_b128 v[228:231], v2 offset:15360\n"
  "v_cvt_pk_bf16_f32 v175, v182, v183\n"
  "v_mfma_f32_16x16x32_bf16 v[188:191], v[236:239], a[156:159], v[188:191]\n"
  "s_waitcnt lgkmcnt(2)\n"
  "buffer_store_dwordx4 v[172:175], v1, s[36:39], s83 offen offset:1024 sc1\n"
  "v_mfma_f32_16x16x32_bf16 v[192:195], v[240:243], a[128:131], v[192:195]\n"
  "ds_read_b128 v[232:235], v2 offset:24576\n"
  "v_mfma_f32_16x16x32_bf16 v[192:195], v[244:247], a[136:139], v[192:195]\n"
  "v_rcp_f32 v76, v76\n"
  "v_mfma_f32_16x16x32_bf16 v[192:195], v[248:251], a[144:147], v[192:195]\n"
  "ds_read_b128 v[236:239], v2 offset:33792\n"
  "v_mfma_f32_16x16x32_bf16 v[192:195], v[252:255], a[152:155], v[192:195]\n"
  "v_rcp_f32 v77, v77\n"
  "v_mfma_f32_16x16x32_bf16 v[196:199], v[240:243], a[132:135], v[196:199]\n"
  "ds_read_b128 v[240:243], v2 offset:43008\n"
  "v_mfma_f32_16x16x32_bf16 v[196:199], v[244:247], a[140:143], v[196:199]\n"
  "v_rcp_f32 v78, v78\n"
  "v_mfma_f32_16x16x32_bf16 v[196:199], v[248:251], a[148:151], v[196:199]\n"
  "ds_read_b128 v[244:247], v2 offset:52224\n"
  "v_cvt_pk_bf16_f32 v184, v184, v185\n"
  "v_mfma_f32_16x16x32_bf16 v[196:199], v[252:255], a[156:159], v[196:199]\n"
  "s_waitcnt lgkmcnt(3)\n"
  "v_cvt_pk_bf16_f32 v185, v186, v187\n"
  "v_cvt_pk_bf16_f32 v186, v192, v193\n"
  "v_mfma_f32_16x16x32_bf16 v[200:203], v[224:227], a[192:195], v[200:203]\n"
  "ds_read_b128 v[248:251], v2 offset:61440\n"
  "v_add_u32 v2, 36864, v2\n"
  "v_mfma_f32_16x16x32_bf16 v[200:203], v[228:231], a[200:203], v[200:203]\n"
  "s_add_u32 s83, s68, 2\n"
  "v_cvt_pk_bf16_f32 v187, v194, v195\n"
  "v_mfma_f32_16x16x32_bf16 v[200:203], v[232:235], a[208:211], v[200:203]\n"
  "s_and_b32 s83, s83, 3\n"
  "ds_read_b128 v[252:255], v2 offset:33792\n"
  "v_add_u32 v2, 0xfffe5000, v2\n"
  "s_waitcnt lgkmcnt(2)\n"
  "v_mfma_f32_16x16x32_bf16 v[200:203], v[236:239], a[216:219], v[200:203]\n"
  "s_mul_i32 s83, s83, 49152\n"
  "v_rcp_f32 v79, v79\n"
  "v_mfma_f32_16x16x32_bf16 v[204:207], v[224:227], a[196:199], v[204:207]\n"
  "s_add_u32 s83, s83, s87\n"
  "ds_read_b128 v[224:227], v2 offset:6144\n"
  "v_mul_f32 v92, v92, v76\n"
  "v_mfma_f32_16x16x32_bf16 v[204:207], v[228:231], a[204:207], v[204:207]\n"
  "buffer_store_dwordx4 v[184:187], v1, s[36:39], s83 offen offset:0 sc1\n"
  "v_mfma_f32_16x16x32_bf16 v[204:207], v[232:235], a[212:215], v[204:207]\n"
  "ds_read_b128 v[228:231], v2 offset:15360\n"
  "v_cvt_pk_bf16_f32 v188, v188, v189\n"
  "v_mfma_f32_16x16x32_bf16 v[204:207], v[236:239], a[220:223], v[204:207]\n"
  "v_cvt_pk_bf16_f32 v189, v190, v191\n"
  "v_cvt_pk_bf16_f32 v190, v196, v197\n"
  "v_mfma_f32_16x16x32_bf16 v[200:203], v[240:243], a[224:227], v[200:203]\n"
  "ds_read_b128 v[232:235], v2 offset:24576\n"
  "v_cvt_pk_bf16_f32 v191, v198, v199\n"
  "v_mfma_f32_16x16x32_bf16 v[200:203], v[244:247], a[232:235], v[200:203]\n"
  "s_waitcnt lgkmcnt(2)\n"
  "buffer_store_dwordx4 v[188:191], v1, s[36:39], s83 offen offset:1024 sc1\n"
  "v_mfma_f32_16x16x32_bf16 v[200:203], v[248:251], a[240:243], v[200:203]\n"
  "ds_read_b128 v[236:239], v2 offset:33792\n"
  "v_add_u32 v2, 73728, v2\n"
  "v_mfma_f32_16x16x32_bf16 v[200:203], v[252:255], a[248:251], v[200:203]\n"
  "v_mul_f32 v93, v93, v77\n"
  "v_mul_f32 v94, v94, v78\n"
  "v_mfma_f32_16x16x32_bf16 v[204:207], v[240:243], a[228:231], v[204:207]\n"
  "ds_read_b128 v[240:243], v2 offset:7168\n"
  "v_mul_f32 v95, v95, v79\n"
  "v_mfma_f32_16x16x32_bf16 v[204:207], v[244:247], a[236:239], v[204:207]\n"
  "v_mul_f32 v72, 0xbfb8aa3b, v144\n"
  "v_mul_f32 v73, 0xbfb8aa3b, v145\n"
  "v_mfma_f32_16x16x32_bf16 v[204:207], v[248:251], a[244:247], v[204:207]\n"
  "ds_read_b128 v[244:247], v2 offset:16384\n"
  "v_mul_f32 v74, 0xbfb8aa3b, v146\n"
  "v_mfma_f32_16x16x32_bf16 v[204:207], v[252:255], a[252:255], v[204:207]\n"
  "v_mul_f32 v75, 0xbfb8aa3b, v147\n"
  "v_mfma_f32_16x16x32_bf16 v[200:203], v[224:227], a[128:131], v[200:203]\n"
  "s_waitcnt lgkmcnt(2)\n"
  "ds_read_b128 v[248:251], v2 offset:25600\n"
  "v_mfma_f32_16x16x32_bf16 v[200:203], v[228:231], a[136:139], v[200:203]\n"
  "v_exp_f32 v72, v72\n"
  "v_mfma_f32_16x16x32_bf16 v[200:203], v[232:235], a[144:147], v[200:203]\n"
  "ds_read_b128 v[252:255], v2 offset:34816\n"
  "v_mfma_f32_16x16x32_bf16 v[200:203], v[236:239], a[152:155], v[200:203]\n"
  "v_exp_f32 v73, v73\n"
  "v_mfma_f32_16x16x32_bf16 v[204:207], v[224:227], a[132:135], v[204:207]\n"
  "ds_read_b128 v[224:227], v2 offset:44032\n"
  "v_mfma_f32_16x16x32_bf16 v[204:207], v[228:231], a[140:143], v[204:207]\n"
  "v_exp_f32 v74, v74\n"
  "v_mfma_f32_16x16x32_bf16 v[204:207], v[232:235], a[148:151], v[204:207]\n"
  "ds_read_b128 v[228:231], v2 offset:53248\n"
  "v_mfma_f32_16x16x32_bf16 v[204:207], v[236:239], a[156:159], v[204:207]\n"
  "s_waitcnt lgkmcnt(3)\n"
  "v_exp_f32 v75, v75\n"
  "v_mfma_f32_16x16x32_bf16 v[208:211], v[240:243], a[192:195], v[208:211]\n"
  "ds_read_b128 v[232:235], v2 offset:62464\n"
  "v_add_u32 v2, 36864, v2\n"
  "v_mfma_f32_16x16x32_bf16 v[208:211], v[244:247], a[200:203], v[208:211]\n"
  "v_add_f32 v72, 1.0, v72\n"
  "v_add_f32 v73, 1.0, v73\n"
  "v_mfma_f32_16x16x32_bf16 v[208:211], v[248:251], a[208:211], v[208:211]\n"
  "s_waitcnt lgkmcnt(2)\n"
  "ds_read_b128 v[236:239], v2 offset:34816\n"
  "v_add_u32 v2, 0xfffe5000, v2\n"
  "v_mfma_f32_16x16x32_bf16 v[208:211], v[252:255], a[216:219], v[208:211]\n"
  "v_add_f32 v74, 1.0, v74\n"
  "v_add_f32 v75, 1.0, v75\n"
  "v_mfma_f32_16x16x32_bf16 v[212:215], v[240:243], a[196:199], v[212:215]\n"
  "ds_read_b128 v[240:243], v2 offset:7168\n"
  "v_mfma_f32_16x16x32_bf16 v[212:215], v[244:247], a[204:207], v[212:215]\n"
  "v_rcp_f32 v72, v72\n"
  "v_mfma_f32_16x16x32_bf16 v[212:215], v[248:251], a[212:215], v[212:215]\n"
  "ds_read_b128 v[244:247], v2 offset:16384\n"
  "v_mfma_f32_16x16x32_bf16 v[212:215], v[252:255], a[220:223], v[212:215]\n"
  "v_rcp_f32 v73, v73\n"
  "v_mfma_f32_16x16x32_bf16 v[208:211], v[224:227], a[224:227], v[208:211]\n"
  "s_waitcnt lgkmcnt(2)\n"
  "ds_read_b128 v[248:251], v2 offset:25600\n"
  "v_mfma_f32_16x16x32_bf16 v[208:211], v[228:231], a[232:235], v[208:211]\n"
  "v_rcp_f32 v74, v74\n"
  "v_mfma_f32_16x16x32_bf16 v[208:211], v[232:235], a[240:243], v[208:211]\n"
  "ds_read_b128 v[252:255], v2 offset:34816\n"
  "v_add_u32 v2, 73728, v2\n"
  "v_mfma_f32_16x16x32_bf16 v[208:211], v[236:239], a[248:251], v[208:211]\n"
  "v_rcp_f32 v75, v75\n"
  "v_mfma_f32_16x16x32_bf16 v[212:215], v[224:227], a[228:231], v[212:215]\n"
  "ds_read_b128 v[224:227], v2 offset:8192\n"
  "v_mul_f32 v144, v144, v72\n"
  "v_mfma_f32_16x16x32_bf16 v[212:215], v[228:231], a[236:239], v[212:215]\n"
  "v_mul_f32 v145, v145, v73\n"
  "v_mul_f32 v146, v146, v74\n"
  "v_mfma_f32_16x16x32_bf16 v[212:215], v[232:235], a[244:247], v[212:215]\n"
  "ds_read_b128 v[228:231], v2 offset:17408\n"
  "v_mul_f32 v147, v147, v75\n"
  "v_mfma_f32_16x16x32_bf16 v[212:215], v[236:239], a[252:255], v[212:215]\n"
  "s_waitcnt lgkmcnt(2)\n"
  "v_mul_f32 v76, 0xbfb8aa3b, v148\n"
  "v_mul_f32 v77, 0xbfb8aa3b, v149\n"
  "v_mfma_f32_16x16x32_bf16 v[208:211], v[240:243], a[128:131], v[208:211]\n"
  "ds_read_b128 v[232:235], v2 offset:26624\n"
  "v_mul_f32 v78, 0xbfb8aa3b, v150\n"
  "v_mfma_f32_16x16x32_bf16 v[208:211], v[244:247], a[136:139], v[208:211]\n"
  "v_mul_f32 v79, 0xbfb8aa3b, v151\n"
  "v_mfma_f32_16x16x32_bf16 v[208:211], v[248:251], a[144:147], v[208:211]\n"
  "ds_read_b128 v[236:239], v2 offset:35840\n"
  "v_mfma_f32_16x16x32_bf16 v[208:211], v[252:255], a[152:155], v[208:211]\n"
  "v_exp_f32 v76, v76\n"
  "v_mfma_f32_16x16x32_bf16 v[212:215], v[240:243], a[132:135], v[212:215]\n"
  "ds_read_b128 v[240:243], v2 offset:45056\n"
  "v_mfma_f32_16x16x32_bf16 v[212:215], v[244:247], a[140:143], v[212:215]\n"
  "v_exp_f32 v77, v77\n"
  "v_mfma_f32_16x16x32_bf16 v[212:215], v[248:251], a[148:151], v[212:215]\n"
  "ds_read_b128 v[244:247], v2 offset:54272\n"
  "v_cvt_pk_bf16_f32 v200, v200, v201\n"
  "v_mfma_f32_16x16x32_bf16 v[212:215], v[252:255], a[156:159], v[212:215]\n"
  "s_waitcnt lgkmcnt(3)\n"
  "v_cvt_pk_bf16_f32 v201, v202, v203\n"
  "v_cvt_pk_bf16_f32 v202, v208, v209\n"
  "v_mfma_f32_16x16x32_bf16 v[216:219], v[224:227], a[192:195], v[216:219]\n"
  "ds_read_b128 v[248:251], v2 offset:63488\n"
  "v_add_u32 v2, 36864, v2\n"
  "v_mfma_f32_16x16x32_bf16 v[216:219], v[228:231], a[200:203], v[216:219]\n"
  "s_add_u32 s83, s68, 3\n"
  "v_cvt_pk_bf16_f32 v203, v210, v211\n"
  "v_mfma_f32_16x16x32_bf16 v[216:219], v[232:235], a[208:211], v[216:219]\n"
  "s_and_b32 s83, s83, 3\n"
  "ds_read_b128 v[252:255], v2 offset:35840\n"
  "v_add_u32 v2, 0xfffe5000, v2\n"
  "s_waitcnt lgkmcnt(2)\n"
  "v_mfma_f32_16x16x32_bf16 v[216:219], v[236:239], a[216:219], v[216:219]\n"
  "s_mul_i32 s83, s83, 49152\n"
  "v_exp_f32 v78, v78\n"
  "v_mfma_f32_16x16x32_bf16 v[220:223], v[224:227], a[196:199], v[220:223]\n"
  "s_add_u32 s83, s83, s87\n"
  "ds_read_b128 v[224:227], v2 offset:8192\n"
  "v_mfma_f32_16x16x32_bf16 v[220:223], v[228:231], a[204:207], v[220:223]\n"
  "buffer_store_dwordx4 v[200:203], v1, s[36:39], s83 offen offset:0 sc1\n"
  "v_mfma_f32_16x16x32_bf16 v[220:223], v[232:235], a[212:215], v[220:223]\n"
  "ds_read_b128 v[228:231], v2 offset:17408\n"
  "v_cvt_pk_bf16_f32 v204, v204, v205\n"
  "v_mfma_f32_16x16x32_bf16 v[220:223], v[236:239], a[220:223], v[220:223]\n"
  "v_cvt_pk_bf16_f32 v205, v206, v207\n"
  "v_cvt_pk_bf16_f32 v206, v212, v213\n"
  "v_mfma_f32_16x16x32_bf16 v[216:219], v[240:243], a[224:227], v[216:219]\n"
  "ds_read_b128 v[232:235], v2 offset:26624\n"
  "v_cvt_pk_bf16_f32 v207, v214, v215\n"
  "v_mfma_f32_16x16x32_bf16 v[216:219], v[244:247], a[232:235], v[216:219]\n"
  "s_waitcnt lgkmcnt(2)\n"
  "buffer_store_dwordx4 v[204:207], v1, s[36:39], s83 offen offset:1024 sc1\n"
  "v_mfma_f32_16x16x32_bf16 v[216:219], v[248:251], a[240:243], v[216:219]\n"
  "ds_read_b128 v[236:239], v2 offset:35840\n"
  "v_mfma_f32_16x16x32_bf16 v[216:219], v[252:255], a[248:251], v[216:219]\n"
  "v_exp_f32 v79, v79\n"
  "v_mfma_f32_16x16x32_bf16 v[220:223], v[240:243], a[228:231], v[220:223]\n"
  "ds_read_b128 v[240:243], v2 offset:36864\n"
  "v_add_f32 v76, 1.0, v76\n"
  "v_mfma_f32_16x16x32_bf16 v[220:223], v[244:247], a[236:239], v[220:223]\n"
  "v_add_f32 v77, 1.0, v77\n"
  "v_add_f32 v78, 1.0, v78\n"
  "v_mfma_f32_16x16x32_bf16 v[220:223], v[248:251], a[244:247], v[220:223]\n"
  "ds_read_b128 v[244:247], v2 offset:46080\n"
  "v_add_f32 v79, 1.0, v79\n"
  "v_mfma_f32_16x16x32_bf16 v[220:223], v[252:255], a[252:255], v[220:223]\n"
  "v_rcp_f32 v76, v76\n"
  "v_mfma_f32_16x16x32_bf16 v[216:219], v[224:227], a[128:131], v[216:219]\n"
  "s_waitcnt lgkmcnt(2)\n"
  "ds_read_b128 v[248:251], v2 offset:55296\n"
  "v_mfma_f32_16x16x32_bf16 v[216:219], v[228:231], a[136:139], v[216:219]\n"
  "v_rcp_f32 v77, v77\n"
  "v_mfma_f32_16x16x32_bf16 v[216:219], v[232:235], a[144:147], v[216:219]\n"
  "ds_read_b128 v[252:255], v2 offset:64512\n"
  "v_add_u32 v2, 73728, v2\n"
  "v_mfma_f32_16x16x32_bf16 v[216:219], v[236:239], a[152:155], v[216:219]\n"
  "v_rcp_f32 v78, v78\n"
  "v_mfma_f32_16x16x32_bf16 v[220:223], v[224:227], a[132:135], v[220:223]\n"
  "ds_read_b128 v[224:227], v2 offset:0\n"
  "v_mfma_f32_16x16x32_bf16 v[220:223], v[228:231], a[140:143], v[220:223]\n"
  "v_rcp_f32 v79, v79\n"
  "v_mfma_f32_16x16x32_bf16 v[220:223], v[232:235], a[148:151], v[220:223]\n"
  "ds_read_b128 v[228:231], v2 offset:9216\n"
  "v_mul_f32 v148, v148, v76\n"
  "v_mfma_f32_16x16x32_bf16 v[220:223], v[236:239], a[156:159], v[220:223]\n"
  "s_waitcnt lgkmcnt(2)\n"
  "v_mul_f32 v149, v149, v77\n"
  "v_mul_f32 v150, v150, v78\n"
  "v_mfma_f32_16x16x32_bf16 v[152:155], v[240:243], a[160:163], v[152:155]\n"
  "ds_read_b128 v[232:235], v2 offset:18432\n"
  "v_mul_f32 v151, v151, v79\n"
  "v_mfma_f32_16x16x32_bf16 v[152:155], v[244:247], a[168:171], v[152:155]\n"
  "v_mfma_f32_16x16x32_bf16 v[152:155], v[248:251], a[176:179], v[152:155]\n"
  "ds_read_b128 v[236:239], v2 offset:27648\n"
  "v_cvt_pk_bf16_f32 v208, v216, v217\n"
  "v_mfma_f32_16x16x32_bf16 v[152:155], v[252:255], a[184:187], v[152:155]\n"
  "v_cvt_pk_bf16_f32 v209, v218, v219\n"
  "v_cvt_pk_bf16_f32 v210, v220, v221\n"
  "v_mfma_f32_16x16x32_bf16 v[156:159], v[240:243], a[164:167], v[156:159]\n"
  "s_mul_i32 s83, s82, 49152\n"
  "ds_read_b128 v[240:243], v2 offset:36864\n"
  "v_cvt_pk_bf16_f32 v211, v222, v223\n"
  "v_mfma_f32_16x16x32_bf16 v[156:159], v[244:247], a[172:175], v[156:159]\n"
  "s_add_u32 s83, s83, s87\n"
  "buffer_store_dwordx4 v[208:211], v1, s[36:39], s83 offen offset:2048 sc1\n"
  "v_mfma_f32_16x16x32_bf16 v[156:159], v[248:251], a[180:183], v[156:159]\n"
  "ds_read_b128 v[244:247], v2 offset:46080\n"
  "v_mfma_f32_16x16x32_bf16 v[156:159], v[252:255], a[188:191], v[156:159]\n"
  "s_waitcnt lgkmcnt(2)\n"
  "v_mfma_f32_16x16x32_bf16 v[152:155], v[224:227], a[192:195], v[152:155]\n"
  "ds_read_b128 v[248:251], v2 offset:55296\n"
  "v_mfma_f32_16x16x32_bf16 v[152:155], v[228:231], a[200:203], v[152:155]\n"
  "s_add_u32 s83, s68, 3\n"
  "v_mfma_f32_16x16x32_bf16 v[152:155], v[232:235], a[208:211], v[152:155]\n"
  "s_and_b32 s83, s83, 3\n"
  "ds_read_b128 v[252:255], v2 offset:64512\n"
  "v_add_u32 v2, 0xfffee000, v2\n"
  "v_mfma_f32_16x16x32_bf16 v[152:155], v[236:239], a[216:219], v[152:155]\n"
  "s_mul_i32 s83, s83, 12288\n"
  "v_mfma_f32_16x16x32_bf16 v[156:159], v[224:227], a[196:199], v[156:159]\n"
  "s_add_u32 s83, s83, s88\n"
  "ds_read_b128 v[224:227], v2 offset:0\n"
  "v_mfma_f32_16x16x32_bf16 v[156:159], v[228:231], a[204:207], v[156:159]\n"
  "v_mfma_f32_16x16x32_bf16 v[156:159], v[232:235], a[212:215], v[156:159]\n"
  "ds_read_b128 v[228:231], v2 offset:9216\n"
  "v_mfma_f32_16x16x32_bf16 v[156:159], v[236:239], a[220:223], v[156:159]\n"
  "s_waitcnt lgkmcnt(3)\n"
  "v_mfma_f32_16x16x32_bf16 v[152:155], v[240:243], a[224:227], v[152:155]\n"
  "ds_read_b128 v[232:235], v2 offset:18432\n"
  "v_mfma_f32_16x16x32_bf16 v[152:155], v[244:247], a[232:235], v[152:155]\n"
  "v_mfma_f32_16x16x32_bf16 v[152:155], v[248:251], a[240:243], v[152:155]\n"
  "s_waitcnt lgkmcnt(3)\n"
  "ds_read_b128 v[236:239], v2 offset:27648\n"
  "v_mfma_f32_16x16x32_bf16 v[152:155], v[252:255], a[248:251], v[152:155]\n"
  "buffer_load_dwordx4 v[64:67], v1, s[36:39], s83 offen offset:0 sc1\n"
  "v_mfma_f32_16x16x32_bf16 v[156:159], v[240:243], a[228:231], v[156:159]\n"
  "ds_read_b128 v[240:243], v2 offset:37888\n"
  "v_mfma_f32_16x16x32_bf16 v[156:159], v[244:247], a[236:239], v[156:159]\n"
  "v_mfma_f32_16x16x32_bf16 v[156:159], v[248:251], a[244:247], v[156:159]\n"
  "ds_read_b128 v[244:247], v2 offset:47104\n"
  "v_mfma_f32_16x16x32_bf16 v[156:159], v[252:255], a[252:255], v[156:159]\n"
  "s_waitcnt lgkmcnt(3)\n"
  "buffer_load_dwordx4 v[68:71], v1, s[36:39], s83 offen offset:1024 sc1\n"
  "v_mfma_f32_16x16x32_bf16 v[152:155], v[224:227], a[128:131], v[152:155]\n"
  "ds_read_b128 v[248:251], v2 offset:56320\n"
  "v_add_u32 v2, 36864, v2\n"
  "v_mfma_f32_16x16x32_bf16 v[152:155], v[228:231], a[136:139], v[152:155]\n"
  "buffer_load_dwordx4 v[40:43], v31, s[36:39], s83 offen offset:2048 sc1\n"
  "v_mfma_f32_16x16x32_bf16 v[152:155], v[232:235], a[144:147], v[152:155]\n"
  "s_add_u32 s83, s68, 2\n"
  "ds_read_b128 v[252:255], v2 offset:28672\n"
  "s_waitcnt lgkmcnt(3)\n"
  "v_mfma_f32_16x16x32_bf16 v[152:155], v[236:239], a[152:155], v[152:155]\n"
  "s_and_b32 s83, s83, 3\n"
  "v_mfma_f32_16x16x32_bf16 v[156:159], v[224:227], a[132:135], v[156:159]\n"
  "s_mul_i32 s83, s83, 12288\n"
  "ds_read_b128 v[224:227], v2 offset:37888\n"
  "v_mfma_f32_16x16x32_bf16 v[156:159], v[228:231], a[140:143], v[156:159]\n"
  "s_add_u32 s83, s83, s88\n"
  "v_mfma_f32_16x16x32_bf16 v[156:159], v[232:235], a[148:151], v[156:159]\n"
  "ds_read_b128 v[228:231], v2 offset:47104\n"
  "v_mfma_f32_16x16x32_bf16 v[156:159], v[236:239], a[156:159], v[156:159]\n"
  "v_mfma_f32_16x16x32_bf16 v[160:163], v[240:243], a[160:163], v[160:163]\n"
  "s_waitcnt lgkmcnt(2)\n"
  "ds_read_b128 v[232:235], v2 offset:56320\n"
  "v_add_u32 v2, 36864, v2\n"
  "v_mfma_f32_16x16x32_bf16 v[160:163], v[244:247], a[168:171], v[160:163]\n"
  "buffer_load_dwordx4 v[56:59], v1, s[36:39], s83 offen offset:0 sc1\n"
  "v_mfma_f32_16x16x32_bf16 v[160:163], v[248:251], a[176:179], v[160:163]\n"
  "ds_read_b128 v[236:239], v2 offset:28672\n"
  "v_mfma_f32_16x16x32_bf16 v[160:163], v[252:255], a[184:187], v[160:163]\n"
  "v_mfma_f32_16x16x32_bf16 v[164:167], v[240:243], a[164:167], v[164:167]\n"
  "ds_read_b128 v[240:243], v2 offset:37888\n"
  "v_mfma_f32_16x16x32_bf16 v[164:167], v[244:247], a[172:175], v[164:167]\n"
  "buffer_load_dwordx4 v[60:63], v1, s[36:39], s83 offen offset:1024 sc1\n"
  "v_mfma_f32_16x16x32_bf16 v[164:167], v[248:251], a[180:183], v[164:167]\n"
  "ds_read_b128 v[244:247], v2 offset:47104\n"
  "v_mfma_f32_16x16x32_bf16 v[164:167], v[252:255], a[188:191], v[164:167]\n"
  "s_waitcnt lgkmcnt(3)\n"
  "buffer_load_dwordx4 v[36:39], v31, s[36:39], s83 offen offset:2048 sc1\n"
  "v_mfma_f32_16x16x32_bf16 v[160:163], v[224:227], a[192:195], v[160:163]\n"
  "s_add_u32 s83, s68, 1\n"
  "ds_read_b128 v[248:251], v2 offset:56320\n"
  "v_add_u32 v2, 36864, v2\n"
  "v_mfma_f32_16x16x32_bf16 v[160:163], v[228:231], a[200:203], v[160:163]\n"
  "s_and_b32 s83, s83, 3\n"
  "v_mfma_f32_16x16x32_bf16 v[160:163], v[232:235], a[208:211], v[160:163]\n"
  "s_mul_i32 s83, s83, 12288\n"
  "ds_read_b128 v[252:255], v2 offset:28672\n"
  "v_add_u32 v2, 0xfffe5000, v2\n"
  "s_waitcnt lgkmcnt(2)\n"
  "v_mfma_f32_16x16x32_bf16 v[160:163], v[236:239], a[216:219], v[160:163]\n"
  "s_add_u32 s83, s83, s88\n"
  "v_mfma_f32_16x16x32_bf16 v[164:167], v[224:227], a[196:199], v[164:167]\n"
  "ds_read_b128 v[224:227], v2 offset:1024\n"
  "v_mfma_f32_16x16x32_bf16 v[164:167], v[228:231], a[204:207], v[164:167]\n"
  "v_mfma_f32_16x16x32_bf16 v[164:167], v[232:235], a[212:215], v[164:167]\n"
  "ds_read_b128 v[228:231], v2 offset:10240\n"
  "v_mfma_f32_16x16x32_bf16 v[164:167], v[236:239], a[220:223], v[164:167]\n"
  "v_mfma_f32_16x16x32_bf16 v[160:163], v[240:243], a[224:227], v[160:163]\n"
  "ds_read_b128 v[232:235], v2 offset:19456\n"
  "v_mfma_f32_16x16x32_bf16 v[160:163], v[244:247], a[232:235], v[160:163]\n"
  "s_waitcnt lgkmcnt(3)\n"
  "buffer_load_dwordx4 v[48:51], v1, s[36:39], s83 offen offset:0 sc1\n"
  "v_mfma_f32_16x16x32_bf16 v[160:163], v[248:251], a[240:243], v[160:163]\n"
  "ds_read_b128 v[236:239], v2 offset:28672\n"
  "v_mfma_f32_16x16x32_bf16 v[160:163], v[252:255], a[248:251], v[160:163]\n"
  "v_mfma_f32_16x16x32_bf16 v[164:167], v[240:243], a[228:231], v[164:167]\n"
  "buffer_load_dwordx4 v[52:55], v1, s[36:39], s83 offen offset:1024 sc1\n"
  "v_mfma_f32_16x16x32_bf16 v[164:167], v[244:247], a[236:239], v[164:167]\n"
  "v_mfma_f32_16x16x32_bf16 v[164:167], v[248:251], a[244:247], v[164:167]\n"
  "v_mfma_f32_16x16x32_bf16 v[164:167], v[252:255], a[252:255], v[164:167]\n"
  "s_waitcnt lgkmcnt(2)\n"
  "buffer_load_dwordx4 v[32:35], v31, s[36:39], s83 offen offset:2048 sc1\n"
  "v_mfma_f32_16x16x32_bf16 v[160:163], v[224:227], a[128:131], v[160:163]\n"
  "v_mfma_f32_16x16x32_bf16 v[160:163], v[228:231], a[136:139], v[160:163]\n"
  "s_waitcnt lgkmcnt(1)\n"
  "v_mfma_f32_16x16x32_bf16 v[160:163], v[232:235], a[144:147], v[160:163]\n"
  "s_waitcnt lgkmcnt(0)\n"
  "v_mfma_f32_16x16x32_bf16 v[160:163], v[236:239], a[152:155], v[160:163]\n"
  "v_mfma_f32_16x16x32_bf16 v[164:167], v[224:227], a[132:135], v[164:167]\n"
  "v_mfma_f32_16x16x32_bf16 v[164:167], v[228:231], a[140:143], v[164:167]\n"
  "v_mfma_f32_16x16x32_bf16 v[164:167], v[232:235], a[148:151], v[164:167]\n"
  "v_mfma_f32_16x16x32_bf16 v[164:167], v[236:239], a[156:159], v[164:167]\n"
  "s_lshl_b32 s83, s68, 6\n"
  "s_add_u32 s81, s83, s95\n"
  "s_add_u32 s80, s81, 18432\n"
  "s_add_u32 s79, s95, 18432\n"
  "s_waitcnt vmcnt(8)\n"
  "v_max3_u32 v72, v64, v65, v66\n"
  "s_waitcnt vmcnt(7)\n"
  "v_max3_u32 v72, v72, v67, v68\n"
  "v_max3_u32 v72, v72, v69, v70\n"
  "s_waitcnt vmcnt(6)\n"
  "v_max3_u32 v72, v72, v71, v40\n"
  "v_max3_u32 v72, v72, v41, v42\n"
  "v_max_u32 v72, v72, v43\n"
  "v_cmp_eq_u32 vcc, -1, v72\n"
  "s_nop 1\n s_cbranch_vccnz Ls4_%=\n Lr4_%=:\n"
  "s_waitcnt vmcnt(5)\n"
  "v_max3_u32 v72, v56, v57, v58\n"
  "s_waitcnt vmcnt(4)\n"
  "v_max3_u32 v72, v72, v59, v60\n"
  "v_max3_u32 v72, v72, v61, v62\n"
  "s_waitcnt vmcnt(3)\n"
  "v_max3_u32 v72, v72, v63, v36\n"
  "v_max3_u32 v72, v72, v37, v38\n"
  "v_max_u32 v72, v72, v39\n"
  "v_cmp_eq_u32 vcc, -1, v72\n"
  "s_nop 1\n s_cbranch_vccnz Ls5_%=\n Lr5_%=:\n"
  "s_waitcnt vmcnt(2)\n"
  "v_max3_u32 v72, v48, v49, v50\n"
  "s_waitcnt vmcnt(1)\n"
  "v_max3_u32 v72, v72, v51, v52\n"
  "v_max3_u32 v72, v72, v53, v54\n"
  "s_waitcnt vmcnt(0)\n"
  "v_max3_u32 v72, v72, v55, v32\n"
  "v_max3_u32 v72, v72, v33, v34\n"
  "v_max_u32 v72, v72, v35\n"
  "v_cmp_eq_u32 vcc, -1, v72\n"
  "s_nop 1\n s_cbranch_vccnz Ls6_%=\n Lr6_%=:\n"
  "v_mfma_f32_16x16x32_bf16 v[152:155], v[26:29], v[48:51], v[152:155]\n"
  "v_mfma_f32_16x16x32_bf16 v[152:155], v[26:29], v[56:59], v[152:155]\n"
  "v_mfma_f32_16x16x32_bf16 v[152:155], v[26:29], v[64:67], v[152:155]\n"
  "v_mfma_f32_16x16x32_bf16 v[156:159], v[26:29], v[52:55], v[156:159]\n"
  "v_mfma_f32_16x16x32_bf16 v[156:159], v[26:29], v[60:63], v[156:159]\n"
  "v_mfma_f32_16x16x32_bf16 v[156:159], v[26:29], v[68:71], v[156:159]\n"
  "v_mfma_f32_16x16x32_bf16 v[160:163], v[24:27], v[48:51], v[160:163]\n"
  "v_mfma_f32_16x16x32_bf16 v[160:163], v[24:27], v[56:59], v[160:163]\n"
  "v_mfma_f32_16x16x32_bf16 v[160:163], v[24:27], v[64:67], v[160:163]\n"
  "v_mfma_f32_16x16x32_bf16 v[164:167], v[24:27], v[52:55], v[164:167]\n"
  "v_mfma_f32_16x16x32_bf16 v[164:167], v[24:27], v[60:63], v[164:167]\n"
  "v_mfma_f32_16x16x32_bf16 v[164:167], v[24:27], v[68:71], v[164:167]\n"
  "v_mfma_f32_16x16x32_bf16 v[216:219], v[26:29], v[32:35], v[216:219]\n"
  "v_mfma_f32_16x16x32_bf16 v[216:219], v[26:29], v[36:39], v[216:219]\n"
  "v_mfma_f32_16x16x32_bf16 v[216:219], v[26:29], v[40:43], v[216:219]\n"
  "v_mfma_f32_16x16x32_bf16 v[220:223], v[24:27], v[32:35], v[220:223]\n"
  "v_mfma_f32_16x16x32_bf16 v[220:223], v[24:27], v[36:39], v[220:223]\n"
  "v_mfma_f32_16x16x32_bf16 v[220:223], v[24:27], v[40:43], v[220:223]\n"
  "v_fmac_f32 v96, v152, v80\n"
  "v_fmac_f32 v97, v153, v81\n"
  "v_fmac_f32 v98, v154, v82\n"
  "v_fmac_f32 v99, v155, v83\n"
  "v_cvt_pk_bf16_f32 v16, v96, v97\n"
  "v_cvt_pk_bf16_f32 v17, v98, v99\n"
  "buffer_store_dwordx2 v[16:17], v14, s[56:59], s81 offen offset:0\n"
  "v_fmac_f32 v104, v156, v84\n"
  "v_fmac_f32 v105, v157, v85\n"
  "v_fmac_f32 v106, v158, v86\n"
  "v_fmac_f32 v107, v159, v87\n"
  "v_cvt_pk_bf16_f32 v20, v104, v105\n"
  "v_cvt_pk_bf16_f32 v21, v106, v107\n"
  "buffer_store_dwordx2 v[20:21], v14, s[56:59], s80 offen offset:0\n"
  "v_fmac_f32 v100, v160, v88\n"
  "v_fmac_f32 v101, v161, v89\n"
  "v_fmac_f32 v102, v162, v90\n"
  "v_fmac_f32 v103, v163, v91\n"
  "v_cvt_pk_bf16_f32 v18, v100, v101\n"
  "v_cvt_pk_bf16_f32 v19, v102, v103\n"
  "buffer_store_dwordx2 v[18:19], v14, s[56:59], s81 offen offset:32\n"
  "v_fmac_f32 v108, v164, v92\n"
  "v_fmac_f32 v109, v165, v93\n"
  "v_fmac_f32 v110, v166, v94\n"
  "v_fmac_f32 v111, v167, v95\n"
  "v_cvt_pk_bf16_f32 v44, v108, v109\n"
  "v_cvt_pk_bf16_f32 v45, v110, v111\n"
  "buffer_store_dwordx2 v[44:45], v14, s[56:59], s80 offen offset:32\n"
  "s_cmp_lg_u32 s82, s68\n"
  "s_cbranch_scc1 Lend_%=\n"
  "v_fmac_f32 v112, v216, v144\n"
  "v_fmac_f32 v113, v217, v145\n"
  "v_fmac_f32 v114, v218, v146\n"
  "v_fmac_f32 v115, v219, v147\n"
  "v_cvt_pk_bf16_f32 v46, v112, v113\n"
  "v_cvt_pk_bf16_f32 v47, v114, v115\n"
  "buffer_store_dwordx2 v[46:47], v14, s[56:59], s95 offen offset:256\n"
  "v_fmac_f32 v116, v220, v148\n"
  "v_fmac_f32 v117, v221, v149\n"
  "v_fmac_f32 v118, v222, v150\n"
  "v_fmac_f32 v119, v223, v151\n"
  "v_cvt_pk_bf16_f32 v6, v116, v117\n"
  "v_cvt_pk_bf16_f32 v7, v118, v119\n"
  "buffer_store_dwordx2 v[6:7], v14, s[56:59], s79 offen offset:256\n"
  "Lend_%=:\n"
  "s_or_b32 s83, s93, s82\n"
  "s_cbranch_scc1 Lend2_%=\n"
  "s_mov_b32 s83, 73252864\n"
  "buffer_store_dword v22, v3, s[36:39], s83 offen offset:0 sc1\n"
  "Lend2_%=:\n"
  "s_waitcnt vmcnt(0)\n"
  "s_branch Ldone_%=\n"
  "Ls0_%=:\n"
  "s_mov_b32 s97, 0x20000\n"
  "Ll0_%=:\n"
  "buffer_load_dwordx4 v[72:75], v1, s[36:39], s69 offen sc1\n"
  "s_sub_u32 s97, s97, 1\n"
  "s_waitcnt vmcnt(0)\n"
  "s_cbranch_scc1 Lr0_%=\n"
  "v_xor_b32 v72, s24, v72\n"
  "v_xor_b32 v73, s24, v73\n"
  "v_xor_b32 v74, s24, v74\n"
  "v_xor_b32 v75, s24, v75\n"
  "v_or3_b32 v72, v72, v73, v74\n"
  "v_or_b32 v72, v72, v75\n"
  "v_cmp_ne_u32 vcc, 0, v72\n"
  "s_sleep 8\n"
  "s_cbranch_vccnz Ll0_%=\n"
  "s_branch Lr0_%=\n"
  "Ls1_%=:\n"
  "s_mov_b32 s97, 0x20000\n"
  "Ll1_%=:\n"
  "buffer_load_dword v12, v13, s[36:39], s65 offen offset:0 sc1\n"
  "s_sub_u32 s97, s97, 1\n"
  "s_waitcnt vmcnt(0)\n"
  "s_cbranch_scc1 Lr1_%=\n"
  "v_cmp_ne_u32 vcc, s24, v12\n"
  "s_sleep 4\n"
  "s_cbranch_vccnz Ll1_%=\n"
  "s_branch Lr1_%=\n"
  "Ls2_%=:\n"
  "s_mov_b32 s97, 0x20000\n"
  "Ll2_%=:\n"
  "buffer_load_dword v12, v13, s[36:39], s65 offen offset:32 sc1\n"
  "s_sub_u32 s97, s97, 1\n"
  "s_waitcnt vmcnt(0)\n"
  "s_cbranch_scc1 Lr2_%=\n"
  "v_cmp_ne_u32 vcc, s24, v12\n"
  "s_sleep 4\n"
  "s_cbranch_vccnz Ll2_%=\n"
  "s_branch Lr2_%=\n"
  "Ls3_%=:\n"
  "s_mov_b32 s97, 0x20000\n"
  "Ll3_%=:\n"
  "buffer_load_dword v12, v13, s[36:39], s78 offen offset:0 sc1\n"
  "s_sub_u32 s97, s97, 1\n"
  "s_waitcnt vmcnt(0)\n"
  "s_cbranch_scc1 Lr3_%=\n"
  "v_cmp_ne_u32 vcc, s24, v12\n"
  "s_sleep 4\n"
  "s_cbranch_vccnz Ll3_%=\n"
  "s_branch Lr3_%=\n"
  "Ls4_%=:\n"
  "s_mov_b32 s97, 0x20000\n"
  "Ll4_%=:\n"
  "s_add_u32 s83, s68, 3\n"
  "s_and_b32 s83, s83, 3\n"
  "s_mul_i32 s83, s83, 12288\n"
  "s_add_u32 s83, s83, s88\n"
  "buffer_load_dwordx4 v[64:67], v1, s[36:39], s83 offen offset:0 sc1\n"
  "buffer_load_dwordx4 v[68:71], v1, s[36:39], s83 offen offset:1024 sc1\n"
  "buffer_load_dwordx4 v[40:43], v31, s[36:39], s83 offen offset:2048 sc1\n"
  "s_sub_u32 s97, s97, 1\n"
  "s_waitcnt vmcnt(0)\n"
  "s_cbranch_scc1 Lr4_%=\n"
  "v_max3_u32 v72, v64, v65, v66\n"
  "v_max3_u32 v72, v72, v67, v68\n"
  "v_max3_u32 v72, v72, v69, v70\n"
  "v_max3_u32 v72, v72, v71, v40\n"
  "v_max3_u32 v72, v72, v41, v42\n"
  "v_max_u32 v72, v72, v43\n"
  "v_cmp_eq_u32 vcc, -1, v72\n"
  "s_nop 1\n"
  "s_cbranch_vccnz Ll4_%=\n"
  "s_branch Lr4_%=\n"
  "Ls5_%=:\n"
  "s_mov_b32 s97, 0x20000\n"
  "Ll5_%=:\n"
  "s_add_u32 s83, s68, 2\n"
  "s_and_b32 s83, s83, 3\n"
  "s_mul_i32 s83, s83, 12288\n"
  "s_add_u32 s83, s83, s88\n"
  "buffer_load_dwordx4 v[56:59], v1, s[36:39], s83 offen offset:0 sc1\n"
  "buffer_load_dwordx4 v[60:63], v1, s[36:39], s83 offen offset:1024 sc1\n"
  "buffer_load_dwordx4 v[36:39], v31, s[36:39], s83 offen offset:2048 sc1\n"
  "s_sub_u32 s97, s97, 1\n"
  "s_waitcnt vmcnt(0)\n"
  "s_cbranch_scc1 Lr5_%=\n"
  "v_max3_u32 v72, v56, v57, v58\n"
  "v_max3_u32 v72, v72, v59, v60\n"
  "v_max3_u32 v72, v72, v61, v62\n"
  "v_max3_u32 v72, v72, v63, v36\n"
  "v_max3_u32 v72, v72, v37, v38\n"
  "v_max_u32 v72, v72, v39\n"
  "v_cmp_eq_u32 vcc, -1, v72\n"
  "s_nop 1\n"
  "s_cbranch_vccnz Ll5_%=\n"
  "s_branch Lr5_%=\n"
  "Ls6_%=:\n"
  "s_mov_b32 s97, 0x20000\n"
  "Ll6_%=:\n"
  "s_add_u32 s83, s68, 1\n"
  "s_and_b32 s83, s83, 3\n"
  "s_mul_i32 s83, s83, 12288\n"
  "s_add_u32 s83, s83, s88\n"
  "buffer_load_dwordx4 v[48:51], v1, s[36:39], s83 offen offset:0 sc1\n"
  "buffer_load_dwordx4 v[52:55], v1, s[36:39], s83 offen offset:1024 sc1\n"
  "buffer_load_dwordx4 v[32:35], v31, s[36:39], s83 offen offset:2048 sc1\n"
  "s_sub_u32 s97, s97, 1\n"
  "s_waitcnt vmcnt(0)\n"
  "s_cbranch_scc1 Lr6_%=\n"
  "v_max3_u32 v72, v48, v49, v50\n"
  "v_max3_u32 v72, v72, v51, v52\n"
  "v_max3_u32 v72, v72, v53, v54\n"
  "v_max3_u32 v72, v72, v55, v32\n"
  "v_max3_u32 v72, v72, v33, v34\n"
  "v_max_u32 v72, v72, v35\n"
  "v_cmp_eq_u32 vcc, -1, v72\n"
  "s_nop 1\n"
  "s_cbranch_vccnz Ll6_%=\n"
  "s_branch Lr6_%=\n"
  "Lfall_%=:\n"
  "s_waitcnt vmcnt(0) lgkmcnt(0)\n"
  "s_barrier\n"
  "s_mov_b32 s34, 1\n"
  "v_lshrrev_b32 v128, 4, v0\n"
  "v_bfe_u32 v129, v0, 2, 2\n"
  "v_and_b32 v130, 3, v0\n"
  "v_and_b32 v131, 15, v0\n"
  "v_lshl_add_u32 v132, v128, 2, v129\n"
  "v_add_u32 v133, 0, v128\n"
  "v_xor_b32 v133, v133, v131\n"
  "v_lshlrev_b32 v133, 4, v133\n"
  "v_lshl_add_u32 v22, v128, 8, v133\n"
  "v_add_u32 v133, 4, v128\n"
  "v_xor_b32 v133, v133, v131\n"
  "v_lshlrev_b32 v133, 4, v133\n"
  "v_lshl_add_u32 v23, v128, 8, v133\n"
  "v_add_u32 v133, 8, v128\n"
  "v_xor_b32 v133, v133, v131\n"
  "v_lshlrev_b32 v133, 4, v133\n"
  "v_lshl_add_u32 v24, v128, 8, v133\n"
  "v_add_u32 v133, 12, v128\n"
  "v_xor_b32 v133, v133, v131\n"
  "v_lshlrev_b32 v133, 4, v133\n"
  "v_lshl_add_u32 v25, v128, 8, v133\n"
  "v_add_u32 v133, 0, v128\n"
  "v_lshlrev_b32 v133, 1, v133\n"
  "v_xor_b32 v133, v133, v131\n"
  "v_lshlrev_b32 v133, 4, v133\n"
  "v_lshl_add_u32 v26, v128, 8, v133\n"
  "v_add_u32 v133, 4, v128\n"
  "v_lshlrev_b32 v133, 1, v133\n"
  "v_xor_b32 v133, v133, v131\n"
  "v_lshlrev_b32 v133, 4, v133\n"
  "v_lshl_add_u32 v27, v128, 8, v133\n"
  "v_add_u32 v133, 0, v128\n"
  "v_xor_b32 v133, v133, v131\n"
  "v_lshlrev_b32 v133, 4, v133\n"
  "v_lshl_add_u32 v32, v131, 8, v133\n"
  "v_add_u32 v133, 4, v128\n"
  "v_xor_b32 v133, v133, v131\n"
  "v_lshlrev_b32 v133, 4, v133\n"
  "v_lshl_add_u32 v33, v131, 8, v133\n"
  "v_add_u32 v133, 8, v128\n"
  "v_xor_b32 v133, v133, v131\n"
  "v_lshlrev_b32 v133, 4, v133\n"
  "v_lshl_add_u32 v34, v131, 8, v133\n"
  "v_add_u32 v133, 12, v128\n"
  "v_xor_b32 v133, v133, v131\n"
  "v_lshlrev_b32 v133, 4, v133\n"
  "v_lshl_add_u32 v35, v131, 8, v133\n"
  "v_add_u32 v32, 131072, v32\n"
  "v_add_u32 v33, 131072, v33\n"
  "v_add_u32 v34, 131072, v34\n"
  "v_add_u32 v35, 131072, v35\n"
  "v_mul_u32_u24 v133, 1152, v132\n"
  "v_lshl_add_u32 v133, v130, 3, v133\n"
  "s_mul_i32 s80, s82, 288\n"
  "v_add_u32 v28, s80, v133\n"
  "v_and_b32 v134, 7, v132\n"
  "v_lshlrev_b32 v132, 8, v132\n"
  "v_lshl_add_u32 v132, v130, 3, v132\n"
  "v_xor_b32 v133, 0, v134\n"
  "v_lshl_add_u32 v36, v133, 5, v132\n"
  "v_xor_b32 v133, 1, v134\n"
  "v_lshl_add_u32 v37, v133, 5, v132\n"
  "v_xor_b32 v133, 2, v134\n"
  "v_lshl_add_u32 v38, v133, 5, v132\n"
  "v_xor_b32 v133, 3, v134\n"
  "v_lshl_add_u32 v39, v133, 5, v132\n"
  "v_xor_b32 v133, 4, v134\n"
  "v_lshl_add_u32 v40, v133, 5, v132\n"
  "v_xor_b32 v133, 5, v134\n"
  "v_lshl_add_u32 v41, v133, 5, v132\n"
  "v_xor_b32 v133, 6, v134\n"
  "v_lshl_add_u32 v42, v133, 5, v132\n"
  "v_xor_b32 v133, 7, v134\n"
  "v_lshl_add_u32 v43, v133, 5, v132\n"
  "s_mov_b32 s97, 0\n"
  "Lfl_%=:\n"
  "s_add_u32 m0, s78, 147456\n"
  "s_add_u32 s79, s77, s97\n"
  "buffer_load_dwordx4 v22, s[48:51], s79 offen offset:0 lds\n"
  "buffer_load_dwordx4 v23, s[48:51], s79 offen offset:1024 lds\n"
  "buffer_load_dwordx4 v24, s[48:51], s79 offen offset:2048 lds\n"
  "buffer_load_dwordx4 v25, s[48:51], s79 offen offset:3072 lds\n"
  "s_waitcnt vmcnt(0)\n"
  "s_barrier\n"
  "ds_read_b128 a[128:131], v32 offset:16384\n"
  "ds_read_b128 a[132:135], v33 offset:16384\n"
  "ds_read_b128 a[136:139], v34 offset:16384\n"
  "ds_read_b128 a[140:143], v35 offset:16384\n"
  "ds_read_b128 a[144:147], v32 offset:20480\n"
  "ds_read_b128 a[148:151], v33 offset:20480\n"
  "ds_read_b128 a[152:155], v34 offset:20480\n"
  "ds_read_b128 a[156:159], v35 offset:20480\n"
  "ds_read_b128 a[160:163], v32 offset:24576\n"
  "ds_read_b128 a[164:167], v33 offset:24576\n"
  "ds_read_b128 a[168:171], v34 offset:24576\n"
  "ds_read_b128 a[172:175], v35 offset:24576\n"
  "ds_read_b128 a[176:179], v32 offset:28672\n"
  "ds_read_b128 a[180:183], v33 offset:28672\n"
  "ds_read_b128 a[184:187], v34 offset:28672\n"
  "ds_read_b128 a[188:191], v35 offset:28672\n"
  "s_waitcnt lgkmcnt(13)\n"
  "v_mfma_f32_16x16x32_bf16 v[128:131], a[128:131], v[208:211], 0\n"
  "v_mfma_f32_16x16x32_bf16 v[128:131], a[132:135], v[80:83], v[128:131]\n"
  "v_mfma_f32_16x16x32_bf16 v[128:131], a[136:139], v[96:99], v[128:131]\n"
  "s_waitcnt lgkmcnt(9)\n"
  "v_mfma_f32_16x16x32_bf16 v[128:131], a[140:143], v[112:115], v[128:131]\n"
  "v_mfma_f32_16x16x32_bf16 v[132:135], a[144:147], v[208:211], 0\n"
  "v_mfma_f32_16x16x32_bf16 v[132:135], a[148:151], v[80:83], v[132:135]\n"
  "v_mfma_f32_16x16x32_bf16 v[132:135], a[152:155], v[96:99], v[132:135]\n"
  "s_waitcnt lgkmcnt(4)\n"
  "v_mfma_f32_16x16x32_bf16 v[132:135], a[156:159], v[112:115], v[132:135]\n"
  "v_mfma_f32_16x16x32_bf16 v[136:139], a[160:163], v[208:211], 0\n"
  "v_mfma_f32_16x16x32_bf16 v[136:139], a[164:167], v[80:83], v[136:139]\n"
  "v_mfma_f32_16x16x32_bf16 v[136:139], a[168:171], v[96:99], v[136:139]\n"
  "v_mfma_f32_16x16x32_bf16 v[136:139], a[172:175], v[112:115], v[136:139]\n"
  "s_waitcnt lgkmcnt(0)\n"
  "v_mfma_f32_16x16x32_bf16 v[140:143], a[176:179], v[208:211], 0\n"
  "v_mfma_f32_16x16x32_bf16 v[140:143], a[180:183], v[80:83], v[140:143]\n"
  "v_mfma_f32_16x16x32_bf16 v[140:143], a[184:187], v[96:99], v[140:143]\n"
  "v_mfma_f32_16x16x32_bf16 v[140:143], a[188:191], v[112:115], v[140:143]\n"
  "v_mfma_f32_16x16x32_bf16 v[144:147], a[128:131], v[212:215], 0\n"
  "v_mfma_f32_16x16x32_bf16 v[144:147], a[132:135], v[84:87], v[144:147]\n"
  "v_mfma_f32_16x16x32_bf16 v[144:147], a[136:139], v[100:103], v[144:147]\n"
  "v_mfma_f32_16x16x32_bf16 v[144:147], a[140:143], v[116:119], v[144:147]\n"
  "v_mfma_f32_16x16x32_bf16 v[148:151], a[144:147], v[212:215], 0\n"
  "v_mfma_f32_16x16x32_bf16 v[148:151], a[148:151], v[84:87], v[148:151]\n"
  "v_mfma_f32_16x16x32_bf16 v[148:151], a[152:155], v[100:103], v[148:151]\n"
  "v_mfma_f32_16x16x32_bf16 v[148:151], a[156:159], v[116:119], v[148:151]\n"
  "v_mfma_f32_16x16x32_bf16 v[152:155], a[160:163], v[212:215], 0\n"
  "v_mfma_f32_16x16x32_bf16 v[152:155], a[164:167], v[84:87], v[152:155]\n"
  "v_mfma_f32_16x16x32_bf16 v[152:155], a[168:171], v[100:103], v[152:155]\n"
  "v_mfma_f32_16x16x32_bf16 v[152:155], a[172:175], v[116:119], v[152:155]\n"
  "v_mfma_f32_16x16x32_bf16 v[156:159], a[176:179], v[212:215], 0\n"
  "v_mfma_f32_16x16x32_bf16 v[156:159], a[180:183], v[84:87], v[156:159]\n"
  "v_mfma_f32_16x16x32_bf16 v[156:159], a[184:187], v[100:103], v[156:159]\n"
  "v_mfma_f32_16x16x32_bf16 v[156:159], a[188:191], v[116:119], v[156:159]\n"
  "v_mfma_f32_16x16x32_bf16 v[160:163], a[128:131], v[216:219], 0\n"
  "v_mfma_f32_16x16x32_bf16 v[160:163], a[132:135], v[88:91], v[160:163]\n"
  "v_mfma_f32_16x16x32_bf16 v[160:163], a[136:139], v[104:107], v[160:163]\n"
  "v_mfma_f32_16x16x32_bf16 v[160:163], a[140:143], v[120:123], v[160:163]\n"
  "v_mfma_f32_16x16x32_bf16 v[164:167], a[144:147], v[216:219], 0\n"
  "v_mfma_f32_16x16x32_bf16 v[164:167], a[148:151], v[88:91], v[164:167]\n"
  "v_mfma_f32_16x16x32_bf16 v[164:167], a[152:155], v[104:107], v[164:167]\n"
  "v_mfma_f32_16x16x32_bf16 v[164:167], a[156:159], v[120:123], v[164:167]\n"
  "v_mfma_f32_16x16x32_bf16 v[168:171], a[160:163], v[216:219], 0\n"
  "v_mfma_f32_16x16x32_bf16 v[168:171], a[164:167], v[88:91], v[168:171]\n"
  "v_mfma_f32_16x16x32_bf16 v[168:171], a[168:171], v[104:107], v[168:171]\n"
  "v_mfma_f32_16x16x32_bf16 v[168:171], a[172:175], v[120:123], v[168:171]\n"
  "v_mfma_f32_16x16x32_bf16 v[172:175], a[176:179], v[216:219], 0\n"
  "v_mfma_f32_16x16x32_bf16 v[172:175], a[180:183], v[88:91], v[172:175]\n"
  "v_mfma_f32_16x16x32_bf16 v[172:175], a[184:187], v[104:107], v[172:175]\n"
  "v_mfma_f32_16x16x32_bf16 v[172:175], a[188:191], v[120:123], v[172:175]\n"
  "v_mfma_f32_16x16x32_bf16 v[176:179], a[128:131], v[220:223], 0\n"
  "v_mfma_f32_16x16x32_bf16 v[176:179], a[132:135], v[92:95], v[176:179]\n"
  "v_mfma_f32_16x16x32_bf16 v[176:179], a[136:139], v[108:111], v[176:179]\n"
  "v_mfma_f32_16x16x32_bf16 v[176:179], a[140:143], v[124:127], v[176:179]\n"
  "v_mfma_f32_16x16x32_bf16 v[180:183], a[144:147], v[220:223], 0\n"
  "v_mfma_f32_16x16x32_bf16 v[180:183], a[148:151], v[92:95], v[180:183]\n"
  "v_mfma_f32_16x16x32_bf16 v[180:183], a[152:155], v[108:111], v[180:183]\n"
  "v_mfma_f32_16x16x32_bf16 v[180:183], a[156:159], v[124:127], v[180:183]\n"
  "v_mfma_f32_16x16x32_bf16 v[184:187], a[160:163], v[220:223], 0\n"
  "v_mfma_f32_16x16x32_bf16 v[184:187], a[164:167], v[92:95], v[184:187]\n"
  "v_mfma_f32_16x16x32_bf16 v[184:187], a[168:171], v[108:111], v[184:187]\n"
  "v_mfma_f32_16x16x32_bf16 v[184:187], a[172:175], v[124:127], v[184:187]\n"
  "v_mfma_f32_16x16x32_bf16 v[188:191], a[176:179], v[220:223], 0\n"
  "v_mfma_f32_16x16x32_bf16 v[188:191], a[180:183], v[92:95], v[188:191]\n"
  "v_mfma_f32_16x16x32_bf16 v[188:191], a[184:187], v[108:111], v[188:191]\n"
  "v_mfma_f32_16x16x32_bf16 v[188:191], a[188:191], v[124:127], v[188:191]\n"
  "v_max3_f32 v68, v68, v128, v129\n"
  "v_max3_f32 v68, v68, v130, v131\n"
  "v_max3_f32 v68, v68, v132, v133\n"
  "v_max3_f32 v68, v68, v134, v135\n"
  "v_max3_f32 v68, v68, v136, v137\n"
  "v_max3_f32 v68, v68, v138, v139\n"
  "v_max3_f32 v68, v68, v140, v141\n"
  "v_max3_f32 v68, v68, v142, v143\n"
  "v_max3_f32 v69, v69, v144, v145\n"
  "v_max3_f32 v69, v69, v146, v147\n"
  "v_max3_f32 v69, v69, v148, v149\n"
  "v_max3_f32 v69, v69, v150, v151\n"
  "v_max3_f32 v69, v69, v152, v153\n"
  "v_max3_f32 v69, v69, v154, v155\n"
  "v_max3_f32 v69, v69, v156, v157\n"
  "v_max3_f32 v69, v69, v158, v159\n"
  "v_max3_f32 v70, v70, v160, v161\n"
  "v_max3_f32 v70, v70, v162, v163\n"
  "v_max3_f32 v70, v70, v164, v165\n"
  "v_max3_f32 v70, v70, v166, v167\n"
  "v_max3_f32 v70, v70, v168, v169\n"
  "v_max3_f32 v70, v70, v170, v171\n"
  "v_max3_f32 v70, v70, v172, v173\n"
  "v_max3_f32 v70, v70, v174, v175\n"
  "v_max3_f32 v71, v71, v176, v177\n"
  "v_max3_f32 v71, v71, v178, v179\n"
  "v_max3_f32 v71, v71, v180, v181\n"
  "v_max3_f32 v71, v71, v182, v183\n"
  "v_max3_f32 v71, v71, v184, v185\n"
  "v_max3_f32 v71, v71, v186, v187\n"
  "v_max3_f32 v71, v71, v188, v189\n"
  "v_max3_f32 v71, v71, v190, v191\n"
  "s_barrier\n"
  "s_add_u32 s97, s97, 16384\n"
  "s_cmp_lt_u32 s97, 131072\n"
  "s_cbranch_scc1 Lfl_%=\n"
  "v_mov_b32 v72, v68\n"
  "s_nop 1\n"
  "v_permlane32_swap_b32 v68, v72\n"
  "v_max_f32 v68, v68, v72\n"
  "v_mov_b32 v72, v68\n"
  "s_nop 1\n"
  "v_permlane16_swap_b32 v68, v72\n"
  "v_max_f32 v68, v68, v72\n"
  "v_mov_b32 v72, v69\n"
  "s_nop 1\n"
  "v_permlane32_swap_b32 v69, v72\n"
  "v_max_f32 v69, v69, v72\n"
  "v_mov_b32 v72, v69\n"
  "s_nop 1\n"
  "v_permlane16_swap_b32 v69, v72\n"
  "v_max_f32 v69, v69, v72\n"
  "v_mov_b32 v72, v70\n"
  "s_nop 1\n"
  "v_permlane32_swap_b32 v70, v72\n"
  "v_max_f32 v70, v70, v72\n"
  "v_mov_b32 v72, v70\n"
  "s_nop 1\n"
  "v_permlane16_swap_b32 v70, v72\n"
  "v_max_f32 v70, v70, v72\n"
  "v_mov_b32 v72, v71\n"
  "s_nop 1\n"
  "v_permlane32_swap_b32 v71, v72\n"
  "v_max_f32 v71, v71, v72\n"
  "v_mov_b32 v72, v71\n"
  "s_nop 1\n"
  "v_permlane16_swap_b32 v71, v72\n"
  "v_max_f32 v71, v71, v72\n"
  "s_add_u32 m0, s78, 147456\n"
  "s_add_u32 s79, s77, 0\n"
  "buffer_load_dwordx4 v22, s[48:51], s79 offen offset:0 lds\n"
  "buffer_load_dwordx4 v23, s[48:51], s79 offen offset:1024 lds\n"
  "buffer_load_dwordx4 v24, s[48:51], s79 offen offset:2048 lds\n"
  "buffer_load_dwordx4 v25, s[48:51], s79 offen offset:3072 lds\n"
  "s_add_u32 m0, s78, 49152\n"
  "s_add_u32 s79, s77, 0\n"
  "buffer_load_dwordx4 v26, s[52:55], s79 offen offset:0 lds\n"
  "buffer_load_dwordx4 v27, s[52:55], s79 offen offset:1024 lds\n"
  "buffer_load_dwordx4 v26, s[52:55], s79 offen offset:2048 lds\n"
  "buffer_load_dwordx4 v27, s[52:55], s79 offen offset:3072 lds\n"
  "s_add_u32 m0, s78, 65536\n"
  "s_add_u32 s79, s77, 16384\n"
  "buffer_load_dwordx4 v22, s[48:51], s79 offen offset:0 lds\n"
  "buffer_load_dwordx4 v23, s[48:51], s79 offen offset:1024 lds\n"
  "buffer_load_dwordx4 v24, s[48:51], s79 offen offset:2048 lds\n"
  "buffer_load_dwordx4 v25, s[48:51], s79 offen offset:3072 lds\n"
  "s_add_u32 m0, s78, 81920\n"
  "s_add_u32 s79, s77, 16384\n"
  "buffer_load_dwordx4 v26, s[52:55], s79 offen offset:0 lds\n"
  "buffer_load_dwordx4 v27, s[52:55], s79 offen offset:1024 lds\n"
  "buffer_load_dwordx4 v26, s[52:55], s79 offen offset:2048 lds\n"
  "buffer_load_dwordx4 v27, s[52:55], s79 offen offset:3072 lds\n"
  "s_waitcnt vmcnt(0)\n"
  "s_barrier\n"
  "ds_read_b128 a[128:131], v32 offset:16384\n"
  "ds_read_b128 a[132:135], v33 offset:16384\n"
  "ds_read_b128 a[136:139], v34 offset:16384\n"
  "ds_read_b128 a[140:143], v35 offset:16384\n"
  "ds_read_b128 a[144:147], v32 offset:20480\n"
  "ds_read_b128 a[148:151], v33 offset:20480\n"
  "ds_read_b128 a[152:155], v34 offset:20480\n"
  "ds_read_b128 a[156:159], v35 offset:20480\n"
  "ds_read_b128 a[160:163], v32 offset:24576\n"
  "ds_read_b128 a[164:167], v33 offset:24576\n"
  "ds_read_b128 a[168:171], v34 offset:24576\n"
  "ds_read_b128 a[172:175], v35 offset:24576\n"
  "ds_read_b128 a[176:179], v32 offset:28672\n"
  "ds_read_b128 a[180:183], v33 offset:28672\n"
  "ds_read_b128 a[184:187], v34 offset:28672\n"
  "ds_read_b128 a[188:191], v35 offset:28672\n"
  "s_waitcnt vmcnt(0) lgkmcnt(0)\n"
  "s_barrier\n"
  "s_branch Lattn_%=\n"
  "Ldone_%=:\n"
  :: "s"(LO(ws)), "s"(HI(ws)), "s"(LO(w1)), "s"(HI(w1)), "s"(LO(wq)), "s"(HI(wq)), "s"(LO(kc)), "s"(HI(kc)), "s"(LO(vc)), "s"(HI(vc)),
     "s"(LO(wo)), "s"(HI(wo)), "s"(LO(w2)), "s"(HI(w2)), "s"(LO(x)), "s"(HI(x)), "s"(LO(out)), "s"(HI(out)), "s"(w), "s"(blockIdx.x)
  :
  "v0","v1","v2","v3","v4","v5","v6","v7","v8","v9","v10","v11","v12","v13","v14","v15","v16","v17","v18","v19","v20","v21","v22","v23",
  "v24","v25","v26","v27","v28","v29","v30","v31","v32","v33","v34","v35","v36","v37","v38","v39","v40","v41","v42","v43","v44","v45","v46","v47",
  "v48","v49","v50","v51","v52","v53","v54","v55","v56","v57","v58","v59","v60","v61","v62","v63","v64","v65","v66","v67","v68","v69","v70","v71",
  "v72","v73","v74","v75","v76","v77","v78","v79","v80","v81","v82","v83","v84","v85","v86","v87","v88","v89","v90","v91","v92","v93","v94","v95",
  "v96","v97","v98","v99","v100","v101","v102","v103","v104","v105","v106","v107","v108","v109","v110","v111","v112","v113","v114","v115","v116","v117","v118","v119",
  "v120","v121","v122","v123","v124","v125","v126","v127","v128","v129","v130","v131","v132","v133","v134","v135","v136","v137","v138","v139","v140","v141","v142","v143",
  "v144","v145","v146","v147","v148","v149","v150","v151","v152","v153","v154","v155","v156","v157","v158","v159","v160","v161","v162","v163","v164","v165","v166","v167",
  "v168","v169","v170","v171","v172","v173","v174","v175","v176","v177","v178","v179","v180","v181","v182","v183","v184","v185","v186","v187","v188","v189","v190","v191",
  "v192","v193","v194","v195","v196","v197","v198","v199","v200","v201","v202","v203","v204","v205","v206","v207","v208","v209","v210","v211","v212","v213","v214","v215",
  "v216","v217","v218","v219","v220","v221","v222","v223","v224","v225","v226","v227","v228","v229","v230","v231","v232","v233","v234","v235","v236","v237","v238","v239",
  "v240","v241","v242","v243","v244","v245","v246","v247","v248","v249","v250","v251","v252","v253","v254","v255","a0","a1","a2","a3","a4","a5","a6","a7",
  "a8","a9","a10","a11","a12","a13","a14","a15","a16","a17","a18","a19","a20","a21","a22","a23","a24","a25","a26","a27","a28","a29","a30","a31",
  "a32","a33","a34","a35","a36","a37","a38","a39","a40","a41","a42","a43","a44","a45","a46","a47","a48","a49","a50","a51","a52","a53","a54","a55",
  "a56","a57","a58","a59","a60","a61","a62","a63","a64","a65","a66","a67","a68","a69","a70","a71","a72","a73","a74","a75","a76","a77","a78","a79",
  "a80","a81","a82","a83","a84","a85","a86","a87","a88","a89","a90","a91","a92","a93","a94","a95","a96","a97","a98","a99","a100","a101","a102","a103",
  "a104","a105","a106","a107","a108","a109","a110","a111","a112","a113","a114","a115","a116","a117","a118","a119","a120","a121","a122","a123","a124","a125","a126","a127",
  "a128","a129","a130","a131","a132","a133","a134","a135","a136","a137","a138","a139","a140","a141","a142","a143","a144","a145","a146","a147","a148","a149","a150","a151",
  "a152","a153","a154","a155","a156","a157","a158","a159","a160","a161","a162","a163","a164","a165","a166","a167","a168","a169","a170","a171","a172","a173","a174","a175",
  "a176","a177","a178","a179","a180","a181","a182","a183","a184","a185","a186","a187","a188","a189","a190","a191","a192","a193","a194","a195","a196","a197","a198","a199",
  "a200","a201","a202","a203","a204","a205","a206","a207","a208","a209","a210","a211","a212","a213","a214","a215","a216","a217","a218","a219","a220","a221","a222","a223",
  "a224","a225","a226","a227","a228","a229","a230","a231","a232","a233","a234","a235","a236","a237","a238","a239","a240","a241","a242","a243","a244","a245","a246","a247",
  "a248","a249","a250","a251","a252","a253","a254","a255","s22","s23","s24","s25","s26","s27","s28","s29","s30","s31","s32","s33","s34","s35","s36","s37",
  "s38","s39","s40","s41","s42","s43","s44","s45","s46","s47","s48","s49","s50","s51","s52","s53","s54","s55","s56","s57","s58","s59","s60","s61",
  "s62","s63","s64","s65","s66","s67","s68","s69","s70","s71","s72","s73","s74","s75","s76","s77","s78","s79","s80","s81","s82","s83","s84","s85",
  "s86","s87","s88","s89","s90","s91","s92","s93","s94","s95","s96","s97","s98","s99","vcc","scc","memory"
  );
}

extern "C" void kernel_launch(void* const* d_in, const int* in_sizes, int n_in, void* d_out, int out_size, void* d_ws, size_t ws_size, hipStream_t stream) {
  char* ws = (char*)d_ws + WS_BASE;
  (void)hipFuncSetAttribute((const void*)km_main, hipFuncAttributeMaxDynamicSharedMemorySize, 163840);
  km_main<<<256, 256, 163840, stream>>>(ws, d_in[6], d_in[3], d_in[1], d_in[2], d_in[4], d_in[7], d_in[0], d_out);
}
```
